```python
import torch

def _softmax(x, axis=-1):
    return torch.softmax(x, dim=axis)
def _gelu(x):
    return torch.nn.functional.gelu(x, approximate="tanh")
def _silu(x):
    return torch.nn.functional.silu(x)
def _sigmoid(x):
    return torch.sigmoid(x)
def _layernorm(x, axis=-1, eps=1e-5):
    m = x.mean(dim=axis, keepdim=True)
    v = x.var(dim=axis, keepdim=True, unbiased=False)
    return (x - m) / torch.sqrt(v + eps)
def _scatter_add(tgt, idx, src, axis=0):
    return tgt.clone().index_add_(axis, idx, src)
def _segment_sum(src, seg, n):
    out = torch.zeros((n,) + src.shape[1:], dtype=src.dtype)
    return out.index_add_(0, seg, src)
def _pad(x, pw):
    flat = tuple(v for pair in reversed(pw) for v in pair)
    return torch.nn.functional.pad(x, flat)

kernel_name = 'interleave_layers__linear_kernel_hedgehog__sliding_local_dilated__prompt'

def setup_inputs():

    _w = (lambda a: (lambda t: t.to(torch.bfloat16) if t.is_floating_point() else t)(torch.as_tensor(a)))
    def _A_setup():
        B, S, D, H = 1, 2048, 1024, 32
        Dh = D // H
        return {
            "x": _w((lambda *s: torch.randn(*s, dtype=torch.bfloat16) * 0.05)(B, S, D)),
            "wqkv": _w((lambda *s: torch.randn(*s, dtype=torch.bfloat16) * 0.2)(D, 3 * D)),
            "w_phi": _w((lambda *s: torch.randn(*s, dtype=torch.bfloat16) * 0.2)(H, Dh, Dh) * (Dh ** -0.5)),
            "wo": _w((lambda *s: torch.randn(*s, dtype=torch.bfloat16) * 0.05)(D, D)),
        }
    _A_in = _A_setup()

    def _B_setup():
        B, S, D = 1, 2048, 1024
        return {
            "x": _w((lambda *s: torch.randn(*s, dtype=torch.bfloat16) * 0.05)(B, S, D)),
            "wqkv": _w((lambda *s: torch.randn(*s, dtype=torch.bfloat16) * 0.1)(D, 3 * D)),
            "wo": _w((lambda *s: torch.randn(*s, dtype=torch.bfloat16) * 0.05)(D, D)),
        }
    _B_in = _B_setup()

    return {
        "A_x": _A_in["x"],
        "A_wqkv": _A_in["wqkv"],
        "A_w_phi": _A_in["w_phi"],
        "A_wo": _A_in["wo"],
        "B_x": _B_in["x"],
        "B_wqkv": _B_in["wqkv"],
        "B_wo": _B_in["wo"],
    }

def reference(A_x, A_wqkv, A_w_phi, A_wo, B_x, B_wqkv, B_wo):
    def _A_ref(x, wqkv, w_phi, wo):
        B, S, D = x.shape
        H = 32; Dh = D // H
        qkv = (lambda x, *d: x.permute(*d))((x @ wqkv).reshape(B, S, 3, H, Dh), 2, 0, 3, 1, 4)
        q, k, v = qkv[0], qkv[1], qkv[2]
        pq = torch.einsum("bhsd,hdf->bhsf", q, w_phi)
        pk = torch.einsum("bhsd,hdf->bhsf", k, w_phi)
        phi_q = torch.concatenate([_softmax(pq, axis=-1), _softmax(-pq, axis=-1)], axis=-1)
        phi_k = torch.concatenate([_softmax(pk, axis=-1), _softmax(-pk, axis=-1)], axis=-1)
        kv = torch.einsum("bhsf,bhsd->bhfd", phi_k, v)
        z  = torch.einsum("bhsf->bhf", phi_k)
        num = torch.einsum("bhqf,bhfd->bhqd", phi_q, kv)
        den = torch.einsum("bhqf,bhf->bhq", phi_q, z)[..., None] + 1e-6
        o = num / den
        return (lambda x, *d: x.permute(*d))(o, 0, 2, 1, 3).reshape(B, S, D) @ wo

    def _B_ref(x, wqkv, wo):
        B, S, D = x.shape
        H = 32; Dh = D // H
        W, dil = 8, 2
        qkv = (lambda x, *d: x.permute(*d))((x @ wqkv).reshape(B, S, 3, H, Dh), 2, 0, 3, 1, 4)
        q, k, v = qkv[0], qkv[1], qkv[2]
        scale = Dh ** -0.5
        pos = torch.arange(S)
        off = torch.arange(W) * dil
        idx = pos[:, None] - off[None, :]
        mask = idx >= 0
        idx_c = torch.where(mask, idx, 0).reshape(-1)
        kg = k[:, :, idx_c, :].reshape(B, H, S, W, Dh)
        vg = v[:, :, idx_c, :].reshape(B, H, S, W, Dh)
        logits = torch.einsum("bhsd,bhswd->bhsw", q, kg) * scale
        logits = torch.where(mask[None, None, :, :], logits, -1e9)
        attn = _softmax(logits, axis=-1)
        o = torch.einsum("bhsw,bhswd->bhsd", attn, vg)
        return (lambda x, *d: x.permute(*d))(o, 0, 2, 1, 3).reshape(B, S, D) @ wo

    x = A_x
    x = x + _A_ref(_layernorm(x, axis=-1), A_wqkv, A_w_phi, A_wo)
    x = x + _B_ref(_layernorm(x, axis=-1), B_wqkv, B_wo)
    x = x + _A_ref(_layernorm(x, axis=-1), A_wqkv, A_w_phi, A_wo)
    x = x + _B_ref(_layernorm(x, axis=-1), B_wqkv, B_wo)
    return x
```

```cpp
#include <hip/hip_runtime.h>
#include <stdint.h>
#include <type_traits>

#define DI __device__ __forceinline__
#define AI __attribute__((always_inline))
typedef unsigned short u16;
typedef __attribute__((ext_vector_type(8))) short bf16x8;
typedef __attribute__((ext_vector_type(4))) short s16x4;
typedef __attribute__((ext_vector_type(4))) float f32x4;
typedef __attribute__((ext_vector_type(2))) float f32x2;
typedef __attribute__((ext_vector_type(2))) __bf16 bf2;
typedef __attribute__((ext_vector_type(4))) unsigned u32x4;
typedef __attribute__((ext_vector_type(2))) unsigned u32x2;
typedef __attribute__((address_space(3))) s16x4* lds_s16x4_ptr;

constexpr float LOG2E = 1.4426950408889634f;

DI unsigned pk2(float a, float b) {
  f32x2 v = {a, b};
  bf2 r = __builtin_convertvector(v, bf2);
  return __builtin_bit_cast(unsigned, r);
}
DI float bflo(unsigned u) { return __uint_as_float(u << 16); }
DI float bfhi(unsigned u) { return __uint_as_float(u & 0xffff0000u); }
DI f32x4 mfma16(bf16x8 a, bf16x8 b, f32x4 c) { return __builtin_amdgcn_mfma_f32_16x16x32_bf16(a, b, c, 0, 0, 0); }
DI void mfma_acc(f32x4& c, bf16x8 a, bf16x8 b) { asm volatile("v_mfma_f32_16x16x32_bf16 %0, %1, %2, %0" : "+v"(c) : "v"(a), "v"(b)); }
DI void mfma_done() { asm volatile("s_nop 7\n\ts_nop 1" ::: "memory"); __builtin_amdgcn_sched_barrier(0); }
DI bf16x8 as_frag(u32x4 v) { return __builtin_bit_cast(bf16x8, v); }
DI float dot2(unsigned a, unsigned b, float c) {
  return __builtin_amdgcn_fdot2_f32_bf16(__builtin_bit_cast(bf2, a), __builtin_bit_cast(bf2, b), c, false);
}
DI u32x2 trread(const char* p) { return __builtin_bit_cast(u32x2, __builtin_amdgcn_ds_read_tr16_b64_v4i16((lds_s16x4_ptr)p)); }
DI float fmx(float a, float b) { return __builtin_elementwise_maximum(a, b); }
DI float fmn(float a, float b) { return __builtin_elementwise_minimum(a, b); }

DI float fma1(float a, float b, float c) { float r; asm("v_fma_f32 %0, %1, %2, %3" : "=v"(r) : "v"(a), "v"(b), "v"(c)); return r; }
DI float mul1(float a, float b) { float r; asm("v_mul_f32 %0, %1, %2" : "=v"(r) : "v"(a), "v"(b)); return r; }
DI float add1(float a, float b) { float r; asm("v_add_f32 %0, %1, %2" : "=v"(r) : "v"(a), "v"(b)); return r; }
template <bool S> DI f32x4 fma4(f32x4 a, float b, f32x4 c) {
  if (S) return f32x4{fma1(a[0], b, c[0]), fma1(a[1], b, c[1]), fma1(a[2], b, c[2]), fma1(a[3], b, c[3])};
  return c + a * b;
}
template <bool S> DI f32x4 mul4(f32x4 a, f32x4 b) {
  if (S) return f32x4{mul1(a[0], b[0]), mul1(a[1], b[1]), mul1(a[2], b[2]), mul1(a[3], b[3])};
  return a * b;
}
template <bool S> DI f32x4 add4(f32x4 a, f32x4 b) {
  if (S) return f32x4{add1(a[0], b[0]), add1(a[1], b[1]), add1(a[2], b[2]), add1(a[3], b[3])};
  return a + b;
}
DI float xsum(float x) {
  auto r = __builtin_amdgcn_permlane16_swap(__float_as_uint(x), __float_as_uint(x), false, false);
  x = __uint_as_float(r[0]) + __uint_as_float(r[1]);
  auto q = __builtin_amdgcn_permlane32_swap(__float_as_uint(x), __float_as_uint(x), false, false);
  return __uint_as_float(q[0]) + __uint_as_float(q[1]);
}
DI float xmax(float x) {
  auto r = __builtin_amdgcn_permlane16_swap(__float_as_uint(x), __float_as_uint(x), false, false);
  x = fmx(__uint_as_float(r[0]), __uint_as_float(r[1]));
  auto q = __builtin_amdgcn_permlane32_swap(__float_as_uint(x), __float_as_uint(x), false, false);
  return fmx(__uint_as_float(q[0]), __uint_as_float(q[1]));
}
template <bool IS_MAX, bool INV = false>
DI void xred2(float& a, float& b) {
  auto r = __builtin_amdgcn_permlane32_swap(__float_as_uint(a), __float_as_uint(b), false, false);
  float c = IS_MAX ? fmx(__uint_as_float(r[0]), __uint_as_float(r[1])) : __uint_as_float(r[0]) + __uint_as_float(r[1]);
  auto q = __builtin_amdgcn_permlane16_swap(__float_as_uint(c), __float_as_uint(c), false, false);
  c = IS_MAX ? fmx(__uint_as_float(q[0]), __uint_as_float(q[1])) : __uint_as_float(q[0]) + __uint_as_float(q[1]);
  if (INV) c = __builtin_amdgcn_rcpf(c);
  auto w = __builtin_amdgcn_permlane32_swap(__float_as_uint(c), __float_as_uint(c), false, false);
  a = __uint_as_float(w[0]); b = __uint_as_float(w[1]);
}

DI unsigned xt_off(int mt, int ks) { return (unsigned)((((mt >> 4) * 32 + ks) * 16 + (mt & 15)) << 10); }

#define DPOL " sc1"
#define GLDS_PRE "s_mov_b32 %0, m0\n\ts_mov_b32 m0, %3\n\ts_nop 2\n\t"
#define GLDS_POST "s_mov_b32 m0, %0"
#define GLDS_OPS : "=&s"(keep) : "v"(voff), "s"(sbase), "s"(lds) : "memory"
#define GL1(c) "global_load_lds_dwordx4 %1, %2" c "\n\t"
#define GL2(c) GL1(c) "global_load_lds_dwordx4 %1, %2 offset:1024" c "\n\t"
#define GL3(c) GL2(c) "global_load_lds_dwordx4 %1, %2 offset:2048" c "\n\t"
#define GL4(c) GL3(c) "global_load_lds_dwordx4 %1, %2 offset:3072" c "\n\t"
template <int N, bool SC1 = false>
DI void glds_run(unsigned voff, const void* sbase, unsigned lds) {
  unsigned keep;
  if constexpr (SC1) {
    if constexpr (N == 1) asm volatile(GLDS_PRE GL1(DPOL) GLDS_POST GLDS_OPS);
    else if constexpr (N == 2) asm volatile(GLDS_PRE GL2(DPOL) GLDS_POST GLDS_OPS);
    else if constexpr (N == 3) asm volatile(GLDS_PRE GL3(DPOL) GLDS_POST GLDS_OPS);
    else asm volatile(GLDS_PRE GL4(DPOL) GLDS_POST GLDS_OPS);
  } else {
    if constexpr (N == 1) asm volatile(GLDS_PRE GL1("") GLDS_POST GLDS_OPS);
    else if constexpr (N == 2) asm volatile(GLDS_PRE GL2("") GLDS_POST GLDS_OPS);
    else if constexpr (N == 3) asm volatile(GLDS_PRE GL3("") GLDS_POST GLDS_OPS);
    else asm volatile(GLDS_PRE GL4("") GLDS_POST GLDS_OPS);
  }
}
DI void glds16v(const void* gsrc, unsigned lds_dst) {
  unsigned keep;
  asm volatile("s_mov_b32 %0, m0\n\ts_mov_b32 m0, %2\n\ts_nop 0\n\tglobal_load_lds_dwordx4 %1, off\n\ts_mov_b32 m0, %0"
               : "=&s"(keep) : "v"(gsrc), "s"(lds_dst) : "memory");
}
template <int N> DI void wait_vm_lgkm0() { asm volatile("s_waitcnt vmcnt(%0) lgkmcnt(0)" ::"n"(N) : "memory"); }
template <int N> DI void wait_vm() { asm volatile("s_waitcnt vmcnt(%0)" ::"n"(N) : "memory"); }
DI void drain() { asm volatile("s_waitcnt vmcnt(0)" ::: "memory"); __builtin_amdgcn_s_waitcnt(0x0f70); }
DI void bar() { asm volatile("" ::: "memory"); __builtin_amdgcn_s_barrier(); asm volatile("" ::: "memory"); }
DI void sync_lds() { asm volatile("s_waitcnt lgkmcnt(0)" ::: "memory"); bar(); }
#define SCHED0 __builtin_amdgcn_sched_barrier(0)
#define STPAD do { SCHED0; asm volatile("s_nop 1"); SCHED0; } while (0)

template <bool IS_MAX, bool INV = false>
DI void xred4(float& a, float& b, float& c, float& d) {
  auto op = [](unsigned x, unsigned y) { return IS_MAX ? fmx(__uint_as_float(x), __uint_as_float(y)) : __uint_as_float(x) + __uint_as_float(y); };
  auto r0 = __builtin_amdgcn_permlane32_swap(__float_as_uint(a), __float_as_uint(b), false, false);
  auto r1 = __builtin_amdgcn_permlane32_swap(__float_as_uint(c), __float_as_uint(d), false, false);
  float ab = op(r0[0], r0[1]), cd = op(r1[0], r1[1]);
  auto q = __builtin_amdgcn_permlane16_swap(__float_as_uint(ab), __float_as_uint(cd), false, false);
  float e = op(q[0], q[1]);
  if (INV) e = __builtin_amdgcn_rcpf(e);
  auto w = __builtin_amdgcn_permlane16_swap(__float_as_uint(e), __float_as_uint(e), false, false);
  auto u = __builtin_amdgcn_permlane32_swap(w[0], w[0], false, false);
  auto v = __builtin_amdgcn_permlane32_swap(w[1], w[1], false, false);
  a = __uint_as_float(u[0]); b = __uint_as_float(u[1]); c = __uint_as_float(v[0]); d = __uint_as_float(v[1]);
}

template <int POS, typename F>
DI void phi_map2(const u32x4* in, const float* rs, bf16x8 wf0, bf16x8 wf1, u32x4 (*out)[2], F mid) {
  const f32x4 zero = f32x4{0.f, 0.f, 0.f, 0.f};
  float x[2][8], mx[2], mn[2];
#pragma unroll
  for (int m = 0; m < 2; ++m) {
    f32x4 r0 = mfma16(wf0, as_frag(in[m]), zero), r1 = mfma16(wf1, as_frag(in[m]), zero);
#pragma unroll
    for (int j = 0; j < 4; ++j) { x[m][j] = r0[j]; x[m][4 + j] = r1[j]; }
  }
#pragma unroll
  for (int m = 0; m < 2; ++m) {
    mx[m] = fmx(fmx(fmx(x[m][0], x[m][1]), x[m][2]), fmx(fmx(x[m][3], x[m][4]), fmx(fmx(x[m][5], x[m][6]), x[m][7])));
    mn[m] = -fmn(fmn(fmn(x[m][0], x[m][1]), x[m][2]), fmn(fmn(x[m][3], x[m][4]), fmn(fmn(x[m][5], x[m][6]), x[m][7])));
  }
  xred4<true>(mx[0], mn[0], mx[1], mn[1]);
  if (POS == 1) { asm volatile("" : "+v"(mx[0]), "+v"(mx[1])); SCHED0; mid(); SCHED0; }
  f32x2 e[2][8];
  float sp[2], sn[2];
#pragma unroll
  for (int m = 0; m < 2; ++m) {
    const float sl = rs[m] * LOG2E;
    const f32x2 l2 = {sl, -sl};
    const f32x2 off = f32x2{mx[m], mn[m]} * (-sl);
#pragma unroll
    for (int j = 0; j < 8; ++j) {
      f32x2 a = __builtin_elementwise_fma(f32x2{x[m][j], x[m][j]}, l2, off);
      e[m][j] = f32x2{__builtin_amdgcn_exp2f(a[0]), __builtin_amdgcn_exp2f(a[1])};
    }
    f32x2 sm = ((e[m][0] + e[m][1]) + (e[m][2] + e[m][3])) + ((e[m][4] + e[m][5]) + (e[m][6] + e[m][7]));
    sp[m] = sm[0]; sn[m] = sm[1];
    if (POS == 2 + m) { asm volatile("" : "+v"(sp[m]), "+v"(mx[1])); SCHED0; mid(); SCHED0; }
  }
  xred4<false, true>(sp[0], sn[0], sp[1], sn[1]);
  if (POS == 4) { asm volatile("" : "+v"(sp[0]), "+v"(sp[1])); SCHED0; mid(); SCHED0; }
#pragma unroll
  for (int m = 0; m < 2; ++m) {
    const f32x2 inv = {sp[m], sn[m]};
#pragma unroll
    for (int j = 0; j < 8; ++j) e[m][j] *= inv;
    out[m][0] = u32x4{pk2(e[m][0][0], e[m][1][0]), pk2(e[m][2][0], e[m][3][0]), pk2(e[m][4][0], e[m][5][0]), pk2(e[m][6][0], e[m][7][0])};
    out[m][1] = u32x4{pk2(e[m][0][1], e[m][1][1]), pk2(e[m][2][1], e[m][3][1]), pk2(e[m][4][1], e[m][5][1]), pk2(e[m][6][1], e[m][7][1])};
  }
}

#define QPR 1
#define WPR 0
#define WP1 2
#define WP2 4
#define KS2 0
#define KPRIO 0
#define TRK 0
#define TRS 4
#define TRS0 0
#define TW 0
#define TSL {0, 0, 3, 6, 9, 12, 14, 16}
#define XPRIO 3
#define UNSAFE 0
#define SLP_W 0
#define WORD 0
#define NPRE 1
#define SLP_X0 8
#define SLP_X1 14
#define WB0 0
#define PRES 3
#define TCH0 0
#define TCH1 0
#define TCHA 1
#define EARLY2 0
#define EARLY5 0
#define APRIO 1
#define ARST 0
#define BPRIO 0
#define GPOS 0
#define SLP_G 0
#define XPRE 0
#define SPRD 0
#define SPR0 2
#define SPRS 0
#define HS1 0
#define OAUX 2
#define OFULL 0
#define OTCH 1
#define TLBW 0
#define XAUX 16
#define SAUX 16
#define RAUX 16
#define PAUX 16
#define NLD 31
#define AAUX 16
#define OLAUX 16
#define WAUX 16
#define KAUX 16
#define STW 1
#define OORD 2
#define SLP_OD 12
#define SLP_OBD SLP_OD
#define SLP_O0D 2
#define SLP_XD0 3
#define SLP_XD1 9
#define PKS 1
#define ZSK 1
#define KPOS 0
#define SLP_K0 0
#define ZW 0
#define WBA 2
#define RA 4
#define SB 0
#define SPLIT 0
#define ESET 4
#define SLP_F 2
#define SLP_OB SLP_O
#define SCAL 8
#define SLP_HX 5
#define SLP_O0 8
#define HPRIO 3
#define HEARLY 0
#define SW0 0
#define EARLYB 1
#define FS 8
#define SLP_Q 4
#define WNA 0
#define WNB 1
#define SLP_P 40
#define SLP_H 16
#define NOB 1
#define P_F2 4
#define P_O 1
#define P_K0 2
#define P_K0W 1
#define EL 7
#define EP 0
#define SLP_EP 0
#define SLP_E 8
#define P_L0 (EL & 1 ? -1 : 0)
#define P_R0 1
#define P_L1 (EL & 2 ? -1 : 6)
#define P_R1 8
#define P_AL (EL & 1 ? -1 : 0)
#define P_AR 2
#define P_WL 6
#define P_W0 8
#define P_W1 (EL & 8 ? 6 : 8)
#define P_X 9
#define P_S 8
#define P_X2 10
#define P_K2 5
#define P_CS 1
#define P_ST 1
#define LPRIO 0
#define LPRIO2 0
#define DLY 0
#define LJ0 10
#define LJ1 9
#define SLP_K 4
#define SLP_R 4
#define SLP_O 22
#define NINIT_O 4
#define WIN_O 4
#define LANEID __builtin_amdgcn_mbcnt_hi(~0u, __builtin_amdgcn_mbcnt_lo(~0u, 0u))
#define STAMP(i) do {} while (0)
#define STAMP_DUMP do {} while (0)
#define COUNT(k) do {} while (0)
#define LDS(T) __attribute__((address_space(3))) T
constexpr unsigned MBY = 1u << 20;
constexpr unsigned WS_WQ = 0;
constexpr unsigned WS_WO = 12 * MBY;
constexpr unsigned WS_AR = 16 * MBY, ARENA = 12 * MBY;
constexpr unsigned AR_X = 0, AR_ST = 4 * MBY, AR_O = 5 * MBY, AR_KV = 9 * MBY;
constexpr unsigned AR_L2 = AR_ST + 256 * 1024, AR_FL = AR_ST + 320 * 1024;
#define L2P 4
#define L2P0 4
constexpr unsigned KVREC = 9 * 1024;
constexpr unsigned WS_CS = WS_AR + 4 * ARENA;
constexpr unsigned WS_F1 = WS_CS + 512 * 1024;
constexpr unsigned WS_F2 = WS_F1 + 64 * 1024;
constexpr unsigned WS_EP = WS_F2 + 2048;
constexpr unsigned WS_END = WS_EP + 128;
constexpr unsigned POISON = 0xffffffffu;

typedef __attribute__((address_space(1))) unsigned gu32;
typedef __attribute__((address_space(1))) unsigned long long gu64;
#define RLX_AGENT __ATOMIC_RELAXED, __HIP_MEMORY_SCOPE_AGENT
typedef __amdgpu_buffer_rsrc_t rsrc_t;
DI rsrc_t mk_rsrc(const void* p, unsigned bytes) { return __builtin_amdgcn_make_buffer_rsrc((void*)p, 0, bytes, 0x00020000); }
DI void st16(rsrc_t r, unsigned off, u32x4 v) { __builtin_amdgcn_raw_buffer_store_b128(v, r, off, 0, SAUX); }
DI void st16f(rsrc_t r, unsigned off, f32x4 v) { st16(r, off, __builtin_bit_cast(u32x4, v)); }
DI u32x4 ld16(rsrc_t r, unsigned off) { return __builtin_amdgcn_raw_buffer_load_b128(r, off, 0, 16); }

template <int AUX = 16>
DI u32x4 ld16s(rsrc_t r, unsigned voff, unsigned soff) { return __builtin_amdgcn_raw_buffer_load_b128(r, voff, soff, AUX); }
DI u32x4 clean(u32x4 v) { return __builtin_elementwise_min(v, u32x4{POISON - 1, POISON - 1, POISON - 1, POISON - 1}); }
DI unsigned umax(unsigned a, unsigned b) { return a > b ? a : b; }
DI unsigned umin(unsigned a, unsigned b) { return a < b ? a : b; }
DI unsigned umax4(u32x4 v) { return umax(umax(umax(v[0], v[1]), v[2]), v[3]); }
DI unsigned umax4(unsigned m, u32x4 v) { return umax(umax(umax(umax(m, v[0]), v[1]), v[2]), v[3]); }
DI bool poisoned(unsigned m) { return __builtin_amdgcn_ballot_w64(m == POISON) != 0; }
#define MEMFENCE asm volatile("" ::: "memory")

#define BL(i, o, c) "buffer_load_dwordx4 %1, %2, %" #i " offen" o c " lds\n\t"
#define BL_OPS(...) : "=&s"(keep) : "v"(voff), "s"(r), "s"(lds), __VA_ARGS__ : "memory"
#define BL2(c) asm volatile(GLDS_PRE BL(4, "", c) BL(5, " offset:1024", c) GLDS_POST BL_OPS("s"(s0), "s"(s1)))
#define BL4(c) asm volatile(GLDS_PRE BL(4, "", c) BL(5, " offset:1024", c) BL(6, " offset:2048", c) BL(7, " offset:3072", c) GLDS_POST BL_OPS("s"(s0), "s"(s1), "s"(s2), "s"(s3)))
template <int N, bool NT>
DI void blds_run(rsrc_t r, unsigned voff, unsigned lds, unsigned s0, unsigned s1 = 0, unsigned s2 = 0, unsigned s3 = 0) {
  unsigned keep;
  if constexpr (N == 2) { if constexpr (NT) BL2(" nt"); else BL2(""); }
  else { if constexpr (NT) BL4(" nt"); else BL4(""); }
}
DI u32x4 ldn(rsrc_t r, unsigned voff, unsigned soff) { return __builtin_amdgcn_raw_buffer_load_b128(r, voff, soff, 0); }

#define CODEB 35704
constexpr int MEGA_CODE_BYTES = CODEB;
DI unsigned code_prefetch(int i) {
  unsigned long long pc; int rel; unsigned d;
  asm volatile("s_getpc_b64 %0\n\ts_mov_b32 %1, _Z11mega_kernel10MegaParams@rel32@lo+4" : "=s"(pc), "=s"(rel));
  const unsigned long long a = pc + (long long)rel + (i * 192 + 192 <= MEGA_CODE_BYTES ? i * 192 : 0);
  asm volatile("s_load_dword %0, %1, 0x0\n\ts_load_dword %0, %1, 0x40\n\ts_load_dword %0, %1, 0x80" : "=&s"(d) : "s"(a) : "memory");
  return d;
}

struct MegaParams {
  char* ws;
  const u16* x;
  const u16* wqkvA;
  const u16* woA;
  const u16* wqkvB;
  const u16* woB;
  const u16* wphi;
  u16* out;
};

constexpr int QSLOT = 6 * 1024, QGRP = 4 * QSLOT;
constexpr int L_STG = QGRP;
constexpr int L_A2 = 16 * 1024, L_Z = 21 * 1024;
constexpr int L_RT = 4 * QGRP;
constexpr int L_PB = 0;
constexpr int L_RED = L_STG + 128 * 1024;
constexpr int L_WP = L_RED + 3072;
constexpr int L_STAMP = L_WP + 2048;
constexpr int MEGA_LDS = L_STAMP + 1536;
static_assert(MEGA_LDS <= 160 * 1024, "LDS map");

template <int I, int N, typename F> DI void static_for_(F&& f) {
  if constexpr (I < N) { f(std::integral_constant<int, I>{}); static_for_<I + 1, N>(f); }
}
template <int N, typename F> DI void static_for(F&& f) { static_for_<0, N>(f); }

#define NCK 3
#define PQ2 1
#define SLP_1 10
#define SLP_1D 4
#define CKJ 8
#define CKB 10
#define OCK 2
#define OC0 1
#define OC1 2
#define ALN
#define CK2 1
constexpr int NCHK = NCK;
constexpr int RO = 8;
constexpr int SPIN_BUDGET = 1 << 15;

__global__ __launch_bounds__(768, 3) void mega_kernel(MegaParams p) {
  extern __shared__ __attribute__((aligned(16))) char smem[];
  const int tid = threadIdx.x;
  const int wave = __builtin_amdgcn_readfirstlane(tid >> 6);
  const int xcd = blockIdx.x & 7, idx = blockIdx.x >> 3;
  int bm = (xcd >> 2) * 4 + (idx >> 3), head = (OORD == 2 ? (xcd & 1) * 2 + ((xcd >> 1) & 1) : xcd & 3) * 8 + (idx & 7);
  int cm = (xcd >> 1) & 1, cn = (xcd & 1) * 8 + (idx & 7);
  char* ws = p.ws;
  const u16 *px = p.x, *pq = p.wqkvA;
  asm volatile("" : "+s"(ws), "+s"(px), "+s"(pq));
  const int swv = wave == SW0 ? 0 : wave == 4 ? 1 : wave == 11 ? 2 : -1; (void)swv;
  float* lmean = (float*)(smem + L_RED);
  float* lrstd = lmean + 272;
  float* lcs = lrstd + 272;
  unsigned* lflag = (unsigned*)lmean;
  const rsrc_t wsr = mk_rsrc(ws, WS_END);
  int budget = SPIN_BUDGET;
  unsigned tag0 = 0, tag1 = 0;
  auto ld_ep = [&]() AI { MEMFENCE; return __builtin_amdgcn_raw_buffer_load_b64(wsr, 0, WS_EP, 16); };
  auto mk_tag = [&](u32x2 ep) AI {
    tag0 = __builtin_amdgcn_readfirstlane(ep[0]) + 0x9e3779b9u;
    tag1 = __builtin_amdgcn_readfirstlane(ep[1]) ^ 0x85ebca6bu;
  };
  const unsigned sbase = (unsigned)(size_t)smem;
  int L = wave, cw = wave - 4;
#define OPAQUE_S asm volatile("" : "+s"(L), "+s"(bm), "+s"(head), "+s"(cm), "+s"(cn));
#define LANE_VARS asm volatile("" : "+s"(cw), "+s"(bm), "+s"(head), "+s"(cm), "+s"(cn)); int lane_ = __builtin_amdgcn_mbcnt_hi(~0u, __builtin_amdgcn_mbcnt_lo(~0u, 0u)); asm volatile("" : "+v"(lane_)); \
  const int lane = lane_, fr = lane & 15, fq = lane >> 4; const unsigned v16 = lane * 16; (void)fr; (void)fq; (void)v16;
  auto put_x = [&](u32x4* xr, unsigned v16, unsigned dst) AI {
    float sm = 0.f, sq = 0.f;
#pragma unroll
    for (int j = 0; j < 2; ++j) {
      xr[j][0] = umin(xr[j][0], POISON - 1); xr[j][2] = umin(xr[j][2], POISON - 1);
      __builtin_amdgcn_raw_buffer_store_b128(xr[j], wsr, v16, dst + j * 16384, XAUX);
    }
    SCHED0;
#pragma unroll
    for (int j = 0; j < 2; ++j)
#pragma unroll
      for (int e = 0; e < 4; ++e) { sm = dot2(xr[j][e], 0x3f803f80u, sm); sq = dot2(xr[j][e], xr[j][e], sq); }
    xred2<false>(sm, sq);
    return u32x2{__float_as_uint(sm), __float_as_uint(sq)};
  };

#define RT_VARS const bool tq = L < 3; const int tsg = tq ? (bm * 2 + (head >> 4)) * 3 + L : bm * 2 + (head >> 4), ks = (head & 15) + 16 * h;
  auto ld_rt = [&](const u16* w, int h, unsigned lds, auto nt) AI {
    RT_VARS
    LANE_VARS
    const unsigned rs = tq ? 6144 : 2048, D = 8 * rs - 1024, o = ks * 32 * rs + tsg * 128;
    blds_run<4, decltype(nt)::value>(mk_rsrc(w, rs * 1024), (lane >> 3) * rs + ((lane & 7) ^ ((lane >> 3) & 6)) * 16, sbase + lds, o, o + D, o + 2 * D, o + 3 * D);
  };
  auto retile = [&](int side, int h, unsigned lds, f32x4* rcs, int p0 = 0, int p1 = 4) AI {
    RT_VARS
    LANE_VARS
    const unsigned dst = tq ? WS_WQ + side * (6 * MBY) + (unsigned)((((tsg & 15) * 2) * 32 + ks) * 6 + (tsg >> 4) * 2) * 1024
                            : WS_WO + side * (2 * MBY) + (unsigned)((tsg * 32 + ks) * 4) * 1024;
    const unsigned gstr = tq ? 32 * 6 * 1024 : 2048;
    const unsigned a0 = (lane >> 2) * 128 + ((lane >> 3) & 3) * 32 + (lane & 3) * 8;
#pragma unroll
    for (int pc = p0; pc < p1; ++pc) {
      const char* aa = smem + lds + (a0 ^ (pc * 32));
      u32x2 r0_ = trread(aa), r1_ = trread(aa + 2048);
      u32x4 v = u32x4{r0_[0], r0_[1], r1_[0], r1_[1]};
      __builtin_amdgcn_raw_buffer_store_b128(v, wsr, v16 + (pc & 1) * 1024, dst + (pc >> 1) * gstr, RAUX);
      if (rcs) rcs[pc] = mfma16(as_frag(u32x4{0x3f803f80u, 0x3f803f80u, 0x3f803f80u, 0x3f803f80u}), as_frag(v), rcs[pc]);
    }
    if (rcs && h == 1 && p1 == 4) {
      const f32x4 c0 = rcs[0], c1 = rcs[1], c2 = rcs[2], c3 = rcs[3];
      const float ca = fq & 1 ? c1[0] : c0[0], cb = fq & 1 ? c3[0] : c2[0], cc = fq & 2 ? cb : ca;
      if (tq) __builtin_amdgcn_raw_buffer_store_b32(__float_as_uint(cc), wsr, ((fq >> 1) * 16 * 96 + (fq & 1) * 16 + fr) * 4,
                                                    WS_CS + (((side * 32 + (tsg & 15) * 2) * 16 + (head & 15)) * 96 + (tsg >> 4) * 32) * 4, 16);
    }
  };
  auto flag = [&](unsigned table) AI {
    if (LANEID == 0) __builtin_amdgcn_raw_buffer_store_b64(u32x2{tag0, tag1}, wsr, 0, table + (bm * 32 + head) * FS, 16);
  };
  auto poll = [&](unsigned table, auto between, int rows = 0) AI {
    LANE_VARS
    const int q = (umin(fq, 2) * 16 + (head >> 1)) * 43 >> 7;
    const unsigned vo = rows ? bm * 256 + (fr & rows) * 16 : fq == 3 ? bm * 256 + fr * 16 : q * 128 + (fr & 7) * 16;
    auto miss = [&](u32x4 f) AI { return __builtin_amdgcn_ballot_w64(((f[0] ^ tag0) | (f[1] ^ tag1) | (f[2] ^ tag0) | (f[3] ^ tag1)) != 0) != 0; };
    MEMFENCE;
    const u32x4 f0 = ld16s(wsr, vo, table);
    MEMFENCE;
    between();
    MEMFENCE;
    bool wait = miss(f0);
#pragma unroll 1
    while (wait && --budget > 0) {
      __builtin_amdgcn_s_sleep(SLP_Q);
      MEMFENCE;
      wait = miss(ld16s(wsr, vo, table));
      asm volatile("" : "+s"(budget) :: "memory");
    }
    MEMFENCE;
  };
  auto ld_x = [&](int cx, unsigned lds, auto nt) AI {
    LANE_VARS
    const unsigned o = (bm * 16 + (head >> 1)) * 32768 + cx * 128;
    blds_run<2, decltype(nt)::value>(mk_rsrc(px, 4 * MBY), (lane >> 3) * 2048 + ((lane & 7) ^ (lane >> 3)) * 16, sbase + lds, o, o + 15360);
  };
  auto st_x = [&](int cx, unsigned lds, int w) AI {
    LANE_VARS
    const int mt = bm * 16 + (head >> 1);
    u32x4 xr[2];
#pragma unroll
    for (int j = 0; j < 2; ++j) {
      const char* a = smem + lds + fr * 128 + (fq & 1) * 8;
      const u32x2 lo = *(const u32x2*)(a + ((j * 4 + (fq >> 1)) ^ (fr & 7)) * 16), hi = *(const u32x2*)(a + ((j * 4 + 2 + (fq >> 1)) ^ (fr & 7)) * 16);
      xr[j] = u32x4{lo[0], lo[1], hi[0], hi[1]};
    }
    const u32x2 stat = put_x(xr, v16, WS_AR + AR_X + xt_off(mt, cx * 2));
    if (fq == 0) *(u32x2*)(lmean + 16 + fr * 4 + w) = stat;
  };

  auto touch = [&](unsigned off) AI {
    unsigned d = 0;
    if (TLBW == 1) asm volatile("s_load_dword %0, %1, %2" : "=&s"(d) : "s"(ws), "s"(off) : "memory");
    if (TLBW == 2) asm volatile("buffer_load_dword %0, off, %1, %2" : "=&v"(d) : "s"(wsr), "s"(off) : "memory");
    return d;
  };
  auto keep = [&](unsigned d) AI {
    if (TLBW == 1) asm volatile("" :: "s"(d));
    if (TLBW == 2) asm volatile("" :: "v"(d));
  };
  const std::integral_constant<bool, (NLD & 16) != 0> nts;
  if (wave >= 8) {
    const unsigned cp = code_prefetch(idx * 9 + cw), cq = code_prefetch(idx * 9 + cw - 4);
    L = wave - 8;
    __builtin_amdgcn_s_sleep(SLP_H);
    ld_x(8 + (head & 1) * 4 + L, L_PB + 32768 + L * 2048, std::integral_constant<bool, (NLD & 4) != 0>{});
    if (L < 3) ld_rt(pq, 1, L_PB + 20480 + L * 4096, std::integral_constant<bool, (NLD & 8) != 0>{});
    sync_lds();
    asm volatile("; pi" :: "s"(cp), "s"(cq) : "memory");
    wait_vm<0>();
    { const int layer = 1; STAMP(15); }
    st_x(8 + (head & 1) * 4 + L, L_PB + 32768 + L * 2048, L * 64 + 2);
    if (L < 3) {
      const f32x4 t = *(const f32x4*)(smem + L_PB + 40960 + L * 1024 + LANEID * 16);
      f32x4 rca[4] = {{t[0], 0.f, 0.f, 0.f}, {t[1], 0.f, 0.f, 0.f}, {t[2], 0.f, 0.f, 0.f}, {t[3], 0.f, 0.f, 0.f}};
      retile(0, 1, L_PB + 20480 + L * 4096, rca);
    }
    wait_vm_lgkm0<0>();
    { const int layer = 2; STAMP(15); }
  } else if (wave >= 4) {
    ld_x((head & 1) * 4 + cw, L_PB + cw * 2048, std::integral_constant<bool, (NLD & 1) != 0>{});
    const unsigned t0 = touch(cw & 1 ? WS_AR + AR_ST + ((bm * 16 + (head >> 1)) * 16 + (head & 1) * 4 + cw) * 128
                                     : WS_AR + AR_X + xt_off(bm * 16 + (head >> 1), ((head & 1) * 4 + cw) * 2));
    if (TLBW == 1) wait_vm_lgkm0<0>(); else wait_vm<TLBW == 2>();
    if (TLBW == 1) keep(t0);
    { const int layer = 1; STAMP(15); }
    st_x((head & 1) * 4 + cw, L_PB + cw * 2048, cw * 64);
    wait_vm_lgkm0<0>();
    if (TLBW == 2) keep(t0);
    { const int layer = 2; STAMP(15); }
    bar();
    asm volatile("; pc" ::: "memory");
    if (EL & 1) {
      if (SLP_E) __builtin_amdgcn_s_sleep(SLP_E);
      L = 3;
      if (cw < 2) ld_rt(p.woA, cw, L_RT + 3 * 14336 + cw * 4096, nts);
      if ((EL & 4) && cw == 2) ld_rt(p.woB, 0, L_RT + 3 * 14336 + 8192, nts);
      if ((EL & 8) && cw == 3) ld_rt(p.woB, 1, L_RT + 8192, nts);
      if ((EL & 16) && cw == 3) glds_run<2>(LANEID * 16, (const char*)p.wphi + head * 2048, sbase + L_WP);
    }
    if (EP) {
      if (SLP_EP) __builtin_amdgcn_s_sleep(SLP_EP);
      const u32x4 pz = u32x4{POISON, POISON, POISON, POISON};
      auto poison = [&](unsigned so) AI { __builtin_amdgcn_raw_buffer_store_b128(pz, wsr, LANEID * 16, so, 16); };
      if (EP & 1) {
        const unsigned o = WS_AR + AR_O + xt_off(bm * 16 + cw * 4, head);
#pragma unroll
        for (int i = 0; i < 4; ++i) poison(o + i * 1024);
      }
      if ((EP & 2) && cw < 3) {
        const unsigned kvo = WS_AR + AR_KV + (unsigned)(bm * 32 + head) * KVREC + cw * 3072;
        poison(kvo); poison(kvo + 1024); poison(kvo + 2048);
      }
      if ((EP & 4) && cw > 0) {
        const unsigned kvo = WS_AR + 2 * ARENA + AR_KV + (unsigned)(bm * 32 + head) * KVREC + (cw - 1) * 3072;
        poison(kvo); poison(kvo + 1024); poison(kvo + 2048);
      }
      MEMFENCE;
    }
  } else if (wave < 3) {
    ld_rt(pq, 0, L_PB + 8192 + L * 4096, std::integral_constant<bool, (NLD & 2) != 0>{});
    const u32x2 ep = ld_ep();
    const int tsg = (bm * 2 + (head >> 4)) * 3 + L;
    const unsigned t0 = touch(WS_WQ + (unsigned)((((tsg & 15) * 2) * 32 + (head & 15)) * 6 + (tsg >> 4) * 2) * 1024);
    if (TLBW == 1) wait_vm_lgkm0<1>(); else wait_vm<1 + (TLBW == 2)>();
    if (TLBW == 1) keep(t0);
    { const int layer = 1; STAMP(15); }
    f32x4 rca[4] = {};
    retile(0, 0, L_PB + 8192 + L * 4096, rca);
    *(f32x4*)(smem + L_PB + 40960 + L * 1024 + LANEID * 16) = f32x4{rca[0][0], rca[1][0], rca[2][0], rca[3][0]};
    drain();
    if (TLBW == 2) keep(t0);
    mk_tag(ep);
    { const int layer = 2; STAMP(15); }
    sync_lds();
    asm volatile("; pl" ::: "memory");
    if (EL & 1) {
      if (SLP_E) __builtin_amdgcn_s_sleep(SLP_E);
      ld_rt(p.wqkvB, 0, L_RT + L * 14336, nts);
      if (EL & 2) ld_rt(p.wqkvB, 1, L_RT + L * 14336 + 4096, nts);
    }
  } else {
    const u32x2 ep = ld_ep();
    const unsigned cp = code_prefetch(idx * 9 + 8);
    if (LANEID < 9) lflag[LANEID] = 0;
    wait_vm_lgkm0<0>();
    asm volatile("" :: "s"(cp));
    mk_tag(ep);
    bar();
    flag(WS_F1);
    if (LANEID == 0) *(u32x2*)(lflag + 10) = u32x2{tag0, tag1};
    { const int layer = 2; STAMP(15); }
    __builtin_amdgcn_s_sleep(SLP_P);
    poll(WS_F1, []() {});
    asm volatile("; pp" ::: "memory");
  }
  bar();
  { const int layer = 3; STAMP(15); }
  int role = wave;
  asm volatile("" : "+s"(role));

  if (__builtin_expect(role < 4, 1)) {
    if (LPRIO) __builtin_amdgcn_s_setprio(LPRIO);
    const unsigned v16 = __builtin_amdgcn_mbcnt_hi(~0u, __builtin_amdgcn_mbcnt_lo(~0u, 0u)) * 16;
#define OPAQUE_LANE int lane = v16 >> 4; asm volatile("" : "+v"(lane));
    auto q_groupW = [&](int layer, int g, int pos) AI {
      const char* gW = ws + WS_WQ + (layer & 1) * (6 * MBY) + head * (192 * 1024) + (g * 4 + L) * 6144;
      const unsigned d = sbase + (pos * 4 + L) * QSLOT;
      glds_run<3, true>(v16, gW, d);
      glds_run<3, true>(v16, gW + 3072, d + 3072);
    };
    u32x4 wn[4][6];
    auto ld_wn = [&](int layer, int g0, int g1) AI {
      const unsigned so = WS_WQ + (layer & 1) * (6 * MBY) + head * (192 * 1024) + L * 6144;
#pragma unroll
      for (int g = g0; g < g1; ++g)
#pragma unroll
        for (int n = 0; n < 6; ++n) wn[g][n] = ld16s<WAUX>(wsr, v16 + (n % 3) * 1024, so + g * 24576 + (n / 3) * 3072);
      MEMFENCE;
    };
    auto wr_wn = [&](int g) AI {
#pragma unroll
      for (int n = 0; n < 6; ++n) *(u32x4*)(smem + (g * 4 + L) * QSLOT + n * 1024 + v16) = wn[g][n];
    };
    const u32x4 pz = u32x4{POISON, POISON, POISON, POISON};
    auto poison = [&](unsigned so) AI { __builtin_amdgcn_raw_buffer_store_b128(pz, wsr, v16, so, PAUX); };
    auto poison_o = [&](int l) AI {
      const unsigned o = WS_AR + l * ARENA + AR_O + xt_off(bm * 16 + L * 4, head);
#pragma unroll
      for (int i = 0; i < 4; ++i) poison(o + i * 1024);
    };
    auto poison_x = [&](int l) AI {
      if (L < NCHK) {
        const unsigned o = WS_AR + l * ARENA + AR_X + bm * (512 * 1024) + head * (NCHK * 2048) + L * 2048;
        poison(o); poison(o + 1024);
      }
    };
    auto poison_s = [&](int l) AI {
      if (L == 3) {
        const unsigned o = WS_AR + l * ARENA + AR_KV + (unsigned)(bm * 32 + head) * KVREC;
        poison(o); poison(o + 1024);
      }
    };
    auto poison_kv = [&](int l, int w0) AI {
      if ((unsigned)(L - w0) < 3) {
        const unsigned kvo = WS_AR + l * ARENA + AR_KV + (unsigned)(bm * 32 + head) * KVREC + (L - w0) * 3072;
        poison(kvo); poison(kvo + 1024); poison(kvo + 2048);
      }
    };
    q_groupW(0, 0, 0);
    q_groupW(0, 1, 1);
    auto put_l2 = [&](float sm, float sq, unsigned vo, unsigned so) AI {
      xred2<false>(sm, sq);
      if (v16 < 256) __builtin_amdgcn_raw_buffer_store_b128(u32x4{__float_as_uint(sm), tag0, __float_as_uint(sq), tag1}, wsr, vo, so, 16);
      MEMFENCE;
    };
    if (L == 3) {
      flag(WS_F1 + 256 * FS);
      MEMFENCE;
      const f32x4 t = *(const f32x4*)((const char*)(lmean + 16) + v16);
      put_l2(t[0] + t[2], t[1] + t[3], v16, WS_AR + AR_L2 + (head & 1) * 32768 + (bm * 16 + (head >> 1)) * 256);
    }
    if (L == 3) wait_vm<8>(); else wait_vm<6>();
    auto body = [&](auto kind, const int layer) AI {
      constexpr bool isB = decltype(kind)::value == 1, is0 = decltype(kind)::value == 0;
      const unsigned ar = WS_AR + layer * ARENA;
      f32x4 rcs[4] = {};
      unsigned tch = 0;
      auto sW = [&]() AI { return WS_WO + (layer & 1) * (2 * MBY) + cn * (128 * 1024); };
      auto stg = [&](unsigned off, auto n) AI { glds_run<decltype(n)::value, true>(v16, ws + sW() + off, sbase + L_STG + off); };
      const std::integral_constant<int, 4> n4; const std::integral_constant<int, 3> n3; const std::integral_constant<int, 2> n2;
      auto stg7 = [&](unsigned o) AI { stg(o, n4); stg(o + 4096, n3); };
      auto stg6 = [&](unsigned o) AI { stg(o, n3); stg(o + 3072, n3); };

      auto flag2 = [&](int b) AI {
        if (is0 && b == P_F2 && L == 0 && v16 == 0) __builtin_amdgcn_raw_buffer_store_b64(u32x2{tag0, tag1}, wsr, 0, WS_F2 + (bm * 32 + head) * 8, 16);
      };
      auto slot = [&](int k) AI {
        const unsigned lu = L_RT + L * 14336;
        const unsigned oo = WS_AR + AR_O + xt_off(bm * 16 + L * 4, head);
        if (k < 4) retile(L < 3, 0, lu, rcs, k, k + 1);
        else if (k < 12) {
          if (L == 3) { if (k < 8) retile(0, 1, lu + 4096, nullptr, k - 4, k - 3); else poison(oo + (k - 8) * 1024); }
          else if (k < 7) poison(WS_AR + AR_KV + (unsigned)(bm * 32 + head) * KVREC + L * 3072 + (k - 4) * 1024);
          else if (k < 11) poison(oo + (k - 7) * 1024);
        } else if (k < 15) {
          if (L < 3) poison(WS_AR + 2 * ARENA + AR_KV + (unsigned)(bm * 32 + head) * KVREC + L * 3072 + (k - 12) * 1024);
          else if (TW) retile(1, 0, lu + 8192, nullptr, k - 12, k - 11);
        } else if (k == 15) { if (TW && L == 3) retile(1, 0, lu + 8192, nullptr, 3, 4); }
        MEMFENCE;
      };
      auto trickle = [&](int b) AI {
        constexpr int st[8] = TSL;
        static_assert(!TRK || st[P_F2 - 1] >= 12, "");
        if (b >= 1 && b <= 6) {
#pragma unroll
          for (int k = 0; k < 16; ++k)
            if (k >= st[b] && k < st[b + 1]) { if (TRS && (k > st[b] || (TRS0 && b > 1))) __builtin_amdgcn_s_sleep(TRS); slot(k); }
        }
      };
      auto hi = [&](int bit) AI { if (is0 && (XPRIO & bit)) { SCHED0; __builtin_amdgcn_s_setprio(3); SCHED0; } };
      auto lo = [&](int bit) AI { if (is0 && (XPRIO & bit)) { SCHED0; __builtin_amdgcn_s_setprio(0); SCHED0; } };
      auto extra_ = [&](int b) AI {
        if (!is0) return;
        const unsigned lu = L_RT + L * 14336;
        if (TRK) {
          trickle(b);
          if (b == P_R1 && L < 3) retile(1, 1, lu + 4096, rcs);
          if (b == P_W0 && L == 3 && !TW) retile(1, 0, lu + 8192, nullptr);
          if (b == P_W1 && L == 3) retile(1, 1, lu + 4096, nullptr);
          if (b == P_WL && L == 3) ld_rt(p.woB, 1, lu + 4096, nts);
          static_assert(!TRK || (EL == 7 && P_WL >= 4), "");
          return;
        }
        if (b == P_R0) retile(L < 3, 0, lu, rcs);
        if (P_R1 == P_AR) { if (b == P_R1) retile(L < 3, 1, lu + 4096, rcs); }
        else {
          if (b == P_R1 && L < 3) retile(1, 1, lu + 4096, rcs);
          if (b == P_AR && L == 3) retile(0, 1, lu + 4096, nullptr);
        }
        if (b == P_W0 && L == 3) retile(1, 0, EL & 4 ? lu + 8192 : lu, nullptr);
        if (b == P_W1 && L == 3) retile(1, 1, EL & 8 ? L_RT + 8192 : lu + 4096, nullptr);
        static_assert(!(EL & 8) || P_W1 <= 7, "");
        if (b == P_L0) ld_rt(L < 3 ? p.wqkvB : p.woA, 0, lu, nts);
        if (P_L1 == P_AL) { if (b == P_L1) ld_rt(L < 3 ? p.wqkvB : p.woA, 1, lu + 4096, nts); }
        else {
          if (b == P_L1 && L < 3) ld_rt(p.wqkvB, 1, lu + 4096, nts);
          if (b == P_AL && L == 3) ld_rt(p.woA, 1, lu + 4096, nts);
        }
        if (b == P_WL && L == 3) { if (!(EL & 4)) ld_rt(p.woB, 0, lu, nts); if (!(EL & 8)) ld_rt(p.woB, 1, lu + 4096, nts); }
      };
      auto extra = [&](int b) AI { if (b < 7) hi(1); extra_(b); if (b < 7) lo(1); };
      auto extra2_ = [&](int b) AI {
        if (!is0) return;
        if (b == P_O && !(EP & 1) && !TRK) poison_o(0);
        if (b == P_K0 && !(EP & 2) && !TRK) poison_kv(0, P_K0W);
        if (b == P_S) poison_s(1);
        if (b == P_X) poison_x(1);
        if (b == P_K2 && !(EP & 4) && !TRK) poison_kv(2, 0);
        if (b == P_X2) { poison_x(2); poison_s(3); }
      };
      auto extra2 = [&](int b) AI { if (b < 7) hi(2); extra2_(b); if (b < 7) lo(2); };
      constexpr auto after = [](int ld, int use) { return use > ld && !(use == ld + 1 && (use == 2 || use == 7)); };
      static_assert(UNSAFE || after(P_O, P_F2 - 1) && after(P_K0, P_F2 - 1) && after(P_R0, P_F2 - 1) && after(P_AR, P_F2 - 1) && P_F2 >= 4 && P_F2 <= 6, "drained one barrier before the flag");
      static_assert(after(P_L0, P_R0) && after(P_L1, P_R1) && after(P_AL, P_AR) && ((EL & 4) || after(P_WL, P_W0)) && ((EL & 8) || after(P_WL, P_W1)) && P_R0 <= P_R1 && P_WL >= P_R0 && P_WL >= P_AR, "order");
      hi(4);
      if (is0 || WB0) sync_lds();
      STAMP(0);
      {
        OPAQUE_S
        OPAQUE_LANE
        float c[16];
        auto ldcs = [&]() AI {
          if (L >= 2) {
#pragma unroll
            for (int i = 0; i < 16; ++i)
              c[i] = __uint_as_float(__builtin_amdgcn_raw_buffer_load_b32(wsr, (lane < 48 ? lane : 0) * 4 + (i & 7) * 384,
                                         WS_CS + (((layer & 1) * 32 + head) * 1536 + (L & 1) * 48 + (i >> 3) * 768) * 4, 16));
          }
        };
        auto wrcs = [&]() AI {
          if (L >= 2) {
            asm volatile("" : "+v"(c[0]));
            float cs = c[0];
#pragma unroll
            for (int i = 1; i < 16; ++i) cs = add1(cs, c[i]);
            if (lane < 48) lcs[(isB ? 96 : 0) + (L & 1) * 48 + lane] = cs;
          }
        };
        if (isB && layer == 1) ldcs();
        if constexpr (!is0) {
#pragma unroll
          for (int g = WB0 ? WB0 : 4; g < 4; ++g) wr_wn(g);
        }
        else {
          q_groupW(0, 2, 2); q_groupW(0, 3, 3);
          extra(0);
          wait_vm<12 + (P_L0 == 0 ? 4 : 0) + (P_L1 == 0 && P_AL == 0 ? 4 : 0)>();
        }
        if (!is0 && NCHK < 3 && L == 1) {
          if (cn < 8) __builtin_amdgcn_s_sleep(SLP_1); else __builtin_amdgcn_s_sleep(SLP_1D);
          poll(ar + AR_FL, []() {}, 11);
        }
        if (is0 && L >= 2 && !(EL & 16)) glds_run<1>(v16, (const char*)p.wphi + head * 2048 + (L - 2) * 1024, sbase + L_WP + (L - 2) * 1024);
        STAMP(1);
        sync_lds();
        STAMP(2);
        OPAQUE_S
        u32x4 s0 = u32x4{0u, 0u, 0u, 0u}, s1 = s0;
        auto ldst = [&]() AI {
          const unsigned so = ar + AR_L2 + (bm * 16 + L * 4) * 256;
          s0 = ld16s(wsr, v16, so);
          if (is0) s1 = ld16s(wsr, v16, so + 32768);
          if (isB && L == 0) s1 = ld16s(wsr, v16 & 255, bm > 0 ? so - 256 : so);
          MEMFENCE;
        };
        auto fin = [&](float sm, float sq, int r, bool own) AI {
          float mean = sm * (1.0f / 1024.0f);
          float var = fmaxf(__builtin_fmaf(sq, 1.0f / 1024.0f, -mul1(mean, mean)), 0.f);
          if (own) lmean[r] = -mean;
          lrstd[r] = __builtin_amdgcn_rsqf(var + 1e-5f);
        };
        auto wrst = [&]() AI {
          while (true) {
            unsigned d = (s0[1] ^ tag0) | (s0[3] ^ tag1);
            if (is0 || (isB && L == 0)) d |= (s1[1] ^ tag0) | (s1[3] ^ tag1);
            if (__builtin_expect(__builtin_amdgcn_ballot_w64(d != 0) == 0, 1) || --budget <= 0) break;
            COUNT(1);
            ldst();
          }
          MEMFENCE;
          float sm = __uint_as_float(s0[0]), sq = __uint_as_float(s0[2]);
          if (is0) { sm += __uint_as_float(s1[0]); sq += __uint_as_float(s1[2]); }
          fin(sm, sq, 16 + L * 64 + lane, true);
          if (isB && L == 0) fin(__uint_as_float(s1[0]), __uint_as_float(s1[2]), lane & 15, false);
        };
        constexpr int LP = is0 ? L2P0 : L2P;
        auto stag = [&](auto nc) AI {
          constexpr int N = decltype(nc)::value;
          if (N) {
            if (L & 1) __builtin_amdgcn_s_sleep(N);
            if (L & 2) __builtin_amdgcn_s_sleep(2 * N);
          }
        };
        auto q_group = [&](int g) AI {
          if (is0) { stag(std::integral_constant<int, SPR0>{}); q_groupW(layer, g, g & 3); }
          else if (g >= 4 + NPRE) { stag(std::integral_constant<int, SPRD>{}); q_groupW(layer, g, g < 6 ? g : g - 4); }
          if (isB && EARLY2 == g - 2) { stg6(L * 6144); stg(120 * 1024 + L * 2048, n2); }
          if (isB && EARLY5 && g == 7) stg6(72 * 1024 + L * 6144);
        };
        auto poll2 = [&]() AI {
          if (!is0 && L == 0) {
            if (SPRS) __builtin_amdgcn_s_sleep(SPRS);
            poll(ar + AR_FL, []() {}, 15);
            if (head < 16) {
              const unsigned so = ar + AR_ST + (bm * 16 + head) * 2048;
              s0 = ld16s(wsr, v16, so); s1 = ld16s(wsr, v16 + 1024, so);
              MEMFENCE;
            }
          }
        };
        auto sum2 = [&]() AI {
          if (!is0 && L == 0 && head < 16) {
            f32x4 t = add4<(SCAL & 8) != 0>(__builtin_bit_cast(f32x4, s0), __builtin_bit_cast(f32x4, s1));
#pragma unroll
            for (int e = 0; e < 4; ++e)
              t[e] += __uint_as_float(__builtin_amdgcn_update_dpp(0u, __float_as_uint(t[e]), 0x128, 0xf, 0xf, false));
            const bool hi = lane & 8;
            put_l2(hi ? t[2] : t[0], hi ? t[3] : t[1], (lane & 7) * 32 + (lane & 8) * 2, ar + AR_L2 + (bm * 16 + head) * 256);
          }
        };
        static_assert(PQ2 == 1 || L2P > 3, "");
        if (PQ2 == 1) poll2();
        else if (!is0 && NCHK == 3 && L == 1) poll(ar + AR_FL, []() {}, 11);
        if (is0) {
          drain();
          poll(WS_F1 + 256 * FS, [&]() AI { extra(1); });
          if (P_CS == 1) ldcs();
          extra2(1);
        }
        if (isB && layer == 1) wrcs();
        MEMFENCE;
        STAMP(3);
        sync_lds();
        STAMP(4);
        OPAQUE_S
        if (!is0) drain();
        if (PQ2 == 1) sum2();
        q_group(4);
        extra(2);
        if (is0 && P_CS == 2) ldcs();
        if (LP == 2) ldst();
        extra2(2);
        if (PQ2 == 2) poll2();
        STAMP(5);
        sync_lds();
        STAMP(6);
        OPAQUE_S
        drain();
        if (PQ2 == 2) sum2();
        q_group(5);
        extra(3);
        if (LP == 3) ldst();
        extra2(3);
        if (is0) wrcs();
        STAMP(7);
        bar();
        OPAQUE_S
        drain();
        flag2(4);
        q_group(6);
        extra(4); extra2(4);
        if (LP == 4) ldst();
        STAMP(8);
        bar();
        STAMP(9);
        OPAQUE_S
        drain();
        flag2(5);
        q_group(7);
        extra(5); extra2(5);
        SCHED0;
        if (LP == 5) ldst(); else wrst();
        STAMP(10);
        bar();
        STAMP(11);
        OPAQUE_S
        drain();
        if (OTCH && isB && layer == 3 && L == 3) {
          asm volatile("global_load_dword %0, %1, %2" : "=v"(tch) : "v"((unsigned)((bm * 256 + cm * 16 + (lane & 7) * 32) * 2048 + cn * 128)), "s"(p.out) : "memory");
        }
        flag2(6);
        extra(6); extra2(6);
        if (isB && EARLYB >= 1) { stag(std::integral_constant<int, SPRD>{}); stg6(96 * 1024 + L * 6144); }
        if (isB && EARLYB >= 2 && !EARLY5) stg6(72 * 1024 + L * 6144);
        if (isB && EARLY2 == 6) { stg6(L * 6144); stg(120 * 1024 + L * 2048, n2); }
        SCHED0;
        if (LP == 5) wrst();
      }
      lo(4);
      STAMP(12);
      sync_lds();
      STAMP(13);
      OPAQUE_S
      u32x4 fl[2];
      fl[0] = fl[1] = u32x4{tag0, tag1, tag0, tag1};
      auto ldfl = [&]() AI { fl[0] = ld16s(wsr, v16, WS_F2); fl[1] = ld16s(wsr, v16 + 1024, WS_F2); };
      if (is0) ldfl();
      extra(7); extra2(7);
      bar();
      constexpr int NR = isB ? 6 : 9;
      const unsigned ro = isB ? 48 * 1024 + L * 6144 : L * 9216;
      u32x4 r[NR];
      bool pend = false;
      auto flags = [&]() AI {
        const u32x4 d = (fl[0] ^ u32x4{tag0, tag1, tag0, tag1}) | (fl[1] ^ u32x4{tag0, tag1, tag0, tag1});
        pend = __builtin_amdgcn_ballot_w64((d[0] | d[1] | d[2] | d[3]) != 0) != 0;
      };
      if (isB) {
        if (!EARLY2) { stg6(L * 6144); stg(120 * 1024 + L * 2048, n2); }
        stg6(24 * 1024 + L * 6144);
        if (EARLYB < 1) stg6(96 * 1024 + L * 6144);
        if (EARLYB < 2 && !EARLY5) stg6(72 * 1024 + L * 6144);
      } else {
        drain();
        if (is0) flags();
        MEMFENCE;
        extra(8); extra2(8);
        if (!is0) poison_x(3);
        while (is0 && __builtin_expect(pend, 0) && --budget > 0) {
          ldfl();
          flags();
          MEMFENCE;
        }
        stg(36 * 1024 + L * 3072, n3); stg7(72 * 1024 + L * 14336); stg7(72 * 1024 + L * 14336 + 7168);
      }
#pragma unroll
      for (int i = 0; i < NR; ++i) r[i] = ld16s<WAUX>(wsr, v16 + (i & 3) * 1024, sW() + ro + (i >> 2) * 4096);
      MEMFENCE;
      if (is0 && bm + head + L == 0 && v16 == 0) __builtin_amdgcn_raw_buffer_store_b32(tag0, wsr, 0, WS_EP, 16);
      auto wr_r = [&]() AI {
        unsigned vv = v16;
        asm volatile("" : "+v"(vv));
#pragma unroll
        for (int i = 0; i < NR; ++i) *(u32x4*)(smem + L_STG + ro + i * 1024 + vv) = r[i];
      };
      auto left = [&]() AI {
        OPAQUE_LANE
        for (;;) {
          MEMFENCE;
          if (__builtin_amdgcn_ballot_w64(lflag[lane & 7] != (unsigned)layer) == 0 || --budget <= 0) break;
          __builtin_amdgcn_s_sleep(SLP_F);
        }
        MEMFENCE;
      };
      if (isB && layer == 3) {
        STAMP(14);
        left();
        wr_r();
        wait_vm_lgkm0<0>();
        asm volatile("" :: "v"(tch));
        bar();
        STAMP_DUMP;
        __builtin_amdgcn_endpgm();
      }
      if (isB) {
        ld_wn(layer + 1, 0, WBA);
        STAMP(14);
        left();
        ld_wn(layer + 1, WBA, 4);
        wr_r();
        wait_vm_lgkm0<24>();
      } else {
        auto tail = [&](int wa, int wb) AI {
          ld_wn(layer + 1, 0, wa);
          if (is0) wait_vm<26 + 6 * WNA>();
          STAMP(14);
          bar();
          stg6(48 * 1024 + L * 6144);
          ld_wn(layer + 1, wa, wb);
          bar();
          extra2(9);
          ld_wn(layer + 1, wb, 4);
        };
        tail(WNA, WNB);
        wr_r();
        if (!is0) drain();
        else if (P_X == 9) wait_vm<6 * (4 - WNB)>();
        static_assert(P_S <= 8 && P_K2 <= 8 && P_X <= 9 && P_X2 <= 10, "");
        bar();
        extra2(10);
        wait_vm_lgkm0<0>();
      }
      bar();
      OPAQUE_S
      if (SLP_W) __builtin_amdgcn_s_sleep(SLP_W);
      if (!WB0) wr_wn(0);
      drain();
      sync_lds();
      OPAQUE_S
      if (WB0 && WORD == 1) { wr_wn(0); SCHED0; }
#pragma unroll
      for (int g = 1; g <= GPOS; ++g) wr_wn(g);
      SCHED0;
      if (SLP_G) __builtin_amdgcn_s_sleep(SLP_G);
      if (NPRE > 0) q_groupW(layer + 1, 4, 4);
      if (NPRE > 1) q_groupW(layer + 1, 5, 5);
#pragma unroll
      for (int g = WB0 && WORD == 0 ? 0 : GPOS + 1; g < (WB0 ? WB0 : 4); ++g) wr_wn(g);
    };
    body(std::integral_constant<int, 0>{}, 0);
#pragma unroll 1
    for (int layer = 1;; layer += 2) {
      body(std::integral_constant<int, 1>{}, layer);
      body(std::integral_constant<int, 2>{}, 2);
    }
  }

  const f32x4 zero = f32x4{0.f, 0.f, 0.f, 0.f};
#pragma unroll 1
  for (int layer = 0; layer < 4; ++layer) {
    const bool isB = layer & 1;
    const unsigned ar = WS_AR + layer * ARENA;
    {
      f32x4 acc[2][6];
      f32x4 ck[2], mv[2], xv[2];
      float mean[2], rs[2], cv[2];
      u32x2 wt[4];
      const float* lcl = lcs + (isB ? 96 : 0);
      { LANE_VARS
      const unsigned va = v16 + cw * 2048;
      const unsigned sa0 = ar + AR_X + bm * (512 * 1024);
      {
        auto lda = [&](unsigned so, u32x4* a) AI {
          a[0] = ld16s<AAUX>(wsr, va, so);
          a[1] = ld16s<AAUX>(wsr, va + 1024, so);
        };
        bf16x8 f[SB ? 1 : 2][6];
        u32x4 a[RA][2];
        static_assert(CK2, "");
        auto unset = [&](const u32x4* e) AI { return poisoned(umax(umax(umax(e[0][0], e[0][2]), e[1][0]), e[1][2])); };
        auto refill = [&](auto ic, unsigned cur) AI {
          constexpr int I = decltype(ic)::value;
          do {
            COUNT(0);
            if (SLP_R) __builtin_amdgcn_s_sleep(SLP_R);
            MEMFENCE;
            lda(cur, a[I]);
            bool u[4];
#pragma unroll
            for (int j = 1; j < 4; ++j) u[j] = unset(a[(I + j) % RA]);
            SCHED0;
#pragma unroll
            for (int j = 1; j < 4; ++j)
              if (u[j]) lda(cur + j * 16384, a[(I + j) % RA]);
            __builtin_amdgcn_s_waitcnt(0x0f70);
            MEMFENCE;
          } while (unset(a[I]) && --budget > 0);
        };
        STAMP(0);
        if (layer > 0) {
          const bool pre = XPRE == 1 ? cn >= 8 && cw < 4 : XPRE == 2 ? cw < 4 : false;
          if (!pre) bar();
          if (cn < 8) { if (cw < 4) __builtin_amdgcn_s_sleep(SLP_X0); else __builtin_amdgcn_s_sleep(SLP_X1); }
          else { if (cw < 4) { if (SLP_XD0) __builtin_amdgcn_s_sleep(SLP_XD0); } else if (SLP_XD1) __builtin_amdgcn_s_sleep(SLP_XD1); }
        }
#pragma unroll
        for (int j = 0; j < 4; ++j) { lda(sa0 + j * 16384, a[j]); SCHED0; }
        if (XPRE && layer > 0 && (XPRE == 1 ? cn >= 8 && cw < 4 : cw < 4)) bar();
#pragma unroll
        for (int m = 0; m < 2; ++m)
#pragma unroll
          for (int n = 0; n < 6; ++n) acc[m][n] = zero;
        asm volatile("" : "+v"(acc[0][0]), "+v"(acc[0][1]), "+v"(acc[0][2]), "+v"(acc[0][3]), "+v"(acc[0][4]), "+v"(acc[0][5]));
        asm volatile("" : "+v"(acc[1][0]), "+v"(acc[1][1]), "+v"(acc[1][2]), "+v"(acc[1][3]), "+v"(acc[1][4]), "+v"(acc[1][5]));
        if (WB0 || layer == 0) sync_lds();
#pragma unroll
        for (int nn = 0; nn < 6; ++nn) { const int n = nn < 4 ? nn + 2 : nn - 4; f[0][n] = *(const bf16x8*)(smem + v16 + n * 1024); }
        SCHED0;
        if (layer > 0) {
          const u32x2 tg = *(const u32x2*)(lflag + 10);
          if (STW) wait_vm<8>(); else drain();
          if (lane == 0) {
            unsigned old;
            asm volatile("ds_add_rtn_u32 %0, %1, %2\n s_waitcnt lgkmcnt(0)" : "=v"(old) : "v"(sbase + L_RED + 32), "v"(1) : "memory");
            if (old == 8u * layer - 1) __builtin_amdgcn_raw_buffer_store_b64(tg, wsr, 0, ar + AR_FL + bm * 256 + (cm * 16 + cn) * 8, 16);
          }
          MEMFENCE;
        }
        SCHED0;
#pragma unroll
        for (int j = 4; j < RA; ++j) { lda(sa0 + j * 16384, a[j]); SCHED0; }
        bool bad = layer > 0 && unset(a[0]);
        const int wrap = layer > 0 ? 6 : 4;
        STAMP(1);
        auto group = [&](auto modec, auto hc, int g) AI {
          constexpr int MODE = decltype(modec)::value, H = decltype(hc)::value;
          constexpr bool LAST = MODE == 3, CHK = MODE == 0, LD = MODE < 2;
          const unsigned gs = (g < wrap ? g : g - 4) * QGRP, gn = (g + 1 < wrap ? g + 1 : g - 3) * QGRP;
          const unsigned sc = sa0 + g * 65536, so = sc + RA * 16384;
          unsigned gl = sbase + gs + v16;
          if constexpr (LAST) asm volatile("" : "+v"(gl));
          static_for<4>([&](auto ic) AI {
            constexpr int i = decltype(ic)::value, I = H * 4 + i;
            constexpr bool END = LAST && i == 3;
            SCHED0;
            if (i == 3 && !LAST) { if (SB) bar(); else sync_lds(); SCHED0; }
            if (i == 0 && LAST) {
              bar();
              if (HPRIO && isB && cw >= ESET) __builtin_amdgcn_s_setprio(HPRIO);
              SCHED0;
            }
            if constexpr (QPR && !LAST) __builtin_amdgcn_s_setprio(i == 3 ? 3 : 2 - i);
            if constexpr (QPR == 2 && LAST) { if (!isB) __builtin_amdgcn_s_setprio(3 - i); else if (i == 0 && cw < ESET) __builtin_amdgcn_s_setprio(0); }
            if constexpr (QPR == 1 && LAST && i == 0) { if (!isB || cw < ESET) __builtin_amdgcn_s_setprio(0); }
            if constexpr (CHK) { if (__builtin_expect(bad, 0)) refill(std::integral_constant<int, I>{}, sc + i * 16384); }
            SCHED0;
            const bf16x8* fc = f[SB ? 0 : i & 1];
            bf16x8* fn = f[SB ? 0 : (i + 1) & 1];
            const u32x4* an = a[(I + 1) % RA];
            const unsigned slot = i < 3 ? gs + (i + 1) * QSLOT : gn;
            const char* pb = LAST ? (const char*)((const LDS(char)*)gl + (i + 1) * QSLOT) : smem + slot + v16;
            unsigned mx = 0;
            static_for<12>([&](auto jc) AI {
              constexpr int j = decltype(jc)::value, nn = j >> 1, m = j & 1, n = nn < 4 ? nn + 2 : nn - 4;
              if constexpr (n < 4) mfma_acc(acc[m][n], fc[n], as_frag(a[I][m]));
              else mfma_acc(acc[m][n], as_frag(a[I][m]), fc[n]);
              SCHED0;
              if constexpr (!END) {
                if constexpr (SB) { if constexpr (m == 1) fn[n] = *(const bf16x8*)(pb + n * 1024); }
                else if constexpr (j < 6) { constexpr int r = j < 4 ? j + 2 : j - 4; fn[r] = *(const bf16x8*)(pb + r * 1024); }
              }
              if constexpr (LAST && i == PRES) {
                if constexpr (j == 0) { ck[0] = *(const f32x4*)(lcl + 32 + fq * 4); ck[1] = *(const f32x4*)(lcl + 48 + fq * 4); }
                if constexpr (j == 1) {
                  unsigned o = sbase + L_RED + 64 + cw * 128;
                  asm volatile("" : "+s"(o));
                  o += fr * 4;
                  asm volatile("" : "+v"(o));
                  const LDS(float)* pm = (const LDS(float)*)o;
                  mean[0] = pm[0]; rs[0] = pm[272];
                  asm volatile("" : "+v"(o));
                  pm = (const LDS(float)*)o;
                  mean[1] = pm[16]; rs[1] = pm[288];
                }
                if constexpr (j == 2) { cv[0] = lcl[64 + fr]; cv[1] = lcl[80 + fr]; }
                if constexpr (j == 3) {
                  unsigned o = sbase + L_RED + 64 + cw * 128;
                  asm volatile("" : "+s"(o));
                  o += fq * 16;
                  asm volatile("" : "+v"(o));
                  mv[0] = *(const LDS(f32x4)*)o; mv[1] = ((const LDS(f32x4)*)o)[4];
                }
                if constexpr (j == 4) { const float* px = (isB ? lcl : lrstd + 16 + cw * 32) + fq * 4; xv[0] = *(const f32x4*)px; xv[1] = *(const f32x4*)(px + 16); }
                if constexpr (j == 5) {
                  const char* wp = smem + L_WP + (lane >> 2) * 64 + (lane & 3) * 8;
                  wt[0] = trread(wp); wt[1] = trread(wp + 1024); wt[2] = trread(wp + 32); wt[3] = trread(wp + 1024 + 32);
                }
              }
              if constexpr (CHK) {
                if constexpr (CK2) { if constexpr (j == CKJ) mx = umax(umax(umax(an[0][0], an[0][2]), an[1][0]), an[1][2]); }
                else {
                  if constexpr (j == 6) mx = umax4(an[0]);
                  if constexpr (j == 8) mx = umax4(mx, an[1]);
                }
                if constexpr (j == CKB) bad = poisoned(mx);
              }
              if constexpr (j == 10 && LD) a[I][0] = ld16s<AAUX>(wsr, va, so + i * 16384);
              if constexpr (j == 11 && LD) a[I][1] = ld16s<AAUX>(wsr, va + 1024, so + i * 16384);
              SCHED0;
            });
          });
        };
        const std::integral_constant<int, 0> c0; const std::integral_constant<int, 1> c1;
        const std::integral_constant<int, 3> c3;
        int g = 0;
        {
          if (layer > 0) {
#pragma unroll 1
            ALN for (; g < NCHK; ++g) group(c0, c0, g);
          }
#pragma unroll 1
          ALN for (; g < 7; ++g) group(c1, c0, g);
          group(c3, c0, 7);
        }
        if (APRIO && !isB && cw >= 4) __builtin_amdgcn_s_setprio(APRIO);
        if (BPRIO && isB && cw < ESET) __builtin_amdgcn_s_setprio(BPRIO);
        mfma_done();
      }
      }
      STAMP(2);
      LANE_VARS
      char* const scr = smem;

      u32x4 qf[2], kf[2];
      u32x2 vf[2][2];
      auto fold_rows = [&](int part, u32x4* out, const f32x4* c, auto sc) AI {
        constexpr bool S = decltype(sc)::value;
#pragma unroll
        for (int m = 0; m < 2; ++m) {
          f32x4 a0 = fma4<S>(c[0], mean[m], acc[m][part * 2]), a1 = fma4<S>(c[1], mean[m], acc[m][part * 2 + 1]);
          out[m] = u32x4{pk2(a0[0], a0[1]), pk2(a0[2], a0[3]), pk2(a1[0], a1[1]), pk2(a1[2], a1[3])};
        }
      };
      auto fold_v = [&](bool scaled) AI {
#pragma unroll
        for (int m = 0; m < 2; ++m) {
          f32x4 a0 = fma4<(SCAL & 2) != 0>(mv[m], cv[0], acc[m][4]), a1 = fma4<(SCAL & 2) != 0>(mv[m], cv[1], acc[m][5]);
          if (scaled) { a0 = mul4<(SCAL & 2) != 0>(a0, xv[m]); a1 = mul4<(SCAL & 2) != 0>(a1, xv[m]); }
          vf[m][0] = u32x2{pk2(a0[0], a0[1]), pk2(a0[2], a0[3])};
          vf[m][1] = u32x2{pk2(a1[0], a1[1]), pk2(a1[2], a1[3])};
        }
      };
      fold_rows(1, kf, ck, std::integral_constant<bool, (SCAL & 1) != 0>{});
      const unsigned otile = WS_AR + (layer & (NOB - 1)) * ARENA + AR_O + xt_off(bm * 16 + cw * 2, head);
      const unsigned opar = OCK >= 4 ? (isB ? 1u : 0x10000u) : NOB == 1 ? layer & 1 : layer >> 1;
      auto put_o = [&](int m, f32x4 a, f32x4 b) AI {
        constexpr unsigned K = OCK >= 4 ? ~0x10001u : ~1u;
        __builtin_amdgcn_raw_buffer_store_b128(u32x4{(pk2(a[0], a[1]) & K) | opar, pk2(a[2], a[3]), (pk2(b[0], b[1]) & K) | opar, pk2(b[2], b[3])}, wsr, v16 + m * 1024, otile, SAUX);
      };

      if (isB) {
        char* ex = scr;
        fold_v(false);
        const unsigned hrec = ar + AR_KV + (unsigned)(bm * 32 + head) * KVREC;
        {
          const u32x4 vl = u32x4{vf[1][0][0], vf[1][0][1], vf[1][1][0], vf[1][1][1]};
          if (cw == 7) {
            __builtin_amdgcn_raw_buffer_store_b128(clean(kf[1]), wsr, v16, hrec, 16);
            STPAD;
            __builtin_amdgcn_raw_buffer_store_b128(clean(vl), wsr, v16 + 1024, hrec, 16);
          }
          char* e2 = ex + (cw + 1) * 2048;
          *(u32x4*)(e2 + lane * 16) = kf[1];
          *(u32x2*)(e2 + 1024 + lane * 8) = vf[1][0];
          *(u32x2*)(e2 + 1536 + lane * 8) = vf[1][1];
          MEMFENCE;
          lflag[cw] = layer;
          MEMFENCE;
          if (HPRIO && (!BPRIO || cw >= ESET)) __builtin_amdgcn_s_setprio(0);
        }
        SCHED0;
        fold_rows(0, qf, xv, std::integral_constant<bool, (SCAL & 4) != 0>{});
        u32x4 kp;
        u32x4 va0 = __builtin_shufflevector(vf[0][0], vf[0][0], -1, -1, 0, 1), vb0 = __builtin_shufflevector(vf[0][1], vf[0][1], -1, -1, 0, 1);
        auto setp = [&](u32x4& d, u32x2 v) AI { d[0] = v[0]; d[1] = v[1]; };
        auto ldh = [&]() AI {
          MEMFENCE;
          kp = ld16s(wsr, v16, hrec - 32 * KVREC);
          setp(va0, __builtin_amdgcn_raw_buffer_load_b64(wsr, v16 + 1024, hrec - 32 * KVREC, 16));
          MEMFENCE;
          setp(vb0, __builtin_amdgcn_raw_buffer_load_b64(wsr, v16 + 1032, hrec - 32 * KVREC, 16));
          MEMFENCE;
        };
        auto prev = [&]() AI {
          if (cw > 0) {
            for (;;) {
              MEMFENCE;
              const unsigned fl = lflag[cw - 1];
              MEMFENCE;
              kp = *(const u32x4*)(ex + cw * 2048 + lane * 16);
              setp(va0, *(const u32x2*)(ex + cw * 2048 + 1024 + lane * 8));
              MEMFENCE;
              setp(vb0, *(const u32x2*)(ex + cw * 2048 + 1536 + lane * 8));
              MEMFENCE;
              if (__builtin_expect(__builtin_amdgcn_readfirstlane(fl) == (unsigned)layer, 1) || --budget <= 0) break;
              __builtin_amdgcn_s_sleep(1);
            }
          } else if (bm > 0) {
            if (SLP_HX) __builtin_amdgcn_s_sleep(SLP_HX);
            if (!HEARLY) ldh();
            while (__builtin_expect(poisoned(umax4(umax4(kp), u32x4{va0[0], va0[1], vb0[0], vb0[1]})), 0) && --budget > 0) { COUNT(2); ldh(); }
          } else {
            kp = u32x4{0u, 0u, 0u, 0u};
            setp(va0, u32x2{0u, 0u}); setp(vb0, u32x2{0u, 0u});
            asm volatile("" : "+v"(kp), "+v"(va0), "+v"(vb0));
          }
        };
        if (HEARLY && cw == 0 && bm > 0) ldh();
        const float scale = 0.17677669529663687f * LOG2E;
        const bool odd = fr & 1;
        const bool own0 = fr >= fq * 4 + (fr & 1), own1 = fr >= fq * 4 + (fr & 1) + 2;
        const bool nopre = (bm == 0) && (cw == 0);
        float l[2][2], mx[2], rk[2][2], sum[2];
#pragma unroll
        for (int m = 0; m < 2; ++m) {
          const float* rb = lrstd + (cw * 2 + m) * 16 + fq * 4 + (fr & 1);
          rk[m][0] = rb[own0 ? 16 : 0];
          rk[m][1] = rb[own1 ? 18 : 2];
        }
        float qk[2][2];
#pragma unroll
        for (int m = 0; m < 2; ++m) { const float qs = rs[m] * scale; qk[m][0] = qs * rk[m][0]; qk[m][1] = qs * rk[m][1]; }
        const unsigned e0 = odd ? 0x01000c0cu : 0x0c0c0100u, e1 = odd ? 0x03020c0cu : 0x0c0c0302u, ez = 0x0c0c0c0cu;
        u32x4 sel = u32x4{own0 ? ez : e0, own1 ? ez : e1, own0 ? e0 : ez, own1 ? e1 : ez};
        u32x4 va1 = u32x4{vf[0][0][0], vf[0][0][1], vf[1][0][0], vf[1][0][1]}, vb1 = u32x4{vf[0][1][0], vf[0][1][1], vf[1][1][0], vf[1][1][1]};
        auto score = [&](int m) AI {
          f32x4 s0 = mfma16(as_frag(m == 0 ? kp : kf[0]), as_frag(qf[m]), zero);
          f32x4 s1 = mfma16(as_frag(kf[m]), as_frag(qf[m]), zero);
          float a = own0 ? (odd ? s1[1] : s1[0]) : (odd ? s0[1] : s0[0]);
          float b = own1 ? (odd ? s1[3] : s1[2]) : (odd ? s0[3] : s0[2]);
          l[m][0] = a * qk[m][0]; l[m][1] = b * qk[m][1];
          if (m == 0) {
            if (nopre && !own0) l[m][0] = -3.0e38f;
            if (nopre && !own1) l[m][1] = -3.0e38f;
          }
          mx[m] = fmx(l[m][0], l[m][1]);
        };
        auto expo = [&](int m) AI {
          l[m][0] = __builtin_amdgcn_exp2f(l[m][0] - mx[m]);
          l[m][1] = __builtin_amdgcn_exp2f(l[m][1] - mx[m]);
          sum[m] = l[m][0] + l[m][1];
        };
        f32x4 ov[2][2];
        auto pv = [&](int m) AI {
          const unsigned pr = pk2(mul1(mul1(l[m][0], rk[m][0]), sum[m]), mul1(mul1(l[m][1], rk[m][1]), sum[m]));
          const u32x4 pf = u32x4{__builtin_amdgcn_perm(pr, pr, sel[0]), __builtin_amdgcn_perm(pr, pr, sel[1]),
                                 __builtin_amdgcn_perm(pr, pr, sel[2]), __builtin_amdgcn_perm(pr, pr, sel[3])};
          ov[m][0] = mfma16(as_frag(m ? va1 : va0), as_frag(pf), zero);
          ov[m][1] = mfma16(as_frag(m ? vb1 : vb0), as_frag(pf), zero);
        };
        score(1);
        asm volatile("" : "+v"(qf[0]), "+v"(qk[0][0]), "+v"(qk[0][1]), "+v"(mx[1]), "+v"(l[1][0]), "+v"(l[1][1]));
        asm volatile("" : "+v"(sel), "+v"(va1), "+v"(vb1), "+v"(va0), "+v"(vb0));
        SCHED0;
        STAMP(3);
        prev();
        score(0);
        xred2<true>(mx[0], mx[1]);
        expo(0); expo(1);
        xred2<false, true>(sum[0], sum[1]);
        pv(0); pv(1);
        SCHED0;
        put_o(0, ov[0][0], ov[0][1]); put_o(1, ov[1][0], ov[1][1]);
        if (BPRIO) __builtin_amdgcn_s_setprio(0);
        STAMP(4);
      } else {
        constexpr int PS = 144;
        fold_v(true);
        char* PK = smem + L_STG;
        char* VF = scr;
        const bf16x8 wf0 = as_frag(u32x4{wt[0][0], wt[0][1], wt[1][0], wt[1][1]}), wf1 = as_frag(u32x4{wt[2][0], wt[2][1], wt[3][0], wt[3][1]});
#pragma unroll
        for (int t = 0; t < 2; ++t)
          *(u32x4*)(VF + (cw * 2 + t) * 1024 + lane * 16) = u32x4{vf[0][t][0], vf[0][t][1], vf[1][t][0], vf[1][t][1]};
        {
          u32x4 pk[2][2];
          phi_map2<0>(kf, rs, wf0, wf1, pk, []() {});
          char* d0 = PK + cw * 4096 + fr * 64 + ((fr >> 2) & 1) * 32 + ((fq ^ ((fr >> 3) * 2)) * 8);
          char* d1 = PK + cw * 4096 + fr * 64 + (((fr >> 2) & 1) ^ 1) * 32 + ((fq ^ ((fr >> 3) * 2)) * 8);
#pragma unroll
          for (int m = 0; m < 2; ++m) {
            *(u32x2*)(d0 + m * 2048) = u32x2{pk[m][0][0], pk[m][0][1]};
            *(u32x2*)(d0 + m * 2048 + 1024) = u32x2{pk[m][0][2], pk[m][0][3]};
            *(u32x2*)(d1 + m * 2048) = u32x2{pk[m][1][0], pk[m][1][1]};
            *(u32x2*)(d1 + m * 2048 + 1024) = u32x2{pk[m][1][2], pk[m][1][3]};
          }
        }
        if (APRIO && ARST == 0) __builtin_amdgcn_s_setprio(0);
        sync_lds();
        STAMP(3);
        const unsigned rec = ar + AR_KV + (unsigned)head * KVREC;
        {
          const u32x4 ones = u32x4{0x3f803f80u, 0x3f803f80u, 0x3f803f80u, 0x3f803f80u};
          f32x4 c0 = zero, cz = zero;
          constexpr int TS = 2048;
          const char* ad = PK + (cw & 1) * 1024 + (fq * 4 + (fr >> 2)) * 64 + (((cw >> 1) ^ fq) & 1) * 32 + (((fr & 3) ^ (fq & 2)) * 8);
          const char* vd = VF + (cw >> 2) * 1024 + lane * 16;
          bf16x8 a[8], b0[8];
#pragma unroll
          for (int ks = 0; ks < 8; ++ks) {
            u32x2 a0 = trread(ad + ks * 2 * TS), a1 = trread(ad + (ks * 2 + 1) * TS);
            a[ks] = as_frag(u32x4{a0[0], a0[1], a1[0], a1[1]});
            b0[ks] = *(const bf16x8*)(vd + ks * 2048);
          }
          SCHED0;
          f32x4 c1 = zero;
#pragma unroll
          for (int ks = 0; ks < 8; ks += 2) {
            c0 = mfma16(a[ks], b0[ks], c0);
            c1 = mfma16(a[ks + 1], b0[ks + 1], c1);
          }
          c0 += c1;
          const unsigned dst = rec + (unsigned)bm * (32 * KVREC);
          st16(wsr, dst + cw * 1024 + v16, clean(__builtin_bit_cast(u32x4, c0)));
          SCHED0;
          if (cw < 4) {
            f32x4 cy = zero;
#pragma unroll
            for (int ks = 0; ks < 8; ks += 2) {
              cz = mfma16(a[ks], as_frag(ones), cz);
              cy = mfma16(a[ks + 1], as_frag(ones), cy);
            }
            cz += cy;
            if (fr == 0) st16(wsr, dst + 8192 + cw * 64 + fq * 16, clean(__builtin_bit_cast(u32x4, cz)));
          }
        }
        SCHED0;
        STAMP(4);
        bar();
        const f32x4 cq[2] = {*(const f32x4*)(lcl + fq * 4), *(const f32x4*)(lcl + 16 + fq * 4)};
        fold_rows(0, qf, cq, std::false_type{});
        u32x4 pq[2][2];
        u32x4 zp = u32x4{0u, 0u, 0u, 0u}, zq = zp;
        u32x4 part[8];
        auto ldz = [&]() AI {
          zp = ld16(wsr, rec + fq * (32 * KVREC) + 8192 + fr * 16);
          zq = ld16(wsr, rec + (4 + fq) * (32 * KVREC) + 8192 + fr * 16);
        };
        auto ldkv = [&]() AI {
          MEMFENCE;
          if (KPOS && SLP_K0 && cw < 4) __builtin_amdgcn_s_sleep(SLP_K0);
          if (cw == ZW) ldz();
#pragma unroll
          for (int rb = 0; rb < 8; ++rb) part[rb] = ld16s<KAUX>(wsr, v16, rec + rb * (32 * KVREC) + cw * 1024);
          MEMFENCE;
        };
        phi_map2<KPOS>(qf, rs, wf0, wf1, pq, ldkv);
        asm volatile("" : "+v"(pq[0][0]), "+v"(pq[0][1]), "+v"(pq[1][0]), "+v"(pq[1][1]));
        SCHED0;
        STAMP(5);
        char* kvf = smem + L_A2;
        if (!KPOS) {
          if (SLP_K) __builtin_amdgcn_s_sleep(SLP_K);
          ldkv();
        }
        f32x4 s4;
        for (;;) {
          MEMFENCE;
          auto nans = [&](f32x4 v) AI { return __builtin_amdgcn_ballot_w64(__builtin_isunordered(v[0], v[1])) | __builtin_amdgcn_ballot_w64(__builtin_isunordered(v[2], v[3])); };
          unsigned long long nan = 0;
          if (cw == ZW) {
            f32x4 z = __builtin_bit_cast(f32x4, zp) + __builtin_bit_cast(f32x4, zq);
            nan = nans(z);
            float z0 = z[0], z1 = z[1], z2 = z[2], z3 = z[3];
            xred4<false>(z0, z1, z2, z3);
            if (fq == 0) *(u32x2*)(smem + L_Z + (fr >> 3) * 64 + (fr & 3) * 16 + ((fr >> 2) & 1) * 8) = u32x2{pk2(z0, z1), pk2(z2, z3)};
          }
          s4 = __builtin_bit_cast(f32x4, part[0]);
#pragma unroll
          for (int rb = 1; rb < 8; ++rb) { s4 += __builtin_bit_cast(f32x4, part[rb]); SCHED0; }
          if (__builtin_expect((nan | nans(s4)) == 0, 1)) break;
          bool again = false;
          if (poisoned(umax4(umax4(zp), zq))) { again = true; ldz(); }
#pragma unroll
          for (int rb = 0; rb < 8; ++rb)
            if (poisoned(umax4(part[rb]))) { again = true; part[rb] = ld16s<KAUX>(wsr, v16, rec + rb * (32 * KVREC) + cw * 1024); }
          __builtin_amdgcn_s_waitcnt(0x0f70);
          if (!again || --budget <= 0) break;
          COUNT(2);
        }
        STAMP(6);
        *(u32x2*)(kvf + (cw >> 1) * 1024 + lane * 16 + (cw & 1) * 8) = u32x2{pk2(s4[0], s4[1]), pk2(s4[2], s4[3])};
        if (APRIO && ARST == 1) __builtin_amdgcn_s_setprio(0);
        sync_lds();
        STAMP(7);
        bf16x8 kvr[2][2], zf[2];
#pragma unroll
        for (int s = 0; s < 2; ++s) {
          kvr[0][s] = *(const bf16x8*)(kvf + (0 * 2 + s) * 1024 + lane * 16);
          kvr[1][s] = *(const bf16x8*)(kvf + (1 * 2 + s) * 1024 + lane * 16);
          zf[s] = *(const bf16x8*)(smem + L_Z + s * 64 + fq * 16);
        }
#pragma unroll
        for (int m = 0; m < 2; ++m) {
          f32x4 c0 = mfma16(kvr[0][1], as_frag(pq[m][1]), mfma16(kvr[0][0], as_frag(pq[m][0]), zero));
          f32x4 c1 = mfma16(kvr[1][1], as_frag(pq[m][1]), mfma16(kvr[1][0], as_frag(pq[m][0]), zero));
          f32x4 cd = mfma16(zf[1], as_frag(pq[m][1]), mfma16(zf[0], as_frag(pq[m][0]), zero));
          const float iden = __builtin_amdgcn_rcpf(cd[0] + 1e-6f);
          c0 *= iden; c1 *= iden;
          put_o(m, c0, c1);
        }
        STAMP(8);
      }
    }
    {
      LANE_VARS
      const int mt = bm * 16 + cw * 2 + cm;
      u32x4 xr[2];
#pragma unroll
      for (int j = 0; j < 2; ++j) xr[j] = ld16s<KAUX>(wsr, v16, ar + AR_X + xt_off(mt, cn * 2 + j));
      const unsigned so0 = WS_AR + (layer & (NOB - 1)) * ARENA + AR_O + xt_off(mt, 0);
      const unsigned opar = OCK >= 4 ? (isB ? 0x10000u : 1u) : NOB == 1 ? layer & 1 : layer >> 1;
      auto diff = [&](u32x4 v) AI { return OCK >= 4 ? (v[0] | v[2]) & opar : ((v[0] ^ opar) | (v[2] ^ opar)) & 1u; };
      auto or3 = [&](unsigned x, unsigned y, unsigned z) AI { return __builtin_amdgcn_bitop3_b32(x, y, z, 0xfe); };
      constexpr int OW = OCK == 5 ? 4 : OCK == 2 || OCK == 4 ? 2 : 1;
      auto dif3 = [&](u32x4 v) AI { return __builtin_amdgcn_bitop3_b32(v[0], v[2], opar, 0x7e); };
      auto any = [&](unsigned d) AI { return __builtin_amdgcn_ballot_w64(d != 0) != 0; };
      const int g0 = OORD == 2 ? cm : OORD ? (head >> 3) & 2 : head >> 3;
      u32x4 a[RO];
      auto refill = [&](auto ic, auto wc, unsigned cur, unsigned nxt) AI {
        constexpr int I = decltype(ic)::value, W = decltype(wc)::value;
        do {
          COUNT(1);
          if (SLP_R) __builtin_amdgcn_s_sleep(SLP_R);
          MEMFENCE;
          a[I] = ld16s(wsr, v16, cur + I * 16384);
          bool u[RO];
#pragma unroll
          for (int j = I + 1; j < I + W; ++j) u[j % RO] = any(diff(a[j % RO]));
          SCHED0;
#pragma unroll
          for (int j = I + 1; j < I + W; ++j)
            if (u[j % RO]) a[j % RO] = ld16s(wsr, v16, (j < RO ? cur : nxt) + (j % RO) * 16384);
          __builtin_amdgcn_s_waitcnt(0x0f70);
          MEMFENCE;
        } while (any(OW == 4 ? diff(a[I]) | diff(a[I + 1]) | diff(a[I + 2]) | diff(a[I + 3]) : OW == 2 ? diff(a[I]) | diff(a[I + 1]) : diff(a[I])) && --budget > 0);
      };
      const unsigned sf = so0 + g0 * (RO * 16384);
      const bool late = isB && cw == 0;
      if (late) bar();
      if (cn < 8) {
        if (late) __builtin_amdgcn_s_sleep(SLP_O0); else if (SLP_OB != SLP_O && isB) __builtin_amdgcn_s_sleep(SLP_OB); else __builtin_amdgcn_s_sleep(SLP_O);
      } else {
        if (late) { if (SLP_O0D) __builtin_amdgcn_s_sleep(SLP_O0D); }
        else if (SLP_OBD != SLP_OD && isB) { if (SLP_OBD) __builtin_amdgcn_s_sleep(SLP_OBD); }
        else if (SLP_OD) __builtin_amdgcn_s_sleep(SLP_OD);
      }
#pragma unroll
      for (int j = 0; j < NINIT_O; ++j) { a[j] = ld16s<OLAUX>(wsr, v16, sf + j * 16384); SCHED0; }
      if (!late) bar();
      STAMP(9);
      bf16x8 wf[2][4];
      const char* pw0 = smem + L_STG + v16;
#pragma unroll
      for (int n = 0; n < 8; ++n) wf[n >> 2][n & 3] = *(const bf16x8*)(pw0 + g0 * (RO * 4096) + n * 1024);
      f32x4 acc[4];
#pragma unroll
      for (int n = 0; n < 4; ++n)
        acc[n] = f32x4{bflo(xr[n >> 1][(n & 1) * 2]), bfhi(xr[n >> 1][(n & 1) * 2]), bflo(xr[n >> 1][(n & 1) * 2 + 1]), bfhi(xr[n >> 1][(n & 1) * 2 + 1])};
      asm volatile("" : "+v"(acc[0]), "+v"(acc[1]), "+v"(acc[2]), "+v"(acc[3]));
      SCHED0;
      STAMP(10);
      if (any(OW == 4 ? diff(a[0]) | diff(a[1]) | diff(a[2]) | diff(a[3]) : OW == 2 ? diff(a[0]) | diff(a[1]) : diff(a[0]))) refill(std::integral_constant<int, 0>{}, std::integral_constant<int, NINIT_O>{}, sf, sf);
      SCHED0;
#pragma unroll
      for (int j = NINIT_O; j < RO; ++j) { a[j] = ld16s<OLAUX>(wsr, v16, sf + j * 16384); SCHED0; }
      STAMP(11);
      bool bad = false;
      auto group = [&](auto lastc, int g) AI {
        constexpr bool LAST = decltype(lastc)::value;
        const int gc = OORD == 2 ? g ^ g0 : OORD == 1 ? (((g & 1) * 2 + (g >> 1)) ^ g0) : (g + g0) & 3;
        const int gn = OORD == 2 ? (g + 1) ^ g0 : OORD == 1 ? ((((g + 1) & 1) * 2 + (((g + 1) >> 1) & 1)) ^ g0) : (g + g0 + 1) & 3;
        const unsigned sc = so0 + gc * (RO * 16384), so = so0 + gn * (RO * 16384);
        const char* pc = pw0 + gc * (RO * 4096);
        const char* pn = pw0 + gn * (RO * 4096);
        static_for<RO>([&](auto ic) AI {
          constexpr int i = decltype(ic)::value;
          SCHED0;
          if constexpr (WPR && !LAST && i == 0) { if (g == 0) __builtin_amdgcn_s_setprio(3); else if (g == 2) __builtin_amdgcn_s_setprio(2); }
          if constexpr (WPR && LAST && i == WP1) __builtin_amdgcn_s_setprio(1);
          if constexpr (WPR && LAST && i == WP2) __builtin_amdgcn_s_setprio(0);
          if constexpr (OCK != 3 && i % OW == 0)
            if (__builtin_expect(bad, 0)) refill(ic, std::integral_constant<int, (LAST && i + WIN_O > RO) ? RO - i : WIN_O>{}, sc, so);
          SCHED0;
          unsigned mx = 0, my = 0;
          static_for<4>([&](auto nc) AI {
            constexpr int n = decltype(nc)::value;
            mfma_acc(acc[n], wf[i & 1][n], as_frag(a[i]));
            SCHED0;
            if constexpr (i + 2 < RO) wf[i & 1][n] = *(const bf16x8*)(pc + (i + 2) * 4096 + n * 1024);
            else if constexpr (!LAST) wf[i & 1][n] = *(const bf16x8*)(pn + (i + 2 - RO) * 4096 + n * 1024);
            if constexpr (!(LAST && i == RO - 1)) {
              if constexpr (OCK == 0) {
                if constexpr (n == 1) mx = diff(a[(i + 1) % RO]);
                if constexpr (n == 2) bad = any(mx);
              }
              if constexpr (OCK == 1) {
                if constexpr (n == 1) mx = dif3(a[(i + 1) % RO]);
                if constexpr (n == 2) bad = any(mx & 1u);
              }
              if constexpr (OCK == 4 && (i & 1)) {
                const u32x4 &p = a[(i + 1) % RO], &q = a[(i + 2) % RO];
                if constexpr (n == OC0) mx = or3(p[0], p[2], q[0]);
                if constexpr (n == OC1) bad = any(__builtin_amdgcn_bitop3_b32(mx, q[2], opar, 0xa8));
              }
              if constexpr (OCK == 5 && (i & 3) == 3) {
                const u32x4 &p = a[(i + 1) % RO], &q = a[(i + 2) % RO], &r = a[(i + 3) % RO], &t = a[(i + 4) % RO];
                if constexpr (n == 0) mx = or3(p[0], p[2], q[0]);
                if constexpr (n == 1) mx = or3(or3(mx, q[2], r[0]), r[2], t[0]);
                if constexpr (n == 2) bad = any(__builtin_amdgcn_bitop3_b32(mx, t[2], opar, 0xa8));
              }
              if constexpr (OCK == 6) { if constexpr (n == OC1) bad = any(diff(a[(i + 1) % RO])); }
              if constexpr (OCK == 2 && (i & 1)) {
                if constexpr (n == 0) mx = dif3(a[(i + 1) % RO]);
                if constexpr (n == 1) my = dif3(a[(i + 2) % RO]);
                if constexpr (n == 2) bad = any((mx | my) & 1u);
              }
            }
            if constexpr (n == 3 && !LAST) a[i] = ld16s<OLAUX>(wsr, v16, so + i * 16384);
            SCHED0;
          });
        });
      };
#pragma unroll 1
      ALN for (int g = 0; g < 3; ++g) group(std::false_type{}, g);
      group(std::true_type{}, 3);
      mfma_done();
      STAMP(12);
#pragma unroll
      for (int j = 0; j < 2; ++j)
#pragma unroll
        for (int e = 0; e < 4; ++e) xr[j][e] = pk2(acc[j * 2 + (e >> 1)][(e & 1) * 2], acc[j * 2 + (e >> 1)][(e & 1) * 2 + 1]);
      if (layer == 3) {
        u32x4 yo[2];
#pragma unroll
        for (int j = 0; j < 2; ++j) {
          auto u = __builtin_amdgcn_permlane16_swap(xr[j][0], xr[j][2], false, false);
          auto v = __builtin_amdgcn_permlane16_swap(xr[j][1], xr[j][3], false, false);
          yo[j] = u32x4{u[0], v[0], u[1], v[1]};
        }
        if (OFULL) {
          u32x4 ya, yb;
#pragma unroll
          for (int e = 0; e < 4; ++e) {
            ya[e] = __builtin_amdgcn_update_dpp(yo[0][e], yo[1][e], 0x128, 0xf, 0xc, false);
            yb[e] = __builtin_amdgcn_update_dpp(yo[1][e], yo[0][e], 0x128, 0xf, 0x3, false);
          }
          const unsigned vo = (fr & 7) * 2048 + (fr >> 3) * 64 + (fq & 1) * 32 + (fq >> 1) * 16;
          __builtin_amdgcn_raw_buffer_store_b128(ya, mk_rsrc(p.out, 4 * MBY), vo, mt * 32768 + cn * 128, OAUX);
          __builtin_amdgcn_raw_buffer_store_b128(yb, mk_rsrc(p.out, 4 * MBY), vo, mt * 32768 + cn * 128 + 16384, OAUX);
        } else {
          const unsigned vo = fr * 2048 + (fq & 1) * 32 + (fq >> 1) * 16;
          __builtin_amdgcn_raw_buffer_store_b128(yo[0], mk_rsrc(p.out, 4 * MBY), vo, mt * 32768 + cn * 128, OAUX);
          __builtin_amdgcn_raw_buffer_store_b128(yo[1], mk_rsrc(p.out, 4 * MBY), vo + 64, mt * 32768 + cn * 128, OAUX);
        }
        STAMP(13);
        STAMP(14);
        STAMP_DUMP;
        return;
      }
      const u32x2 stat = put_x(xr, v16, ar + ARENA + AR_X + xt_off(mt, cn * 2));
      if (fq == 0) __builtin_amdgcn_raw_buffer_store_b64(stat, wsr, fr * 8, ar + ARENA + AR_ST + (mt * 16 + cn) * 128, 16);
      MEMFENCE;
      STAMP(14);
    }
  }
}

extern "C" void kernel_launch(void* const* d_in, const int* in_sizes, int n_in, void* d_out, int out_size,
                              void* d_ws, size_t ws_size, hipStream_t stream) {
  (void)hipFuncSetAttribute((const void*)mega_kernel, hipFuncAttributeMaxDynamicSharedMemorySize, MEGA_LDS);
  MegaParams m;
  m.ws = (char*)d_ws; m.out = (u16*)d_out; m.x = (const u16*)d_in[0]; m.wphi = (const u16*)d_in[2];
  m.wqkvA = (const u16*)d_in[1]; m.wqkvB = (const u16*)d_in[5];
  m.woA = (const u16*)d_in[3]; m.woB = (const u16*)d_in[6];
  mega_kernel<<<256, 768, MEGA_LDS, stream>>>(m);
}
```

```cpp
#include <hip/hip_runtime.h>
#include <stdint.h>
#include <type_traits>

#define DI __device__ __forceinline__
#define AI __attribute__((always_inline))
typedef unsigned short u16;
typedef __attribute__((ext_vector_type(8))) short bf16x8;
typedef __attribute__((ext_vector_type(4))) short s16x4;
typedef __attribute__((ext_vector_type(4))) float f32x4;
typedef __attribute__((ext_vector_type(2))) float f32x2;
typedef __attribute__((ext_vector_type(2))) __bf16 bf2;
typedef __attribute__((ext_vector_type(4))) unsigned u32x4;
typedef __attribute__((ext_vector_type(2))) unsigned u32x2;
typedef __attribute__((address_space(3))) s16x4* lds_s16x4_ptr;
#define LDS(T) __attribute__((address_space(3))) T
template <int N> using IC = std::integral_constant<int, N>;

constexpr float LOG2E = 1.4426950408889634f;

DI unsigned pk2(float a, float b) {
  f32x2 v = {a, b};
  bf2 r = __builtin_convertvector(v, bf2);
  return __builtin_bit_cast(unsigned, r);
}
DI float bflo(unsigned u) { return __uint_as_float(u << 16); }
DI float bfhi(unsigned u) { return __uint_as_float(u & 0xffff0000u); }
DI f32x4 mfma16(bf16x8 a, bf16x8 b, f32x4 c) { return __builtin_amdgcn_mfma_f32_16x16x32_bf16(a, b, c, 0, 0, 0); }
DI void mfma_acc(f32x4& c, bf16x8 a, bf16x8 b) { asm volatile("v_mfma_f32_16x16x32_bf16 %0, %1, %2, %0" : "+v"(c) : "v"(a), "v"(b)); }
DI void mfma_done() { asm volatile("s_nop 7\n\ts_nop 1" ::: "memory"); __builtin_amdgcn_sched_barrier(0); }
DI bf16x8 as_frag(u32x4 v) { return __builtin_bit_cast(bf16x8, v); }
DI float dot2(unsigned a, unsigned b, float c) {
  return __builtin_amdgcn_fdot2_f32_bf16(__builtin_bit_cast(bf2, a), __builtin_bit_cast(bf2, b), c, false);
}
DI u32x2 trread(const char* p) { return __builtin_bit_cast(u32x2, __builtin_amdgcn_ds_read_tr16_b64_v4i16((lds_s16x4_ptr)p)); }
DI u32x2 trl(unsigned a) { return __builtin_bit_cast(u32x2, __builtin_amdgcn_ds_read_tr16_b64_v4i16((lds_s16x4_ptr)a)); }
DI float fmx(float a, float b) { return __builtin_elementwise_maximum(a, b); }
DI float fmn(float a, float b) { return __builtin_elementwise_minimum(a, b); }

DI float mul1(float a, float b) { float r; asm("v_mul_f32 %0, %1, %2" : "=v"(r) : "v"(a), "v"(b)); return r; }
DI float add1(float a, float b) { float r; asm("v_add_f32 %0, %1, %2" : "=v"(r) : "v"(a), "v"(b)); return r; }
DI f32x4 add4(f32x4 a, f32x4 b) { return f32x4{add1(a[0], b[0]), add1(a[1], b[1]), add1(a[2], b[2]), add1(a[3], b[3])}; }

template <bool IS_MAX, bool INV = false>
DI void xred2(float& a, float& b) {
  auto r = __builtin_amdgcn_permlane32_swap(__float_as_uint(a), __float_as_uint(b), false, false);
  float c = IS_MAX ? fmx(__uint_as_float(r[0]), __uint_as_float(r[1])) : __uint_as_float(r[0]) + __uint_as_float(r[1]);
  auto q = __builtin_amdgcn_permlane16_swap(__float_as_uint(c), __float_as_uint(c), false, false);
  c = IS_MAX ? fmx(__uint_as_float(q[0]), __uint_as_float(q[1])) : __uint_as_float(q[0]) + __uint_as_float(q[1]);
  if (INV) c = __builtin_amdgcn_rcpf(c);
  auto w = __builtin_amdgcn_permlane32_swap(__float_as_uint(c), __float_as_uint(c), false, false);
  a = __uint_as_float(w[0]); b = __uint_as_float(w[1]);
}
template <bool IS_MAX, bool INV = false>
DI void xred4(float& a, float& b, float& c, float& d) {
  auto op = [](unsigned x, unsigned y) { return IS_MAX ? fmx(__uint_as_float(x), __uint_as_float(y)) : __uint_as_float(x) + __uint_as_float(y); };
  auto r0 = __builtin_amdgcn_permlane32_swap(__float_as_uint(a), __float_as_uint(b), false, false);
  auto r1 = __builtin_amdgcn_permlane32_swap(__float_as_uint(c), __float_as_uint(d), false, false);
  float ab = op(r0[0], r0[1]), cd = op(r1[0], r1[1]);
  auto q = __builtin_amdgcn_permlane16_swap(__float_as_uint(ab), __float_as_uint(cd), false, false);
  float e = op(q[0], q[1]);
  if (INV) e = __builtin_amdgcn_rcpf(e);
  auto w = __builtin_amdgcn_permlane16_swap(__float_as_uint(e), __float_as_uint(e), false, false);
  auto u = __builtin_amdgcn_permlane32_swap(w[0], w[0], false, false);
  auto v = __builtin_amdgcn_permlane32_swap(w[1], w[1], false, false);
  a = __uint_as_float(u[0]); b = __uint_as_float(u[1]); c = __uint_as_float(v[0]); d = __uint_as_float(v[1]);
}

DI unsigned xt_off(int mt, int ks) { return (unsigned)((((mt >> 4) * 32 + ks) * 16 + (mt & 15)) << 10); }

#define GLDS_PRE "s_mov_b32 %0, m0\n\ts_mov_b32 m0, %3\n\ts_nop 2\n\t"
#define GLDS_POST "s_mov_b32 m0, %0"
#define GLDS_OPS : "=&s"(keep) : "v"(voff), "s"(sbase), "s"(lds) : "memory"
#define GL1(c) "global_load_lds_dwordx4 %1, %2" c "\n\t"
#define GL2(c) GL1(c) "global_load_lds_dwordx4 %1, %2 offset:1024" c "\n\t"
#define GL3(c) GL2(c) "global_load_lds_dwordx4 %1, %2 offset:2048" c "\n\t"
#define GL4(c) GL3(c) "global_load_lds_dwordx4 %1, %2 offset:3072" c "\n\t"
template <int N, bool SC1 = false>
DI void glds_run(unsigned voff, const void* sbase, unsigned lds) {
  unsigned keep;
  if constexpr (SC1) {
    if constexpr (N == 2) asm volatile(GLDS_PRE GL2(" sc1") GLDS_POST GLDS_OPS);
    else if constexpr (N == 3) asm volatile(GLDS_PRE GL3(" sc1") GLDS_POST GLDS_OPS);
    else asm volatile(GLDS_PRE GL4(" sc1") GLDS_POST GLDS_OPS);
  } else {
    static_assert(SC1 || N == 1, "");
    asm volatile(GLDS_PRE GL1("") GLDS_POST GLDS_OPS);
  }
}
template <int N> DI void wait_vm_lgkm0() { asm volatile("s_waitcnt vmcnt(%0) lgkmcnt(0)" ::"n"(N) : "memory"); }
template <int N> DI void wait_vm() { asm volatile("s_waitcnt vmcnt(%0)" ::"n"(N) : "memory"); }
DI void drain() { asm volatile("s_waitcnt vmcnt(0)" ::: "memory"); __builtin_amdgcn_s_waitcnt(0x0f70); }
DI void bar() { asm volatile("" ::: "memory"); __builtin_amdgcn_s_barrier(); asm volatile("" ::: "memory"); }
DI void sync_lds() { asm volatile("s_waitcnt lgkmcnt(0)" ::: "memory"); bar(); }
#define SCHED0 __builtin_amdgcn_sched_barrier(0)
#define STPAD do { SCHED0; asm volatile("s_nop 1"); SCHED0; } while (0)
#define SLEEP(n) do { if (n) __builtin_amdgcn_s_sleep(n); } while (0)
#define PRIO(n) __builtin_amdgcn_s_setprio(n)

DI void phi_map2(const u32x4* in, const float* rs, bf16x8 wf0, bf16x8 wf1, u32x4 (*out)[2]) {
  const f32x4 zero = f32x4{0.f, 0.f, 0.f, 0.f};
  float x[2][8], mx[2], mn[2];
#pragma unroll
  for (int m = 0; m < 2; ++m) {
    f32x4 r0 = mfma16(wf0, as_frag(in[m]), zero), r1 = mfma16(wf1, as_frag(in[m]), zero);
#pragma unroll
    for (int j = 0; j < 4; ++j) { x[m][j] = r0[j]; x[m][4 + j] = r1[j]; }
  }
#pragma unroll
  for (int m = 0; m < 2; ++m) {
    mx[m] = fmx(fmx(fmx(x[m][0], x[m][1]), x[m][2]), fmx(fmx(x[m][3], x[m][4]), fmx(fmx(x[m][5], x[m][6]), x[m][7])));
    mn[m] = -fmn(fmn(fmn(x[m][0], x[m][1]), x[m][2]), fmn(fmn(x[m][3], x[m][4]), fmn(fmn(x[m][5], x[m][6]), x[m][7])));
  }
  xred4<true>(mx[0], mn[0], mx[1], mn[1]);
  f32x2 e[2][8];
  float sp[2], sn[2];
#pragma unroll
  for (int m = 0; m < 2; ++m) {
    const float sl = rs[m] * LOG2E;
    const f32x2 l2 = {sl, -sl};
    const f32x2 off = f32x2{mx[m], mn[m]} * (-sl);
#pragma unroll
    for (int j = 0; j < 8; ++j) {
      f32x2 a = __builtin_elementwise_fma(f32x2{x[m][j], x[m][j]}, l2, off);
      e[m][j] = f32x2{__builtin_amdgcn_exp2f(a[0]), __builtin_amdgcn_exp2f(a[1])};
    }
    f32x2 sm = ((e[m][0] + e[m][1]) + (e[m][2] + e[m][3])) + ((e[m][4] + e[m][5]) + (e[m][6] + e[m][7]));
    sp[m] = sm[0]; sn[m] = sm[1];
  }
  xred4<false, true>(sp[0], sn[0], sp[1], sn[1]);
#pragma unroll
  for (int m = 0; m < 2; ++m) {
    const f32x2 inv = {sp[m], sn[m]};
#pragma unroll
    for (int j = 0; j < 8; ++j) e[m][j] *= inv;
    out[m][0] = u32x4{pk2(e[m][0][0], e[m][1][0]), pk2(e[m][2][0], e[m][3][0]), pk2(e[m][4][0], e[m][5][0]), pk2(e[m][6][0], e[m][7][0])};
    out[m][1] = u32x4{pk2(e[m][0][1], e[m][1][1]), pk2(e[m][2][1], e[m][3][1]), pk2(e[m][4][1], e[m][5][1]), pk2(e[m][6][1], e[m][7][1])};
  }
}

#define SLP_X0 8
#define SLP_X1 14
#define SLP_XD0 3
#define SLP_XD1 9
#define SLP_O 22
#define SLP_OD 12
#define SLP_O0 8
#define SLP_O0D 2
#define SLP_K 4
#define SLP_R 4
#define SLP_HX 5
#define SLP_Q 4
#define SLP_P 40
#define SLP_H 16
#define SLP_E 8
#define SPR0 2
#define SW0 0
#define LANEID __builtin_amdgcn_mbcnt_hi(~0u, __builtin_amdgcn_mbcnt_lo(~0u, 0u))
#define STAMP(i) do {} while (0)
#define STAMP_DUMP do {} while (0)
#define COUNT(k) do {} while (0)
#define PSTAMP(l) do { const int layer = l; (void)layer; STAMP(15); } while (0)
constexpr unsigned MBY = 1u << 20;
constexpr unsigned WS_WQ = 0;
constexpr unsigned WS_WO = 12 * MBY;
constexpr unsigned WS_AR = 16 * MBY, ARENA = 12 * MBY;
constexpr unsigned AR_X = 0, AR_ST = 4 * MBY, AR_O = 5 * MBY, AR_KV = 9 * MBY;
constexpr unsigned AR_L2 = AR_ST + 256 * 1024, AR_FL = AR_ST + 320 * 1024;
constexpr unsigned KVREC = 9 * 1024;
constexpr unsigned WS_CS = WS_AR + 4 * ARENA;
constexpr unsigned WS_F1 = WS_CS + 512 * 1024;
constexpr unsigned WS_F2 = WS_F1 + 64 * 1024;
constexpr unsigned WS_EP = WS_F2 + 2048;
constexpr unsigned WS_END = WS_EP + 128;
constexpr unsigned POISON = 0xffffffffu;

typedef __amdgpu_buffer_rsrc_t rsrc_t;
DI rsrc_t mk_rsrc(const void* p, unsigned bytes) { return __builtin_amdgcn_make_buffer_rsrc((void*)p, 0, bytes, 0x00020000); }
DI void st16(rsrc_t r, unsigned off, u32x4 v) { __builtin_amdgcn_raw_buffer_store_b128(v, r, off, 0, 16); }
DI void st16s(rsrc_t r, unsigned voff, unsigned soff, u32x4 v) { __builtin_amdgcn_raw_buffer_store_b128(v, r, voff, soff, 16); }
DI u32x4 ld16(rsrc_t r, unsigned off) { return __builtin_amdgcn_raw_buffer_load_b128(r, off, 0, 16); }
DI u32x4 ld16s(rsrc_t r, unsigned voff, unsigned soff) { return __builtin_amdgcn_raw_buffer_load_b128(r, voff, soff, 16); }
DI u32x4 clean(u32x4 v) { return __builtin_elementwise_min(v, u32x4{POISON - 1, POISON - 1, POISON - 1, POISON - 1}); }
DI unsigned umax(unsigned a, unsigned b) { return a > b ? a : b; }
DI unsigned umin(unsigned a, unsigned b) { return a < b ? a : b; }
DI unsigned umax4(u32x4 v) { return umax(umax(umax(v[0], v[1]), v[2]), v[3]); }
DI unsigned umax4(unsigned m, u32x4 v) { return umax(umax(umax(umax(m, v[0]), v[1]), v[2]), v[3]); }
DI bool poisoned(unsigned m) { return __builtin_amdgcn_ballot_w64(m == POISON) != 0; }
#define MEMFENCE asm volatile("" ::: "memory")

#define BL(i, o) "buffer_load_dwordx4 %1, %2, %" #i " offen" o " nt lds\n\t"
#define BL_OPS(...) : "=&s"(keep) : "v"(voff), "s"(r), "s"(lds), __VA_ARGS__ : "memory"
template <int N>
DI void blds_run(rsrc_t r, unsigned voff, unsigned lds, unsigned s0, unsigned s1 = 0, unsigned s2 = 0, unsigned s3 = 0) {
  unsigned keep;
  if constexpr (N == 2) asm volatile(GLDS_PRE BL(4, "") BL(5, " offset:1024") GLDS_POST BL_OPS("s"(s0), "s"(s1)));
  else asm volatile(GLDS_PRE BL(4, "") BL(5, " offset:1024") BL(6, " offset:2048") BL(7, " offset:3072") GLDS_POST BL_OPS("s"(s0), "s"(s1), "s"(s2), "s"(s3)));
}

#define CODEB 35656
constexpr int MEGA_CODE_BYTES = CODEB;
DI unsigned code_prefetch(int i) {
  unsigned long long pc; int rel; unsigned d;
  asm volatile("s_getpc_b64 %0\n\ts_mov_b32 %1, _Z11mega_kernel10MegaParams@rel32@lo+4" : "=s"(pc), "=s"(rel));
  const unsigned long long a = pc + (long long)rel + (i * 192 + 192 <= MEGA_CODE_BYTES ? i * 192 : 0);
  asm volatile("s_load_dword %0, %1, 0x0\n\ts_load_dword %0, %1, 0x40\n\ts_load_dword %0, %1, 0x80" : "=&s"(d) : "s"(a) : "memory");
  return d;
}

struct MegaParams {
  char* ws;
  const u16* x;
  const u16* wqkvA;
  const u16* woA;
  const u16* wqkvB;
  const u16* woB;
  const u16* wphi;
  u16* out;
};

constexpr int QSLOT = 6 * 1024, QGRP = 4 * QSLOT;
constexpr int L_STG = QGRP;
constexpr int L_A2 = 16 * 1024, L_Z = 21 * 1024;
constexpr int L_RT = 4 * QGRP;
constexpr int L_PB = 0;
constexpr int L_RED = L_STG + 128 * 1024;
constexpr int L_WP = L_RED + 3072;
constexpr int L_STAMP = L_WP + 2048;
constexpr int MEGA_LDS = L_STAMP + 1536;
static_assert(MEGA_LDS <= 160 * 1024, "LDS map");

template <int I, int N, typename F> DI void static_for_(F&& f) {
  if constexpr (I < N) { f(IC<I>{}); static_for_<I + 1, N>(f); }
}
template <int N, typename F> DI void static_for(F&& f) { static_for_<0, N>(f); }

constexpr int NCHK = 3;
constexpr int RO = 8;
constexpr int SPIN_BUDGET = 1 << 15;

__global__ __launch_bounds__(768, 3) void mega_kernel(MegaParams p) {
  extern __shared__ __attribute__((aligned(16))) char smem[];
  const int tid = threadIdx.x;
  const int wave = __builtin_amdgcn_readfirstlane(tid >> 6);
  const int xcd = blockIdx.x & 7, idx = blockIdx.x >> 3;
  int bm = (xcd >> 2) * 4 + (idx >> 3), head = ((xcd & 1) * 2 + ((xcd >> 1) & 1)) * 8 + (idx & 7);
  int cm = (xcd >> 1) & 1, cn = (xcd & 1) * 8 + (idx & 7);
  char* ws = p.ws;
  const u16 *px = p.x, *pq = p.wqkvA;
  asm volatile("" : "+s"(ws), "+s"(px), "+s"(pq));
  const int swv = wave == SW0 ? 0 : wave == 4 ? 1 : wave == 11 ? 2 : -1; (void)swv;
  float* lmean = (float*)(smem + L_RED);
  float* lrstd = lmean + 272;
  float* lcs = lrstd + 272;
  unsigned* lflag = (unsigned*)lmean;
  const rsrc_t wsr = mk_rsrc(ws, WS_END);
  int budget = SPIN_BUDGET;
  unsigned tag0 = 0, tag1 = 0;
  auto ld_ep = [&]() AI { MEMFENCE; return __builtin_amdgcn_raw_buffer_load_b64(wsr, 0, WS_EP, 16); };
  auto mk_tag = [&](u32x2 ep) AI {
    tag0 = __builtin_amdgcn_readfirstlane(ep[0]) + 0x9e3779b9u;
    tag1 = __builtin_amdgcn_readfirstlane(ep[1]) ^ 0x85ebca6bu;
  };
  const unsigned sbase = (unsigned)(size_t)smem;
  int L = wave, cw = wave - 4;
#define OPAQUE_S asm volatile("" : "+s"(L), "+s"(bm), "+s"(head), "+s"(cm), "+s"(cn));
#define LANE_VARS asm volatile("" : "+s"(cw), "+s"(bm), "+s"(head), "+s"(cm), "+s"(cn)); int lane_ = __builtin_amdgcn_mbcnt_hi(~0u, __builtin_amdgcn_mbcnt_lo(~0u, 0u)); asm volatile("" : "+v"(lane_)); \
  const int lane = lane_, fr = lane & 15, fq = lane >> 4; const unsigned v16 = lane * 16; (void)fr; (void)fq; (void)v16;
  auto put_x = [&](u32x4* xr, unsigned v16, unsigned dst) AI {
    float sm = 0.f, sq = 0.f;
#pragma unroll
    for (int j = 0; j < 2; ++j) {
      xr[j][0] = umin(xr[j][0], POISON - 1); xr[j][2] = umin(xr[j][2], POISON - 1);
      st16s(wsr, v16, dst + j * 16384, xr[j]);
    }
    SCHED0;
#pragma unroll
    for (int j = 0; j < 2; ++j)
#pragma unroll
      for (int e = 0; e < 4; ++e) { sm = dot2(xr[j][e], 0x3f803f80u, sm); sq = dot2(xr[j][e], xr[j][e], sq); }
    xred2<false>(sm, sq);
    return u32x2{__float_as_uint(sm), __float_as_uint(sq)};
  };

#define RT_VARS const bool tq = L < 3; const int tsg = tq ? (bm * 2 + (head >> 4)) * 3 + L : bm * 2 + (head >> 4), ks = (head & 15) + 16 * h;
  auto ld_rt = [&](const u16* w, int h, unsigned lds) AI {
    RT_VARS
    LANE_VARS
    const unsigned rs = tq ? 6144 : 2048, D = 8 * rs - 1024, o = ks * 32 * rs + tsg * 128;
    blds_run<4>(mk_rsrc(w, rs * 1024), (lane >> 3) * rs + ((lane & 7) ^ ((lane >> 3) & 6)) * 16, sbase + lds, o, o + D, o + 2 * D, o + 3 * D);
  };
  auto retile = [&](int side, int h, unsigned lds, f32x4* rcs) AI {
    RT_VARS
    LANE_VARS
    const unsigned dst = tq ? WS_WQ + side * (6 * MBY) + (unsigned)((((tsg & 15) * 2) * 32 + ks) * 6 + (tsg >> 4) * 2) * 1024
                            : WS_WO + side * (2 * MBY) + (unsigned)((tsg * 32 + ks) * 4) * 1024;
    const unsigned gstr = tq ? 32 * 6 * 1024 : 2048;
    const unsigned a0 = (lane >> 2) * 128 + ((lane >> 3) & 3) * 32 + (lane & 3) * 8;
#pragma unroll
    for (int pc = 0; pc < 4; ++pc) {
      const char* aa = smem + lds + (a0 ^ (pc * 32));
      u32x2 r0_ = trread(aa), r1_ = trread(aa + 2048);
      u32x4 v = u32x4{r0_[0], r0_[1], r1_[0], r1_[1]};
      st16s(wsr, v16 + (pc & 1) * 1024, dst + (pc >> 1) * gstr, v);
      if (rcs) rcs[pc] = mfma16(as_frag(u32x4{0x3f803f80u, 0x3f803f80u, 0x3f803f80u, 0x3f803f80u}), as_frag(v), rcs[pc]);
    }
    if (rcs && h == 1) {
      const f32x4 c0 = rcs[0], c1 = rcs[1], c2 = rcs[2], c3 = rcs[3];
      const float ca = fq & 1 ? c1[0] : c0[0], cb = fq & 1 ? c3[0] : c2[0], cc = fq & 2 ? cb : ca;
      if (tq) __builtin_amdgcn_raw_buffer_store_b32(__float_as_uint(cc), wsr, ((fq >> 1) * 16 * 96 + (fq & 1) * 16 + fr) * 4,
                                                    WS_CS + (((side * 32 + (tsg & 15) * 2) * 16 + (head & 15)) * 96 + (tsg >> 4) * 32) * 4, 16);
    }
  };
  auto flag = [&](unsigned table) AI {
    if (LANEID == 0) __builtin_amdgcn_raw_buffer_store_b64(u32x2{tag0, tag1}, wsr, 0, table + (bm * 32 + head) * 8, 16);
  };
  auto poll = [&](unsigned table, auto between, int rows = 0) AI {
    LANE_VARS
    const int q = (umin(fq, 2) * 16 + (head >> 1)) * 43 >> 7;
    const unsigned vo = rows ? bm * 256 + (fr & rows) * 16 : fq == 3 ? bm * 256 + fr * 16 : q * 128 + (fr & 7) * 16;
    auto miss = [&](u32x4 f) AI { return __builtin_amdgcn_ballot_w64(((f[0] ^ tag0) | (f[1] ^ tag1) | (f[2] ^ tag0) | (f[3] ^ tag1)) != 0) != 0; };
    MEMFENCE;
    const u32x4 f0 = ld16s(wsr, vo, table);
    MEMFENCE;
    between();
    MEMFENCE;
    bool wait = miss(f0);
#pragma unroll 1
    while (wait && --budget > 0) {
      SLEEP(SLP_Q);
      MEMFENCE;
      wait = miss(ld16s(wsr, vo, table));
      asm volatile("" : "+s"(budget) :: "memory");
    }
    MEMFENCE;
  };
  auto ld_x = [&](int cx, unsigned lds) AI {
    LANE_VARS
    const unsigned o = (bm * 16 + (head >> 1)) * 32768 + cx * 128;
    blds_run<2>(mk_rsrc(px, 4 * MBY), (lane >> 3) * 2048 + ((lane & 7) ^ (lane >> 3)) * 16, sbase + lds, o, o + 15360);
  };
  auto st_x = [&](int cx, unsigned lds, int w) AI {
    LANE_VARS
    const int mt = bm * 16 + (head >> 1);
    u32x4 xr[2];
#pragma unroll
    for (int j = 0; j < 2; ++j) {
      const char* a = smem + lds + fr * 128 + (fq & 1) * 8;
      const u32x2 lo = *(const u32x2*)(a + ((j * 4 + (fq >> 1)) ^ (fr & 7)) * 16), hi = *(const u32x2*)(a + ((j * 4 + 2 + (fq >> 1)) ^ (fr & 7)) * 16);
      xr[j] = u32x4{lo[0], lo[1], hi[0], hi[1]};
    }
    const u32x2 stat = put_x(xr, v16, WS_AR + AR_X + xt_off(mt, cx * 2));
    if (fq == 0) *(u32x2*)(lmean + 16 + fr * 4 + w) = stat;
  };

  if (wave >= 8) {
    const unsigned cp = code_prefetch(idx * 9 + cw), cq = code_prefetch(idx * 9 + cw - 4);
    L = wave - 8;
    SLEEP(SLP_H);
    ld_x(8 + (head & 1) * 4 + L, L_PB + 32768 + L * 2048);
    if (L < 3) ld_rt(pq, 1, L_PB + 20480 + L * 4096);
    sync_lds();
    asm volatile("; pi" :: "s"(cp), "s"(cq) : "memory");
    wait_vm<0>();
    PSTAMP(1);
    st_x(8 + (head & 1) * 4 + L, L_PB + 32768 + L * 2048, L * 64 + 2);
    if (L < 3) {
      const f32x4 t = *(const f32x4*)(smem + L_PB + 40960 + L * 1024 + LANEID * 16);
      f32x4 rca[4] = {{t[0], 0.f, 0.f, 0.f}, {t[1], 0.f, 0.f, 0.f}, {t[2], 0.f, 0.f, 0.f}, {t[3], 0.f, 0.f, 0.f}};
      retile(0, 1, L_PB + 20480 + L * 4096, rca);
    }
    wait_vm_lgkm0<0>();
    PSTAMP(2);
  } else if (wave >= 4) {
    ld_x((head & 1) * 4 + cw, L_PB + cw * 2048);
    wait_vm<0>();
    PSTAMP(1);
    st_x((head & 1) * 4 + cw, L_PB + cw * 2048, cw * 64);
    wait_vm_lgkm0<0>();
    PSTAMP(2);
    bar();
    asm volatile("; pc" ::: "memory");
    SLEEP(SLP_E);
    L = 3;
    if (cw < 2) ld_rt(p.woA, cw, L_RT + 3 * 14336 + cw * 4096);
    if (cw == 2) ld_rt(p.woB, 0, L_RT + 3 * 14336 + 8192);
  } else if (wave < 3) {
    ld_rt(pq, 0, L_PB + 8192 + L * 4096);
    const u32x2 ep = ld_ep();
    wait_vm<1>();
    PSTAMP(1);
    f32x4 rca[4] = {};
    retile(0, 0, L_PB + 8192 + L * 4096, rca);
    *(f32x4*)(smem + L_PB + 40960 + L * 1024 + LANEID * 16) = f32x4{rca[0][0], rca[1][0], rca[2][0], rca[3][0]};
    drain();
    mk_tag(ep);
    PSTAMP(2);
    sync_lds();
    asm volatile("; pl" ::: "memory");
    SLEEP(SLP_E);
    ld_rt(p.wqkvB, 0, L_RT + L * 14336);
    ld_rt(p.wqkvB, 1, L_RT + L * 14336 + 4096);
  } else {
    const u32x2 ep = ld_ep();
    const unsigned cp = code_prefetch(idx * 9 + 8);
    if (LANEID < 9) lflag[LANEID] = 0;
    wait_vm_lgkm0<0>();
    asm volatile("" :: "s"(cp));
    mk_tag(ep);
    bar();
    flag(WS_F1);
    if (LANEID == 0) *(u32x2*)(lflag + 10) = u32x2{tag0, tag1};
    PSTAMP(2);
    SLEEP(SLP_P);
    poll(WS_F1, []() {});
    asm volatile("; pp" ::: "memory");
  }
  bar();
  PSTAMP(3);
  int role = wave;
  asm volatile("" : "+s"(role));

  if (__builtin_expect(role < 4, 1)) {
    const unsigned v16 = __builtin_amdgcn_mbcnt_hi(~0u, __builtin_amdgcn_mbcnt_lo(~0u, 0u)) * 16;
#define OPAQUE_LANE int lane = v16 >> 4; asm volatile("" : "+v"(lane));
    auto q_groupW = [&](int layer, int g, int pos) AI {
      const char* gW = ws + WS_WQ + (layer & 1) * (6 * MBY) + head * (192 * 1024) + (g * 4 + L) * 6144;
      const unsigned d = sbase + (pos * 4 + L) * QSLOT;
      glds_run<3, true>(v16, gW, d);
      glds_run<3, true>(v16, gW + 3072, d + 3072);
    };
    u32x4 wn[4][6];
    auto ld_wn = [&](int layer, int g0, int g1) AI {
      const unsigned so = WS_WQ + (layer & 1) * (6 * MBY) + head * (192 * 1024) + L * 6144;
#pragma unroll
      for (int g = g0; g < g1; ++g)
#pragma unroll
        for (int n = 0; n < 6; ++n) wn[g][n] = ld16s(wsr, v16 + (n % 3) * 1024, so + g * 24576 + (n / 3) * 3072);
      MEMFENCE;
    };
    auto wr_wn = [&](int g) AI {
#pragma unroll
      for (int n = 0; n < 6; ++n) *(u32x4*)(smem + (g * 4 + L) * QSLOT + n * 1024 + v16) = wn[g][n];
    };
    const u32x4 pz = u32x4{POISON, POISON, POISON, POISON};
    auto poison = [&](unsigned so) AI { st16s(wsr, v16, so, pz); };
    auto poison_o = [&]() AI {
      const unsigned o = WS_AR + AR_O + xt_off(bm * 16 + L * 4, head);
#pragma unroll
      for (int i = 0; i < 4; ++i) poison(o + i * 1024);
    };
    auto poison_x = [&](int l) AI {
      if (L < NCHK) {
        const unsigned o = WS_AR + l * ARENA + AR_X + bm * (512 * 1024) + head * (NCHK * 2048) + L * 2048;
        poison(o); poison(o + 1024);
      }
    };
    auto poison_s = [&](int l) AI {
      if (L == 3) {
        const unsigned o = WS_AR + l * ARENA + AR_KV + (unsigned)(bm * 32 + head) * KVREC;
        poison(o); poison(o + 1024);
      }
    };
    auto poison_kv = [&](int l, int w0) AI {
      if ((unsigned)(L - w0) < 3) {
        const unsigned kvo = WS_AR + l * ARENA + AR_KV + (unsigned)(bm * 32 + head) * KVREC + (L - w0) * 3072;
        poison(kvo); poison(kvo + 1024); poison(kvo + 2048);
      }
    };
    q_groupW(0, 0, 0);
    q_groupW(0, 1, 1);
    auto put_l2 = [&](float sm, float sq, unsigned vo, unsigned so) AI {
      xred2<false>(sm, sq);
      if (v16 < 256) st16s(wsr, vo, so, u32x4{__float_as_uint(sm), tag0, __float_as_uint(sq), tag1});
      MEMFENCE;
    };
    if (L == 3) {
      flag(WS_F1 + 2048);
      MEMFENCE;
      const f32x4 t = *(const f32x4*)((const char*)(lmean + 16) + v16);
      put_l2(t[0] + t[2], t[1] + t[3], v16, WS_AR + AR_L2 + (head & 1) * 32768 + (bm * 16 + (head >> 1)) * 256);
    }
    if (L == 3) wait_vm<8>(); else wait_vm<6>();
    auto body = [&](auto kind, const int layer) AI {
      constexpr bool isB = decltype(kind)::value == 1, is0 = decltype(kind)::value == 0;
      const unsigned ar = WS_AR + layer * ARENA;
      f32x4 rcs[4] = {};
      unsigned tch = 0;
      auto sW = [&]() AI { return WS_WO + (layer & 1) * (2 * MBY) + cn * (128 * 1024); };
      auto stg = [&](unsigned off, auto n) AI { glds_run<decltype(n)::value, true>(v16, ws + sW() + off, sbase + L_STG + off); };
      auto stg7 = [&](unsigned o) AI { stg(o, IC<4>{}); stg(o + 4096, IC<3>{}); };
      auto stg6 = [&](unsigned o) AI { stg(o, IC<3>{}); stg(o + 3072, IC<3>{}); };

      auto extra = [&](int b) AI {
        if (!is0) return;
        const unsigned lu = L_RT + L * 14336;
        if (b < 7) { SCHED0; PRIO(3); SCHED0; }
        if (b == 1) retile(L < 3, 0, lu, rcs);
        if (b == 2 && L == 3) retile(0, 1, lu + 4096, nullptr);
        if (b == 6 && L == 3) ld_rt(p.woB, 1, lu + 4096);
        if (b == 8) {
          if (L < 3) retile(1, 1, lu + 4096, rcs);
          if (L == 3) retile(1, 0, lu + 8192, nullptr);
          if (L == 3) retile(1, 1, lu + 4096, nullptr);
        }
        if (b < 7) { SCHED0; PRIO(0); SCHED0; }
      };
      auto extra2 = [&](int b) AI {
        if (!is0) return;
        if (b < 7) { SCHED0; PRIO(3); SCHED0; }
        if (b == 1) poison_o();
        if (b == 2) poison_kv(0, 1);
        if (b == 5) poison_kv(2, 0);
        if (b == 8) poison_s(1);
        if (b == 9) poison_x(1);
        if (b == 10) { poison_x(2); poison_s(3); }
        if (b < 7) { SCHED0; PRIO(0); SCHED0; }
      };
      if (is0) sync_lds();
      STAMP(0);
      {
        OPAQUE_S
        OPAQUE_LANE
        float c[16];
        auto ldcs = [&]() AI {
          if (L >= 2) {
#pragma unroll
            for (int i = 0; i < 16; ++i)
              c[i] = __uint_as_float(__builtin_amdgcn_raw_buffer_load_b32(wsr, (lane < 48 ? lane : 0) * 4 + (i & 7) * 384,
                                         WS_CS + (((layer & 1) * 32 + head) * 1536 + (L & 1) * 48 + (i >> 3) * 768) * 4, 16));
          }
        };
        auto wrcs = [&]() AI {
          if (L >= 2) {
            asm volatile("" : "+v"(c[0]));
            float cs = c[0];
#pragma unroll
            for (int i = 1; i < 16; ++i) cs = add1(cs, c[i]);
            if (lane < 48) lcs[(isB ? 96 : 0) + (L & 1) * 48 + lane] = cs;
          }
        };
        if (isB && layer == 1) ldcs();
        if constexpr (is0) {
          q_groupW(0, 2, 2); q_groupW(0, 3, 3);
          wait_vm<12>();
          if (L >= 2) glds_run<1>(v16, (const char*)p.wphi + head * 2048 + (L - 2) * 1024, sbase + L_WP + (L - 2) * 1024);
        }
        STAMP(1);
        sync_lds();
        STAMP(2);
        OPAQUE_S
        u32x4 s0 = u32x4{0u, 0u, 0u, 0u}, s1 = s0;
        auto ldst = [&]() AI {
          const unsigned so = ar + AR_L2 + (bm * 16 + L * 4) * 256;
          s0 = ld16s(wsr, v16, so);
          if (is0) s1 = ld16s(wsr, v16, so + 32768);
          if (isB && L == 0) s1 = ld16s(wsr, v16 & 255, bm > 0 ? so - 256 : so);
          MEMFENCE;
        };
        auto fin = [&](float sm, float sq, int r, bool own) AI {
          float mean = sm * (1.0f / 1024.0f);
          float var = fmaxf(__builtin_fmaf(sq, 1.0f / 1024.0f, -mul1(mean, mean)), 0.f);
          if (own) lmean[r] = -mean;
          lrstd[r] = __builtin_amdgcn_rsqf(var + 1e-5f);
        };
        auto wrst = [&]() AI {
          while (true) {
            unsigned d = (s0[1] ^ tag0) | (s0[3] ^ tag1);
            if (is0 || (isB && L == 0)) d |= (s1[1] ^ tag0) | (s1[3] ^ tag1);
            if (__builtin_expect(__builtin_amdgcn_ballot_w64(d != 0) == 0, 1) || --budget <= 0) break;
            COUNT(1);
            ldst();
          }
          MEMFENCE;
          float sm = __uint_as_float(s0[0]), sq = __uint_as_float(s0[2]);
          if (is0) { sm += __uint_as_float(s1[0]); sq += __uint_as_float(s1[2]); }
          fin(sm, sq, 16 + L * 64 + lane, true);
          if (isB && L == 0) fin(__uint_as_float(s1[0]), __uint_as_float(s1[2]), lane & 15, false);
        };
        auto q_group = [&](int g) AI {
          if (is0) {
            if (L & 1) SLEEP(SPR0);
            if (L & 2) SLEEP(2 * SPR0);
            q_groupW(layer, g, g & 3);
          } else if (g >= 5) q_groupW(layer, g, g < 6 ? g : g - 4);
        };
        if (!is0 && L == 0) {
          poll(ar + AR_FL, []() {}, 15);
          if (head < 16) {
            const unsigned so = ar + AR_ST + (bm * 16 + head) * 2048;
            s0 = ld16s(wsr, v16, so); s1 = ld16s(wsr, v16 + 1024, so);
            MEMFENCE;
          }
        }
        if (is0) {
          drain();
          poll(WS_F1 + 2048, [&]() AI { extra(1); });
          ldcs();
          extra2(1);
        }
        if (isB && layer == 1) wrcs();
        MEMFENCE;
        STAMP(3);
        sync_lds();
        STAMP(4);
        OPAQUE_S
        if (!is0) drain();
        if (!is0 && L == 0 && head < 16) {
          f32x4 t = add4(__builtin_bit_cast(f32x4, s0), __builtin_bit_cast(f32x4, s1));
#pragma unroll
          for (int e = 0; e < 4; ++e)
            t[e] += __uint_as_float(__builtin_amdgcn_update_dpp(0u, __float_as_uint(t[e]), 0x128, 0xf, 0xf, false));
          const bool hi = lane & 8;
          put_l2(hi ? t[2] : t[0], hi ? t[3] : t[1], (lane & 7) * 32 + (lane & 8) * 2, ar + AR_L2 + (bm * 16 + head) * 256);
        }
        q_group(4);
        extra(2); extra2(2);
        STAMP(5);
        sync_lds();
        STAMP(6);
        OPAQUE_S
        drain();
        q_group(5);
        if (is0) wrcs();
        STAMP(7);
        bar();
        OPAQUE_S
        drain();
        if (is0 && L == 0 && v16 == 0) __builtin_amdgcn_raw_buffer_store_b64(u32x2{tag0, tag1}, wsr, 0, WS_F2 + (bm * 32 + head) * 8, 16);
        q_group(6);
        ldst();
        STAMP(8);
        bar();
        STAMP(9);
        OPAQUE_S
        drain();
        q_group(7);
        extra2(5);
        SCHED0;
        wrst();
        STAMP(10);
        bar();
        STAMP(11);
        OPAQUE_S
        drain();
        if (isB && layer == 3 && L == 3) {
          asm volatile("global_load_dword %0, %1, %2" : "=v"(tch) : "v"((unsigned)((bm * 256 + cm * 16 + (lane & 7) * 32) * 2048 + cn * 128)), "s"(p.out) : "memory");
        }
        extra(6);
        if (isB) stg6(96 * 1024 + L * 6144);
        SCHED0;
      }
      STAMP(12);
      sync_lds();
      STAMP(13);
      OPAQUE_S
      u32x4 fl[2];
      fl[0] = fl[1] = u32x4{tag0, tag1, tag0, tag1};
      auto ldfl = [&]() AI { fl[0] = ld16s(wsr, v16, WS_F2); fl[1] = ld16s(wsr, v16 + 1024, WS_F2); };
      if (is0) ldfl();
      bar();
      constexpr int NR = isB ? 6 : 9;
      const unsigned ro = isB ? 48 * 1024 + L * 6144 : L * 9216;
      u32x4 r[NR];
      bool pend = false;
      auto flags = [&]() AI {
        const u32x4 d = (fl[0] ^ u32x4{tag0, tag1, tag0, tag1}) | (fl[1] ^ u32x4{tag0, tag1, tag0, tag1});
        pend = __builtin_amdgcn_ballot_w64((d[0] | d[1] | d[2] | d[3]) != 0) != 0;
      };
      if (isB) {
        stg6(L * 6144); stg(120 * 1024 + L * 2048, IC<2>{});
        stg6(24 * 1024 + L * 6144);
        stg6(72 * 1024 + L * 6144);
      } else {
        drain();
        if (is0) flags();
        MEMFENCE;
        extra(8); extra2(8);
        if (!is0) poison_x(3);
        while (is0 && __builtin_expect(pend, 0) && --budget > 0) {
          ldfl();
          flags();
          MEMFENCE;
        }
        stg(36 * 1024 + L * 3072, IC<3>{}); stg7(72 * 1024 + L * 14336); stg7(72 * 1024 + L * 14336 + 7168);
      }
#pragma unroll
      for (int i = 0; i < NR; ++i) r[i] = ld16s(wsr, v16 + (i & 3) * 1024, sW() + ro + (i >> 2) * 4096);
      MEMFENCE;
      if (is0 && bm + head + L == 0 && v16 == 0) __builtin_amdgcn_raw_buffer_store_b32(tag0, wsr, 0, WS_EP, 16);
      auto wr_r = [&]() AI {
        unsigned vv = v16;
        asm volatile("" : "+v"(vv));
#pragma unroll
        for (int i = 0; i < NR; ++i) *(u32x4*)(smem + L_STG + ro + i * 1024 + vv) = r[i];
      };
      auto left = [&]() AI {
        OPAQUE_LANE
        for (;;) {
          MEMFENCE;
          if (__builtin_amdgcn_ballot_w64(lflag[lane & 7] != (unsigned)layer) == 0 || --budget <= 0) break;
          SLEEP(2);
        }
        MEMFENCE;
      };
      if (isB && layer == 3) {
        STAMP(14);
        left();
        wr_r();
        wait_vm_lgkm0<0>();
        asm volatile("" :: "v"(tch));
        bar();
        STAMP_DUMP;
        __builtin_amdgcn_endpgm();
      }
      if (isB) {
        ld_wn(layer + 1, 0, 2);
        STAMP(14);
        left();
        ld_wn(layer + 1, 2, 4);
        wr_r();
        wait_vm_lgkm0<24>();
      } else {
        if (is0) wait_vm<26>();
        STAMP(14);
        bar();
        stg6(48 * 1024 + L * 6144);
        ld_wn(layer + 1, 0, 1);
        bar();
        extra2(9);
        ld_wn(layer + 1, 1, 4);
        wr_r();
        if (!is0) drain();
        else wait_vm<18>();
        bar();
        extra2(10);
        wait_vm_lgkm0<0>();
      }
      bar();
      OPAQUE_S
      wr_wn(0);
      drain();
      sync_lds();
      OPAQUE_S
      SCHED0;
      q_groupW(layer + 1, 4, 4);
#pragma unroll
      for (int g = 1; g < 4; ++g) wr_wn(g);
    };
    body(IC<0>{}, 0);
#pragma unroll 1
    for (int layer = 1;; layer += 2) {
      body(IC<1>{}, layer);
      body(IC<2>{}, 2);
    }
  }

  const f32x4 zero = f32x4{0.f, 0.f, 0.f, 0.f};
#pragma unroll 1
  for (int layer = 0; layer < 4; ++layer) {
    const bool isB = layer & 1;
    const unsigned ar = WS_AR + layer * ARENA;
    {
      f32x4 acc[2][6];
      f32x4 ck[2], mv[2], xv[2];
      float mean[2], rs[2], cv[2];
      u32x2 wt[4];
      const float* lcl = lcs + (isB ? 96 : 0);
      {
        LANE_VARS
        const unsigned va = v16 + cw * 2048;
        const unsigned sa0 = ar + AR_X + bm * (512 * 1024);
        auto lda = [&](unsigned so, u32x4* a) AI {
          a[0] = ld16s(wsr, va, so);
          a[1] = ld16s(wsr, va + 1024, so);
        };
        bf16x8 f[2][6];
        u32x4 a[4][2];
        auto unset = [&](const u32x4* e) AI { return poisoned(umax(umax(umax(e[0][0], e[0][2]), e[1][0]), e[1][2])); };
        auto refill = [&](auto ic, unsigned cur) AI {
          constexpr int I = decltype(ic)::value;
          do {
            COUNT(0);
            SLEEP(SLP_R);
            MEMFENCE;
            lda(cur, a[I]);
            bool u[4];
#pragma unroll
            for (int j = 1; j < 4; ++j) u[j] = unset(a[(I + j) % 4]);
            SCHED0;
#pragma unroll
            for (int j = 1; j < 4; ++j)
              if (u[j]) lda(cur + j * 16384, a[(I + j) % 4]);
            __builtin_amdgcn_s_waitcnt(0x0f70);
            MEMFENCE;
          } while (unset(a[I]) && --budget > 0);
        };
        STAMP(0);
        if (layer > 0) {
          bar();
          if (cn < 8) { if (cw < 4) SLEEP(SLP_X0); else SLEEP(SLP_X1); }
          else { if (cw < 4) SLEEP(SLP_XD0); else SLEEP(SLP_XD1); }
        }
#pragma unroll
        for (int j = 0; j < 4; ++j) { lda(sa0 + j * 16384, a[j]); SCHED0; }
#pragma unroll
        for (int m = 0; m < 2; ++m)
#pragma unroll
          for (int n = 0; n < 6; ++n) acc[m][n] = zero;
        asm volatile("" : "+v"(acc[0][0]), "+v"(acc[0][1]), "+v"(acc[0][2]), "+v"(acc[0][3]), "+v"(acc[0][4]), "+v"(acc[0][5]));
        asm volatile("" : "+v"(acc[1][0]), "+v"(acc[1][1]), "+v"(acc[1][2]), "+v"(acc[1][3]), "+v"(acc[1][4]), "+v"(acc[1][5]));
        if (layer == 0) sync_lds();
#pragma unroll
        for (int nn = 0; nn < 6; ++nn) { const int n = nn < 4 ? nn + 2 : nn - 4; f[0][n] = *(const bf16x8*)(smem + v16 + n * 1024); }
        SCHED0;
        if (layer > 0) {
          const u32x2 tg = *(const u32x2*)(lflag + 10);
          wait_vm<8>();
          if (lane == 0) {
            unsigned old;
            asm volatile("ds_add_rtn_u32 %0, %1, %2\n s_waitcnt lgkmcnt(0)" : "=v"(old) : "v"(sbase + L_RED + 32), "v"(1) : "memory");
            if (old == 8u * layer - 1) __builtin_amdgcn_raw_buffer_store_b64(tg, wsr, 0, ar + AR_FL + bm * 256 + (cm * 16 + cn) * 8, 16);
          }
          MEMFENCE;
        }
        SCHED0;
        bool bad = layer > 0 && unset(a[0]);
        const int wrap = layer > 0 ? 6 : 4;
        unsigned ps[4] = {sbase + L_RED + 64 + cw * 128, sbase + L_RED + 2176 + (layer & 1) * 384, 0, sbase + L_WP};
        ps[2] = ps[0] + 1088 + (layer & 1) * (2176 + 384 - 1088 - 64 - cw * 128);
        static_assert(L_WP % 1024 == 0, "");
        int early = (layer & 1) * cw;
        asm volatile("" : "+s"(ps[0]), "+s"(ps[1]), "+s"(ps[2]), "+s"(ps[3]), "+s"(early));
        STAMP(1);
        auto group = [&](auto modec, int g) AI {
          constexpr int MODE = decltype(modec)::value;
          constexpr bool LAST = MODE == 3, CHK = MODE == 0;
          const unsigned gs = (g < wrap ? g : g - 4) * QGRP, gn = (g + 1 < wrap ? g + 1 : g - 3) * QGRP;
          const unsigned sc = sa0 + g * 65536, so = sc + 4 * 16384;
          unsigned gl = sbase + gs + v16;
          if constexpr (LAST) asm volatile("" : "+v"(gl));
          unsigned pa[5];
          static_for<4>([&](auto ic) AI {
            constexpr int i = decltype(ic)::value;
            constexpr bool END = LAST && i == 3;
            SCHED0;
            if (i == 3 && !LAST) { sync_lds(); SCHED0; }
            if (i == 0 && LAST) {
              PRIO(0);
              if (early >= 4) PRIO(3);
              SCHED0;
              bar();
              SCHED0;
            }
            if constexpr (!LAST) PRIO(i == 3 ? 3 : 2 - i);
            if constexpr (CHK) { if (__builtin_expect(bad, 0)) refill(ic, sc + i * 16384); }
            SCHED0;
            const bf16x8* fc = f[i & 1];
            bf16x8* fn = f[(i + 1) & 1];
            const u32x4* an = a[(i + 1) % 4];
            const unsigned slot = i < 3 ? gs + (i + 1) * QSLOT : gn;
            const char* pb = LAST ? (const char*)((const LDS(char)*)gl + (i + 1) * QSLOT) : smem + slot + v16;
            unsigned mx = 0;
            static_for<12>([&](auto jc) AI {
              constexpr int j = decltype(jc)::value, nn = j >> 1, m = j & 1, n = nn < 4 ? nn + 2 : nn - 4;
              if constexpr (n < 4) mfma_acc(acc[m][n], fc[n], as_frag(a[i][m]));
              else mfma_acc(acc[m][n], as_frag(a[i][m]), fc[n]);
              SCHED0;
              if constexpr (!END && j < 6) { constexpr int r = j < 4 ? j + 2 : j - 4; fn[r] = *(const bf16x8*)(pb + r * 1024); }
              if constexpr (LAST && i == 0) {
                if constexpr (j == 7) { pa[4] = v16 >> 1; asm volatile("" : "+v"(pa[4])); }
                if constexpr (j == 9) { pa[4] = (pa[4] & 24) | ps[3]; asm volatile("" : "+v"(pa[4])); }
                if constexpr (j == 11) { pa[4] |= v16 & ~63u; asm volatile("" : "+v"(pa[4])); }
              }
              if constexpr (LAST && i == 1) {
                if constexpr (j == 6) { pa[0] = lane & 48; asm volatile("" : "+v"(pa[0])); }
                if constexpr (j == 7) { pa[1] = v16 >> 2; asm volatile("" : "+v"(pa[1])); }
                if constexpr (j == 8) { pa[1] &= 60; asm volatile("" : "+v"(pa[1])); }
                if constexpr (j == 9) { pa[2] = pa[1] + ps[0]; asm volatile("" : "+v"(pa[2])); }
                if constexpr (j == 10) { pa[1] += ps[1]; asm volatile("" : "+v"(pa[1])); }
                if constexpr (j == 11) { pa[3] = pa[0] + ps[1]; asm volatile("" : "+v"(pa[3])); }
              }
              if constexpr (LAST && i == 2) {
                if constexpr (j == 6) ck[0] = ((const LDS(f32x4)*)pa[3])[8];
                if constexpr (j == 7) ck[1] = ((const LDS(f32x4)*)pa[3])[12];
                if constexpr (j == 8) { mean[0] = ((const LDS(float)*)pa[2])[0]; asm volatile("" : "+v"(pa[2])); }
                if constexpr (j == 9) { mean[1] = ((const LDS(float)*)pa[2])[16]; asm volatile("" : "+v"(pa[2])); }
                if constexpr (j == 10) { cv[0] = ((const LDS(float)*)pa[1])[64]; cv[1] = ((const LDS(float)*)pa[1])[80]; }
                if constexpr (j == 11) { pa[1] = pa[0] + ps[0]; asm volatile("" : "+v"(pa[1])); }
              }
              if constexpr (LAST && i == 3) {
                if constexpr (j == 0) mv[0] = ((const LDS(f32x4)*)pa[1])[0];
                if constexpr (j == 1) mv[1] = ((const LDS(f32x4)*)pa[1])[4];
                if constexpr (j == 2) { pa[0] += ps[2]; asm volatile("" : "+v"(pa[0])); }
                if constexpr (j == 3) { rs[0] = ((const LDS(float)*)pa[2])[272]; asm volatile("" : "+v"(pa[2])); }
                if constexpr (j == 4) xv[0] = ((const LDS(f32x4)*)pa[0])[0];
                if constexpr (j == 5) xv[1] = ((const LDS(f32x4)*)pa[0])[4];
                if constexpr (j == 6) rs[1] = ((const LDS(float)*)pa[2])[288];
                if constexpr (j == 8) wt[0] = trl(pa[4]);
                if constexpr (j == 9) wt[1] = trl(pa[4] + 1024);
                if constexpr (j == 10) wt[2] = trl(pa[4] + 32);
                if constexpr (j == 11) wt[3] = trl(pa[4] + 1056);
              }
              if constexpr (CHK) {
                if constexpr (j == 8) mx = umax(umax(umax(an[0][0], an[0][2]), an[1][0]), an[1][2]);
                if constexpr (j == 10) bad = poisoned(mx);
              }
              if constexpr (j == 10 && !LAST) a[i][0] = ld16s(wsr, va, so + i * 16384);
              if constexpr (j == 11 && !LAST) a[i][1] = ld16s(wsr, va + 1024, so + i * 16384);
              SCHED0;
            });
          });
        };
        int g = 0;
        if (layer > 0) {
#pragma unroll 1
          for (; g < NCHK; ++g) group(IC<0>{}, g);
        }
#pragma unroll 1
        for (; g < 7; ++g) group(IC<1>{}, g);
        group(IC<3>{}, 7);
        if (!isB && cw >= 4) PRIO(1);
        mfma_done();
      }
      STAMP(2);
      LANE_VARS
      char* const scr = smem;

      u32x4 qf[2], kf[2];
      u32x2 vf[2][2];
      auto fold_rows = [&](int part, u32x4* out, const f32x4* c) AI {
#pragma unroll
        for (int m = 0; m < 2; ++m) {
          f32x4 a0 = acc[m][part * 2] + c[0] * mean[m], a1 = acc[m][part * 2 + 1] + c[1] * mean[m];
          out[m] = u32x4{pk2(a0[0], a0[1]), pk2(a0[2], a0[3]), pk2(a1[0], a1[1]), pk2(a1[2], a1[3])};
        }
      };
      auto fold_v = [&](bool scaled) AI {
#pragma unroll
        for (int m = 0; m < 2; ++m) {
          f32x4 a0 = acc[m][4] + mv[m] * cv[0], a1 = acc[m][5] + mv[m] * cv[1];
          if (scaled) { a0 = a0 * xv[m]; a1 = a1 * xv[m]; }
          vf[m][0] = u32x2{pk2(a0[0], a0[1]), pk2(a0[2], a0[3])};
          vf[m][1] = u32x2{pk2(a1[0], a1[1]), pk2(a1[2], a1[3])};
        }
      };
      fold_rows(1, kf, ck);
      const unsigned otile = WS_AR + AR_O + xt_off(bm * 16 + cw * 2, head);
      const unsigned opar = layer & 1;
      auto put_o = [&](int m, f32x4 a, f32x4 b) AI {
        st16s(wsr, v16 + m * 1024, otile, u32x4{(pk2(a[0], a[1]) & ~1u) | opar, pk2(a[2], a[3]), (pk2(b[0], b[1]) & ~1u) | opar, pk2(b[2], b[3])});
      };

      if (isB) {
        char* ex = scr;
        fold_v(false);
        const unsigned hrec = ar + AR_KV + (unsigned)(bm * 32 + head) * KVREC;
        {
          const u32x4 vl = u32x4{vf[1][0][0], vf[1][0][1], vf[1][1][0], vf[1][1][1]};
          if (cw == 7) {
            st16s(wsr, v16, hrec, clean(kf[1]));
            STPAD;
            st16s(wsr, v16 + 1024, hrec, clean(vl));
          }
          char* e2 = ex + (cw + 1) * 2048;
          *(u32x4*)(e2 + lane * 16) = kf[1];
          *(u32x2*)(e2 + 1024 + lane * 8) = vf[1][0];
          *(u32x2*)(e2 + 1536 + lane * 8) = vf[1][1];
          MEMFENCE;
          lflag[cw] = layer;
          MEMFENCE;
          PRIO(0);
        }
        SCHED0;
        fold_rows(0, qf, xv);
        u32x4 kp;
        u32x4 va0 = __builtin_shufflevector(vf[0][0], vf[0][0], -1, -1, 0, 1), vb0 = __builtin_shufflevector(vf[0][1], vf[0][1], -1, -1, 0, 1);
        auto setp = [&](u32x4& d, u32x2 v) AI { d[0] = v[0]; d[1] = v[1]; };
        auto ldh = [&]() AI {
          MEMFENCE;
          kp = ld16s(wsr, v16, hrec - 32 * KVREC);
          setp(va0, __builtin_amdgcn_raw_buffer_load_b64(wsr, v16 + 1024, hrec - 32 * KVREC, 16));
          MEMFENCE;
          setp(vb0, __builtin_amdgcn_raw_buffer_load_b64(wsr, v16 + 1032, hrec - 32 * KVREC, 16));
          MEMFENCE;
        };
        auto prev = [&]() AI {
          if (cw > 0) {
            for (;;) {
              MEMFENCE;
              const unsigned fl = lflag[cw - 1];
              MEMFENCE;
              kp = *(const u32x4*)(ex + cw * 2048 + lane * 16);
              setp(va0, *(const u32x2*)(ex + cw * 2048 + 1024 + lane * 8));
              MEMFENCE;
              setp(vb0, *(const u32x2*)(ex + cw * 2048 + 1536 + lane * 8));
              MEMFENCE;
              if (__builtin_expect(__builtin_amdgcn_readfirstlane(fl) == (unsigned)layer, 1) || --budget <= 0) break;
              SLEEP(1);
            }
          } else if (bm > 0) {
            SLEEP(SLP_HX);
            ldh();
            while (__builtin_expect(poisoned(umax4(umax4(kp), u32x4{va0[0], va0[1], vb0[0], vb0[1]})), 0) && --budget > 0) { COUNT(2); ldh(); }
          } else {
            kp = u32x4{0u, 0u, 0u, 0u};
            setp(va0, u32x2{0u, 0u}); setp(vb0, u32x2{0u, 0u});
            asm volatile("" : "+v"(kp), "+v"(va0), "+v"(vb0));
          }
        };
        const float scale = 0.17677669529663687f * LOG2E;
        const bool odd = fr & 1;
        const bool own0 = fr >= fq * 4 + (fr & 1), own1 = fr >= fq * 4 + (fr & 1) + 2;
        const bool nopre = (bm == 0) && (cw == 0);
        float l[2][2], mx[2], rk[2][2], sum[2];
#pragma unroll
        for (int m = 0; m < 2; ++m) {
          const float* rb = lrstd + (cw * 2 + m) * 16 + fq * 4 + (fr & 1);
          rk[m][0] = rb[own0 ? 16 : 0];
          rk[m][1] = rb[own1 ? 18 : 2];
        }
        float qk[2][2];
#pragma unroll
        for (int m = 0; m < 2; ++m) { const float qs = rs[m] * scale; qk[m][0] = qs * rk[m][0]; qk[m][1] = qs * rk[m][1]; }
        const unsigned e0 = odd ? 0x01000c0cu : 0x0c0c0100u, e1 = odd ? 0x03020c0cu : 0x0c0c0302u, ez = 0x0c0c0c0cu;
        u32x4 sel = u32x4{own0 ? ez : e0, own1 ? ez : e1, own0 ? e0 : ez, own1 ? e1 : ez};
        u32x4 va1 = u32x4{vf[0][0][0], vf[0][0][1], vf[1][0][0], vf[1][0][1]}, vb1 = u32x4{vf[0][1][0], vf[0][1][1], vf[1][1][0], vf[1][1][1]};
        auto score = [&](int m) AI {
          f32x4 s0 = mfma16(as_frag(m == 0 ? kp : kf[0]), as_frag(qf[m]), zero);
          f32x4 s1 = mfma16(as_frag(kf[m]), as_frag(qf[m]), zero);
          float a = own0 ? (odd ? s1[1] : s1[0]) : (odd ? s0[1] : s0[0]);
          float b = own1 ? (odd ? s1[3] : s1[2]) : (odd ? s0[3] : s0[2]);
          l[m][0] = a * qk[m][0]; l[m][1] = b * qk[m][1];
          if (m == 0) {
            if (nopre && !own0) l[m][0] = -3.0e38f;
            if (nopre && !own1) l[m][1] = -3.0e38f;
          }
          mx[m] = fmx(l[m][0], l[m][1]);
        };
        auto expo = [&](int m) AI {
          l[m][0] = __builtin_amdgcn_exp2f(l[m][0] - mx[m]);
          l[m][1] = __builtin_amdgcn_exp2f(l[m][1] - mx[m]);
          sum[m] = l[m][0] + l[m][1];
        };
        f32x4 ov[2][2];
        auto pv = [&](int m) AI {
          const unsigned pr = pk2(mul1(mul1(l[m][0], rk[m][0]), sum[m]), mul1(mul1(l[m][1], rk[m][1]), sum[m]));
          const u32x4 pf = u32x4{__builtin_amdgcn_perm(pr, pr, sel[0]), __builtin_amdgcn_perm(pr, pr, sel[1]),
                                 __builtin_amdgcn_perm(pr, pr, sel[2]), __builtin_amdgcn_perm(pr, pr, sel[3])};
          ov[m][0] = mfma16(as_frag(m ? va1 : va0), as_frag(pf), zero);
          ov[m][1] = mfma16(as_frag(m ? vb1 : vb0), as_frag(pf), zero);
        };
        score(1);
        asm volatile("" : "+v"(qf[0]), "+v"(qk[0][0]), "+v"(qk[0][1]), "+v"(mx[1]), "+v"(l[1][0]), "+v"(l[1][1]));
        asm volatile("" : "+v"(sel), "+v"(va1), "+v"(vb1), "+v"(va0), "+v"(vb0));
        SCHED0;
        STAMP(3);
        prev();
        score(0);
        xred2<true>(mx[0], mx[1]);
        expo(0); expo(1);
        xred2<false, true>(sum[0], sum[1]);
        pv(0); pv(1);
        SCHED0;
        put_o(0, ov[0][0], ov[0][1]); put_o(1, ov[1][0], ov[1][1]);
        STAMP(4);
      } else {
        fold_v(true);
        char* PK = smem + L_STG;
        char* VF = scr;
        const bf16x8 wf0 = as_frag(u32x4{wt[0][0], wt[0][1], wt[1][0], wt[1][1]}), wf1 = as_frag(u32x4{wt[2][0], wt[2][1], wt[3][0], wt[3][1]});
#pragma unroll
        for (int t = 0; t < 2; ++t)
          *(u32x4*)(VF + (cw * 2 + t) * 1024 + lane * 16) = u32x4{vf[0][t][0], vf[0][t][1], vf[1][t][0], vf[1][t][1]};
        {
          u32x4 pk[2][2];
          phi_map2(kf, rs, wf0, wf1, pk);
          char* d0 = PK + cw * 4096 + fr * 64 + ((fr >> 2) & 1) * 32 + ((fq ^ ((fr >> 3) * 2)) * 8);
          char* d1 = PK + cw * 4096 + fr * 64 + (((fr >> 2) & 1) ^ 1) * 32 + ((fq ^ ((fr >> 3) * 2)) * 8);
#pragma unroll
          for (int m = 0; m < 2; ++m) {
            *(u32x2*)(d0 + m * 2048) = u32x2{pk[m][0][0], pk[m][0][1]};
            *(u32x2*)(d0 + m * 2048 + 1024) = u32x2{pk[m][0][2], pk[m][0][3]};
            *(u32x2*)(d1 + m * 2048) = u32x2{pk[m][1][0], pk[m][1][1]};
            *(u32x2*)(d1 + m * 2048 + 1024) = u32x2{pk[m][1][2], pk[m][1][3]};
          }
        }
        PRIO(0);
        sync_lds();
        STAMP(3);
        const unsigned rec = ar + AR_KV + (unsigned)head * KVREC;
        {
          const u32x4 ones = u32x4{0x3f803f80u, 0x3f803f80u, 0x3f803f80u, 0x3f803f80u};
          f32x4 c0 = zero, c1 = zero;
          const char* ad = PK + (cw & 1) * 1024 + (fq * 4 + (fr >> 2)) * 64 + (((cw >> 1) ^ fq) & 1) * 32 + (((fr & 3) ^ (fq & 2)) * 8);
          const char* vd = VF + (cw >> 2) * 1024 + lane * 16;
          bf16x8 a[8], b0[8];
#pragma unroll
          for (int ks = 0; ks < 8; ++ks) {
            u32x2 a0 = trread(ad + ks * 4096), a1 = trread(ad + ks * 4096 + 2048);
            a[ks] = as_frag(u32x4{a0[0], a0[1], a1[0], a1[1]});
            b0[ks] = *(const bf16x8*)(vd + ks * 2048);
          }
          SCHED0;
#pragma unroll
          for (int ks = 0; ks < 8; ks += 2) {
            c0 = mfma16(a[ks], b0[ks], c0);
            c1 = mfma16(a[ks + 1], b0[ks + 1], c1);
          }
          c0 += c1;
          const unsigned dst = rec + (unsigned)bm * (32 * KVREC);
          st16(wsr, dst + cw * 1024 + v16, clean(__builtin_bit_cast(u32x4, c0)));
          SCHED0;
          if (cw < 4) {
            f32x4 cz = zero, cy = zero;
#pragma unroll
            for (int ks = 0; ks < 8; ks += 2) {
              cz = mfma16(a[ks], as_frag(ones), cz);
              cy = mfma16(a[ks + 1], as_frag(ones), cy);
            }
            cz += cy;
            if (fr == 0) st16(wsr, dst + 8192 + cw * 64 + fq * 16, clean(__builtin_bit_cast(u32x4, cz)));
          }
        }
        SCHED0;
        STAMP(4);
        bar();
        const f32x4 cq[2] = {*(const f32x4*)(lcl + fq * 4), *(const f32x4*)(lcl + 16 + fq * 4)};
        fold_rows(0, qf, cq);
        u32x4 pq[2][2];
        u32x4 zp = u32x4{0u, 0u, 0u, 0u}, zq = zp;
        u32x4 part[8];
        auto ldz = [&]() AI {
          zp = ld16(wsr, rec + fq * (32 * KVREC) + 8192 + fr * 16);
          zq = ld16(wsr, rec + (4 + fq) * (32 * KVREC) + 8192 + fr * 16);
        };
        phi_map2(qf, rs, wf0, wf1, pq);
        asm volatile("" : "+v"(pq[0][0]), "+v"(pq[0][1]), "+v"(pq[1][0]), "+v"(pq[1][1]));
        SCHED0;
        STAMP(5);
        char* kvf = smem + L_A2;
        SLEEP(SLP_K);
        MEMFENCE;
        if (cw == 0) ldz();
#pragma unroll
        for (int rb = 0; rb < 8; ++rb) part[rb] = ld16s(wsr, v16, rec + rb * (32 * KVREC) + cw * 1024);
        MEMFENCE;
        f32x4 s4;
        for (;;) {
          MEMFENCE;
          auto nans = [&](f32x4 v) AI { return __builtin_amdgcn_ballot_w64(__builtin_isunordered(v[0], v[1])) | __builtin_amdgcn_ballot_w64(__builtin_isunordered(v[2], v[3])); };
          unsigned long long nan = 0;
          if (cw == 0) {
            f32x4 z = __builtin_bit_cast(f32x4, zp) + __builtin_bit_cast(f32x4, zq);
            nan = nans(z);
            float z0 = z[0], z1 = z[1], z2 = z[2], z3 = z[3];
            xred4<false>(z0, z1, z2, z3);
            if (fq == 0) *(u32x2*)(smem + L_Z + (fr >> 3) * 64 + (fr & 3) * 16 + ((fr >> 2) & 1) * 8) = u32x2{pk2(z0, z1), pk2(z2, z3)};
          }
          s4 = __builtin_bit_cast(f32x4, part[0]);
#pragma unroll
          for (int rb = 1; rb < 8; ++rb) { s4 += __builtin_bit_cast(f32x4, part[rb]); SCHED0; }
          if (__builtin_expect((nan | nans(s4)) == 0, 1)) break;
          bool again = false;
          if (poisoned(umax4(umax4(zp), zq))) { again = true; ldz(); }
#pragma unroll
          for (int rb = 0; rb < 8; ++rb)
            if (poisoned(umax4(part[rb]))) { again = true; part[rb] = ld16s(wsr, v16, rec + rb * (32 * KVREC) + cw * 1024); }
          __builtin_amdgcn_s_waitcnt(0x0f70);
          if (!again || --budget <= 0) break;
          COUNT(2);
        }
        STAMP(6);
        *(u32x2*)(kvf + (cw >> 1) * 1024 + lane * 16 + (cw & 1) * 8) = u32x2{pk2(s4[0], s4[1]), pk2(s4[2], s4[3])};
        sync_lds();
        STAMP(7);
        bf16x8 kvr[2][2], zf[2];
#pragma unroll
        for (int s = 0; s < 2; ++s) {
          kvr[0][s] = *(const bf16x8*)(kvf + (0 * 2 + s) * 1024 + lane * 16);
          kvr[1][s] = *(const bf16x8*)(kvf + (1 * 2 + s) * 1024 + lane * 16);
          zf[s] = *(const bf16x8*)(smem + L_Z + s * 64 + fq * 16);
        }
#pragma unroll
        for (int m = 0; m < 2; ++m) {
          f32x4 c0 = mfma16(kvr[0][1], as_frag(pq[m][1]), mfma16(kvr[0][0], as_frag(pq[m][0]), zero));
          f32x4 c1 = mfma16(kvr[1][1], as_frag(pq[m][1]), mfma16(kvr[1][0], as_frag(pq[m][0]), zero));
          f32x4 cd = mfma16(zf[1], as_frag(pq[m][1]), mfma16(zf[0], as_frag(pq[m][0]), zero));
          const float iden = __builtin_amdgcn_rcpf(cd[0] + 1e-6f);
          c0 *= iden; c1 *= iden;
          put_o(m, c0, c1);
        }
        STAMP(8);
      }
    }
    {
      LANE_VARS
      const int mt = bm * 16 + cw * 2 + cm;
      u32x4 xr[2];
#pragma unroll
      for (int j = 0; j < 2; ++j) xr[j] = ld16s(wsr, v16, ar + AR_X + xt_off(mt, cn * 2 + j));
      const unsigned so0 = WS_AR + AR_O + xt_off(mt, 0);
      const unsigned opar = layer & 1;
      auto diff = [&](u32x4 v) AI { return ((v[0] ^ opar) | (v[2] ^ opar)) & 1u; };
      auto dif3 = [&](u32x4 v) AI { return __builtin_amdgcn_bitop3_b32(v[0], v[2], opar, 0x7e); };
      auto any = [&](unsigned d) AI { return __builtin_amdgcn_ballot_w64(d != 0) != 0; };
      const int g0 = cm;
      u32x4 a[RO];
      auto refill = [&](auto ic, auto wc, unsigned cur, unsigned nxt) AI {
        constexpr int I = decltype(ic)::value, W = decltype(wc)::value;
        do {
          COUNT(1);
          SLEEP(SLP_R);
          MEMFENCE;
          a[I] = ld16s(wsr, v16, cur + I * 16384);
          bool u[RO];
#pragma unroll
          for (int j = I + 1; j < I + W; ++j) u[j % RO] = any(diff(a[j % RO]));
          SCHED0;
#pragma unroll
          for (int j = I + 1; j < I + W; ++j)
            if (u[j % RO]) a[j % RO] = ld16s(wsr, v16, (j < RO ? cur : nxt) + (j % RO) * 16384);
          __builtin_amdgcn_s_waitcnt(0x0f70);
          MEMFENCE;
        } while (any(diff(a[I]) | diff(a[I + 1])) && --budget > 0);
      };
      const unsigned sf = so0 + g0 * (RO * 16384);
      const bool late = isB && cw == 0;
      if (late) bar();
      if (cn < 8) { if (late) SLEEP(SLP_O0); else SLEEP(SLP_O); }
      else { if (late) SLEEP(SLP_O0D); else SLEEP(SLP_OD); }
#pragma unroll
      for (int j = 0; j < 4; ++j) { a[j] = ld16s(wsr, v16, sf + j * 16384); SCHED0; }
      if (!late) bar();
      STAMP(9);
      bf16x8 wf[2][4];
      const char* pw0 = smem + L_STG + v16;
#pragma unroll
      for (int n = 0; n < 8; ++n) wf[n >> 2][n & 3] = *(const bf16x8*)(pw0 + g0 * (RO * 4096) + n * 1024);
      f32x4 acc[4];
#pragma unroll
      for (int n = 0; n < 4; ++n)
        acc[n] = f32x4{bflo(xr[n >> 1][(n & 1) * 2]), bfhi(xr[n >> 1][(n & 1) * 2]), bflo(xr[n >> 1][(n & 1) * 2 + 1]), bfhi(xr[n >> 1][(n & 1) * 2 + 1])};
      asm volatile("" : "+v"(acc[0]), "+v"(acc[1]), "+v"(acc[2]), "+v"(acc[3]));
      SCHED0;
      STAMP(10);
      if (any(diff(a[0]) | diff(a[1]))) refill(IC<0>{}, IC<4>{}, sf, sf);
      SCHED0;
#pragma unroll
      for (int j = 4; j < RO; ++j) { a[j] = ld16s(wsr, v16, sf + j * 16384); SCHED0; }
      STAMP(11);
      bool bad = false;
      auto group = [&](auto lastc, int g) AI {
        constexpr bool LAST = decltype(lastc)::value;
        const int gc = g ^ g0, gn = (g + 1) ^ g0;
        const unsigned sc = so0 + gc * (RO * 16384), so = so0 + gn * (RO * 16384);
        const char* pc = pw0 + gc * (RO * 4096);
        const char* pn = pw0 + gn * (RO * 4096);
        static_for<RO>([&](auto ic) AI {
          constexpr int i = decltype(ic)::value;
          SCHED0;
          if constexpr (i % 2 == 0)
            if (__builtin_expect(bad, 0)) refill(ic, IC<(LAST && i + 4 > RO) ? RO - i : 4>{}, sc, so);
          SCHED0;
          unsigned mx = 0, my = 0;
          static_for<4>([&](auto nc) AI {
            constexpr int n = decltype(nc)::value;
            mfma_acc(acc[n], wf[i & 1][n], as_frag(a[i]));
            SCHED0;
            if constexpr (i + 2 < RO) wf[i & 1][n] = *(const bf16x8*)(pc + (i + 2) * 4096 + n * 1024);
            else if constexpr (!LAST) wf[i & 1][n] = *(const bf16x8*)(pn + (i + 2 - RO) * 4096 + n * 1024);
            if constexpr (!(LAST && i == RO - 1) && (i & 1)) {
              if constexpr (n == 0) mx = dif3(a[(i + 1) % RO]);
              if constexpr (n == 1) my = dif3(a[(i + 2) % RO]);
              if constexpr (n == 2) bad = any((mx | my) & 1u);
            }
            if constexpr (n == 3 && !LAST) a[i] = ld16s(wsr, v16, so + i * 16384);
            SCHED0;
          });
        });
      };
#pragma unroll 1
      for (int g = 0; g < 3; ++g) group(std::false_type{}, g);
      group(std::true_type{}, 3);
      mfma_done();
      STAMP(12);
#pragma unroll
      for (int j = 0; j < 2; ++j)
#pragma unroll
        for (int e = 0; e < 4; ++e) xr[j][e] = pk2(acc[j * 2 + (e >> 1)][(e & 1) * 2], acc[j * 2 + (e >> 1)][(e & 1) * 2 + 1]);
      if (layer == 3) {
        u32x4 yo[2];
#pragma unroll
        for (int j = 0; j < 2; ++j) {
          auto u = __builtin_amdgcn_permlane16_swap(xr[j][0], xr[j][2], false, false);
          auto v = __builtin_amdgcn_permlane16_swap(xr[j][1], xr[j][3], false, false);
          yo[j] = u32x4{u[0], v[0], u[1], v[1]};
        }
        const unsigned vo = fr * 2048 + (fq & 1) * 32 + (fq >> 1) * 16;
        __builtin_amdgcn_raw_buffer_store_b128(yo[0], mk_rsrc(p.out, 4 * MBY), vo, mt * 32768 + cn * 128, 2);
        __builtin_amdgcn_raw_buffer_store_b128(yo[1], mk_rsrc(p.out, 4 * MBY), vo + 64, mt * 32768 + cn * 128, 2);
        STAMP(13);
        STAMP(14);
        STAMP_DUMP;
        return;
      }
      const u32x2 stat = put_x(xr, v16, ar + ARENA + AR_X + xt_off(mt, cn * 2));
      if (fq == 0) __builtin_amdgcn_raw_buffer_store_b64(stat, wsr, fr * 8, ar + ARENA + AR_ST + (mt * 16 + cn) * 128, 16);
      MEMFENCE;
      STAMP(14);
    }
  }
}

extern "C" void kernel_launch(void* const* d_in, const int* in_sizes, int n_in, void* d_out, int out_size,
                              void* d_ws, size_t ws_size, hipStream_t stream) {
  (void)hipFuncSetAttribute((const void*)mega_kernel, hipFuncAttributeMaxDynamicSharedMemorySize, MEGA_LDS);
  MegaParams m;
  m.ws = (char*)d_ws; m.out = (u16*)d_out; m.x = (const u16*)d_in[0]; m.wphi = (const u16*)d_in[2];
  m.wqkvA = (const u16*)d_in[1]; m.wqkvB = (const u16*)d_in[5];
  m.woA = (const u16*)d_in[3]; m.woB = (const u16*)d_in[6];
  mega_kernel<<<256, 768, MEGA_LDS, stream>>>(m);
}
```

```cpp
#include <hip/hip_runtime.h>
#include <stdint.h>
#include <type_traits>

#define DI __device__ __forceinline__
#define AI __attribute__((always_inline))
typedef unsigned short u16;
typedef __attribute__((ext_vector_type(8))) short bf16x8;
typedef __attribute__((ext_vector_type(4))) short s16x4;
typedef __attribute__((ext_vector_type(4))) float f32x4;
typedef __attribute__((ext_vector_type(2))) float f32x2;
typedef __attribute__((ext_vector_type(2))) __bf16 bf2;
typedef __attribute__((ext_vector_type(4))) unsigned u32x4;
typedef __attribute__((ext_vector_type(2))) unsigned u32x2;
typedef __attribute__((address_space(3))) s16x4* lds_s16x4_ptr;
#define LDS(T) __attribute__((address_space(3))) T
template <int N> using IC = std::integral_constant<int, N>;

constexpr float LOG2E = 1.4426950408889634f;

DI unsigned pk2(float a, float b) {
  f32x2 v = {a, b};
  bf2 r = __builtin_convertvector(v, bf2);
  return __builtin_bit_cast(unsigned, r);
}
DI float bflo(unsigned u) { return __uint_as_float(u << 16); }
DI float bfhi(unsigned u) { return __uint_as_float(u & 0xffff0000u); }
DI f32x4 mfma16(bf16x8 a, bf16x8 b, f32x4 c) { return __builtin_amdgcn_mfma_f32_16x16x32_bf16(a, b, c, 0, 0, 0); }
DI void mfma_acc(f32x4& c, bf16x8 a, bf16x8 b) { asm volatile("v_mfma_f32_16x16x32_bf16 %0, %1, %2, %0" : "+v"(c) : "v"(a), "v"(b)); }
DI void mfma_done() { asm volatile("s_nop 7\n\ts_nop 1" ::: "memory"); __builtin_amdgcn_sched_barrier(0); }
DI bf16x8 as_frag(u32x4 v) { return __builtin_bit_cast(bf16x8, v); }
DI float dot2(unsigned a, unsigned b, float c) {
  return __builtin_amdgcn_fdot2_f32_bf16(__builtin_bit_cast(bf2, a), __builtin_bit_cast(bf2, b), c, false);
}
DI u32x2 trread(const char* p) { return __builtin_bit_cast(u32x2, __builtin_amdgcn_ds_read_tr16_b64_v4i16((lds_s16x4_ptr)p)); }
DI u32x2 trl(unsigned a) { return __builtin_bit_cast(u32x2, __builtin_amdgcn_ds_read_tr16_b64_v4i16((lds_s16x4_ptr)a)); }
DI float fmx(float a, float b) { return __builtin_elementwise_maximum(a, b); }
DI float fmn(float a, float b) { return __builtin_elementwise_minimum(a, b); }

DI float mul1(float a, float b) { float r; asm("v_mul_f32 %0, %1, %2" : "=v"(r) : "v"(a), "v"(b)); return r; }
DI float add1(float a, float b) { float r; asm("v_add_f32 %0, %1, %2" : "=v"(r) : "v"(a), "v"(b)); return r; }
DI f32x4 mul4(f32x4 a, f32x4 b) {
  float r0, r1, r2, r3;
  asm("v_mul_f32 %0, %4, %8\n\tv_mul_f32 %1, %5, %9\n\tv_mul_f32 %2, %6, %10\n\tv_mul_f32 %3, %7, %11"
      : "=&v"(r0), "=&v"(r1), "=&v"(r2), "=&v"(r3) : "v"(a[0]), "v"(a[1]), "v"(a[2]), "v"(a[3]), "v"(b[0]), "v"(b[1]), "v"(b[2]), "v"(b[3]));
  return f32x4{r0, r1, r2, r3};
}
DI f32x4 add4(f32x4 a, f32x4 b) { return f32x4{add1(a[0], b[0]), add1(a[1], b[1]), add1(a[2], b[2]), add1(a[3], b[3])}; }

template <bool IS_MAX, bool INV = false>
DI void xred2(float& a, float& b) {
  auto r = __builtin_amdgcn_permlane32_swap(__float_as_uint(a), __float_as_uint(b), false, false);
  float c = IS_MAX ? fmx(__uint_as_float(r[0]), __uint_as_float(r[1])) : __uint_as_float(r[0]) + __uint_as_float(r[1]);
  auto q = __builtin_amdgcn_permlane16_swap(__float_as_uint(c), __float_as_uint(c), false, false);
  c = IS_MAX ? fmx(__uint_as_float(q[0]), __uint_as_float(q[1])) : __uint_as_float(q[0]) + __uint_as_float(q[1]);
  if (INV) c = __builtin_amdgcn_rcpf(c);
  auto w = __builtin_amdgcn_permlane32_swap(__float_as_uint(c), __float_as_uint(c), false, false);
  a = __uint_as_float(w[0]); b = __uint_as_float(w[1]);
}
template <bool IS_MAX, bool INV = false>
DI void xred4(float& a, float& b, float& c, float& d) {
  auto op = [](unsigned x, unsigned y) { return IS_MAX ? fmx(__uint_as_float(x), __uint_as_float(y)) : __uint_as_float(x) + __uint_as_float(y); };
  auto r0 = __builtin_amdgcn_permlane32_swap(__float_as_uint(a), __float_as_uint(b), false, false);
  auto r1 = __builtin_amdgcn_permlane32_swap(__float_as_uint(c), __float_as_uint(d), false, false);
  float ab = op(r0[0], r0[1]), cd = op(r1[0], r1[1]);
  auto q = __builtin_amdgcn_permlane16_swap(__float_as_uint(ab), __float_as_uint(cd), false, false);
  float e = op(q[0], q[1]);
  if (INV) e = __builtin_amdgcn_rcpf(e);
  auto w = __builtin_amdgcn_permlane16_swap(__float_as_uint(e), __float_as_uint(e), false, false);
  auto u = __builtin_amdgcn_permlane32_swap(w[0], w[0], false, false);
  auto v = __builtin_amdgcn_permlane32_swap(w[1], w[1], false, false);
  a = __uint_as_float(u[0]); b = __uint_as_float(u[1]); c = __uint_as_float(v[0]); d = __uint_as_float(v[1]);
}

DI unsigned xt_off(int mt, int ks) { return (unsigned)((((mt >> 4) * 32 + ks) * 16 + (mt & 15)) << 10); }

#define GLDS_PRE "s_mov_b32 %0, m0\n\ts_mov_b32 m0, %3\n\ts_nop 2\n\t"
#define GLDS_POST "s_mov_b32 m0, %0"
#define GLDS_OPS : "=&s"(keep) : "v"(voff), "s"(sbase), "s"(lds) : "memory"
#define GL1(c) "global_load_lds_dwordx4 %1, %2" c "\n\t"
#define GL2(c) GL1(c) "global_load_lds_dwordx4 %1, %2 offset:1024" c "\n\t"
#define GL3(c) GL2(c) "global_load_lds_dwordx4 %1, %2 offset:2048" c "\n\t"
#define GL4(c) GL3(c) "global_load_lds_dwordx4 %1, %2 offset:3072" c "\n\t"
template <int N, bool SC1 = false>
DI void glds_run(unsigned voff, const void* sbase, unsigned lds) {
  unsigned keep;
  if constexpr (SC1) {
    if constexpr (N == 2) asm volatile(GLDS_PRE GL2(" sc1") GLDS_POST GLDS_OPS);
    else if constexpr (N == 3) asm volatile(GLDS_PRE GL3(" sc1") GLDS_POST GLDS_OPS);
    else asm volatile(GLDS_PRE GL4(" sc1") GLDS_POST GLDS_OPS);
  } else {
    static_assert(SC1 || N == 1, "");
    asm volatile(GLDS_PRE GL1("") GLDS_POST GLDS_OPS);
  }
}
template <int N> DI void wait_vm_lgkm0() { asm volatile("s_waitcnt vmcnt(%0) lgkmcnt(0)" ::"n"(N) : "memory"); }
template <int N> DI void wait_vm() { asm volatile("s_waitcnt vmcnt(%0)" ::"n"(N) : "memory"); }
DI void drain() { asm volatile("s_waitcnt vmcnt(0)" ::: "memory"); __builtin_amdgcn_s_waitcnt(0x0f70); }
DI void bar() { asm volatile("" ::: "memory"); __builtin_amdgcn_s_barrier(); asm volatile("" ::: "memory"); }
DI void sync_lds() { asm volatile("s_waitcnt lgkmcnt(0)" ::: "memory"); bar(); }
#define SCHED0 __builtin_amdgcn_sched_barrier(0)
#define STPAD do { SCHED0; asm volatile("s_nop 1"); SCHED0; } while (0)
#define SLEEP(n) do { if (n) __builtin_amdgcn_s_sleep(n); } while (0)
#define PRIO(n) __builtin_amdgcn_s_setprio(n)

DI void phi_map2(const u32x4* in, const float* rs, bf16x8 wf0, bf16x8 wf1, u32x4 (*out)[2]) {
  const f32x4 zero = f32x4{0.f, 0.f, 0.f, 0.f};
  float x[2][8], mx[2], mn[2];
#pragma unroll
  for (int m = 0; m < 2; ++m) {
    f32x4 r0 = mfma16(wf0, as_frag(in[m]), zero), r1 = mfma16(wf1, as_frag(in[m]), zero);
#pragma unroll
    for (int j = 0; j < 4; ++j) { x[m][j] = r0[j]; x[m][4 + j] = r1[j]; }
  }
#pragma unroll
  for (int m = 0; m < 2; ++m) {
    mx[m] = fmx(fmx(fmx(x[m][0], x[m][1]), x[m][2]), fmx(fmx(x[m][3], x[m][4]), fmx(fmx(x[m][5], x[m][6]), x[m][7])));
    mn[m] = -fmn(fmn(fmn(x[m][0], x[m][1]), x[m][2]), fmn(fmn(x[m][3], x[m][4]), fmn(fmn(x[m][5], x[m][6]), x[m][7])));
  }
  xred4<true>(mx[0], mn[0], mx[1], mn[1]);
  f32x2 e[2][8];
  float sp[2], sn[2];
#pragma unroll
  for (int m = 0; m < 2; ++m) {
    const float sl = rs[m] * LOG2E;
    const f32x2 l2 = {sl, -sl};
    const f32x2 off = f32x2{mx[m], mn[m]} * (-sl);
#pragma unroll
    for (int j = 0; j < 8; ++j) {
      f32x2 a = __builtin_elementwise_fma(f32x2{x[m][j], x[m][j]}, l2, off);
      e[m][j] = f32x2{__builtin_amdgcn_exp2f(a[0]), __builtin_amdgcn_exp2f(a[1])};
    }
    f32x2 sm = ((e[m][0] + e[m][1]) + (e[m][2] + e[m][3])) + ((e[m][4] + e[m][5]) + (e[m][6] + e[m][7]));
    sp[m] = sm[0]; sn[m] = sm[1];
  }
  xred4<false, true>(sp[0], sn[0], sp[1], sn[1]);
#pragma unroll
  for (int m = 0; m < 2; ++m) {
    const f32x2 inv = {sp[m], sn[m]};
#pragma unroll
    for (int j = 0; j < 8; ++j) e[m][j] *= inv;
    out[m][0] = u32x4{pk2(e[m][0][0], e[m][1][0]), pk2(e[m][2][0], e[m][3][0]), pk2(e[m][4][0], e[m][5][0]), pk2(e[m][6][0], e[m][7][0])};
    out[m][1] = u32x4{pk2(e[m][0][1], e[m][1][1]), pk2(e[m][2][1], e[m][3][1]), pk2(e[m][4][1], e[m][5][1]), pk2(e[m][6][1], e[m][7][1])};
  }
}

#define SLP_X0 8
#define SLP_X1 14
#define SLP_XD0 3
#define SLP_XD1 9
#define SLP_O 22
#define SLP_OD 12
#define SLP_O0 8
#define SLP_O0D 2
#define SLP_K 4
#define SLP_R 4
#define SLP_HX 1
#define SLP_Q 4
#define SLP_P 40
#define SLP_E 8
#define SPR0 2
#define SW0 0
#define LANEID __builtin_amdgcn_mbcnt_hi(~0u, __builtin_amdgcn_mbcnt_lo(~0u, 0u))
#define STAMP(i) do {} while (0)
#define STAMP_DUMP do {} while (0)
#define COUNT(k) do {} while (0)
#define PSTAMP(l) do { const int layer = l; (void)layer; STAMP(15); } while (0)
constexpr unsigned MBY = 1u << 20;
constexpr unsigned WS_WQ = 0;
constexpr unsigned WS_WO = 12 * MBY;
constexpr unsigned WS_AR = 16 * MBY, ARENA = 12 * MBY;
constexpr unsigned AR_X = 0, AR_ST = 4 * MBY, AR_O = 5 * MBY, AR_KV = 9 * MBY;
constexpr unsigned AR_L2 = AR_ST + 256 * 1024, AR_FL = AR_ST + 320 * 1024;
constexpr unsigned KVREC = 9 * 1024;
constexpr unsigned WS_CS = WS_AR + 4 * ARENA;
constexpr unsigned WS_F1 = WS_CS + 512 * 1024;
constexpr unsigned WS_F2 = WS_F1 + 64 * 1024;
constexpr unsigned WS_EP = WS_F2 + 2048;
constexpr unsigned WS_END = WS_EP + 128;
constexpr unsigned POISON = 0xffffffffu;
constexpr unsigned OOB = 0x80000000u;

typedef __amdgpu_buffer_rsrc_t rsrc_t;
DI rsrc_t mk_rsrc(const void* p, unsigned bytes) { return __builtin_amdgcn_make_buffer_rsrc((void*)p, 0, bytes, 0x00020000); }
DI void st16(rsrc_t r, unsigned off, u32x4 v) { __builtin_amdgcn_raw_buffer_store_b128(v, r, off, 0, 16); }
DI void st16s(rsrc_t r, unsigned voff, unsigned soff, u32x4 v) { __builtin_amdgcn_raw_buffer_store_b128(v, r, voff, soff, 16); }
DI u32x4 ld16(rsrc_t r, unsigned off) { return __builtin_amdgcn_raw_buffer_load_b128(r, off, 0, 16); }
DI u32x4 ld16s(rsrc_t r, unsigned voff, unsigned soff) { return __builtin_amdgcn_raw_buffer_load_b128(r, voff, soff, 16); }
DI u32x4 clean(u32x4 v) { return __builtin_elementwise_min(v, u32x4{POISON - 1, POISON - 1, POISON - 1, POISON - 1}); }
DI unsigned umax(unsigned a, unsigned b) { return a > b ? a : b; }
DI unsigned umin(unsigned a, unsigned b) { return a < b ? a : b; }
DI unsigned umax4(u32x4 v) { return umax(umax(umax(v[0], v[1]), v[2]), v[3]); }
DI unsigned umax4(unsigned m, u32x4 v) { return umax(umax(umax(umax(m, v[0]), v[1]), v[2]), v[3]); }
DI bool poisoned(unsigned m) { return __builtin_amdgcn_ballot_w64(m == POISON) != 0; }
#define MEMFENCE asm volatile("" ::: "memory")

#define BL(i, o) "buffer_load_dwordx4 %1, %2, %" #i " offen" o " nt lds\n\t"
#define BL_OPS(...) : "=&s"(keep) : "v"(voff), "s"(r), "s"(lds), __VA_ARGS__ : "memory"
template <int N>
DI void blds_run(rsrc_t r, unsigned voff, unsigned lds, unsigned s0, unsigned s1 = 0, unsigned s2 = 0, unsigned s3 = 0) {
  unsigned keep;
  if constexpr (N == 2) asm volatile(GLDS_PRE BL(4, "") BL(5, " offset:1024") GLDS_POST BL_OPS("s"(s0), "s"(s1)));
  else asm volatile(GLDS_PRE BL(4, "") BL(5, " offset:1024") BL(6, " offset:2048") BL(7, " offset:3072") GLDS_POST BL_OPS("s"(s0), "s"(s1), "s"(s2), "s"(s3)));
}

#define CODEB 35988
constexpr int MEGA_CODE_BYTES = CODEB;
DI unsigned code_prefetch(int i) {
  unsigned long long pc; int rel; unsigned d;
  asm volatile("s_getpc_b64 %0\n\ts_mov_b32 %1, _Z11mega_kernel10MegaParams@rel32@lo+4" : "=s"(pc), "=s"(rel));
  const unsigned long long a = pc + (long long)rel + (i * 192 + 192 <= MEGA_CODE_BYTES ? i * 192 : 0);
  asm volatile("s_load_dword %0, %1, 0x0\n\ts_load_dword %0, %1, 0x40\n\ts_load_dword %0, %1, 0x80" : "=&s"(d) : "s"(a) : "memory");
  return d;
}

struct MegaParams {
  char* ws;
  const u16* x;
  const u16* wqkvA;
  const u16* woA;
  const u16* wqkvB;
  const u16* woB;
  const u16* wphi;
  u16* out;
};

constexpr int QSLOT = 6 * 1024, QGRP = 4 * QSLOT;
constexpr int L_STG = QGRP;
constexpr int L_A2 = 16 * 1024, L_Z = 21 * 1024;
constexpr int L_RT = 4 * QGRP;
constexpr int L_PB = 0;
constexpr int L_RED = L_STG + 128 * 1024;
constexpr int L_WP = L_RED + 3072;
constexpr int L_STAMP = L_WP + 2048;
constexpr int MEGA_LDS = L_STAMP + 1536;
static_assert(MEGA_LDS <= 160 * 1024, "LDS map");

template <int I, int N, typename F> DI void static_for_(F&& f) {
  if constexpr (I < N) { f(IC<I>{}); static_for_<I + 1, N>(f); }
}
template <int N, typename F> DI void static_for(F&& f) { static_for_<0, N>(f); }

constexpr int NCHK = 3;
constexpr int RO = 8;
constexpr int SPIN_BUDGET = 1 << 15;

__global__ __launch_bounds__(768, 3) void mega_kernel(MegaParams p) {
  extern __shared__ __attribute__((aligned(16))) char smem[];
  const int tid = threadIdx.x;
  const int wave = __builtin_amdgcn_readfirstlane(tid >> 6);
  const int xcd = blockIdx.x & 7, idx = blockIdx.x >> 3;
  int bm = (xcd >> 2) * 4 + (idx >> 3), head = ((xcd & 1) * 2 + ((xcd >> 1) & 1)) * 8 + (idx & 7);
  int cm = (xcd >> 1) & 1, cn = (xcd & 1) * 8 + (idx & 7);
  char* ws = p.ws;
  const u16 *px = p.x, *pq = p.wqkvA;
  asm volatile("" : "+s"(ws), "+s"(px), "+s"(pq));
  const int swv = wave == SW0 ? 0 : wave == 4 ? 1 : wave == 11 ? 2 : -1; (void)swv;
  float* lmean = (float*)(smem + L_RED);
  float* lrstd = lmean + 272;
  float* lcs = lrstd + 272;
  unsigned* lflag = (unsigned*)lmean;
  const rsrc_t wsr = mk_rsrc(ws, WS_END);
  int budget = SPIN_BUDGET;
  unsigned tag0 = 0, tag1 = 0;
  auto ld_ep = [&]() AI { MEMFENCE; return __builtin_amdgcn_raw_buffer_load_b64(wsr, 0, WS_EP, 16); };
  auto mk_tag = [&](u32x2 ep) AI {
    tag0 = __builtin_amdgcn_readfirstlane(ep[0]) + 0x9e3779b9u;
    tag1 = __builtin_amdgcn_readfirstlane(ep[1]) ^ 0x85ebca6bu;
  };
  const unsigned sbase = (unsigned)(size_t)smem;
  int L = wave, cw = wave - 4;
#define OPAQUE_S asm volatile("" : "+s"(L), "+s"(bm), "+s"(head), "+s"(cm), "+s"(cn));
#define LANE_VARS asm volatile("" : "+s"(cw), "+s"(bm), "+s"(head), "+s"(cm), "+s"(cn)); int lane_ = __builtin_amdgcn_mbcnt_hi(~0u, __builtin_amdgcn_mbcnt_lo(~0u, 0u)); asm volatile("" : "+v"(lane_)); \
  const int lane = lane_, fr = lane & 15, fq = lane >> 4; const unsigned v16 = lane * 16; (void)fr; (void)fq; (void)v16;
  auto put_x = [&](u32x4* xr, unsigned v16, unsigned dst) AI {
    float sm = 0.f, sq = 0.f;
#pragma unroll
    for (int j = 0; j < 2; ++j) {
      xr[j][0] = umin(xr[j][0], POISON - 1); xr[j][2] = umin(xr[j][2], POISON - 1);
      st16s(wsr, v16, dst + j * 16384, xr[j]);
    }
    SCHED0;
#pragma unroll
    for (int j = 0; j < 2; ++j)
#pragma unroll
      for (int e = 0; e < 4; ++e) { sm = dot2(xr[j][e], 0x3f803f80u, sm); sq = dot2(xr[j][e], xr[j][e], sq); }
    xred2<false>(sm, sq);
    return u32x2{__float_as_uint(sm), __float_as_uint(sq)};
  };

#define RT_VARS const bool tq = L < 3; const int tsg = tq ? (bm * 2 + (head >> 4)) * 3 + L : bm * 2 + (head >> 4), ks = (head & 15) + 16 * h;
  auto ld_rt = [&](const u16* w, int h, unsigned lds) AI {
    RT_VARS
    LANE_VARS
    const unsigned rs = tq ? 6144 : 2048, D = 8 * rs - 1024, o = ks * 32 * rs + tsg * 128;
    blds_run<4>(mk_rsrc(w, rs * 1024), (lane >> 3) * rs + ((lane & 7) ^ ((lane >> 3) & 6)) * 16, sbase + lds, o, o + D, o + 2 * D, o + 3 * D);
  };
  auto retile = [&](int side, int h, unsigned lds, f32x4* rcs) AI {
    RT_VARS
    LANE_VARS
    const unsigned dst = tq ? WS_WQ + side * (6 * MBY) + (unsigned)((((tsg & 15) * 2) * 32 + ks) * 6 + (tsg >> 4) * 2) * 1024
                            : WS_WO + side * (2 * MBY) + (unsigned)((tsg * 32 + ks) * 4) * 1024;
    const unsigned gstr = tq ? 32 * 6 * 1024 : 2048;
    const unsigned a0 = (lane >> 2) * 128 + ((lane >> 3) & 3) * 32 + (lane & 3) * 8;
#pragma unroll
    for (int pc = 0; pc < 4; ++pc) {
      const char* aa = smem + lds + (a0 ^ (pc * 32));
      u32x2 r0_ = trread(aa), r1_ = trread(aa + 2048);
      u32x4 v = u32x4{r0_[0], r0_[1], r1_[0], r1_[1]};
      st16s(wsr, v16 + (pc & 1) * 1024, dst + (pc >> 1) * gstr, v);
      if (rcs) rcs[pc] = mfma16(as_frag(u32x4{0x3f803f80u, 0x3f803f80u, 0x3f803f80u, 0x3f803f80u}), as_frag(v), rcs[pc]);
    }
    if (rcs && h == 1) {
      const f32x4 c0 = rcs[0], c1 = rcs[1], c2 = rcs[2], c3 = rcs[3];
      const float ca = fq & 1 ? c1[0] : c0[0], cb = fq & 1 ? c3[0] : c2[0], cc = fq & 2 ? cb : ca;
      if (tq) __builtin_amdgcn_raw_buffer_store_b32(__float_as_uint(cc), wsr, ((fq >> 1) * 16 * 96 + (fq & 1) * 16 + fr) * 4,
                                                    WS_CS + (((side * 32 + (tsg & 15) * 2) * 16 + (head & 15)) * 96 + (tsg >> 4) * 32) * 4, 16);
    }
  };
  auto flag = [&](unsigned table) AI {
    if (LANEID == 0) __builtin_amdgcn_raw_buffer_store_b64(u32x2{tag0, tag1}, wsr, 0, table + (bm * 32 + head) * 8, 16);
  };
  auto poll = [&](unsigned table, auto between, int rows = 0) AI {
    LANE_VARS
    const int q = (umin(fq, 2) * 16 + (head >> 1)) * 43 >> 7;
    const unsigned vo = rows ? bm * 256 + (fr & rows) * 16 : fq == 3 ? bm * 256 + fr * 16 : q * 128 + (fr & 7) * 16;
    auto miss = [&](u32x4 f) AI { return __builtin_amdgcn_ballot_w64(((f[0] ^ tag0) | (f[1] ^ tag1) | (f[2] ^ tag0) | (f[3] ^ tag1)) != 0) != 0; };
    MEMFENCE;
    const u32x4 f0 = ld16s(wsr, vo, table);
    MEMFENCE;
    between();
    MEMFENCE;
    bool wait = miss(f0);
#pragma unroll 1
    while (wait && --budget > 0) {
      SLEEP(SLP_Q);
      MEMFENCE;
      wait = miss(ld16s(wsr, vo, table));
      asm volatile("" : "+s"(budget) :: "memory");
    }
    MEMFENCE;
  };
  auto ld_x = [&](int cx, unsigned lds) AI {
    LANE_VARS
    const unsigned o = (bm * 16 + (head >> 1)) * 32768 + cx * 128;
    blds_run<2>(mk_rsrc(px, 4 * MBY), (lane >> 3) * 2048 + ((lane & 7) ^ (lane >> 3)) * 16, sbase + lds, o, o + 15360);
  };
  auto st_x = [&](int cx, unsigned lds, int w) AI {
    LANE_VARS
    const int mt = bm * 16 + (head >> 1);
    u32x4 xr[2];
#pragma unroll
    for (int j = 0; j < 2; ++j) {
      const char* a = smem + lds + fr * 128 + (fq & 1) * 8;
      const u32x2 lo = *(const u32x2*)(a + ((j * 4 + (fq >> 1)) ^ (fr & 7)) * 16), hi = *(const u32x2*)(a + ((j * 4 + 2 + (fq >> 1)) ^ (fr & 7)) * 16);
      xr[j] = u32x4{lo[0], lo[1], hi[0], hi[1]};
    }
    const u32x2 stat = put_x(xr, v16, WS_AR + AR_X + xt_off(mt, cx * 2));
    if (fq == 0) *(u32x2*)(lmean + 16 + fr * 4 + w) = stat;
  };

  {
    const int lane = LANEID;
    const bool tq = wave < 4;
    const unsigned rs = tq ? 6144 : 2048, D = 8 * rs - 1024;
    const unsigned o = (tq ? (head & 15) * 32 : (bm * 16 + (head >> 1)) * 16) * rs + (tq ? (bm * 2 + (head >> 4)) * 3 + wave : (head & 1) * 4 + cw) * 128;
    const unsigned lds = sbase + L_PB + (tq ? 8192 + wave * 4096 : cw * 2048);
    const unsigned vo = (lane >> 3) * rs + ((lane & 7) ^ ((lane >> 3) & (tq ? 6 : 7))) * 16;
    const rsrc_t r = mk_rsrc(tq ? pq : px, tq ? 6 * MBY : 4 * MBY);
    if ((wave & 7) != 3 && wave < 8) blds_run<2>(r, vo, lds, o, o + D);
    if (wave < 3) blds_run<2>(r, vo, lds + 2048, o + 2 * D + 2048, o + 3 * D + 2048);
  }
  if (wave >= 8) {
    const unsigned cp = code_prefetch(idx * 9 + cw), cq = code_prefetch(idx * 9 + cw - 4);
    L = wave - 8;
    bar();
    ld_x(8 + (head & 1) * 4 + L, L_PB + 32768 + L * 2048);
    if (L < 3) ld_rt(pq, 1, L_PB + 20480 + L * 4096);
    sync_lds();
    asm volatile("; pi" :: "s"(cp), "s"(cq) : "memory");
    wait_vm<0>();
    PSTAMP(1);
    st_x(8 + (head & 1) * 4 + L, L_PB + 32768 + L * 2048, L * 64 + 2);
    if (L < 3) {
      const f32x4 t = *(const f32x4*)(smem + L_PB + 40960 + L * 1024 + LANEID * 16);
      f32x4 rca[4] = {{t[0], 0.f, 0.f, 0.f}, {t[1], 0.f, 0.f, 0.f}, {t[2], 0.f, 0.f, 0.f}, {t[3], 0.f, 0.f, 0.f}};
      retile(0, 1, L_PB + 20480 + L * 4096, rca);
    }
    asm volatile("s_waitcnt lgkmcnt(0)" ::: "memory");
    PSTAMP(2);
  } else if (wave >= 4) {
    wait_vm<0>();
    PSTAMP(1);
    st_x((head & 1) * 4 + cw, L_PB + cw * 2048, cw * 64);
    bar();
    wait_vm_lgkm0<0>();
    PSTAMP(2);
    bar();
    asm volatile("; pc" ::: "memory");
    SLEEP(SLP_E);
    L = 3;
    if (cw < 2) ld_rt(p.woA, cw, L_RT + 3 * 14336 + cw * 4096);
    if (cw == 2) ld_rt(p.woB, 0, L_RT + 3 * 14336 + 8192);
  } else if (wave < 3) {
    const u32x2 ep = ld_ep();
    wait_vm<1>();
    PSTAMP(1);
    f32x4 rca[4] = {};
    retile(0, 0, L_PB + 8192 + L * 4096, rca);
    *(f32x4*)(smem + L_PB + 40960 + L * 1024 + LANEID * 16) = f32x4{rca[0][0], rca[1][0], rca[2][0], rca[3][0]};
    bar();
    drain();
    mk_tag(ep);
    PSTAMP(2);
    sync_lds();
    asm volatile("; pl" ::: "memory");
    SLEEP(SLP_E);
    ld_rt(p.wqkvB, 0, L_RT + L * 14336);
    ld_rt(p.wqkvB, 1, L_RT + L * 14336 + 4096);
  } else {
    const u32x2 ep = ld_ep();
    const unsigned cp = code_prefetch(idx * 9 + 8);
    if (LANEID < 9) lflag[LANEID] = 0;
    wait_vm_lgkm0<0>();
    asm volatile("" :: "s"(cp));
    mk_tag(ep);
    bar();
    bar();
    flag(WS_F1);
    if (LANEID == 0) *(u32x2*)(lflag + 10) = u32x2{tag0, tag1};
    PSTAMP(2);
    SLEEP(SLP_P);
    poll(WS_F1, []() {});
    asm volatile("; pp" ::: "memory");
  }
  bar();
  PSTAMP(3);
  int role = wave;
  asm volatile("" : "+s"(role));

  if (__builtin_expect(role < 4, 1)) {
    const unsigned v16 = __builtin_amdgcn_mbcnt_hi(~0u, __builtin_amdgcn_mbcnt_lo(~0u, 0u)) * 16;
#define OPAQUE_LANE int lane = v16 >> 4; asm volatile("" : "+v"(lane));
    auto q_groupW = [&](int layer, int g, int pos) AI {
      const char* gW = ws + WS_WQ + (layer & 1) * (6 * MBY) + head * (192 * 1024) + (g * 4 + L) * 6144;
      const unsigned d = sbase + (pos * 4 + L) * QSLOT;
      glds_run<3, true>(v16, gW, d);
      glds_run<3, true>(v16, gW + 3072, d + 3072);
    };
    u32x4 wn[4][6];
    auto ld_wn = [&](int layer, int g0, int g1) AI {
      const unsigned so = WS_WQ + (layer & 1) * (6 * MBY) + head * (192 * 1024) + L * 6144;
#pragma unroll
      for (int g = g0; g < g1; ++g)
#pragma unroll
        for (int n = 0; n < 6; ++n) wn[g][n] = ld16s(wsr, v16 + (n % 3) * 1024, so + g * 24576 + (n / 3) * 3072);
      MEMFENCE;
    };
    auto wr_wn = [&](int g) AI {
#pragma unroll
      for (int n = 0; n < 6; ++n) *(u32x4*)(smem + (g * 4 + L) * QSLOT + n * 1024 + v16) = wn[g][n];
    };
    const u32x4 pz = u32x4{POISON, POISON, POISON, POISON};
    auto poison = [&](unsigned so) AI { st16s(wsr, v16, so, pz); };
    auto poison_o = [&]() AI {
      const unsigned o = WS_AR + AR_O + xt_off(bm * 16 + L * 4, head);
#pragma unroll
      for (int i = 0; i < 4; ++i) poison(o + i * 1024);
    };
    auto poison_x = [&](int l) AI {
      if (L < NCHK) {
        const unsigned o = WS_AR + l * ARENA + AR_X + bm * (512 * 1024) + head * (NCHK * 2048) + L * 2048;
        poison(o); poison(o + 1024);
      }
    };
    auto poison_s = [&](int l) AI {
      if (L == 3) {
        const unsigned o = WS_AR + l * ARENA + AR_KV + (unsigned)(bm * 32 + head) * KVREC;
        poison(o); poison(o + 1024);
      }
    };
    auto poison_kv = [&](int l, int w0) AI {
      if ((unsigned)(L - w0) < 3) {
        const unsigned kvo = WS_AR + l * ARENA + AR_KV + (unsigned)(bm * 32 + head) * KVREC + (L - w0) * 3072;
        poison(kvo); poison(kvo + 1024); poison(kvo + 2048);
      }
    };
    q_groupW(0, 0, 0);
    q_groupW(0, 1, 1);
    auto put_l2 = [&](float sm, float sq, unsigned vo, unsigned so) AI {
      xred2<false>(sm, sq);
      if (v16 < 256) st16s(wsr, vo, so, u32x4{__float_as_uint(sm), tag0, __float_as_uint(sq), tag1});
      MEMFENCE;
    };
    if (L == 3) {
      const f32x4 t = *(const f32x4*)((const char*)(lmean + 16) + v16);
      put_l2(t[0] + t[2], t[1] + t[3], v16, WS_AR + AR_L2 + (head & 1) * 32768 + (bm * 16 + (head >> 1)) * 256);
    }
    if (L == 3) wait_vm<7>(); else wait_vm<6>();
    auto body = [&](auto kind, const int layer) AI {
      constexpr bool isB = decltype(kind)::value == 1, is0 = decltype(kind)::value == 0;
      const unsigned ar = WS_AR + layer * ARENA;
      f32x4 rcs[4] = {};
      unsigned tch = 0;
      auto sW = [&]() AI { return WS_WO + (layer & 1) * (2 * MBY) + cn * (128 * 1024); };
      auto stg = [&](unsigned off, auto n) AI { glds_run<decltype(n)::value, true>(v16, ws + sW() + off, sbase + L_STG + off); };
      auto stg7 = [&](unsigned o) AI { stg(o, IC<4>{}); stg(o + 4096, IC<3>{}); };
      auto stg6 = [&](unsigned o) AI { stg(o, IC<3>{}); stg(o + 3072, IC<3>{}); };

      auto extra = [&](int b) AI {
        if (!is0) return;
        const unsigned lu = L_RT + L * 14336;
        if (b < 7) { SCHED0; PRIO(3); SCHED0; }
        if (b == 1) retile(L < 3, 0, lu, rcs);
        if (b == 2 && L == 3) retile(0, 1, lu + 4096, nullptr);
        if (b == 6 && L == 3) ld_rt(p.woB, 1, lu + 4096);
        if (b == 8) {
          if (L < 3) retile(1, 1, lu + 4096, rcs);
          if (L == 3) retile(1, 0, lu + 8192, nullptr);
          if (L == 3) retile(1, 1, lu + 4096, nullptr);
        }
        if (b < 7) { SCHED0; PRIO(0); SCHED0; }
      };
      auto extra2 = [&](int b) AI {
        if (!is0) return;
        if (b < 7) { SCHED0; PRIO(3); SCHED0; }
        if (b == 1) poison_o();
        if (b == 2) poison_kv(0, 1);
        if (b == 5) poison_kv(2, 0);
        if (b == 8) poison_s(1);
        if (b == 9) poison_x(1);
        if (b == 10) { poison_x(2); poison_s(3); }
        if (b < 7) { SCHED0; PRIO(0); SCHED0; }
      };
      if (is0) sync_lds();
      STAMP(0);
      {
        OPAQUE_S
        OPAQUE_LANE
        float c[16];
#pragma unroll
        for (int i = 0; i < 16; ++i) asm volatile("" : "=v"(c[i]));
        auto ldcs = [&]() AI {
          if (L >= 2) {
#pragma unroll
            for (int i = 0; i < 16; ++i)
              c[i] = __uint_as_float(__builtin_amdgcn_raw_buffer_load_b32(wsr, (lane < 48 ? lane : 0) * 4 + (i & 7) * 384,
                                         WS_CS + (((layer & 1) * 32 + head) * 1536 + (L & 1) * 48 + (i >> 3) * 768) * 4, 16));
          }
        };
        auto wrcs = [&]() AI {
          if (L >= 2) {
            asm volatile("" : "+v"(c[0]));
            float cs = c[0];
#pragma unroll
            for (int i = 1; i < 16; ++i) cs = add1(cs, c[i]);
            if (lane < 48) lcs[(isB ? 96 : 0) + (L & 1) * 48 + lane] = cs;
          }
        };
        if (isB && layer == 1) ldcs();
        if constexpr (is0) {
          if (L == 3) flag(WS_F1 + 2048);
          q_groupW(0, 2, 2); q_groupW(0, 3, 3);
          wait_vm<12>();
          if (L >= 2) glds_run<1>(v16, (const char*)p.wphi + head * 2048 + (L - 2) * 1024, sbase + L_WP + (L - 2) * 1024);
        }
        STAMP(1);
        sync_lds();
        STAMP(2);
        OPAQUE_S
        u32x4 s0 = u32x4{0u, 0u, 0u, 0u}, s1 = s0;
        auto ldst = [&]() AI {
          const unsigned so = ar + AR_L2 + (bm * 16 + L * 4) * 256;
          s0 = ld16s(wsr, v16, so);
          if (is0) s1 = ld16s(wsr, v16, so + 32768);
          if (isB && L == 0) s1 = ld16s(wsr, v16 & 255, bm > 0 ? so - 256 : so);
          MEMFENCE;
        };
        auto fin = [&](float sm, float sq, int r, bool own) AI {
          float mean = sm * (1.0f / 1024.0f);
          float var = fmaxf(__builtin_fmaf(sq, 1.0f / 1024.0f, -mul1(mean, mean)), 0.f);
          if (own) lmean[r] = -mean;
          lrstd[r] = __builtin_amdgcn_rsqf(var + 1e-5f);
        };
        auto wrst = [&]() AI {
          while (true) {
            unsigned d = (s0[1] ^ tag0) | (s0[3] ^ tag1);
            if (is0 || (isB && L == 0)) d |= (s1[1] ^ tag0) | (s1[3] ^ tag1);
            if (__builtin_expect(__builtin_amdgcn_ballot_w64(d != 0) == 0, 1) || --budget <= 0) break;
            COUNT(1);
            ldst();
          }
          MEMFENCE;
          float sm = __uint_as_float(s0[0]), sq = __uint_as_float(s0[2]);
          if (is0) { sm += __uint_as_float(s1[0]); sq += __uint_as_float(s1[2]); }
          fin(sm, sq, 16 + L * 64 + lane, true);
          if (isB && L == 0) fin(__uint_as_float(s1[0]), __uint_as_float(s1[2]), lane & 15, false);
        };
        auto q_group = [&](int g) AI {
          if (is0) {
            if (L & 1) SLEEP(SPR0);
            if (L & 2) SLEEP(2 * SPR0);
            q_groupW(layer, g, g & 3);
          } else if (g >= 5) q_groupW(layer, g, g < 6 ? g : g - 4);
        };
        if (!is0 && L == 0) {
          poll(ar + AR_FL, []() {}, 15);
          if (head < 16) {
            const unsigned so = ar + AR_ST + (bm * 16 + head) * 2048;
            s0 = ld16s(wsr, v16, so); s1 = ld16s(wsr, v16 + 1024, so);
            MEMFENCE;
          }
        }
        if (is0) {
          drain();
          poll(WS_F1 + 2048, [&]() AI { extra(1); });
          ldcs();
          extra2(1);
        }
        if (isB && layer == 1) wrcs();
        MEMFENCE;
        STAMP(3);
        sync_lds();
        STAMP(4);
        OPAQUE_S
        if (!is0) drain();
        if (!is0 && L == 0 && head < 16) {
          f32x4 t = add4(__builtin_bit_cast(f32x4, s0), __builtin_bit_cast(f32x4, s1));
#pragma unroll
          for (int e = 0; e < 4; ++e)
            t[e] += __uint_as_float(__builtin_amdgcn_update_dpp(0u, __float_as_uint(t[e]), 0x128, 0xf, 0xf, false));
          const bool hi = lane & 8;
          put_l2(hi ? t[2] : t[0], hi ? t[3] : t[1], (lane & 7) * 32 + (lane & 8) * 2, ar + AR_L2 + (bm * 16 + head) * 256);
        }
        q_group(4);
        extra(2); extra2(2);
        STAMP(5);
        sync_lds();
        STAMP(6);
        OPAQUE_S
        drain();
        q_group(5);
        if (is0) wrcs();
        STAMP(7);
        bar();
        OPAQUE_S
        drain();
        if (is0 && L == 0 && v16 == 0) __builtin_amdgcn_raw_buffer_store_b64(u32x2{tag0, tag1}, wsr, 0, WS_F2 + (bm * 32 + head) * 8, 16);
        q_group(6);
        ldst();
        STAMP(8);
        bar();
        STAMP(9);
        OPAQUE_S
        drain();
        q_group(7);
        extra2(5);
        SCHED0;
        wrst();
        STAMP(10);
        bar();
        STAMP(11);
        OPAQUE_S
        drain();
        if (isB && layer == 3 && L == 3) {
          asm volatile("global_load_dword %0, %1, %2" : "=v"(tch) : "v"((unsigned)((bm * 256 + cm * 16 + (lane & 7) * 32) * 2048 + cn * 128)), "s"(p.out) : "memory");
        }
        extra(6);
        if (isB) stg6(96 * 1024 + L * 6144);
        SCHED0;
      }
      STAMP(12);
      sync_lds();
      STAMP(13);
      OPAQUE_S
      u32x4 fl[2];
      fl[0] = fl[1] = u32x4{tag0, tag1, tag0, tag1};
      auto ldfl = [&]() AI { fl[0] = ld16s(wsr, v16, WS_F2); fl[1] = ld16s(wsr, v16 + 1024, WS_F2); };
      if (is0) ldfl();
      bar();
      constexpr int NR = isB ? 6 : 9;
      const unsigned ro = isB ? 48 * 1024 + L * 6144 : L * 9216;
      u32x4 r[NR];
      bool pend = false;
      auto flags = [&]() AI {
        const u32x4 d = (fl[0] ^ u32x4{tag0, tag1, tag0, tag1}) | (fl[1] ^ u32x4{tag0, tag1, tag0, tag1});
        pend = __builtin_amdgcn_ballot_w64((d[0] | d[1] | d[2] | d[3]) != 0) != 0;
      };
      if (isB) {
        stg6(L * 6144); stg(120 * 1024 + L * 2048, IC<2>{});
        stg6(24 * 1024 + L * 6144);
        stg6(72 * 1024 + L * 6144);
      } else {
        drain();
        if (is0) flags();
        MEMFENCE;
        extra(8); extra2(8);
        if (!is0) poison_x(3);
        while (is0 && __builtin_expect(pend, 0) && --budget > 0) {
          ldfl();
          flags();
          MEMFENCE;
        }
        stg(36 * 1024 + L * 3072, IC<3>{}); stg7(72 * 1024 + L * 14336); stg7(72 * 1024 + L * 14336 + 7168);
      }
#pragma unroll
      for (int i = 0; i < NR; ++i) r[i] = ld16s(wsr, v16 + (i & 3) * 1024, sW() + ro + (i >> 2) * 4096);
      MEMFENCE;
      if (is0 && bm + head + L == 0 && v16 == 0) __builtin_amdgcn_raw_buffer_store_b32(tag0, wsr, 0, WS_EP, 16);
      auto wr_r = [&]() AI {
        unsigned vv = v16;
        asm volatile("" : "+v"(vv));
#pragma unroll
        for (int i = 0; i < NR; ++i) *(u32x4*)(smem + L_STG + ro + i * 1024 + vv) = r[i];
      };
      auto left = [&]() AI {
        OPAQUE_LANE
        for (;;) {
          MEMFENCE;
          if (__builtin_amdgcn_ballot_w64(lflag[lane & 7] != (unsigned)layer) == 0 || --budget <= 0) break;
          SLEEP(2);
        }
        MEMFENCE;
      };
      if (isB) {
#pragma unroll
        for (int i = 0; i < 24; ++i) asm volatile("" : "=v"(wn[i / 6][i % 6]));
        if (layer == 1) ld_wn(layer + 1, 0, 2);
        STAMP(14);
        left();
        wr_r();
        MEMFENCE;
        if (layer == 1) ld_wn(layer + 1, 2, 4);
        wait_vm_lgkm0<12>();
        if (layer == 3) wait_vm<0>();
        asm volatile("" :: "v"(tch));
      } else {
        if (is0) wait_vm<26>();
        STAMP(14);
        bar();
        stg6(48 * 1024 + L * 6144);
        ld_wn(layer + 1, 0, 1);
        bar();
        extra2(9);
        ld_wn(layer + 1, 1, 4);
        wr_r();
        if (!is0) drain();
        else wait_vm<18>();
        bar();
        extra2(10);
        wait_vm_lgkm0<0>();
      }
      bar();
      if (isB && layer == 3) { STAMP_DUMP; __builtin_amdgcn_endpgm(); }
      OPAQUE_S
      wr_wn(0);
      drain();
      sync_lds();
      OPAQUE_S
      SCHED0;
      q_groupW(layer + 1, 4, 4);
#pragma unroll
      for (int g = 1; g < 4; ++g) wr_wn(g);
    };
    body(IC<0>{}, 0);
#pragma unroll 1
    for (int layer = 1;; layer += 2) {
      body(IC<1>{}, layer);
      body(IC<2>{}, 2);
    }
  }

  const f32x4 zero = f32x4{0.f, 0.f, 0.f, 0.f};
#pragma unroll 1
  for (int layer = 0; layer < 4; ++layer) {
    const bool isB = layer & 1;
    const unsigned ar = WS_AR + layer * ARENA;
    {
      f32x4 acc[2][6];
      f32x4 ck[2], mv[2], xv[2];
      float mean[2], rs[2], cv[2];
      u32x2 wt[4];
      const float* lcl = lcs + (isB ? 96 : 0);
      {
        LANE_VARS
        const unsigned va = v16 + cw * 2048;
        const unsigned sa0 = ar + AR_X + bm * (512 * 1024);
        auto lda = [&](unsigned so, u32x4* a) AI {
          a[0] = ld16s(wsr, va, so);
          a[1] = ld16s(wsr, va + 1024, so);
        };
        bf16x8 f[2][6];
        u32x4 a[4][2];
        auto unset = [&](const u32x4* e) AI { return poisoned(umax(umax(umax(e[0][0], e[0][2]), e[1][0]), e[1][2])); };
        auto refill = [&](auto ic, unsigned cur) AI {
          constexpr int I = decltype(ic)::value;
          do {
            COUNT(0);
            SLEEP(SLP_R);
            MEMFENCE;
            lda(cur, a[I]);
            bool u[4];
#pragma unroll
            for (int j = 1; j < 4; ++j) u[j] = unset(a[(I + j) % 4]);
            SCHED0;
#pragma unroll
            for (int j = 1; j < 4; ++j)
              if (u[j]) lda(cur + j * 16384, a[(I + j) % 4]);
            __builtin_amdgcn_s_waitcnt(0x0f70);
            MEMFENCE;
          } while (unset(a[I]) && --budget > 0);
        };
        STAMP(0);
        if (layer > 0) {
          bar();
          if (cn < 8) { if (cw < 4) SLEEP(SLP_X0); else SLEEP(SLP_X1); }
          else { if (cw < 4) SLEEP(SLP_XD0); else SLEEP(SLP_XD1); }
        }
#pragma unroll
        for (int j = 0; j < 4; ++j) { lda(sa0 + j * 16384, a[j]); SCHED0; }
#pragma unroll
        for (int m = 0; m < 2; ++m)
#pragma unroll
          for (int n = 0; n < 6; ++n) acc[m][n] = zero;
        asm volatile("" : "+v"(acc[0][0]), "+v"(acc[0][1]), "+v"(acc[0][2]), "+v"(acc[0][3]), "+v"(acc[0][4]), "+v"(acc[0][5]));
        asm volatile("" : "+v"(acc[1][0]), "+v"(acc[1][1]), "+v"(acc[1][2]), "+v"(acc[1][3]), "+v"(acc[1][4]), "+v"(acc[1][5]));
        if (layer == 0) { wait_vm<8>(); sync_lds(); }
#pragma unroll
        for (int nn = 0; nn < 6; ++nn) { const int n = nn < 4 ? nn + 2 : nn - 4; f[0][n] = *(const bf16x8*)(smem + v16 + n * 1024); }
        SCHED0;
        if (layer > 0) {
          const u32x2 tg = *(const u32x2*)(lflag + 10);
          wait_vm<8>();
          unsigned old;
          asm volatile("s_mov_b64 exec, 1\n\tds_add_rtn_u32 %0, %1, %2\n\ts_mov_b64 exec, -1\n\ts_waitcnt lgkmcnt(0)" : "=v"(old) : "v"(sbase + L_RED + 32), "v"(1) : "memory");
          if (__builtin_amdgcn_readfirstlane(old) == 8u * layer - 1)
            __builtin_amdgcn_raw_buffer_store_b64(tg, wsr, lane ? OOB : 0, ar + AR_FL + bm * 256 + (cm * 16 + cn) * 8, 16);
          MEMFENCE;
        }
        SCHED0;
        bool bad = layer > 0 && unset(a[0]);
        const int wrap = layer > 0 ? 6 : 4;
        unsigned ps[4] = {sbase + L_RED + 64 + cw * 128, sbase + L_RED + 2176 + (layer & 1) * 384, 0, sbase + L_WP};
        ps[2] = ps[0] + 1088 + (layer & 1) * (2176 + 384 - 1088 - 64 - cw * 128);
        static_assert(L_WP % 1024 == 0, "");
        int early = (layer & 1) * cw;
        asm volatile("" : "+s"(ps[0]), "+s"(ps[1]), "+s"(ps[2]), "+s"(ps[3]), "+s"(early));
        STAMP(1);
        auto group = [&](auto modec, int g) AI {
          constexpr int MODE = decltype(modec)::value;
          constexpr bool LAST = MODE == 3, CHK = MODE == 0;
          const unsigned gs = (g < wrap ? g : g - 4) * QGRP, gn = (g + 1 < wrap ? g + 1 : g - 3) * QGRP;
          const unsigned sc = sa0 + g * 65536, so = sc + 4 * 16384;
          unsigned gl = sbase + gs + v16;
          if constexpr (LAST) asm volatile("" : "+v"(gl));
          unsigned pa[5];
          static_for<4>([&](auto ic) AI {
            constexpr int i = decltype(ic)::value;
            constexpr bool END = LAST && i == 3;
            SCHED0;
            if (i == 3 && !LAST) { sync_lds(); SCHED0; }
            if (i == 0 && LAST) {
              PRIO(0);
              if (early >= 4) PRIO(3);
              SCHED0;
              bar();
              SCHED0;
            }
            if constexpr (!LAST) PRIO(i == 3 ? 3 : 2 - i);
            if constexpr (CHK) { if (__builtin_expect(bad, 0)) refill(ic, sc + i * 16384); }
            SCHED0;
            const bf16x8* fc = f[i & 1];
            bf16x8* fn = f[(i + 1) & 1];
            const u32x4* an = a[(i + 1) % 4];
            const unsigned slot = i < 3 ? gs + (i + 1) * QSLOT : gn;
            const char* pb = LAST ? (const char*)((const LDS(char)*)gl + (i + 1) * QSLOT) : smem + slot + v16;
            unsigned mx = 0;
            static_for<12>([&](auto jc) AI {
              constexpr int j = decltype(jc)::value, nn = j >> 1, m = j & 1, n = nn < 4 ? nn + 2 : nn - 4;
              if constexpr (n < 4) mfma_acc(acc[m][n], fc[n], as_frag(a[i][m]));
              else mfma_acc(acc[m][n], as_frag(a[i][m]), fc[n]);
              SCHED0;
              if constexpr (!END && j < 6) { constexpr int r = j < 4 ? j + 2 : j - 4; fn[r] = *(const bf16x8*)(pb + r * 1024); }
              if constexpr (LAST && i == 0) {
                if constexpr (j == 7) { pa[4] = v16 >> 1; asm volatile("" : "+v"(pa[4])); }
                if constexpr (j == 9) { pa[4] = (pa[4] & 24) | ps[3]; asm volatile("" : "+v"(pa[4])); }
                if constexpr (j == 11) { pa[4] |= v16 & ~63u; asm volatile("" : "+v"(pa[4])); }
              }
              if constexpr (LAST && i == 1) {
                if constexpr (j == 6) { pa[0] = lane & 48; asm volatile("" : "+v"(pa[0])); }
                if constexpr (j == 7) { pa[1] = v16 >> 2; asm volatile("" : "+v"(pa[1])); }
                if constexpr (j == 8) { pa[1] &= 60; asm volatile("" : "+v"(pa[1])); }
                if constexpr (j == 9) { pa[2] = pa[1] + ps[0]; asm volatile("" : "+v"(pa[2])); }
                if constexpr (j == 10) { pa[1] += ps[1]; asm volatile("" : "+v"(pa[1])); }
                if constexpr (j == 11) { pa[3] = pa[0] + ps[1]; asm volatile("" : "+v"(pa[3])); }
              }
              if constexpr (LAST && i == 2) {
                if constexpr (j == 6) ck[0] = ((const LDS(f32x4)*)pa[3])[8];
                if constexpr (j == 7) ck[1] = ((const LDS(f32x4)*)pa[3])[12];
                if constexpr (j == 8) { mean[0] = ((const LDS(float)*)pa[2])[0]; asm volatile("" : "+v"(pa[2])); }
                if constexpr (j == 9) { mean[1] = ((const LDS(float)*)pa[2])[16]; asm volatile("" : "+v"(pa[2])); }
                if constexpr (j == 10) { cv[0] = ((const LDS(float)*)pa[1])[64]; cv[1] = ((const LDS(float)*)pa[1])[80]; }
                if constexpr (j == 11) { pa[1] = pa[0] + ps[0]; asm volatile("" : "+v"(pa[1])); }
              }
              if constexpr (LAST && i == 3) {
                if constexpr (j == 0) mv[0] = ((const LDS(f32x4)*)pa[1])[0];
                if constexpr (j == 1) mv[1] = ((const LDS(f32x4)*)pa[1])[4];
                if constexpr (j == 2) { pa[0] += ps[2]; asm volatile("" : "+v"(pa[0])); }
                if constexpr (j == 3) { rs[0] = ((const LDS(float)*)pa[2])[272]; asm volatile("" : "+v"(pa[2])); }
                if constexpr (j == 4) xv[0] = ((const LDS(f32x4)*)pa[0])[0];
                if constexpr (j == 5) xv[1] = ((const LDS(f32x4)*)pa[0])[4];
                if constexpr (j == 6) rs[1] = ((const LDS(float)*)pa[2])[288];
                if constexpr (j == 8) wt[0] = trl(pa[4]);
                if constexpr (j == 9) wt[1] = trl(pa[4] + 1024);
                if constexpr (j == 10) wt[2] = trl(pa[4] + 32);
                if constexpr (j == 11) wt[3] = trl(pa[4] + 1056);
              }
              if constexpr (CHK) {
                if constexpr (j == 8) mx = umax(umax(umax(an[0][0], an[0][2]), an[1][0]), an[1][2]);
                if constexpr (j == 10) bad = poisoned(mx);
              }
              if constexpr (j == 10 && !LAST) a[i][0] = ld16s(wsr, va, so + i * 16384);
              if constexpr (j == 11 && !LAST) a[i][1] = ld16s(wsr, va + 1024, so + i * 16384);
              SCHED0;
            });
          });
        };
        int g = 0;
        if (layer > 0) {
#pragma unroll 1
          for (; g < NCHK; ++g) group(IC<0>{}, g);
        }
#pragma unroll 1
        for (; g < 7; ++g) group(IC<1>{}, g);
        group(IC<3>{}, 7);
        if (cw < 4) PRIO(1); else if (!isB) PRIO(2);
        mfma_done();
      }
      STAMP(2);
      LANE_VARS
      char* const scr = smem;

      u32x4 qf[2], kf[2];
      u32x2 vf[2][2];
      auto fold_rows = [&](int part, u32x4* out, const f32x4* c) AI {
#pragma unroll
        for (int m = 0; m < 2; ++m) {
          f32x4 a0 = acc[m][part * 2] + c[0] * mean[m], a1 = acc[m][part * 2 + 1] + c[1] * mean[m];
          out[m] = u32x4{pk2(a0[0], a0[1]), pk2(a0[2], a0[3]), pk2(a1[0], a1[1]), pk2(a1[2], a1[3])};
        }
      };
      auto fold_v = [&](bool scaled) AI {
#pragma unroll
        for (int m = 0; m < 2; ++m) {
          f32x4 a0 = acc[m][4] + mv[m] * cv[0], a1 = acc[m][5] + mv[m] * cv[1];
          if (scaled) { a0 = mul4(a0, xv[m]); a1 = mul4(a1, xv[m]); }
          vf[m][0] = u32x2{pk2(a0[0], a0[1]), pk2(a0[2], a0[3])};
          vf[m][1] = u32x2{pk2(a1[0], a1[1]), pk2(a1[2], a1[3])};
        }
      };
      const unsigned otile = WS_AR + AR_O + xt_off(bm * 16 + cw * 2, head);
      auto put_o = [&](int m, f32x4 a, f32x4 b, unsigned opar) AI {
        const u32x4 v = u32x4{(pk2(a[0], a[1]) & ~1u) | opar, pk2(a[2], a[3]), (pk2(b[0], b[1]) & ~1u) | opar, pk2(b[2], b[3])};
        st16s(wsr, v16 + m * 1024, otile, v);
        return v;
      };
      auto hold = [&](u32x4 a, u32x4 b) AI { SCHED0; asm volatile("s_nop 1" :: "v"(a), "v"(b)); SCHED0; };

      if (!isB) {
        fold_rows(1, kf, ck); fold_v(true);
        char* PK = smem + L_STG;
        char* VF = scr;
        const bf16x8 wf0 = as_frag(u32x4{wt[0][0], wt[0][1], wt[1][0], wt[1][1]}), wf1 = as_frag(u32x4{wt[2][0], wt[2][1], wt[3][0], wt[3][1]});
#pragma unroll
        for (int t = 0; t < 2; ++t)
          *(u32x4*)(VF + (cw * 2 + t) * 1024 + lane * 16) = u32x4{vf[0][t][0], vf[0][t][1], vf[1][t][0], vf[1][t][1]};
        {
          u32x4 pk[2][2];
          phi_map2(kf, rs, wf0, wf1, pk);
          char* d0 = PK + cw * 4096 + fr * 64 + ((fr >> 2) & 1) * 32 + ((fq ^ ((fr >> 3) * 2)) * 8);
          char* d1 = PK + cw * 4096 + fr * 64 + (((fr >> 2) & 1) ^ 1) * 32 + ((fq ^ ((fr >> 3) * 2)) * 8);
#pragma unroll
          for (int m = 0; m < 2; ++m) {
            *(u32x2*)(d0 + m * 2048) = u32x2{pk[m][0][0], pk[m][0][1]};
            *(u32x2*)(d0 + m * 2048 + 1024) = u32x2{pk[m][0][2], pk[m][0][3]};
            *(u32x2*)(d1 + m * 2048) = u32x2{pk[m][1][0], pk[m][1][1]};
            *(u32x2*)(d1 + m * 2048 + 1024) = u32x2{pk[m][1][2], pk[m][1][3]};
          }
        }
        PRIO(1);
        sync_lds();
        STAMP(3);
        const unsigned rec = ar + AR_KV + (unsigned)head * KVREC;
        {
          const u32x4 ones = u32x4{0x3f803f80u, 0x3f803f80u, 0x3f803f80u, 0x3f803f80u};
          f32x4 c0 = zero, c1 = zero;
          const char* ad = PK + (cw & 1) * 1024 + (fq * 4 + (fr >> 2)) * 64 + (((cw >> 1) ^ fq) & 1) * 32 + (((fr & 3) ^ (fq & 2)) * 8);
          const char* vd = VF + (cw >> 2) * 1024 + lane * 16;
          bf16x8 a[8], b0[8];
#pragma unroll
          for (int ks = 0; ks < 8; ++ks) {
            u32x2 a0 = trread(ad + ks * 4096), a1 = trread(ad + ks * 4096 + 2048);
            a[ks] = as_frag(u32x4{a0[0], a0[1], a1[0], a1[1]});
            b0[ks] = *(const bf16x8*)(vd + ks * 2048);
          }
          SCHED0;
#pragma unroll
          for (int ks = 0; ks < 8; ks += 2) {
            c0 = mfma16(a[ks], b0[ks], c0);
            c1 = mfma16(a[ks + 1], b0[ks + 1], c1);
          }
          c0 += c1;
          const unsigned dst = rec + (unsigned)bm * (32 * KVREC);
          st16(wsr, dst + cw * 1024 + v16, clean(__builtin_bit_cast(u32x4, c0)));
          SCHED0;
          if (cw < 4) {
            f32x4 cz = zero, cy = zero;
#pragma unroll
            for (int ks = 0; ks < 8; ks += 2) {
              cz = mfma16(a[ks], as_frag(ones), cz);
              cy = mfma16(a[ks + 1], as_frag(ones), cy);
            }
            cz += cy;
            st16(wsr, fr ? OOB : dst + 8192 + cw * 64 + fq * 16, clean(__builtin_bit_cast(u32x4, cz)));
          }
        }
        SCHED0;
        STAMP(4);
        bar();
        const f32x4 cq[2] = {*(const f32x4*)(lcl + fq * 4), *(const f32x4*)(lcl + 16 + fq * 4)};
        fold_rows(0, qf, cq);
        u32x4 pq[2][2];
        u32x4 zp, zq;
        u32x4 part[8];
        auto ldz = [&]() AI {
          zp = ld16(wsr, rec + fq * (32 * KVREC) + 8192 + fr * 16);
          zq = ld16(wsr, rec + (4 + fq) * (32 * KVREC) + 8192 + fr * 16);
        };
        phi_map2(qf, rs, wf0, wf1, pq);
        asm volatile("" : "+v"(pq[0][0]), "+v"(pq[0][1]), "+v"(pq[1][0]), "+v"(pq[1][1]));
        SCHED0;
        STAMP(5);
        char* kvf = smem + L_A2;
        SLEEP(SLP_K);
        MEMFENCE;
        if (cw == 0) ldz();
#pragma unroll
        for (int rb = 0; rb < 8; ++rb) part[rb] = ld16s(wsr, v16, rec + rb * (32 * KVREC) + cw * 1024);
        MEMFENCE;
        unsigned kw = sbase + L_A2 + v16 + (cw >> 1) * 1024 + (cw & 1) * 8;
        asm volatile("" : "+v"(kw));
        f32x4 s4;
        auto nans = [&](f32x4 v) AI { return __builtin_amdgcn_ballot_w64(__builtin_isunordered(v[0], v[1])) | __builtin_amdgcn_ballot_w64(__builtin_isunordered(v[2], v[3])); };
        auto sums = [&]() AI {
          unsigned long long nan = 0;
          if (cw == 0) {
            f32x4 z = __builtin_bit_cast(f32x4, zp) + __builtin_bit_cast(f32x4, zq);
            nan = nans(z);
            float z0 = z[0], z1 = z[1], z2 = z[2], z3 = z[3];
            xred4<false>(z0, z1, z2, z3);
            *(u32x2*)(smem + L_Z + (fr >> 3) * 64 + (fr & 3) * 16 + ((fr >> 2) & 1) * 8) = u32x2{pk2(z0, z1), pk2(z2, z3)};
          }
          s4 = __builtin_bit_cast(f32x4, part[0]);
#pragma unroll
          for (int rb = 1; rb < 8; ++rb) { s4 += __builtin_bit_cast(f32x4, part[rb]); SCHED0; }
          return nan | nans(s4);
        };
        if (__builtin_expect(sums() != 0, 0)) {
          for (;;) {
            bool again = false;
            if (cw == 0 && poisoned(umax4(umax4(zp), zq))) { again = true; ldz(); }
#pragma unroll
            for (int rb = 0; rb < 8; ++rb)
              if (poisoned(umax4(part[rb]))) { again = true; part[rb] = ld16s(wsr, v16, rec + rb * (32 * KVREC) + cw * 1024); }
            __builtin_amdgcn_s_waitcnt(0x0f70);
            MEMFENCE;
            if (!again || --budget <= 0) break;
            COUNT(2);
          }
          sums();
        }
        STAMP(6);
        *(LDS(u32x2)*)kw = u32x2{pk2(s4[0], s4[1]), pk2(s4[2], s4[3])};
        sync_lds();
        STAMP(7);
        bf16x8 kvr[2][2], zf[2];
#pragma unroll
        for (int s = 0; s < 2; ++s) {
          kvr[0][s] = *(const bf16x8*)(kvf + (0 * 2 + s) * 1024 + lane * 16);
          kvr[1][s] = *(const bf16x8*)(kvf + (1 * 2 + s) * 1024 + lane * 16);
          zf[s] = *(const bf16x8*)(smem + L_Z + s * 64 + fq * 16);
        }
        u32x4 oo[2];
#pragma unroll
        for (int m = 0; m < 2; ++m) {
          f32x4 c0 = mfma16(kvr[0][1], as_frag(pq[m][1]), mfma16(kvr[0][0], as_frag(pq[m][0]), zero));
          f32x4 c1 = mfma16(kvr[1][1], as_frag(pq[m][1]), mfma16(kvr[1][0], as_frag(pq[m][0]), zero));
          f32x4 cd = mfma16(zf[1], as_frag(pq[m][1]), mfma16(zf[0], as_frag(pq[m][0]), zero));
          const float iden = __builtin_amdgcn_rcpf(cd[0] + 1e-6f);
          c0 *= iden; c1 *= iden;
          oo[m] = put_o(m, c0, c1, 0);
        }
        hold(oo[0], oo[1]);
        STAMP(8);
        asm volatile("" : "=v"(acc[0][0]), "=v"(acc[0][1]), "=v"(acc[0][2]), "=v"(acc[0][3]), "=v"(acc[0][4]), "=v"(acc[0][5]));
        asm volatile("" : "=v"(acc[1][0]), "=v"(acc[1][1]), "=v"(acc[1][2]), "=v"(acc[1][3]), "=v"(acc[1][4]), "=v"(acc[1][5]));
        asm volatile("" : "=v"(ck[0]), "=v"(ck[1]), "=v"(mv[0]), "=v"(mv[1]), "=v"(xv[0]), "=v"(xv[1]));
        asm volatile("" : "=v"(mean[0]), "=v"(mean[1]), "=v"(rs[0]), "=v"(rs[1]), "=v"(cv[0]), "=v"(cv[1]));
      }
      int kind = layer & 1;
      asm volatile("" : "+s"(kind));
      if (kind) {
        char* ex = scr;
        fold_rows(1, kf, ck); fold_v(false);
        const unsigned hrec = ar + AR_KV + (unsigned)(bm * 32 + head) * KVREC;
        {
          const u32x4 vl = u32x4{vf[1][0][0], vf[1][0][1], vf[1][1][0], vf[1][1][1]};
          if (cw == 7) {
            st16s(wsr, v16, hrec, clean(kf[1]));
            STPAD;
            st16s(wsr, v16 + 1024, hrec, clean(vl));
            STPAD;
          }
          char* e2 = ex + (cw + 1) * 2048;
          *(u32x4*)(e2 + lane * 16) = kf[1];
          *(u32x2*)(e2 + 1024 + lane * 8) = vf[1][0];
          *(u32x2*)(e2 + 1536 + lane * 8) = vf[1][1];
          MEMFENCE;
          lflag[cw] = layer;
          MEMFENCE;
          PRIO(1);
        }
        SCHED0;
        fold_rows(0, qf, xv);
        u32x4 kp;
        u32x4 va0 = __builtin_shufflevector(vf[0][0], vf[0][0], -1, -1, 0, 1), vb0 = __builtin_shufflevector(vf[0][1], vf[0][1], -1, -1, 0, 1);
        auto setp = [&](u32x4& d, u32x2 v) AI { d[0] = v[0]; d[1] = v[1]; };
        auto ldh = [&]() AI {
          MEMFENCE;
          kp = ld16s(wsr, v16, hrec - 32 * KVREC);
          setp(va0, __builtin_amdgcn_raw_buffer_load_b64(wsr, v16 + 1024, hrec - 32 * KVREC, 16));
          MEMFENCE;
          setp(vb0, __builtin_amdgcn_raw_buffer_load_b64(wsr, v16 + 1032, hrec - 32 * KVREC, 16));
          MEMFENCE;
        };
        unsigned fl = 0;
        auto rdl = [&]() AI {
          MEMFENCE;
          fl = lflag[cw - 1];
          MEMFENCE;
          kp = *(const u32x4*)(ex + cw * 2048 + lane * 16);
          setp(va0, *(const u32x2*)(ex + cw * 2048 + 1024 + lane * 8));
          MEMFENCE;
          setp(vb0, *(const u32x2*)(ex + cw * 2048 + 1536 + lane * 8));
          MEMFENCE;
        };
        auto prev = [&]() AI {
          if (cw > 0) {
            do { SLEEP(1); rdl(); __builtin_amdgcn_s_waitcnt(0xc07f); } while (__builtin_amdgcn_readfirstlane(fl) != (unsigned)layer && --budget > 0);
          } else if (bm > 0) {
            SLEEP(SLP_HX);
            do { ldh(); __builtin_amdgcn_s_waitcnt(0x0f70); }
            while (poisoned(umax4(umax4(kp), u32x4{va0[0], va0[1], vb0[0], vb0[1]})) && --budget > 0);
          } else {
            kp = u32x4{0u, 0u, 0u, 0u};
            setp(va0, u32x2{0u, 0u}); setp(vb0, u32x2{0u, 0u});
          }
          asm volatile("" : "+v"(kp), "+v"(va0), "+v"(vb0));
        };
        const float scale = 0.17677669529663687f * LOG2E;
        const bool odd = fr & 1;
        const bool own0 = fr >= fq * 4 + (fr & 1), own1 = fr >= fq * 4 + (fr & 1) + 2;
        const bool nopre = (bm == 0) && (cw == 0);
        float l[2][2], mx[2], rk[2][2], sum[2];
#pragma unroll
        for (int m = 0; m < 2; ++m) {
          const float* rb = lrstd + (cw * 2 + m) * 16 + fq * 4 + (fr & 1);
          rk[m][0] = rb[own0 ? 16 : 0];
          rk[m][1] = rb[own1 ? 18 : 2];
        }
        float qk[2][2];
#pragma unroll
        for (int m = 0; m < 2; ++m) { const float qs = rs[m] * scale; qk[m][0] = qs * rk[m][0]; qk[m][1] = qs * rk[m][1]; }
        const unsigned e0 = odd ? 0x01000c0cu : 0x0c0c0100u, e1 = odd ? 0x03020c0cu : 0x0c0c0302u, ez = 0x0c0c0c0cu;
        u32x4 sel = u32x4{own0 ? ez : e0, own1 ? ez : e1, own0 ? e0 : ez, own1 ? e1 : ez};
        u32x4 va1 = u32x4{vf[0][0][0], vf[0][0][1], vf[1][0][0], vf[1][0][1]}, vb1 = u32x4{vf[0][1][0], vf[0][1][1], vf[1][1][0], vf[1][1][1]};
        float off0 = nopre && !own0 ? -3.0e38f : 0.f, off1 = nopre && !own1 ? -3.0e38f : 0.f;
        auto score = [&](int m) AI {
          f32x4 s0 = mfma16(as_frag(m == 0 ? kp : kf[0]), as_frag(qf[m]), zero);
          f32x4 s1 = mfma16(as_frag(kf[m]), as_frag(qf[m]), zero);
          float a = own0 ? (odd ? s1[1] : s1[0]) : (odd ? s0[1] : s0[0]);
          float b = own1 ? (odd ? s1[3] : s1[2]) : (odd ? s0[3] : s0[2]);
          if (m == 0) { l[m][0] = __builtin_fmaf(a, qk[m][0], off0); l[m][1] = __builtin_fmaf(b, qk[m][1], off1); }
          else { l[m][0] = a * qk[m][0]; l[m][1] = b * qk[m][1]; }
          mx[m] = fmx(l[m][0], l[m][1]);
        };
        auto expo = [&](int m) AI {
          l[m][0] = __builtin_amdgcn_exp2f(l[m][0] - mx[m]);
          l[m][1] = __builtin_amdgcn_exp2f(l[m][1] - mx[m]);
          sum[m] = l[m][0] + l[m][1];
        };
        f32x4 ov[2][2];
        auto pv = [&](int m) AI {
          const unsigned pr = pk2(mul1(mul1(l[m][0], rk[m][0]), sum[m]), mul1(mul1(l[m][1], rk[m][1]), sum[m]));
          const u32x4 pf = u32x4{__builtin_amdgcn_perm(pr, pr, sel[0]), __builtin_amdgcn_perm(pr, pr, sel[1]),
                                 __builtin_amdgcn_perm(pr, pr, sel[2]), __builtin_amdgcn_perm(pr, pr, sel[3])};
          ov[m][0] = mfma16(as_frag(m ? va1 : va0), as_frag(pf), zero);
          ov[m][1] = mfma16(as_frag(m ? vb1 : vb0), as_frag(pf), zero);
        };
        asm volatile("" : "+v"(qf[0]), "+v"(qf[1]), "+v"(va0), "+v"(vb0));
        SCHED0;
        if (cw > 0) rdl();
        SCHED0;
        score(1);
        asm volatile("" : "+v"(qf[0]), "+v"(qk[0][0]), "+v"(qk[0][1]), "+v"(mx[1]), "+v"(l[1][0]), "+v"(l[1][1]));
        asm volatile("" : "+v"(sel), "+v"(va1), "+v"(vb1), "+v"(off0), "+v"(off1));
        SCHED0;
        STAMP(3);
        if (__builtin_expect(cw == 0 || __builtin_amdgcn_readfirstlane(fl) != (unsigned)layer, 0)) prev();
        score(0);
        xred2<true>(mx[0], mx[1]);
        expo(0); expo(1);
        xred2<false, true>(sum[0], sum[1]);
        pv(0); pv(1);
        SCHED0;
        const u32x4 o0 = put_o(0, ov[0][0], ov[0][1], 1), o1 = put_o(1, ov[1][0], ov[1][1], 1);
        hold(o0, o1);
        STAMP(4);
      }
    }
    {
      LANE_VARS
      const int mt = bm * 16 + cw * 2 + cm;
      u32x4 xr[2];
#pragma unroll
      for (int j = 0; j < 2; ++j) xr[j] = ld16s(wsr, v16, ar + AR_X + xt_off(mt, cn * 2 + j));
      const unsigned so0 = WS_AR + AR_O + xt_off(mt, 0);
      const unsigned opar = layer & 1;
      auto diff = [&](u32x4 v) AI { return ((v[0] ^ opar) | (v[2] ^ opar)) & 1u; };
      auto dif3 = [&](u32x4 v) AI { return __builtin_amdgcn_bitop3_b32(v[0], v[2], opar, 0x7e); };
      auto any = [&](unsigned d) AI { return __builtin_amdgcn_ballot_w64(d != 0) != 0; };
      const int g0 = cm;
      u32x4 a[RO];
      auto refill = [&](auto ic, auto wc, unsigned cur, unsigned nxt) AI {
        constexpr int I = decltype(ic)::value, W = decltype(wc)::value;
        do {
          COUNT(1);
          SLEEP(SLP_R);
          MEMFENCE;
          a[I] = ld16s(wsr, v16, cur + I * 16384);
          bool u[RO];
#pragma unroll
          for (int j = I + 1; j < I + W; ++j) u[j % RO] = any(diff(a[j % RO]));
          SCHED0;
#pragma unroll
          for (int j = I + 1; j < I + W; ++j)
            if (u[j % RO]) a[j % RO] = ld16s(wsr, v16, (j < RO ? cur : nxt) + (j % RO) * 16384);
          __builtin_amdgcn_s_waitcnt(0x0f70);
          MEMFENCE;
        } while (any(diff(a[I]) | diff(a[I + 1])) && --budget > 0);
      };
      const unsigned sf = so0 + g0 * (RO * 16384);
      const bool late = isB && cw == 0;
      if (late) bar();
      if (cn < 8) { if (late) SLEEP(SLP_O0); else SLEEP(SLP_O); }
      else { if (late) SLEEP(SLP_O0D); else SLEEP(SLP_OD); }
#pragma unroll
      for (int j = 0; j < 4; ++j) { a[j] = ld16s(wsr, v16, sf + j * 16384); SCHED0; }
      if (!late) bar();
      STAMP(9);
      bf16x8 wf[2][4];
      const char* pw0 = smem + L_STG + v16;
#pragma unroll
      for (int n = 0; n < 8; ++n) wf[n >> 2][n & 3] = *(const bf16x8*)(pw0 + g0 * (RO * 4096) + n * 1024);
      f32x4 acc[4];
#pragma unroll
      for (int n = 0; n < 4; ++n)
        acc[n] = f32x4{bflo(xr[n >> 1][(n & 1) * 2]), bfhi(xr[n >> 1][(n & 1) * 2]), bflo(xr[n >> 1][(n & 1) * 2 + 1]), bfhi(xr[n >> 1][(n & 1) * 2 + 1])};
      asm volatile("" : "+v"(acc[0]), "+v"(acc[1]), "+v"(acc[2]), "+v"(acc[3]));
      SCHED0;
      STAMP(10);
      if (any(diff(a[0]) | diff(a[1]))) refill(IC<0>{}, IC<4>{}, sf, sf);
      SCHED0;
#pragma unroll
      for (int j = 4; j < RO; ++j) { a[j] = ld16s(wsr, v16, sf + j * 16384); SCHED0; }
      STAMP(11);
      bool bad = false;
      auto group = [&](auto lastc, int g) AI {
        constexpr bool LAST = decltype(lastc)::value;
        const int gc = g ^ g0, gn = (g + 1) ^ g0;
        const unsigned sc = so0 + gc * (RO * 16384), so = so0 + gn * (RO * 16384);
        const char* pc = pw0 + gc * (RO * 4096);
        const char* pn = pw0 + gn * (RO * 4096);
        static_for<RO>([&](auto ic) AI {
          constexpr int i = decltype(ic)::value;
          SCHED0;
          if constexpr (i % 2 == 0)
            if (__builtin_expect(bad, 0)) refill(ic, IC<(LAST && i + 4 > RO) ? RO - i : 4>{}, sc, so);
          SCHED0;
          unsigned mx = 0, my = 0;
          static_for<4>([&](auto nc) AI {
            constexpr int n = decltype(nc)::value;
            mfma_acc(acc[n], wf[i & 1][n], as_frag(a[i]));
            SCHED0;
            if constexpr (i + 2 < RO) wf[i & 1][n] = *(const bf16x8*)(pc + (i + 2) * 4096 + n * 1024);
            else if constexpr (!LAST) wf[i & 1][n] = *(const bf16x8*)(pn + (i + 2 - RO) * 4096 + n * 1024);
            if constexpr (!(LAST && i == RO - 1) && (i & 1)) {
              if constexpr (n == 0) mx = dif3(a[(i + 1) % RO]);
              if constexpr (n == 1) my = dif3(a[(i + 2) % RO]);
              if constexpr (n == 2) bad = any((mx | my) & 1u);
            }
            if constexpr (n == 3 && !LAST) a[i] = ld16s(wsr, v16, so + i * 16384);
            SCHED0;
          });
        });
      };
#pragma unroll 1
      for (int g = 0; g < 3; ++g) group(std::false_type{}, g);
      group(std::true_type{}, 3);
      mfma_done();
      STAMP(12);
#pragma unroll
      for (int j = 0; j < 2; ++j)
#pragma unroll
        for (int e = 0; e < 4; ++e) xr[j][e] = pk2(acc[j * 2 + (e >> 1)][(e & 1) * 2], acc[j * 2 + (e >> 1)][(e & 1) * 2 + 1]);
      if (layer == 3) {
        u32x4 yo[2];
#pragma unroll
        for (int j = 0; j < 2; ++j) {
          auto u = __builtin_amdgcn_permlane16_swap(xr[j][0], xr[j][2], false, false);
          auto v = __builtin_amdgcn_permlane16_swap(xr[j][1], xr[j][3], false, false);
          yo[j] = u32x4{u[0], v[0], u[1], v[1]};
        }
        const unsigned vo = fr * 2048 + (fq & 1) * 32 + (fq >> 1) * 16;
        __builtin_amdgcn_raw_buffer_store_b128(yo[0], mk_rsrc(p.out, 4 * MBY), vo, mt * 32768 + cn * 128, 2);
        __builtin_amdgcn_raw_buffer_store_b128(yo[1], mk_rsrc(p.out, 4 * MBY), vo + 64, mt * 32768 + cn * 128, 2);
        STAMP(13);
        STAMP(14);
        STAMP_DUMP;
        __builtin_amdgcn_endpgm();
      }
      const u32x2 stat = put_x(xr, v16, ar + ARENA + AR_X + xt_off(mt, cn * 2));
      __builtin_amdgcn_raw_buffer_store_b64(stat, wsr, fq ? OOB : fr * 8, ar + ARENA + AR_ST + (mt * 16 + cn) * 128, 16);
      MEMFENCE;
      STAMP(14);
    }
  }
}

extern "C" void kernel_launch(void* const* d_in, const int* in_sizes, int n_in, void* d_out, int out_size,
                              void* d_ws, size_t ws_size, hipStream_t stream) {
  (void)hipFuncSetAttribute((const void*)mega_kernel, hipFuncAttributeMaxDynamicSharedMemorySize, MEGA_LDS);
  MegaParams m;
  m.ws = (char*)d_ws; m.out = (u16*)d_out; m.x = (const u16*)d_in[0]; m.wphi = (const u16*)d_in[2];
  m.wqkvA = (const u16*)d_in[1]; m.wqkvB = (const u16*)d_in[5];
  m.woA = (const u16*)d_in[3]; m.woB = (const u16*)d_in[6];
  mega_kernel<<<256, 768, MEGA_LDS, stream>>>(m);
}
```

```cpp
#include <hip/hip_runtime.h>
#include <stdint.h>
#include <type_traits>

#define DI __device__ __forceinline__
#define AI __attribute__((always_inline))
typedef unsigned short u16;
typedef __attribute__((ext_vector_type(8))) short bf16x8;
typedef __attribute__((ext_vector_type(4))) short s16x4;
typedef __attribute__((ext_vector_type(4))) float f32x4;
typedef __attribute__((ext_vector_type(2))) float f32x2;
typedef __attribute__((ext_vector_type(2))) __bf16 bf2;
typedef __attribute__((ext_vector_type(4))) unsigned u32x4;
typedef __attribute__((ext_vector_type(2))) unsigned u32x2;
typedef __attribute__((address_space(3))) s16x4* lds_s16x4_ptr;
#define LDS(T) __attribute__((address_space(3))) T
template <int N> using IC = std::integral_constant<int, N>;

constexpr float LOG2E = 1.4426950408889634f;

DI unsigned pk2(float a, float b) {
  f32x2 v = {a, b};
  bf2 r = __builtin_convertvector(v, bf2);
  return __builtin_bit_cast(unsigned, r);
}
DI float bflo(unsigned u) { return __uint_as_float(u << 16); }
DI float bfhi(unsigned u) { return __uint_as_float(u & 0xffff0000u); }
DI f32x4 mfma16(bf16x8 a, bf16x8 b, f32x4 c) { return __builtin_amdgcn_mfma_f32_16x16x32_bf16(a, b, c, 0, 0, 0); }
DI void mfma_acc(f32x4& c, bf16x8 a, bf16x8 b) { asm volatile("v_mfma_f32_16x16x32_bf16 %0, %1, %2, %0" : "+v"(c) : "v"(a), "v"(b)); }
DI void mfma_done() { asm volatile("s_nop 7\n\ts_nop 1" ::: "memory"); __builtin_amdgcn_sched_barrier(0); }
DI bf16x8 as_frag(u32x4 v) { return __builtin_bit_cast(bf16x8, v); }
DI float dot2(unsigned a, unsigned b, float c) {
  return __builtin_amdgcn_fdot2_f32_bf16(__builtin_bit_cast(bf2, a), __builtin_bit_cast(bf2, b), c, false);
}
DI u32x2 trread(const char* p) { return __builtin_bit_cast(u32x2, __builtin_amdgcn_ds_read_tr16_b64_v4i16((lds_s16x4_ptr)p)); }
DI u32x2 trl(unsigned a) { return __builtin_bit_cast(u32x2, __builtin_amdgcn_ds_read_tr16_b64_v4i16((lds_s16x4_ptr)a)); }
DI float fmx(float a, float b) { return __builtin_elementwise_maximum(a, b); }
DI float fmn(float a, float b) { return __builtin_elementwise_minimum(a, b); }

DI float mul1(float a, float b) { float r; asm("v_mul_f32 %0, %1, %2" : "=v"(r) : "v"(a), "v"(b)); return r; }
DI float add1(float a, float b) { float r; asm("v_add_f32 %0, %1, %2" : "=v"(r) : "v"(a), "v"(b)); return r; }
DI f32x4 mul4(f32x4 a, f32x4 b) {
  float r0, r1, r2, r3;
  asm("v_mul_f32 %0, %4, %8\n\tv_mul_f32 %1, %5, %9\n\tv_mul_f32 %2, %6, %10\n\tv_mul_f32 %3, %7, %11"
      : "=&v"(r0), "=&v"(r1), "=&v"(r2), "=&v"(r3) : "v"(a[0]), "v"(a[1]), "v"(a[2]), "v"(a[3]), "v"(b[0]), "v"(b[1]), "v"(b[2]), "v"(b[3]));
  return f32x4{r0, r1, r2, r3};
}
DI f32x4 add4(f32x4 a, f32x4 b) { return f32x4{add1(a[0], b[0]), add1(a[1], b[1]), add1(a[2], b[2]), add1(a[3], b[3])}; }

template <bool IS_MAX, bool INV = false>
DI void xred2(float& a, float& b) {
  auto r = __builtin_amdgcn_permlane32_swap(__float_as_uint(a), __float_as_uint(b), false, false);
  float c = IS_MAX ? fmx(__uint_as_float(r[0]), __uint_as_float(r[1])) : __uint_as_float(r[0]) + __uint_as_float(r[1]);
  auto q = __builtin_amdgcn_permlane16_swap(__float_as_uint(c), __float_as_uint(c), false, false);
  c = IS_MAX ? fmx(__uint_as_float(q[0]), __uint_as_float(q[1])) : __uint_as_float(q[0]) + __uint_as_float(q[1]);
  if (INV) c = __builtin_amdgcn_rcpf(c);
  auto w = __builtin_amdgcn_permlane32_swap(__float_as_uint(c), __float_as_uint(c), false, false);
  a = __uint_as_float(w[0]); b = __uint_as_float(w[1]);
}
template <bool IS_MAX, bool INV = false>
DI void xred4(float& a, float& b, float& c, float& d) {
  auto op = [](unsigned x, unsigned y) { return IS_MAX ? fmx(__uint_as_float(x), __uint_as_float(y)) : __uint_as_float(x) + __uint_as_float(y); };
  auto r0 = __builtin_amdgcn_permlane32_swap(__float_as_uint(a), __float_as_uint(b), false, false);
  auto r1 = __builtin_amdgcn_permlane32_swap(__float_as_uint(c), __float_as_uint(d), false, false);
  float ab = op(r0[0], r0[1]), cd = op(r1[0], r1[1]);
  auto q = __builtin_amdgcn_permlane16_swap(__float_as_uint(ab), __float_as_uint(cd), false, false);
  float e = op(q[0], q[1]);
  if (INV) e = __builtin_amdgcn_rcpf(e);
  auto w = __builtin_amdgcn_permlane16_swap(__float_as_uint(e), __float_as_uint(e), false, false);
  auto u = __builtin_amdgcn_permlane32_swap(w[0], w[0], false, false);
  auto v = __builtin_amdgcn_permlane32_swap(w[1], w[1], false, false);
  a = __uint_as_float(u[0]); b = __uint_as_float(u[1]); c = __uint_as_float(v[0]); d = __uint_as_float(v[1]);
}

DI unsigned xt_off(int mt, int ks) { return (unsigned)((((mt >> 4) * 32 + ks) * 16 + (mt & 15)) << 10); }

#define GLDS_PRE "s_mov_b32 %0, m0\n\ts_mov_b32 m0, %3\n\ts_nop 2\n\t"
#define GLDS_POST "s_mov_b32 m0, %0"
#define GLDS_OPS : "=&s"(keep) : "v"(voff), "s"(sbase), "s"(lds) : "memory"
#define GL1(c) "global_load_lds_dwordx4 %1, %2" c "\n\t"
#define GL2(c) GL1(c) "global_load_lds_dwordx4 %1, %2 offset:1024" c "\n\t"
#define GL3(c) GL2(c) "global_load_lds_dwordx4 %1, %2 offset:2048" c "\n\t"
#define GL4(c) GL3(c) "global_load_lds_dwordx4 %1, %2 offset:3072" c "\n\t"
template <int N, bool SC1 = false>
DI void glds_run(unsigned voff, const void* sbase, unsigned lds) {
  unsigned keep;
  if constexpr (SC1) {
    if constexpr (N == 2) asm volatile(GLDS_PRE GL2(" sc1") GLDS_POST GLDS_OPS);
    else if constexpr (N == 3) asm volatile(GLDS_PRE GL3(" sc1") GLDS_POST GLDS_OPS);
    else asm volatile(GLDS_PRE GL4(" sc1") GLDS_POST GLDS_OPS);
  } else {
    static_assert(SC1 || N == 1, "");
    asm volatile(GLDS_PRE GL1("") GLDS_POST GLDS_OPS);
  }
}
template <int N> DI void wait_vm_lgkm0() { asm volatile("s_waitcnt vmcnt(%0) lgkmcnt(0)" ::"n"(N) : "memory"); }
template <int N> DI void wait_vm() { asm volatile("s_waitcnt vmcnt(%0)" ::"n"(N) : "memory"); }
DI void drain() { asm volatile("s_waitcnt vmcnt(0)" ::: "memory"); __builtin_amdgcn_s_waitcnt(0x0f70); }
DI void bar() { asm volatile("" ::: "memory"); __builtin_amdgcn_s_barrier(); asm volatile("" ::: "memory"); }
DI void sync_lds() { asm volatile("s_waitcnt lgkmcnt(0)" ::: "memory"); bar(); }
#define SCHED0 __builtin_amdgcn_sched_barrier(0)
#define STPAD do { SCHED0; asm volatile("s_nop 1"); SCHED0; } while (0)
#define SLEEP(n) do { if (n) __builtin_amdgcn_s_sleep(n); } while (0)
#define PRIO(n) __builtin_amdgcn_s_setprio(n)

template <int POS, int PR = 0, typename F>
DI void phi_map2(const u32x4* in, const float* rs, bf16x8 wf0, bf16x8 wf1, u32x4 (*out)[2], F mid) {
  const f32x4 zero = f32x4{0.f, 0.f, 0.f, 0.f};
  float x[2][8], mx[2], mn[2];
#pragma unroll
  for (int m = 0; m < 2; ++m) {
    f32x4 r0 = mfma16(wf0, as_frag(in[m]), zero), r1 = mfma16(wf1, as_frag(in[m]), zero);
#pragma unroll
    for (int j = 0; j < 4; ++j) { x[m][j] = r0[j]; x[m][4 + j] = r1[j]; }
  }
#pragma unroll
  for (int m = 0; m < 2; ++m) {
    mx[m] = fmx(fmx(fmx(x[m][0], x[m][1]), x[m][2]), fmx(fmx(x[m][3], x[m][4]), fmx(fmx(x[m][5], x[m][6]), x[m][7])));
    mn[m] = -fmn(fmn(fmn(x[m][0], x[m][1]), x[m][2]), fmn(fmn(x[m][3], x[m][4]), fmn(fmn(x[m][5], x[m][6]), x[m][7])));
  }
  xred4<true>(mx[0], mn[0], mx[1], mn[1]);
  if (PR == 3) { asm volatile("" : "+v"(mx[0]), "+v"(mx[1])); SCHED0; PRIO(2); SCHED0; }
  f32x2 e[2][8];
  float sp[2], sn[2];
#pragma unroll
  for (int m = 0; m < 2; ++m) {
    const float sl = rs[m] * LOG2E;
    const f32x2 l2 = {sl, -sl};
    const f32x2 off = f32x2{mx[m], mn[m]} * (-sl);
#pragma unroll
    for (int j = 0; j < 8; ++j) {
      f32x2 a = __builtin_elementwise_fma(f32x2{x[m][j], x[m][j]}, l2, off);
      e[m][j] = f32x2{__builtin_amdgcn_exp2f(a[0]), __builtin_amdgcn_exp2f(a[1])};
    }
    f32x2 sm = ((e[m][0] + e[m][1]) + (e[m][2] + e[m][3])) + ((e[m][4] + e[m][5]) + (e[m][6] + e[m][7]));
    sp[m] = sm[0]; sn[m] = sm[1];
    if (PR && m == (PR & 1)) { asm volatile("" : "+v"(sp[m]), "+v"(mx[1])); SCHED0; PRIO(PR == 3 ? 1 : 2); SCHED0; }
    if (PR == 2 && m == 1) { asm volatile("" : "+v"(sp[m])); SCHED0; PRIO(1); SCHED0; }
    if (POS == 2 + m) { asm volatile("" : "+v"(sp[m]), "+v"(mx[1])); SCHED0; mid(); SCHED0; }
  }
  xred4<false, true>(sp[0], sn[0], sp[1], sn[1]);
  if (POS == 4) { asm volatile("" : "+v"(sp[0]), "+v"(sp[1])); SCHED0; mid(); SCHED0; }
#pragma unroll
  for (int m = 0; m < 2; ++m) {
    const f32x2 inv = {sp[m], sn[m]};
#pragma unroll
    for (int j = 0; j < 8; ++j) e[m][j] *= inv;
    out[m][0] = u32x4{pk2(e[m][0][0], e[m][1][0]), pk2(e[m][2][0], e[m][3][0]), pk2(e[m][4][0], e[m][5][0]), pk2(e[m][6][0], e[m][7][0])};
    out[m][1] = u32x4{pk2(e[m][0][1], e[m][1][1]), pk2(e[m][2][1], e[m][3][1]), pk2(e[m][4][1], e[m][5][1]), pk2(e[m][6][1], e[m][7][1])};
  }
}

#define SLP_X0 8
#define SLP_X1 14
#define SLP_XD0 3
#define SLP_XD1 9
#define SLP_O 22
#define SLP_OD 12
#define SLP_O0 8
#define SLP_O0D 2
#define SLP_K 4
#define SLP_R 4
#define SLP_HX 1
#define SLP_Q 4
#define SLP_P 40
#define SLP_E 8
#define WA 1
#define WB 4
#define SLP_N 0
#define SLP_4 13
#define KPOS 4
#define WP0 0
#define WP2 2
#define QP 0
#define SPR0 2
#define SW0 0
#define LANEID __builtin_amdgcn_mbcnt_hi(~0u, __builtin_amdgcn_mbcnt_lo(~0u, 0u))
#define STAMP(i) do {} while (0)
#define STAMP_DUMP do {} while (0)
#define COUNT(k) do {} while (0)
#define PSTAMP(l) do { const int layer = l; (void)layer; STAMP(15); } while (0)
constexpr unsigned MBY = 1u << 20;
constexpr unsigned WS_WQ = 0;
constexpr unsigned WS_WO = 12 * MBY;
constexpr unsigned WS_AR = 16 * MBY, ARENA = 12 * MBY;
constexpr unsigned AR_X = 0, AR_ST = 4 * MBY, AR_O = 5 * MBY, AR_KV = 9 * MBY;
constexpr unsigned AR_L2 = AR_ST + 256 * 1024, AR_FL = AR_ST + 320 * 1024;
constexpr unsigned KVREC = 9 * 1024;
constexpr unsigned WS_CS = WS_AR + 4 * ARENA;
constexpr unsigned WS_F1 = WS_CS + 512 * 1024;
constexpr unsigned WS_F2 = WS_F1 + 64 * 1024;
constexpr unsigned WS_EP = WS_F2 + 2048;
constexpr unsigned WS_END = WS_EP + 128;
constexpr unsigned POISON = 0xffffffffu;
constexpr unsigned OOB = 0x80000000u;

typedef __amdgpu_buffer_rsrc_t rsrc_t;
DI rsrc_t mk_rsrc(const void* p, unsigned bytes) { return __builtin_amdgcn_make_buffer_rsrc((void*)p, 0, bytes, 0x00020000); }
DI void st16(rsrc_t r, unsigned off, u32x4 v) { __builtin_amdgcn_raw_buffer_store_b128(v, r, off, 0, 16); }
DI void st16s(rsrc_t r, unsigned voff, unsigned soff, u32x4 v) { __builtin_amdgcn_raw_buffer_store_b128(v, r, voff, soff, 16); }
DI u32x4 ld16(rsrc_t r, unsigned off) { return __builtin_amdgcn_raw_buffer_load_b128(r, off, 0, 16); }
DI u32x4 ld16s(rsrc_t r, unsigned voff, unsigned soff) { return __builtin_amdgcn_raw_buffer_load_b128(r, voff, soff, 16); }
DI u32x4 clean(u32x4 v) { return __builtin_elementwise_min(v, u32x4{POISON - 1, POISON - 1, POISON - 1, POISON - 1}); }
DI unsigned umax(unsigned a, unsigned b) { return a > b ? a : b; }
DI unsigned umin(unsigned a, unsigned b) { return a < b ? a : b; }
DI unsigned umax4(u32x4 v) { return umax(umax(umax(v[0], v[1]), v[2]), v[3]); }
DI unsigned umax4(unsigned m, u32x4 v) { return umax(umax(umax(umax(m, v[0]), v[1]), v[2]), v[3]); }
DI bool poisoned(unsigned m) { return __builtin_amdgcn_ballot_w64(m == POISON) != 0; }
#define MEMFENCE asm volatile("" ::: "memory")

#define BL(i, o) "buffer_load_dwordx4 %1, %2, %" #i " offen" o " nt lds\n\t"
#define BL_OPS(...) : "=&s"(keep) : "v"(voff), "s"(r), "s"(lds), __VA_ARGS__ : "memory"
template <int N>
DI void blds_run(rsrc_t r, unsigned voff, unsigned lds, unsigned s0, unsigned s1 = 0, unsigned s2 = 0, unsigned s3 = 0) {
  unsigned keep;
  if constexpr (N == 2) asm volatile(GLDS_PRE BL(4, "") BL(5, " offset:1024") GLDS_POST BL_OPS("s"(s0), "s"(s1)));
  else asm volatile(GLDS_PRE BL(4, "") BL(5, " offset:1024") BL(6, " offset:2048") BL(7, " offset:3072") GLDS_POST BL_OPS("s"(s0), "s"(s1), "s"(s2), "s"(s3)));
}

#define CODEB 35528
constexpr int MEGA_CODE_BYTES = CODEB;
DI unsigned code_prefetch(int i) {
  unsigned long long pc; int rel; unsigned d;
  asm volatile("s_getpc_b64 %0\n\ts_mov_b32 %1, _Z11mega_kernel10MegaParams@rel32@lo+4" : "=s"(pc), "=s"(rel));
  const unsigned long long a = pc + (long long)rel + (i * 192 + 192 <= MEGA_CODE_BYTES ? i * 192 : 0);
  asm volatile("s_load_dword %0, %1, 0x0\n\ts_load_dword %0, %1, 0x40\n\ts_load_dword %0, %1, 0x80" : "=&s"(d) : "s"(a) : "memory");
  return d;
}

struct MegaParams {
  char* ws;
  const u16* x;
  const u16* wqkvA;
  const u16* woA;
  const u16* wqkvB;
  const u16* woB;
  const u16* wphi;
  u16* out;
};

constexpr int QSLOT = 6 * 1024, QGRP = 4 * QSLOT;
constexpr int L_STG = QGRP;
constexpr int L_A2 = 16 * 1024, L_Z = 21 * 1024;
constexpr int L_RT = 4 * QGRP;
constexpr int L_PB = 0;
constexpr int L_RED = L_STG + 128 * 1024;
constexpr int L_WP = L_RED + 3072;
constexpr int L_STAMP = L_WP + 2048;
constexpr int MEGA_LDS = L_STAMP + 1536;
static_assert(MEGA_LDS <= 160 * 1024, "LDS map");

template <int I, int N, typename F> DI void static_for_(F&& f) {
  if constexpr (I < N) { f(IC<I>{}); static_for_<I + 1, N>(f); }
}
template <int N, typename F> DI void static_for(F&& f) { static_for_<0, N>(f); }

constexpr int NCHK = 3;
constexpr int RO = 8;
constexpr int SPIN_BUDGET = 1 << 15;

__global__ __launch_bounds__(768, 3) void mega_kernel(MegaParams p) {
  extern __shared__ __attribute__((aligned(16))) char smem[];
  const int tid = threadIdx.x;
  const int wave = __builtin_amdgcn_readfirstlane(tid >> 6);
  const int xcd = blockIdx.x & 7, idx = blockIdx.x >> 3;
  int bm = (xcd >> 2) * 4 + (idx >> 3), head = ((xcd & 1) * 2 + ((xcd >> 1) & 1)) * 8 + (idx & 7);
  int cm = (xcd >> 1) & 1, cn = (xcd & 1) * 8 + (idx & 7);
  char* ws = p.ws;
  const u16 *px = p.x, *pq = p.wqkvA;
  asm volatile("" : "+s"(ws), "+s"(px), "+s"(pq));
  const int swv = wave == SW0 ? 0 : wave == 4 ? 1 : wave == 11 ? 2 : -1; (void)swv;
  float* lmean = (float*)(smem + L_RED);
  float* lrstd = lmean + 272;
  float* lcs = lrstd + 272;
  unsigned* lflag = (unsigned*)lmean;
  const rsrc_t wsr = mk_rsrc(ws, WS_END);
  int budget = SPIN_BUDGET;
  unsigned tag0 = 0, tag1 = 0;
  auto ld_ep = [&]() AI { MEMFENCE; return __builtin_amdgcn_raw_buffer_load_b64(wsr, 0, WS_EP, 16); };
  auto mk_tag = [&](u32x2 ep) AI {
    tag0 = __builtin_amdgcn_readfirstlane(ep[0]) + 0x9e3779b9u;
    tag1 = __builtin_amdgcn_readfirstlane(ep[1]) ^ 0x85ebca6bu;
  };
  const unsigned sbase = (unsigned)(size_t)smem;
  int L = wave, cw = wave - 4;
#define OPAQUE_S asm volatile("" : "+s"(L), "+s"(bm), "+s"(head), "+s"(cm), "+s"(cn));
#define LANE_VARS asm volatile("" : "+s"(cw), "+s"(bm), "+s"(head), "+s"(cm), "+s"(cn)); int lane_ = __builtin_amdgcn_mbcnt_hi(~0u, __builtin_amdgcn_mbcnt_lo(~0u, 0u)); asm volatile("" : "+v"(lane_)); \
  const int lane = lane_, fr = lane & 15, fq = lane >> 4; const unsigned v16 = lane * 16; (void)fr; (void)fq; (void)v16;
  auto put_x = [&](u32x4* xr, unsigned v16, unsigned dst) AI {
    float sm = 0.f, sq = 0.f;
#pragma unroll
    for (int j = 0; j < 2; ++j) {
      xr[j][0] = umin(xr[j][0], POISON - 1); xr[j][2] = umin(xr[j][2], POISON - 1);
      st16s(wsr, v16, dst + j * 16384, xr[j]);
    }
    SCHED0;
#pragma unroll
    for (int j = 0; j < 2; ++j)
#pragma unroll
      for (int e = 0; e < 4; ++e) { sm = dot2(xr[j][e], 0x3f803f80u, sm); sq = dot2(xr[j][e], xr[j][e], sq); }
    xred2<false>(sm, sq);
    return u32x2{__float_as_uint(sm), __float_as_uint(sq)};
  };

#define RT_VARS const bool tq = L < 3; const int tsg = tq ? (bm * 2 + (head >> 4)) * 3 + L : bm * 2 + (head >> 4), ks = (head & 15) + 16 * h;
  auto ld_rt = [&](const u16* w, int h, unsigned lds) AI {
    RT_VARS
    LANE_VARS
    const unsigned rs = tq ? 6144 : 2048, D = 8 * rs - 1024, o = ks * 32 * rs + tsg * 128;
    blds_run<4>(mk_rsrc(w, rs * 1024), (lane >> 3) * rs + ((lane & 7) ^ ((lane >> 3) & 6)) * 16, sbase + lds, o, o + D, o + 2 * D, o + 3 * D);
  };
  auto retile = [&](int side, int h, unsigned lds, f32x4* rcs) AI {
    RT_VARS
    LANE_VARS
    const unsigned dst = tq ? WS_WQ + side * (6 * MBY) + (unsigned)((((tsg & 15) * 2) * 32 + ks) * 6 + (tsg >> 4) * 2) * 1024
                            : WS_WO + side * (2 * MBY) + (unsigned)((tsg * 32 + ks) * 4) * 1024;
    const unsigned gstr = tq ? 32 * 6 * 1024 : 2048;
    const unsigned a0 = (lane >> 2) * 128 + ((lane >> 3) & 3) * 32 + (lane & 3) * 8;
#pragma unroll
    for (int pc = 0; pc < 4; ++pc) {
      const char* aa = smem + lds + (a0 ^ (pc * 32));
      u32x2 r0_ = trread(aa), r1_ = trread(aa + 2048);
      u32x4 v = u32x4{r0_[0], r0_[1], r1_[0], r1_[1]};
      st16s(wsr, v16 + (pc & 1) * 1024, dst + (pc >> 1) * gstr, v);
      if (rcs) rcs[pc] = mfma16(as_frag(u32x4{0x3f803f80u, 0x3f803f80u, 0x3f803f80u, 0x3f803f80u}), as_frag(v), rcs[pc]);
    }
    if (rcs && h == 1) {
      const f32x4 c0 = rcs[0], c1 = rcs[1], c2 = rcs[2], c3 = rcs[3];
      const float ca = fq & 1 ? c1[0] : c0[0], cb = fq & 1 ? c3[0] : c2[0], cc = fq & 2 ? cb : ca;
      if (tq) __builtin_amdgcn_raw_buffer_store_b32(__float_as_uint(cc), wsr, ((fq >> 1) * 1536 + (fq & 1) * 16 + fr) * 4,
                                                    WS_CS + ((side * 32 + (tsg & 15) * 2) * 1536 + (tsg >> 4) * 512 + (head & 15) * 32) * 4, 16);
    }
  };
  auto flag = [&](unsigned table) AI {
    if (LANEID == 0) __builtin_amdgcn_raw_buffer_store_b64(u32x2{tag0, tag1}, wsr, 0, table + (bm * 32 + head) * 8, 16);
  };
  auto poll = [&](unsigned table, auto between, int rows = 0) AI {
    LANE_VARS
    const int q = (umin(fq, 2) * 16 + (head >> 1)) * 43 >> 7;
    const unsigned vo = rows ? bm * 256 + (fr & rows) * 16 : fq == 3 ? bm * 256 + fr * 16 : q * 128 + (fr & 7) * 16;
    auto miss = [&](u32x4 f) AI { return __builtin_amdgcn_ballot_w64(((f[0] ^ tag0) | (f[1] ^ tag1) | (f[2] ^ tag0) | (f[3] ^ tag1)) != 0) != 0; };
    MEMFENCE;
    const u32x4 f0 = ld16s(wsr, vo, table);
    MEMFENCE;
    between();
    MEMFENCE;
    bool wait = miss(f0);
#pragma unroll 1
    while (wait && --budget > 0) {
      SLEEP(SLP_Q);
      MEMFENCE;
      wait = miss(ld16s(wsr, vo, table));
      asm volatile("" : "+s"(budget) :: "memory");
    }
    MEMFENCE;
  };
  auto ld_x = [&](int cx, unsigned lds) AI {
    LANE_VARS
    const unsigned o = (bm * 16 + (head >> 1)) * 32768 + cx * 128;
    blds_run<2>(mk_rsrc(px, 4 * MBY), (lane >> 3) * 2048 + ((lane & 7) ^ (lane >> 3)) * 16, sbase + lds, o, o + 15360);
  };
  auto st_x = [&](int cx, unsigned lds, int w) AI {
    LANE_VARS
    const int mt = bm * 16 + (head >> 1);
    u32x4 xr[2];
#pragma unroll
    for (int j = 0; j < 2; ++j) {
      const char* a = smem + lds + fr * 128 + (fq & 1) * 8;
      const u32x2 lo = *(const u32x2*)(a + ((j * 4 + (fq >> 1)) ^ (fr & 7)) * 16), hi = *(const u32x2*)(a + ((j * 4 + 2 + (fq >> 1)) ^ (fr & 7)) * 16);
      xr[j] = u32x4{lo[0], lo[1], hi[0], hi[1]};
    }
    const u32x2 stat = put_x(xr, v16, WS_AR + AR_X + xt_off(mt, cx * 2));
    if (fq == 0) *(u32x2*)(lmean + 16 + fr * 4 + w) = stat;
  };

  {
    const int lane = LANEID;
    const bool tq = wave < 4;
    const unsigned rs = tq ? 6144 : 2048, D = 8 * rs - 1024;
    const unsigned o = (tq ? (head & 15) * 32 : (bm * 16 + (head >> 1)) * 16) * rs + (tq ? (bm * 2 + (head >> 4)) * 3 + wave : (head & 1) * 4 + cw) * 128;
    const unsigned lds = sbase + L_PB + (tq ? 8192 + wave * 4096 : cw * 2048);
    const unsigned vo = (lane >> 3) * rs + ((lane & 7) ^ ((lane >> 3) & (tq ? 6 : 7))) * 16;
    const rsrc_t r = mk_rsrc(tq ? pq : px, tq ? 6 * MBY : 4 * MBY);
    if ((wave & 7) != 3 && wave < 8) blds_run<2>(r, vo, lds, o, o + D);
    if (wave < 3) blds_run<2>(r, vo, lds + 2048, o + 2 * D + 2048, o + 3 * D + 2048);
  }
  if (wave >= 8) {
    const unsigned cp = code_prefetch(idx * 9 + cw), cq = code_prefetch(idx * 9 + cw - 4);
    L = wave - 8;
    bar();
    ld_x(8 + (head & 1) * 4 + L, L_PB + 32768 + L * 2048);
    if (L < 3) ld_rt(pq, 1, L_PB + 20480 + L * 4096);
    sync_lds();
    asm volatile("; pi" :: "s"(cp), "s"(cq) : "memory");
    wait_vm<0>();
    PSTAMP(1);
    st_x(8 + (head & 1) * 4 + L, L_PB + 32768 + L * 2048, L * 64 + 2);
    if (L < 3) {
      const f32x4 t = *(const f32x4*)(smem + L_PB + 40960 + L * 1024 + LANEID * 16);
      f32x4 rca[4] = {{t[0], 0.f, 0.f, 0.f}, {t[1], 0.f, 0.f, 0.f}, {t[2], 0.f, 0.f, 0.f}, {t[3], 0.f, 0.f, 0.f}};
      retile(0, 1, L_PB + 20480 + L * 4096, rca);
    }
    asm volatile("s_waitcnt lgkmcnt(0)" ::: "memory");
    PSTAMP(2);
  } else if (wave >= 4) {
    wait_vm<0>();
    PSTAMP(1);
    st_x((head & 1) * 4 + cw, L_PB + cw * 2048, cw * 64);
    bar();
    wait_vm_lgkm0<0>();
    PSTAMP(2);
    bar();
    asm volatile("; pc" ::: "memory");
    SLEEP(SLP_E);
    L = 3;
    if (cw < 2) ld_rt(p.woA, cw, L_RT + 3 * 14336 + cw * 4096);
    if (cw == 2) ld_rt(p.woB, 0, L_RT + 3 * 14336 + 8192);
  } else if (wave < 3) {
    const u32x2 ep = ld_ep();
    wait_vm<1>();
    PSTAMP(1);
    f32x4 rca[4] = {};
    retile(0, 0, L_PB + 8192 + L * 4096, rca);
    *(f32x4*)(smem + L_PB + 40960 + L * 1024 + LANEID * 16) = f32x4{rca[0][0], rca[1][0], rca[2][0], rca[3][0]};
    bar();
    drain();
    mk_tag(ep);
    PSTAMP(2);
    sync_lds();
    asm volatile("; pl" ::: "memory");
    SLEEP(SLP_E);
    ld_rt(p.wqkvB, 0, L_RT + L * 14336);
    ld_rt(p.wqkvB, 1, L_RT + L * 14336 + 4096);
  } else {
    const u32x2 ep = ld_ep();
    const unsigned cp = code_prefetch(idx * 9 + 8);
    if (LANEID < 9) lflag[LANEID] = 0;
    wait_vm_lgkm0<0>();
    asm volatile("" :: "s"(cp));
    mk_tag(ep);
    bar();
    bar();
    flag(WS_F1);
    if (LANEID == 0) *(u32x2*)(lflag + 10) = u32x2{tag0, tag1};
    PSTAMP(2);
    SLEEP(SLP_P);
    poll(WS_F1, []() {});
    asm volatile("; pp" ::: "memory");
  }
  bar();
  PSTAMP(3);
  int role = wave;
  asm volatile("" : "+s"(role));

  if (__builtin_expect(role < 4, 1)) {
    const unsigned v16 = __builtin_amdgcn_mbcnt_hi(~0u, __builtin_amdgcn_mbcnt_lo(~0u, 0u)) * 16;
#define OPAQUE_LANE int lane = v16 >> 4; asm volatile("" : "+v"(lane));
    auto q_groupW = [&](int layer, int g, int pos) AI {
      const char* gW = ws + WS_WQ + (layer & 1) * (6 * MBY) + head * (192 * 1024) + (g * 4 + L) * 6144;
      const unsigned d = sbase + (pos * 4 + L) * QSLOT;
      glds_run<3, true>(v16, gW, d);
      glds_run<3, true>(v16, gW + 3072, d + 3072);
    };
    u32x4 wn[4][6];
    auto ld_wn = [&](int layer, int g0, int g1) AI {
      const unsigned so = WS_WQ + (layer & 1) * (6 * MBY) + head * (192 * 1024) + L * 6144;
#pragma unroll
      for (int g = g0; g < g1; ++g)
#pragma unroll
        for (int n = 0; n < 6; ++n) wn[g][n] = ld16s(wsr, v16 + (n % 3) * 1024, so + g * 24576 + (n / 3) * 3072);
      MEMFENCE;
    };
    auto wr_wn = [&](int g) AI {
#pragma unroll
      for (int n = 0; n < 6; ++n) *(u32x4*)(smem + (g * 4 + L) * QSLOT + n * 1024 + v16) = wn[g][n];
    };
    const u32x4 pz = u32x4{POISON, POISON, POISON, POISON};
    auto poison = [&](unsigned so) AI { st16s(wsr, v16, so, pz); };
    auto poison_o = [&]() AI {
      const unsigned o = WS_AR + AR_O + xt_off(bm * 16 + L * 4, head);
#pragma unroll
      for (int i = 0; i < 4; ++i) poison(o + i * 1024);
      STPAD;
    };
    auto poison_x = [&](int l) AI {
      if (L < NCHK) {
        const unsigned o = WS_AR + l * ARENA + AR_X + bm * (512 * 1024) + head * (NCHK * 2048) + L * 2048;
        poison(o); poison(o + 1024);
        STPAD;
      }
    };
    auto poison_s = [&](int l) AI {
      if (L == 3) {
        const unsigned o = WS_AR + l * ARENA + AR_KV + (unsigned)(bm * 32 + head) * KVREC;
        poison(o); poison(o + 1024);
        STPAD;
      }
    };
    auto poison_kv = [&](int l) AI {
      const unsigned kvo = WS_AR + l * ARENA + AR_KV + (unsigned)(bm * 32 + head) * KVREC;
      poison(kvo + L * 1024);
      if (L == (l ? 0 : 1)) st16s(wsr, v16 < 256 ? v16 : OOB, kvo + 4096, pz);
      STPAD;
    };
    q_groupW(0, 0, 0);
    q_groupW(0, 1, 1);
    auto put_l2 = [&](float sm, float sq, unsigned vo, unsigned so) AI {
      xred2<false>(sm, sq);
      if (v16 < 256) st16s(wsr, vo, so, u32x4{__float_as_uint(sm), tag0, __float_as_uint(sq), tag1});
      MEMFENCE;
    };
    if (L == 3) {
      const f32x4 t = *(const f32x4*)((const char*)(lmean + 16) + v16);
      put_l2(t[0] + t[2], t[1] + t[3], v16, WS_AR + AR_L2 + (head & 1) * 32768 + (bm * 16 + (head >> 1)) * 256);
    }
    if (L == 3) wait_vm<7>(); else wait_vm<6>();
    auto body = [&](auto kind, const int layer) AI {
      constexpr bool isB = decltype(kind)::value == 1, is0 = decltype(kind)::value == 0;
      const unsigned ar = WS_AR + layer * ARENA;
      f32x4 rcs[4] = {};
      unsigned tch = 0;
      auto sW = [&]() AI { return WS_WO + (layer & 1) * (2 * MBY) + cn * (128 * 1024); };
      auto stg = [&](unsigned off, auto n) AI { glds_run<decltype(n)::value, true>(v16, ws + sW() + off, sbase + L_STG + off); };
      auto stg7 = [&](unsigned o) AI { stg(o, IC<4>{}); stg(o + 4096, IC<3>{}); };
      auto stg6 = [&](unsigned o) AI { stg(o, IC<3>{}); stg(o + 3072, IC<3>{}); };

      auto extra = [&](int b) AI {
        if (!is0) return;
        const unsigned lu = L_RT + L * 14336;
        if (b < 7) { SCHED0; PRIO(3); SCHED0; }
        if (b == 1) retile(L < 3, 0, lu, rcs);
        if (b == 2 && L == 3) retile(0, 1, lu + 4096, nullptr);
        if (b == 6 && L == 3) ld_rt(p.woB, 1, lu + 4096);
        if (b == 8) {
          if (L < 3) retile(1, 1, lu + 4096, rcs);
          if (L == 3) retile(1, 0, lu + 8192, nullptr);
          if (L == 3) retile(1, 1, lu + 4096, nullptr);
        }
        if (b < 7) { SCHED0; PRIO(0); SCHED0; }
      };
      auto extra2 = [&](int b) AI {
        if (!is0) return;
        if (b < 7) { SCHED0; PRIO(3); SCHED0; }
        if (b == 1) poison_o();
        if (b == 2) poison_kv(0);
        if (b == 5) poison_kv(2);
        if (b == 8) poison_s(1);
        if (b == 9) poison_x(1);
        if (b == 10) { poison_x(2); poison_s(3); }
        if (b < 7) { SCHED0; PRIO(0); SCHED0; }
      };
      if (is0) sync_lds();
      STAMP(0);
      {
        OPAQUE_S
        OPAQUE_LANE
        u32x4 c0, c1;
        asm volatile("" : "=v"(c0), "=v"(c1));
        auto ldcs = [&]() AI {
          if (L > 0) {
            const unsigned so = WS_CS + (((layer & 1) * 32 + head) * 1536 + (L - 1) * 512) * 4;
            c0 = ld16s(wsr, v16, so); c1 = ld16s(wsr, v16 + 1024, so);
          }
          MEMFENCE;
        };
        auto wrcs = [&]() AI {
          if (L > 0) {
            f32x4 t = add4(__builtin_bit_cast(f32x4, c0), __builtin_bit_cast(f32x4, c1));
#pragma unroll
            for (int e = 0; e < 4; ++e)
              t[e] += __uint_as_float(__builtin_amdgcn_update_dpp(0u, __float_as_uint(t[e]), 0x128, 0xf, 0xf, false));
            float t0 = t[0], t1 = t[1], t2 = t[2], t3 = t[3];
            xred4<false>(t0, t1, t2, t3);
            *(f32x4*)(lcs + (isB ? 96 : 0) + (L - 1) * 32 + (lane & 7) * 4) = f32x4{t0, t1, t2, t3};
          }
        };
        if (isB && layer == 1) ldcs();
        if constexpr (is0) {
          if (L == 3) flag(WS_F1 + 2048);
          q_groupW(0, 2, 2); q_groupW(0, 3, 3);
          wait_vm<12>();
          if (L >= 2) glds_run<1>(v16, (const char*)p.wphi + head * 2048 + (L - 2) * 1024, sbase + L_WP + (L - 2) * 1024);
        }
        STAMP(1);
        sync_lds();
        STAMP(2);
        OPAQUE_S
        u32x4 s0 = u32x4{0u, 0u, 0u, 0u}, s1 = s0;
        auto ldst = [&]() AI {
          const unsigned so = ar + AR_L2 + (bm * 16 + L * 4) * 256;
          s0 = ld16s(wsr, v16, so);
          if (is0) s1 = ld16s(wsr, v16, so + 32768);
          if (isB && L == 0) s1 = ld16s(wsr, v16 & 255, bm > 0 ? so - 256 : so);
          MEMFENCE;
        };
        auto fin = [&](float sm, float sq, int r, bool own) AI {
          float mean = sm * (1.0f / 1024.0f);
          float var = fmaxf(__builtin_fmaf(sq, 1.0f / 1024.0f, -mul1(mean, mean)), 0.f);
          if (own) lmean[r] = -mean;
          lrstd[r] = __builtin_amdgcn_rsqf(var + 1e-5f);
        };
        auto wrst = [&]() AI {
          while (true) {
            unsigned d = (s0[1] ^ tag0) | (s0[3] ^ tag1);
            if (is0 || (isB && L == 0)) d |= (s1[1] ^ tag0) | (s1[3] ^ tag1);
            if (__builtin_expect(__builtin_amdgcn_ballot_w64(d != 0) == 0, 1) || --budget <= 0) break;
            COUNT(1);
            ldst();
          }
          MEMFENCE;
          float sm = __uint_as_float(s0[0]), sq = __uint_as_float(s0[2]);
          if (is0) { sm += __uint_as_float(s1[0]); sq += __uint_as_float(s1[2]); }
          fin(sm, sq, 16 + L * 64 + lane, true);
          if (isB && L == 0) fin(__uint_as_float(s1[0]), __uint_as_float(s1[2]), lane & 15, false);
        };
        auto q_group = [&](int g) AI {
          if (is0) {
            if (L & 1) SLEEP(SPR0);
            if (L & 2) SLEEP(2 * SPR0);
            q_groupW(layer, g, g & 3);
          } else if (g >= 5) q_groupW(layer, g, g < 6 ? g : g - 4);
        };
        if (!is0 && L == 0) {
          poll(ar + AR_FL, []() {}, 15);
          if (head < 16) {
            const unsigned so = ar + AR_ST + (bm * 16 + head) * 2048;
            s0 = ld16s(wsr, v16, so); s1 = ld16s(wsr, v16 + 1024, so);
            MEMFENCE;
          }
        }
        if (is0) {
          drain();
          poll(WS_F1 + 2048, [&]() AI { extra(1); });
          ldcs();
          extra2(1);
        }
        if (isB && layer == 1) wrcs();
        MEMFENCE;
        STAMP(3);
        sync_lds();
        STAMP(4);
        OPAQUE_S
        if (!is0) drain();
        if (!is0 && L == 0 && head < 16) {
          f32x4 t = add4(__builtin_bit_cast(f32x4, s0), __builtin_bit_cast(f32x4, s1));
#pragma unroll
          for (int e = 0; e < 4; ++e)
            t[e] += __uint_as_float(__builtin_amdgcn_update_dpp(0u, __float_as_uint(t[e]), 0x128, 0xf, 0xf, false));
          const bool hi = lane & 8;
          put_l2(hi ? t[2] : t[0], hi ? t[3] : t[1], (lane & 7) * 32 + (lane & 8) * 2, ar + AR_L2 + (bm * 16 + head) * 256);
        }
        q_group(4);
        extra(2); extra2(2);
        STAMP(5);
        sync_lds();
        STAMP(6);
        OPAQUE_S
        drain();
        q_group(5);
        if (is0) wrcs();
        STAMP(7);
        bar();
        OPAQUE_S
        drain();
        if (is0 && L == 0 && v16 == 0) __builtin_amdgcn_raw_buffer_store_b64(u32x2{tag0, tag1}, wsr, 0, WS_F2 + (bm * 32 + head) * 8, 16);
        q_group(6);
        ldst();
        STAMP(8);
        bar();
        STAMP(9);
        OPAQUE_S
        drain();
        q_group(7);
        extra2(5);
        SCHED0;
        wrst();
        STAMP(10);
        bar();
        STAMP(11);
        OPAQUE_S
        drain();
        if (isB && layer == 3 && L == 3) {
          asm volatile("global_load_dword %0, %1, %2" : "=v"(tch) : "v"((unsigned)((bm * 256 + cm * 16 + (lane & 7) * 32) * 2048 + cn * 128)), "s"(p.out) : "memory");
        }
        extra(6);
        if (isB) stg6(96 * 1024 + L * 6144);
        SCHED0;
      }
      STAMP(12);
      sync_lds();
      STAMP(13);
      OPAQUE_S
      u32x4 fl[2];
      fl[0] = fl[1] = u32x4{tag0, tag1, tag0, tag1};
      auto ldfl = [&]() AI { fl[0] = ld16s(wsr, v16, WS_F2); fl[1] = ld16s(wsr, v16 + 1024, WS_F2); };
      if (is0) ldfl();
      bar();
      constexpr int NR = isB ? 6 : 9;
      const unsigned ro = isB ? 48 * 1024 + L * 6144 : L * 9216;
      u32x4 r[NR];
      bool pend = false;
      auto flags = [&]() AI {
        const u32x4 d = (fl[0] ^ u32x4{tag0, tag1, tag0, tag1}) | (fl[1] ^ u32x4{tag0, tag1, tag0, tag1});
        pend = __builtin_amdgcn_ballot_w64((d[0] | d[1] | d[2] | d[3]) != 0) != 0;
      };
      if (isB) {
        stg6(L * 6144); stg(120 * 1024 + L * 2048, IC<2>{});
        stg6(24 * 1024 + L * 6144);
        stg6(72 * 1024 + L * 6144);
      } else {
        drain();
        if (is0) flags();
        MEMFENCE;
        extra(8); extra2(8);
        if (!is0) poison_x(3);
        while (is0 && __builtin_expect(pend, 0) && --budget > 0) {
          ldfl();
          flags();
          MEMFENCE;
        }
        stg(36 * 1024 + L * 3072, IC<3>{}); stg7(72 * 1024 + L * 14336); stg7(72 * 1024 + L * 14336 + 7168);
      }
#pragma unroll
      for (int i = 0; i < NR; ++i) r[i] = ld16s(wsr, v16 + (i & 3) * 1024, sW() + ro + (i >> 2) * 4096);
      MEMFENCE;
      if (is0 && bm + head + L == 0 && v16 == 0) __builtin_amdgcn_raw_buffer_store_b32(tag0, wsr, 0, WS_EP, 16);
      auto wr_r = [&]() AI {
        unsigned vv = v16;
        asm volatile("" : "+v"(vv));
#pragma unroll
        for (int i = 0; i < NR; ++i) *(u32x4*)(smem + L_STG + ro + i * 1024 + vv) = r[i];
      };
      auto left = [&]() AI {
        OPAQUE_LANE
        for (;;) {
          MEMFENCE;
          if (__builtin_amdgcn_ballot_w64(lflag[lane & 7] != (unsigned)layer) == 0 || --budget <= 0) break;
          SLEEP(2);
        }
        MEMFENCE;
      };
      if (isB) {
#pragma unroll
        for (int i = 0; i < 24; ++i) asm volatile("" : "=v"(wn[i / 6][i % 6]));
        if (layer == 1) ld_wn(layer + 1, 0, 2);
        STAMP(14);
        left();
        wr_r();
        MEMFENCE;
        if (layer == 1) ld_wn(layer + 1, 2, 4);
        wait_vm_lgkm0<12>();
        if (layer == 3) wait_vm<0>();
        asm volatile("" :: "v"(tch));
      } else {
        constexpr int W0 = is0 ? WP0 : WP2, W1 = W0 > WA ? W0 : WA;
        ld_wn(layer + 1, 0, W0);
        if (is0) wait_vm<26 + 6 * W0>();
        STAMP(14);
        bar();
        stg6(48 * 1024 + L * 6144);
        ld_wn(layer + 1, W0, W1);
        bar();
        extra2(9);
        if (SLP_N) { wr_r(); MEMFENCE; SLEEP(SLP_N); }
        ld_wn(layer + 1, W1, WB);
        if (!SLP_N) wr_r();
        wait_vm<6 * (WB - W1)>();
        bar();
        if (WB < 4) SLEEP(SLP_4);
        extra2(10);
        ld_wn(layer + 1, WB, 4);
        wait_vm_lgkm0<6 * (4 - WB)>();
      }
      bar();
      if (isB && layer == 3) { STAMP_DUMP; __builtin_amdgcn_endpgm(); }
      OPAQUE_S
      wr_wn(0);
      drain();
      sync_lds();
      OPAQUE_S
      SCHED0;
      q_groupW(layer + 1, 4, 4);
#pragma unroll
      for (int g = 1; g < 4; ++g) wr_wn(g);
    };
    body(IC<0>{}, 0);
#pragma unroll 1
    for (int layer = 1;; layer += 2) {
      body(IC<1>{}, layer);
      body(IC<2>{}, 2);
    }
  }

  const f32x4 zero = f32x4{0.f, 0.f, 0.f, 0.f};
#pragma unroll 1
  for (int layer = 0; layer < 4; ++layer) {
    const bool isB = layer & 1;
    const unsigned ar = WS_AR + layer * ARENA;
    {
      f32x4 acc[2][6];
      f32x4 ck[2], mv[2], xv[2];
      float mean[2], rs[2], cv[2];
      u32x2 wt[4];
      const float* lcl = lcs + (isB ? 96 : 0);
      {
        LANE_VARS
        const unsigned va = v16 + cw * 2048;
        const unsigned sa0 = ar + AR_X + bm * (512 * 1024);
        auto lda = [&](unsigned so, u32x4* a) AI {
          a[0] = ld16s(wsr, va, so);
          a[1] = ld16s(wsr, va + 1024, so);
        };
        bf16x8 f[2][6];
        u32x4 a[4][2];
        auto unset = [&](const u32x4* e) AI { return poisoned(umax(umax(umax(e[0][0], e[0][2]), e[1][0]), e[1][2])); };
        auto refill = [&](auto ic, unsigned cur) AI {
          constexpr int I = decltype(ic)::value;
          do {
            COUNT(0);
            SLEEP(SLP_R);
            MEMFENCE;
            lda(cur, a[I]);
            bool u[4];
#pragma unroll
            for (int j = 1; j < 4; ++j) u[j] = unset(a[(I + j) % 4]);
            SCHED0;
#pragma unroll
            for (int j = 1; j < 4; ++j)
              if (u[j]) lda(cur + j * 16384, a[(I + j) % 4]);
            __builtin_amdgcn_s_waitcnt(0x0f70);
            MEMFENCE;
          } while (unset(a[I]) && --budget > 0);
        };
        STAMP(0);
        if (layer > 0) {
          bar();
          if (cn < 8) { if (cw < 4) SLEEP(SLP_X0); else SLEEP(SLP_X1); }
          else { if (cw < 4) SLEEP(SLP_XD0); else SLEEP(SLP_XD1); }
        }
#pragma unroll
        for (int j = 0; j < 4; ++j) { lda(sa0 + j * 16384, a[j]); SCHED0; }
#pragma unroll
        for (int m = 0; m < 2; ++m)
#pragma unroll
          for (int n = 0; n < 6; ++n) acc[m][n] = zero;
        asm volatile("" : "+v"(acc[0][0]), "+v"(acc[0][1]), "+v"(acc[0][2]), "+v"(acc[0][3]), "+v"(acc[0][4]), "+v"(acc[0][5]));
        asm volatile("" : "+v"(acc[1][0]), "+v"(acc[1][1]), "+v"(acc[1][2]), "+v"(acc[1][3]), "+v"(acc[1][4]), "+v"(acc[1][5]));
        if (layer == 0) { wait_vm<8>(); sync_lds(); }
#pragma unroll
        for (int nn = 0; nn < 6; ++nn) { const int n = nn < 4 ? nn + 2 : nn - 4; f[0][n] = *(const bf16x8*)(smem + v16 + n * 1024); }
        SCHED0;
        if (layer > 0) {
          const u32x2 tg = *(const u32x2*)(lflag + 10);
          wait_vm<8>();
          unsigned old;
          asm volatile("s_mov_b64 exec, 1\n\tds_add_rtn_u32 %0, %1, %2\n\ts_mov_b64 exec, -1\n\ts_waitcnt lgkmcnt(0)" : "=v"(old) : "v"(sbase + L_RED + 32), "v"(1) : "memory");
          if (__builtin_amdgcn_readfirstlane(old) == 8u * layer - 1)
            __builtin_amdgcn_raw_buffer_store_b64(tg, wsr, lane ? OOB : 0, ar + AR_FL + bm * 256 + (cm * 16 + cn) * 8, 16);
          MEMFENCE;
        }
        SCHED0;
        bool bad = layer > 0 && unset(a[0]);
        const int wrap = layer > 0 ? 6 : 4;
        unsigned ps[4] = {sbase + L_RED + 64 + cw * 128, sbase + L_RED + 2176 + (layer & 1) * 384, 0, sbase + L_WP};
        ps[2] = ps[0] + 1088 + (layer & 1) * (2176 + 384 - 1088 - 64 - cw * 128);
        static_assert(L_WP % 1024 == 0, "");
        int early = (layer & 1) * cw;
        asm volatile("" : "+s"(ps[0]), "+s"(ps[1]), "+s"(ps[2]), "+s"(ps[3]), "+s"(early));
        STAMP(1);
        auto group = [&](auto modec, int g) AI {
          constexpr int MODE = decltype(modec)::value;
          constexpr bool LAST = MODE == 3, CHK = MODE == 0;
          const unsigned gs = (g < wrap ? g : g - 4) * QGRP, gn = (g + 1 < wrap ? g + 1 : g - 3) * QGRP;
          const unsigned sc = sa0 + g * 65536, so = sc + 4 * 16384;
          unsigned gl = sbase + gs + v16;
          if constexpr (LAST) asm volatile("" : "+v"(gl));
          unsigned pa[5];
          static_for<4>([&](auto ic) AI {
            constexpr int i = decltype(ic)::value;
            constexpr bool END = LAST && i == 3;
            SCHED0;
            if (i == 3 && !LAST) { sync_lds(); SCHED0; }
            if (i == 0 && LAST) {
              PRIO(0);
              if (early >= 4) PRIO(3);
              SCHED0;
              bar();
              SCHED0;
            }
            if constexpr (!LAST) PRIO(i == 3 ? 3 : 2 - i);
            if constexpr (CHK) { if (__builtin_expect(bad, 0)) refill(ic, sc + i * 16384); }
            SCHED0;
            const bf16x8* fc = f[i & 1];
            bf16x8* fn = f[(i + 1) & 1];
            const u32x4* an = a[(i + 1) % 4];
            const unsigned slot = i < 3 ? gs + (i + 1) * QSLOT : gn;
            const char* pb = LAST ? (const char*)((const LDS(char)*)gl + (i + 1) * QSLOT) : smem + slot + v16;
            unsigned mx = 0;
            static_for<12>([&](auto jc) AI {
              constexpr int j = decltype(jc)::value, nn = j >> 1, m = j & 1, n = nn < 4 ? nn + 2 : nn - 4;
              if constexpr (n < 4) mfma_acc(acc[m][n], fc[n], as_frag(a[i][m]));
              else mfma_acc(acc[m][n], as_frag(a[i][m]), fc[n]);
              SCHED0;
              if constexpr (!END && j < 6) { constexpr int r = j < 4 ? j + 2 : j - 4; fn[r] = *(const bf16x8*)(pb + r * 1024); }
              if constexpr (LAST && i == 0) {
                if constexpr (j == 7) { pa[4] = v16 >> 1; asm volatile("" : "+v"(pa[4])); }
                if constexpr (j == 9) { pa[4] = (pa[4] & 24) | ps[3]; asm volatile("" : "+v"(pa[4])); }
                if constexpr (j == 11) { pa[4] |= v16 & ~63u; asm volatile("" : "+v"(pa[4])); }
              }
              if constexpr (LAST && i == 1) {
                if constexpr (j == 6) { pa[0] = lane & 48; asm volatile("" : "+v"(pa[0])); }
                if constexpr (j == 7) { pa[1] = v16 >> 2; asm volatile("" : "+v"(pa[1])); }
                if constexpr (j == 8) { pa[1] &= 60; asm volatile("" : "+v"(pa[1])); }
                if constexpr (j == 9) { pa[2] = pa[1] + ps[0]; asm volatile("" : "+v"(pa[2])); }
                if constexpr (j == 10) { pa[1] += ps[1]; asm volatile("" : "+v"(pa[1])); }
                if constexpr (j == 11) { pa[3] = pa[0] + ps[1]; asm volatile("" : "+v"(pa[3])); }
              }
              if constexpr (LAST && i == 2) {
                if constexpr (j == 6) ck[0] = ((const LDS(f32x4)*)pa[3])[8];
                if constexpr (j == 7) ck[1] = ((const LDS(f32x4)*)pa[3])[12];
                if constexpr (j == 8) { mean[0] = ((const LDS(float)*)pa[2])[0]; asm volatile("" : "+v"(pa[2])); }
                if constexpr (j == 9) { mean[1] = ((const LDS(float)*)pa[2])[16]; asm volatile("" : "+v"(pa[2])); }
                if constexpr (j == 10) { cv[0] = ((const LDS(float)*)pa[1])[64]; cv[1] = ((const LDS(float)*)pa[1])[80]; }
                if constexpr (j == 11) { pa[1] = pa[0] + ps[0]; asm volatile("" : "+v"(pa[1])); }
              }
              if constexpr (LAST && i == 3) {
                if constexpr (j == 0) mv[0] = ((const LDS(f32x4)*)pa[1])[0];
                if constexpr (j == 1) mv[1] = ((const LDS(f32x4)*)pa[1])[4];
                if constexpr (j == 2) { pa[0] += ps[2]; asm volatile("" : "+v"(pa[0])); }
                if constexpr (j == 3) { rs[0] = ((const LDS(float)*)pa[2])[272]; asm volatile("" : "+v"(pa[2])); }
                if constexpr (j == 4) xv[0] = ((const LDS(f32x4)*)pa[0])[0];
                if constexpr (j == 5) xv[1] = ((const LDS(f32x4)*)pa[0])[4];
                if constexpr (j == 6) rs[1] = ((const LDS(float)*)pa[2])[288];
                if constexpr (j == 8) wt[0] = trl(pa[4]);
                if constexpr (j == 9) wt[1] = trl(pa[4] + 1024);
                if constexpr (j == 10) wt[2] = trl(pa[4] + 32);
                if constexpr (j == 11) wt[3] = trl(pa[4] + 1056);
              }
              if constexpr (CHK) {
                if constexpr (j == 8) mx = umax(umax(umax(an[0][0], an[0][2]), an[1][0]), an[1][2]);
                if constexpr (j == 10) bad = poisoned(mx);
              }
              if constexpr (j == 10 && !LAST) a[i][0] = ld16s(wsr, va, so + i * 16384);
              if constexpr (j == 11 && !LAST) a[i][1] = ld16s(wsr, va + 1024, so + i * 16384);
              SCHED0;
            });
          });
        };
        int g = 0;
        if (layer > 0) {
#pragma unroll 1
          for (; g < NCHK; ++g) group(IC<0>{}, g);
        }
#pragma unroll 1
        for (; g < 7; ++g) group(IC<1>{}, g);
        group(IC<3>{}, 7);
        if (cw < 4) PRIO(1); else if (!isB) PRIO(2);
        mfma_done();
      }
      STAMP(2);
      LANE_VARS
      char* const scr = smem;

      u32x4 qf[2], kf[2];
      u32x2 vf[2][2];
      auto fold_rows = [&](int part, u32x4* out, const f32x4* c) AI {
#pragma unroll
        for (int m = 0; m < 2; ++m) {
          f32x4 a0 = acc[m][part * 2] + c[0] * mean[m], a1 = acc[m][part * 2 + 1] + c[1] * mean[m];
          out[m] = u32x4{pk2(a0[0], a0[1]), pk2(a0[2], a0[3]), pk2(a1[0], a1[1]), pk2(a1[2], a1[3])};
        }
      };
      auto fold_v = [&](bool scaled) AI {
#pragma unroll
        for (int m = 0; m < 2; ++m) {
          f32x4 a0 = acc[m][4] + mv[m] * cv[0], a1 = acc[m][5] + mv[m] * cv[1];
          if (scaled) { a0 = mul4(a0, xv[m]); a1 = mul4(a1, xv[m]); }
          vf[m][0] = u32x2{pk2(a0[0], a0[1]), pk2(a0[2], a0[3])};
          vf[m][1] = u32x2{pk2(a1[0], a1[1]), pk2(a1[2], a1[3])};
        }
      };
      const unsigned otile = WS_AR + AR_O + xt_off(bm * 16 + cw * 2, head);
      auto put_o = [&](int m, f32x4 a, f32x4 b, unsigned opar) AI {
        const u32x4 v = u32x4{(pk2(a[0], a[1]) & ~1u) | opar, pk2(a[2], a[3]), (pk2(b[0], b[1]) & ~1u) | opar, pk2(b[2], b[3])};
        st16s(wsr, v16 + m * 1024, otile, v);
        return v;
      };
      auto hold = [&](u32x4 a, u32x4 b) AI { SCHED0; asm volatile("s_nop 1" :: "v"(a), "v"(b)); SCHED0; };

      if (!isB) {
        fold_rows(1, kf, ck); fold_v(true);
        char* PK = smem + L_STG;
        char* VF = scr;
        const bf16x8 wf0 = as_frag(u32x4{wt[0][0], wt[0][1], wt[1][0], wt[1][1]}), wf1 = as_frag(u32x4{wt[2][0], wt[2][1], wt[3][0], wt[3][1]});
#pragma unroll
        for (int t = 0; t < 2; ++t)
          *(u32x4*)(VF + (cw * 2 + t) * 1024 + lane * 16) = u32x4{vf[0][t][0], vf[0][t][1], vf[1][t][0], vf[1][t][1]};
        {
          u32x4 pk[2][2];
          phi_map2<0>(kf, rs, wf0, wf1, pk, []() {});
          char* d0 = PK + cw * 4096 + fr * 64 + ((fr >> 2) & 1) * 32 + ((fq ^ ((fr >> 3) * 2)) * 8);
          char* d1 = PK + cw * 4096 + fr * 64 + (((fr >> 2) & 1) ^ 1) * 32 + ((fq ^ ((fr >> 3) * 2)) * 8);
#pragma unroll
          for (int m = 0; m < 2; ++m) {
            *(u32x2*)(d0 + m * 2048) = u32x2{pk[m][0][0], pk[m][0][1]};
            *(u32x2*)(d0 + m * 2048 + 1024) = u32x2{pk[m][0][2], pk[m][0][3]};
            *(u32x2*)(d1 + m * 2048) = u32x2{pk[m][1][0], pk[m][1][1]};
            *(u32x2*)(d1 + m * 2048 + 1024) = u32x2{pk[m][1][2], pk[m][1][3]};
          }
        }
        PRIO(1);
        sync_lds();
        STAMP(3);
        const unsigned rec = ar + AR_KV + (unsigned)head * KVREC;
        {
          const u32x4 ones = u32x4{0x3f803f80u, 0x3f803f80u, 0x3f803f80u, 0x3f803f80u};
          f32x4 c0 = zero, c1 = zero;
          const char* ad = PK + (cw & 1) * 1024 + (fq * 4 + (fr >> 2)) * 64 + (((cw >> 1) ^ fq) & 1) * 32 + (((fr & 3) ^ (fq & 2)) * 8);
          const char* vd = VF + (cw >> 2) * 1024 + lane * 16;
          bf16x8 a[8], b0[8];
#pragma unroll
          for (int ks = 0; ks < 8; ++ks) {
            u32x2 a0 = trread(ad + ks * 4096), a1 = trread(ad + ks * 4096 + 2048);
            a[ks] = as_frag(u32x4{a0[0], a0[1], a1[0], a1[1]});
            b0[ks] = *(const bf16x8*)(vd + ks * 2048);
          }
          SCHED0;
#pragma unroll
          for (int ks = 0; ks < 8; ks += 2) {
            c0 = mfma16(a[ks], b0[ks], c0);
            c1 = mfma16(a[ks + 1], b0[ks + 1], c1);
          }
          c0 += c1;
          const unsigned dst = rec + (unsigned)bm * (32 * KVREC);
          const unsigned p0 = pk2(c0[0], c0[1]), p1 = pk2(c0[2], c0[3]);
          const auto q0 = __builtin_amdgcn_permlane16_swap(p0, p0, false, false), q1 = __builtin_amdgcn_permlane16_swap(p1, p1, false, false);
          st16(wsr, fq & 1 ? OOB : dst + cw * 512 + (fq >> 1) * 256 + fr * 16, clean(u32x4{q0[0], q1[0], q0[1], q1[1]}));
          SCHED0;
          if (cw < 4) {
            f32x4 cz = zero, cy = zero;
#pragma unroll
            for (int ks = 0; ks < 8; ks += 2) {
              cz = mfma16(a[ks], as_frag(ones), cz);
              cy = mfma16(a[ks + 1], as_frag(ones), cy);
            }
            cz += cy;
            st16(wsr, fr ? OOB : dst + 4096 + cw * 64 + fq * 16, clean(__builtin_bit_cast(u32x4, cz)));
          }
        }
        SCHED0;
        STAMP(4);
        bar();
        if (QP) PRIO(3);
        const f32x4 cq[2] = {*(const f32x4*)(lcl + fq * 4), *(const f32x4*)(lcl + 16 + fq * 4)};
        fold_rows(0, qf, cq);
        u32x4 pq[2][2];
        u32x4 zp, zq;
        u32x4 part[4];
        const unsigned vk = (fq >> 1) * (32 * KVREC) + (fq & 1) * 256 + fr * 16;
        auto ldp = [&](int i) AI { part[i] = ld16s(wsr, vk, rec + i * (64 * KVREC) + cw * 512); };
        auto ldz = [&]() AI {
          zp = ld16(wsr, rec + fq * (32 * KVREC) + 4096 + fr * 16);
          zq = ld16(wsr, rec + (4 + fq) * (32 * KVREC) + 4096 + fr * 16);
        };
#pragma unroll
        for (int i = 0; i < 4; ++i) asm volatile("" : "=v"(part[i]));
        asm volatile("" : "=v"(zp), "=v"(zq));
        phi_map2<KPOS, QP>(qf, rs, wf0, wf1, pq, [&]() AI {
          if (QP || cw >= 4) {
            MEMFENCE;
            if (QP && cw == 0) ldz();
#pragma unroll
            for (int i = 0; i < 4; ++i) ldp(i);
            MEMFENCE;
          }
        });
        asm volatile("" : "+v"(pq[0][0]), "+v"(pq[0][1]), "+v"(pq[1][0]), "+v"(pq[1][1]));
        SCHED0;
        STAMP(5);
        char* kvf = smem + L_A2;
        if (!KPOS || (!QP && cw < 4)) {
          SLEEP(SLP_K);
          MEMFENCE;
          if (cw == 0) ldz();
#pragma unroll
          for (int i = 0; i < 4; ++i) ldp(i);
          MEMFENCE;
        }
        unsigned kw = sbase + L_A2 + v16 + (cw >> 1) * 1024 + (cw & 1) * 8;
        const unsigned one = (fq & 1) == (fr >> 3) ? (fr & 1 ? 0x3f800000u : 0x00003f80u) : 0u;
        u32x4 eye = u32x4{(fr & 6) == 0 ? one : 0u, (fr & 6) == 2 ? one : 0u, (fr & 6) == 4 ? one : 0u, (fr & 6) == 6 ? one : 0u};
        asm volatile("" : "+v"(kw), "+v"(eye));
        u32x2 kvs;
        auto nans = [&](f32x4 v) AI { return __builtin_amdgcn_ballot_w64(__builtin_isunordered(v[0], v[1])) | __builtin_amdgcn_ballot_w64(__builtin_isunordered(v[2], v[3])); };
        auto sums = [&]() AI {
          unsigned long long nan = 0;
          if (cw == 0) {
            f32x4 z = __builtin_bit_cast(f32x4, zp) + __builtin_bit_cast(f32x4, zq);
            nan = nans(z);
            float z0 = z[0], z1 = z[1], z2 = z[2], z3 = z[3];
            xred4<false>(z0, z1, z2, z3);
            *(u32x2*)(smem + L_Z + (fr >> 3) * 64 + (fr & 3) * 16 + ((fr >> 2) & 1) * 8) = u32x2{pk2(z0, z1), pk2(z2, z3)};
          }
          f32x4 s4 = zero;
#pragma unroll
          for (int i = 0; i < 4; ++i) { s4 = mfma16(as_frag(eye), as_frag(part[i]), s4); SCHED0; }
          kvs = u32x2{pk2(s4[0], s4[1]), pk2(s4[2], s4[3])};
          asm volatile("" : "+v"(kvs));
          return nan | __builtin_amdgcn_ballot_w64(s4[0] != s4[0]);
        };
        if (__builtin_expect(sums() != 0, 0)) {
          for (;;) {
            bool again = false;
            if (cw == 0 && poisoned(umax4(umax4(zp), zq))) { again = true; ldz(); }
#pragma unroll
            for (int i = 0; i < 4; ++i)
              if (poisoned(umax4(part[i]))) { again = true; ldp(i); }
            __builtin_amdgcn_s_waitcnt(0x0f70);
            MEMFENCE;
            if (!again || --budget <= 0) break;
            COUNT(2);
          }
          sums();
        }
        STAMP(6);
        *(LDS(u32x2)*)kw = kvs;
        sync_lds();
        STAMP(7);
        bf16x8 kvr[2][2], zf[2];
#pragma unroll
        for (int s = 0; s < 2; ++s) {
          kvr[0][s] = *(const bf16x8*)(kvf + (0 * 2 + s) * 1024 + lane * 16);
          kvr[1][s] = *(const bf16x8*)(kvf + (1 * 2 + s) * 1024 + lane * 16);
          zf[s] = *(const bf16x8*)(smem + L_Z + s * 64 + fq * 16);
        }
        u32x4 oo[2];
#pragma unroll
        for (int m = 0; m < 2; ++m) {
          f32x4 c0 = mfma16(kvr[0][1], as_frag(pq[m][1]), mfma16(kvr[0][0], as_frag(pq[m][0]), zero));
          f32x4 c1 = mfma16(kvr[1][1], as_frag(pq[m][1]), mfma16(kvr[1][0], as_frag(pq[m][0]), zero));
          f32x4 cd = mfma16(zf[1], as_frag(pq[m][1]), mfma16(zf[0], as_frag(pq[m][0]), zero));
          const float iden = __builtin_amdgcn_rcpf(cd[0] + 1e-6f);
          c0 *= iden; c1 *= iden;
          oo[m] = put_o(m, c0, c1, 0);
        }
        hold(oo[0], oo[1]);
        STAMP(8);
        asm volatile("" : "=v"(acc[0][0]), "=v"(acc[0][1]), "=v"(acc[0][2]), "=v"(acc[0][3]), "=v"(acc[0][4]), "=v"(acc[0][5]));
        asm volatile("" : "=v"(acc[1][0]), "=v"(acc[1][1]), "=v"(acc[1][2]), "=v"(acc[1][3]), "=v"(acc[1][4]), "=v"(acc[1][5]));
        asm volatile("" : "=v"(ck[0]), "=v"(ck[1]), "=v"(mv[0]), "=v"(mv[1]), "=v"(xv[0]), "=v"(xv[1]));
        asm volatile("" : "=v"(mean[0]), "=v"(mean[1]), "=v"(rs[0]), "=v"(rs[1]), "=v"(cv[0]), "=v"(cv[1]));
      }
      int kind = layer & 1;
      asm volatile("" : "+s"(kind));
      if (kind) {
        char* ex = scr;
        fold_rows(1, kf, ck); fold_v(false);
        const unsigned hrec = ar + AR_KV + (unsigned)(bm * 32 + head) * KVREC;
        {
          const u32x4 vl = u32x4{vf[1][0][0], vf[1][0][1], vf[1][1][0], vf[1][1][1]};
          if (cw == 7) {
            st16s(wsr, v16, hrec, clean(kf[1]));
            STPAD;
            st16s(wsr, v16 + 1024, hrec, clean(vl));
            STPAD;
          }
          char* e2 = ex + (cw + 1) * 2048;
          *(u32x4*)(e2 + lane * 16) = kf[1];
          *(u32x2*)(e2 + 1024 + lane * 8) = vf[1][0];
          *(u32x2*)(e2 + 1536 + lane * 8) = vf[1][1];
          MEMFENCE;
          lflag[cw] = layer;
          MEMFENCE;
          PRIO(1);
        }
        SCHED0;
        fold_rows(0, qf, xv);
        u32x4 kp;
        u32x4 va0 = __builtin_shufflevector(vf[0][0], vf[0][0], -1, -1, 0, 1), vb0 = __builtin_shufflevector(vf[0][1], vf[0][1], -1, -1, 0, 1);
        auto setp = [&](u32x4& d, u32x2 v) AI { d[0] = v[0]; d[1] = v[1]; };
        auto ldh = [&]() AI {
          MEMFENCE;
          kp = ld16s(wsr, v16, hrec - 32 * KVREC);
          setp(va0, __builtin_amdgcn_raw_buffer_load_b64(wsr, v16 + 1024, hrec - 32 * KVREC, 16));
          MEMFENCE;
          setp(vb0, __builtin_amdgcn_raw_buffer_load_b64(wsr, v16 + 1032, hrec - 32 * KVREC, 16));
          MEMFENCE;
        };
        unsigned fl = 0;
        auto rdl = [&]() AI {
          MEMFENCE;
          fl = lflag[cw - 1];
          MEMFENCE;
          kp = *(const u32x4*)(ex + cw * 2048 + lane * 16);
          setp(va0, *(const u32x2*)(ex + cw * 2048 + 1024 + lane * 8));
          MEMFENCE;
          setp(vb0, *(const u32x2*)(ex + cw * 2048 + 1536 + lane * 8));
          MEMFENCE;
        };
        auto prev = [&]() AI {
          if (cw > 0) {
            do { SLEEP(1); rdl(); __builtin_amdgcn_s_waitcnt(0xc07f); } while (__builtin_amdgcn_readfirstlane(fl) != (unsigned)layer && --budget > 0);
          } else if (bm > 0) {
            SLEEP(SLP_HX);
            do { ldh(); __builtin_amdgcn_s_waitcnt(0x0f70); }
            while (poisoned(umax4(umax4(kp), u32x4{va0[0], va0[1], vb0[0], vb0[1]})) && --budget > 0);
          } else {
            kp = u32x4{0u, 0u, 0u, 0u};
            setp(va0, u32x2{0u, 0u}); setp(vb0, u32x2{0u, 0u});
          }
          asm volatile("" : "+v"(kp), "+v"(va0), "+v"(vb0));
        };
        const float scale = 0.17677669529663687f * LOG2E;
        const bool odd = fr & 1;
        const bool own0 = fr >= fq * 4 + (fr & 1), own1 = fr >= fq * 4 + (fr & 1) + 2;
        const bool nopre = (bm == 0) && (cw == 0);
        float l[2][2], mx[2], rk[2][2], sum[2];
#pragma unroll
        for (int m = 0; m < 2; ++m) {
          const float* rb = lrstd + (cw * 2 + m) * 16 + fq * 4 + (fr & 1);
          rk[m][0] = rb[own0 ? 16 : 0];
          rk[m][1] = rb[own1 ? 18 : 2];
        }
        float qk[2][2];
#pragma unroll
        for (int m = 0; m < 2; ++m) { const float qs = rs[m] * scale; qk[m][0] = qs * rk[m][0]; qk[m][1] = qs * rk[m][1]; }
        const unsigned e0 = odd ? 0x01000c0cu : 0x0c0c0100u, e1 = odd ? 0x03020c0cu : 0x0c0c0302u, ez = 0x0c0c0c0cu;
        u32x4 sel = u32x4{own0 ? ez : e0, own1 ? ez : e1, own0 ? e0 : ez, own1 ? e1 : ez};
        u32x4 va1 = u32x4{vf[0][0][0], vf[0][0][1], vf[1][0][0], vf[1][0][1]}, vb1 = u32x4{vf[0][1][0], vf[0][1][1], vf[1][1][0], vf[1][1][1]};
        float off0 = nopre && !own0 ? -3.0e38f : 0.f, off1 = nopre && !own1 ? -3.0e38f : 0.f;
        auto score = [&](int m) AI {
          f32x4 s0 = mfma16(as_frag(m == 0 ? kp : kf[0]), as_frag(qf[m]), zero);
          f32x4 s1 = mfma16(as_frag(kf[m]), as_frag(qf[m]), zero);
          float a = own0 ? (odd ? s1[1] : s1[0]) : (odd ? s0[1] : s0[0]);
          float b = own1 ? (odd ? s1[3] : s1[2]) : (odd ? s0[3] : s0[2]);
          if (m == 0) { l[m][0] = __builtin_fmaf(a, qk[m][0], off0); l[m][1] = __builtin_fmaf(b, qk[m][1], off1); }
          else { l[m][0] = a * qk[m][0]; l[m][1] = b * qk[m][1]; }
          mx[m] = fmx(l[m][0], l[m][1]);
        };
        auto expo = [&](int m) AI {
          l[m][0] = __builtin_amdgcn_exp2f(l[m][0] - mx[m]);
          l[m][1] = __builtin_amdgcn_exp2f(l[m][1] - mx[m]);
          sum[m] = l[m][0] + l[m][1];
        };
        f32x4 ov[2][2];
        auto pv = [&](int m) AI {
          const unsigned pr = pk2(mul1(mul1(l[m][0], rk[m][0]), sum[m]), mul1(mul1(l[m][1], rk[m][1]), sum[m]));
          const u32x4 pf = u32x4{__builtin_amdgcn_perm(pr, pr, sel[0]), __builtin_amdgcn_perm(pr, pr, sel[1]),
                                 __builtin_amdgcn_perm(pr, pr, sel[2]), __builtin_amdgcn_perm(pr, pr, sel[3])};
          ov[m][0] = mfma16(as_frag(m ? va1 : va0), as_frag(pf), zero);
          ov[m][1] = mfma16(as_frag(m ? vb1 : vb0), as_frag(pf), zero);
        };
        asm volatile("" : "+v"(qf[0]), "+v"(qf[1]), "+v"(va0), "+v"(vb0));
        SCHED0;
        if (cw > 0) rdl();
        SCHED0;
        score(1);
        asm volatile("" : "+v"(qf[0]), "+v"(qk[0][0]), "+v"(qk[0][1]), "+v"(mx[1]), "+v"(l[1][0]), "+v"(l[1][1]));
        asm volatile("" : "+v"(sel), "+v"(va1), "+v"(vb1), "+v"(off0), "+v"(off1));
        SCHED0;
        STAMP(3);
        if (__builtin_expect(cw == 0 || __builtin_amdgcn_readfirstlane(fl) != (unsigned)layer, 0)) prev();
        score(0);
        xred2<true>(mx[0], mx[1]);
        expo(0); expo(1);
        xred2<false, true>(sum[0], sum[1]);
        pv(0); pv(1);
        SCHED0;
        const u32x4 o0 = put_o(0, ov[0][0], ov[0][1], 1), o1 = put_o(1, ov[1][0], ov[1][1], 1);
        hold(o0, o1);
        STAMP(4);
      }
    }
    {
      LANE_VARS
      const int mt = bm * 16 + cw * 2 + cm;
      u32x4 xr[2];
#pragma unroll
      for (int j = 0; j < 2; ++j) xr[j] = ld16s(wsr, v16, ar + AR_X + xt_off(mt, cn * 2 + j));
      const unsigned so0 = WS_AR + AR_O + xt_off(mt, 0);
      const unsigned opar = layer & 1;
      auto diff = [&](u32x4 v) AI { return ((v[0] ^ opar) | (v[2] ^ opar)) & 1u; };
      auto dif3 = [&](u32x4 v) AI { return __builtin_amdgcn_bitop3_b32(v[0], v[2], opar, 0x7e); };
      auto any = [&](unsigned d) AI { return __builtin_amdgcn_ballot_w64(d != 0) != 0; };
      const int g0 = cm;
      u32x4 a[RO];
      auto refill = [&](auto ic, auto wc, unsigned cur, unsigned nxt) AI {
        constexpr int I = decltype(ic)::value, W = decltype(wc)::value;
        do {
          COUNT(1);
          SLEEP(SLP_R);
          MEMFENCE;
          a[I] = ld16s(wsr, v16, cur + I * 16384);
          bool u[RO];
#pragma unroll
          for (int j = I + 1; j < I + W; ++j) u[j % RO] = any(diff(a[j % RO]));
          SCHED0;
#pragma unroll
          for (int j = I + 1; j < I + W; ++j)
            if (u[j % RO]) a[j % RO] = ld16s(wsr, v16, (j < RO ? cur : nxt) + (j % RO) * 16384);
          __builtin_amdgcn_s_waitcnt(0x0f70);
          MEMFENCE;
        } while (any(diff(a[I]) | diff(a[I + 1])) && --budget > 0);
      };
      const unsigned sf = so0 + g0 * (RO * 16384);
      const bool late = isB && cw == 0;
      if (late) bar();
      if (cn < 8) { if (late) SLEEP(SLP_O0); else SLEEP(SLP_O); }
      else { if (late) SLEEP(SLP_O0D); else SLEEP(SLP_OD); }
#pragma unroll
      for (int j = 0; j < 4; ++j) { a[j] = ld16s(wsr, v16, sf + j * 16384); SCHED0; }
      if (!late) bar();
      STAMP(9);
      bf16x8 wf[2][4];
      const char* pw0 = smem + L_STG + v16;
#pragma unroll
      for (int n = 0; n < 8; ++n) wf[n >> 2][n & 3] = *(const bf16x8*)(pw0 + g0 * (RO * 4096) + n * 1024);
      f32x4 acc[4];
#pragma unroll
      for (int n = 0; n < 4; ++n)
        acc[n] = f32x4{bflo(xr[n >> 1][(n & 1) * 2]), bfhi(xr[n >> 1][(n & 1) * 2]), bflo(xr[n >> 1][(n & 1) * 2 + 1]), bfhi(xr[n >> 1][(n & 1) * 2 + 1])};
      asm volatile("" : "+v"(acc[0]), "+v"(acc[1]), "+v"(acc[2]), "+v"(acc[3]));
      SCHED0;
      STAMP(10);
      if (any(diff(a[0]) | diff(a[1]))) refill(IC<0>{}, IC<4>{}, sf, sf);
      SCHED0;
#pragma unroll
      for (int j = 4; j < RO; ++j) { a[j] = ld16s(wsr, v16, sf + j * 16384); SCHED0; }
      STAMP(11);
      bool bad = false;
      auto group = [&](auto lastc, int g) AI {
        constexpr bool LAST = decltype(lastc)::value;
        const int gc = g ^ g0, gn = (g + 1) ^ g0;
        const unsigned sc = so0 + gc * (RO * 16384), so = so0 + gn * (RO * 16384);
        const char* pc = pw0 + gc * (RO * 4096);
        const char* pn = pw0 + gn * (RO * 4096);
        static_for<RO>([&](auto ic) AI {
          constexpr int i = decltype(ic)::value;
          SCHED0;
          if constexpr (i % 2 == 0)
            if (__builtin_expect(bad, 0)) refill(ic, IC<(LAST && i + 4 > RO) ? RO - i : 4>{}, sc, so);
          SCHED0;
          unsigned mx = 0, my = 0;
          static_for<4>([&](auto nc) AI {
            constexpr int n = decltype(nc)::value;
            mfma_acc(acc[n], wf[i & 1][n], as_frag(a[i]));
            SCHED0;
            if constexpr (i + 2 < RO) wf[i & 1][n] = *(const bf16x8*)(pc + (i + 2) * 4096 + n * 1024);
            else if constexpr (!LAST) wf[i & 1][n] = *(const bf16x8*)(pn + (i + 2 - RO) * 4096 + n * 1024);
            if constexpr (!(LAST && i == RO - 1) && (i & 1)) {
              if constexpr (n == 0) mx = dif3(a[(i + 1) % RO]);
              if constexpr (n == 1) my = dif3(a[(i + 2) % RO]);
              if constexpr (n == 2) bad = any((mx | my) & 1u);
            }
            if constexpr (n == 3 && !LAST) a[i] = ld16s(wsr, v16, so + i * 16384);
            SCHED0;
          });
        });
      };
#pragma unroll 1
      for (int g = 0; g < 3; ++g) group(std::false_type{}, g);
      group(std::true_type{}, 3);
      mfma_done();
      STAMP(12);
#pragma unroll
      for (int j = 0; j < 2; ++j)
#pragma unroll
        for (int e = 0; e < 4; ++e) xr[j][e] = pk2(acc[j * 2 + (e >> 1)][(e & 1) * 2], acc[j * 2 + (e >> 1)][(e & 1) * 2 + 1]);
      if (layer == 3) {
        u32x4 yo[2];
#pragma unroll
        for (int j = 0; j < 2; ++j) {
          auto u = __builtin_amdgcn_permlane16_swap(xr[j][0], xr[j][2], false, false);
          auto v = __builtin_amdgcn_permlane16_swap(xr[j][1], xr[j][3], false, false);
          yo[j] = u32x4{u[0], v[0], u[1], v[1]};
        }
        const unsigned vo = fr * 2048 + (fq & 1) * 32 + (fq >> 1) * 16;
        __builtin_amdgcn_raw_buffer_store_b128(yo[0], mk_rsrc(p.out, 4 * MBY), vo, mt * 32768 + cn * 128, 2);
        __builtin_amdgcn_raw_buffer_store_b128(yo[1], mk_rsrc(p.out, 4 * MBY), vo + 64, mt * 32768 + cn * 128, 2);
        STAMP(13);
        STAMP(14);
        STAMP_DUMP;
        __builtin_amdgcn_endpgm();
      }
      const u32x2 stat = put_x(xr, v16, ar + ARENA + AR_X + xt_off(mt, cn * 2));
      __builtin_amdgcn_raw_buffer_store_b64(stat, wsr, fq ? OOB : fr * 8, ar + ARENA + AR_ST + (mt * 16 + cn) * 128, 16);
      MEMFENCE;
      STAMP(14);
    }
  }
}

extern "C" void kernel_launch(void* const* d_in, const int* in_sizes, int n_in, void* d_out, int out_size,
                              void* d_ws, size_t ws_size, hipStream_t stream) {
  (void)hipFuncSetAttribute((const void*)mega_kernel, hipFuncAttributeMaxDynamicSharedMemorySize, MEGA_LDS);
  MegaParams m;
  m.ws = (char*)d_ws; m.out = (u16*)d_out; m.x = (const u16*)d_in[0]; m.wphi = (const u16*)d_in[2];
  m.wqkvA = (const u16*)d_in[1]; m.wqkvB = (const u16*)d_in[5];
  m.woA = (const u16*)d_in[3]; m.woB = (const u16*)d_in[6];
  mega_kernel<<<256, 768, MEGA_LDS, stream>>>(m);
}
```

```cpp
#include <hip/hip_runtime.h>
#include <stdint.h>
#include <type_traits>

#define DI __device__ __forceinline__
#define AI __attribute__((always_inline))
typedef unsigned short u16;
typedef __attribute__((ext_vector_type(8))) short bf16x8;
typedef __attribute__((ext_vector_type(4))) short s16x4;
typedef __attribute__((ext_vector_type(4))) float f32x4;
typedef __attribute__((ext_vector_type(2))) float f32x2;
typedef __attribute__((ext_vector_type(2))) __bf16 bf2;
typedef __attribute__((ext_vector_type(4))) unsigned u32x4;
typedef __attribute__((ext_vector_type(2))) unsigned u32x2;
typedef __attribute__((address_space(3))) s16x4* lds_s16x4_ptr;
#define LDS(T) __attribute__((address_space(3))) T
template <int N> using IC = std::integral_constant<int, N>;

constexpr float LOG2E = 1.4426950408889634f;

DI unsigned pk2(float a, float b) {
  f32x2 v = {a, b};
  bf2 r = __builtin_convertvector(v, bf2);
  return __builtin_bit_cast(unsigned, r);
}
DI float bflo(unsigned u) { return __uint_as_float(u << 16); }
DI float bfhi(unsigned u) { return __uint_as_float(u & 0xffff0000u); }
DI f32x4 mfma16(bf16x8 a, bf16x8 b, f32x4 c) { return __builtin_amdgcn_mfma_f32_16x16x32_bf16(a, b, c, 0, 0, 0); }
DI void mfma_acc(f32x4& c, bf16x8 a, bf16x8 b) { asm volatile("v_mfma_f32_16x16x32_bf16 %0, %1, %2, %0" : "+v"(c) : "v"(a), "v"(b)); }
DI void mfma_done() { asm volatile("s_nop 7\n\ts_nop 1" ::: "memory"); __builtin_amdgcn_sched_barrier(0); }
DI bf16x8 as_frag(u32x4 v) { return __builtin_bit_cast(bf16x8, v); }
DI float dot2(unsigned a, unsigned b, float c) {
  return __builtin_amdgcn_fdot2_f32_bf16(__builtin_bit_cast(bf2, a), __builtin_bit_cast(bf2, b), c, false);
}
DI u32x2 trread(const char* p) { return __builtin_bit_cast(u32x2, __builtin_amdgcn_ds_read_tr16_b64_v4i16((lds_s16x4_ptr)p)); }
DI u32x2 trl(unsigned a) { return __builtin_bit_cast(u32x2, __builtin_amdgcn_ds_read_tr16_b64_v4i16((lds_s16x4_ptr)a)); }
DI float fmx(float a, float b) { return __builtin_elementwise_maximum(a, b); }
DI float fmn(float a, float b) { return __builtin_elementwise_minimum(a, b); }

DI float mul1(float a, float b) { float r; asm("v_mul_f32 %0, %1, %2" : "=v"(r) : "v"(a), "v"(b)); return r; }
DI float add1(float a, float b) { float r; asm("v_add_f32 %0, %1, %2" : "=v"(r) : "v"(a), "v"(b)); return r; }
DI f32x4 mul4(f32x4 a, f32x4 b) {
  float r0, r1, r2, r3;
  asm("v_mul_f32 %0, %4, %8\n\tv_mul_f32 %1, %5, %9\n\tv_mul_f32 %2, %6, %10\n\tv_mul_f32 %3, %7, %11"
      : "=&v"(r0), "=&v"(r1), "=&v"(r2), "=&v"(r3) : "v"(a[0]), "v"(a[1]), "v"(a[2]), "v"(a[3]), "v"(b[0]), "v"(b[1]), "v"(b[2]), "v"(b[3]));
  return f32x4{r0, r1, r2, r3};
}
DI f32x4 add4(f32x4 a, f32x4 b) { return f32x4{add1(a[0], b[0]), add1(a[1], b[1]), add1(a[2], b[2]), add1(a[3], b[3])}; }

template <bool IS_MAX, bool INV = false>
DI void xred2(float& a, float& b) {
  auto r = __builtin_amdgcn_permlane32_swap(__float_as_uint(a), __float_as_uint(b), false, false);
  float c = IS_MAX ? fmx(__uint_as_float(r[0]), __uint_as_float(r[1])) : __uint_as_float(r[0]) + __uint_as_float(r[1]);
  auto q = __builtin_amdgcn_permlane16_swap(__float_as_uint(c), __float_as_uint(c), false, false);
  c = IS_MAX ? fmx(__uint_as_float(q[0]), __uint_as_float(q[1])) : __uint_as_float(q[0]) + __uint_as_float(q[1]);
  if (INV) c = __builtin_amdgcn_rcpf(c);
  auto w = __builtin_amdgcn_permlane32_swap(__float_as_uint(c), __float_as_uint(c), false, false);
  a = __uint_as_float(w[0]); b = __uint_as_float(w[1]);
}
template <bool IS_MAX, bool INV = false>
DI void xred4(float& a, float& b, float& c, float& d) {
  auto op = [](unsigned x, unsigned y) { return IS_MAX ? fmx(__uint_as_float(x), __uint_as_float(y)) : __uint_as_float(x) + __uint_as_float(y); };
  auto r0 = __builtin_amdgcn_permlane32_swap(__float_as_uint(a), __float_as_uint(b), false, false);
  auto r1 = __builtin_amdgcn_permlane32_swap(__float_as_uint(c), __float_as_uint(d), false, false);
  float ab = op(r0[0], r0[1]), cd = op(r1[0], r1[1]);
  auto q = __builtin_amdgcn_permlane16_swap(__float_as_uint(ab), __float_as_uint(cd), false, false);
  float e = op(q[0], q[1]);
  if (INV) e = __builtin_amdgcn_rcpf(e);
  auto w = __builtin_amdgcn_permlane16_swap(__float_as_uint(e), __float_as_uint(e), false, false);
  auto u = __builtin_amdgcn_permlane32_swap(w[0], w[0], false, false);
  auto v = __builtin_amdgcn_permlane32_swap(w[1], w[1], false, false);
  a = __uint_as_float(u[0]); b = __uint_as_float(u[1]); c = __uint_as_float(v[0]); d = __uint_as_float(v[1]);
}

DI unsigned xt_off(int mt, int ks) { return (unsigned)((((mt >> 4) * 32 + ks) * 16 + (mt & 15)) << 10); }

#define GLDS_PRE "s_mov_b32 %0, m0\n\ts_mov_b32 m0, %3\n\ts_nop 2\n\t"
#define GLDS_POST "s_mov_b32 m0, %0"
#define GLDS_OPS : "=&s"(keep) : "v"(voff), "s"(sbase), "s"(lds) : "memory"
#define GL1(c) "global_load_lds_dwordx4 %1, %2" c "\n\t"
#define GL2(c) GL1(c) "global_load_lds_dwordx4 %1, %2 offset:1024" c "\n\t"
#define GL3(c) GL2(c) "global_load_lds_dwordx4 %1, %2 offset:2048" c "\n\t"
#define GL4(c) GL3(c) "global_load_lds_dwordx4 %1, %2 offset:3072" c "\n\t"
template <int N, bool SC1 = false>
DI void glds_run(unsigned voff, const void* sbase, unsigned lds) {
  unsigned keep;
  if constexpr (SC1) {
    if constexpr (N == 2) asm volatile(GLDS_PRE GL2(" sc1") GLDS_POST GLDS_OPS);
    else if constexpr (N == 3) asm volatile(GLDS_PRE GL3(" sc1") GLDS_POST GLDS_OPS);
    else asm volatile(GLDS_PRE GL4(" sc1") GLDS_POST GLDS_OPS);
  } else {
    static_assert(SC1 || N == 1, "");
    asm volatile(GLDS_PRE GL1("") GLDS_POST GLDS_OPS);
  }
}
template <int N> DI void wait_vm_lgkm0() { asm volatile("s_waitcnt vmcnt(%0) lgkmcnt(0)" ::"n"(N) : "memory"); }
template <int N> DI void wait_vm() { asm volatile("s_waitcnt vmcnt(%0)" ::"n"(N) : "memory"); }
DI void drain() { asm volatile("s_waitcnt vmcnt(0)" ::: "memory"); __builtin_amdgcn_s_waitcnt(0x0f70); }
DI void bar() { asm volatile("" ::: "memory"); __builtin_amdgcn_s_barrier(); asm volatile("" ::: "memory"); }
DI void sync_lds() { asm volatile("s_waitcnt lgkmcnt(0)" ::: "memory"); bar(); }
#define SCHED0 __builtin_amdgcn_sched_barrier(0)
#define STPAD do { SCHED0; asm volatile("s_nop 1"); SCHED0; } while (0)
#define SLEEP(n) do { if (n) __builtin_amdgcn_s_sleep(n); } while (0)
#define PRIO(n) __builtin_amdgcn_s_setprio(n)

template <int POS, int PR = 0, typename F>
DI void phi_map2(const u32x4* in, const float* rs, bf16x8 wf0, bf16x8 wf1, u32x4 (*out)[2], F mid) {
  const f32x4 zero = f32x4{0.f, 0.f, 0.f, 0.f};
  float x[2][8], mx[2], mn[2];
#pragma unroll
  for (int m = 0; m < 2; ++m) {
    f32x4 r0 = mfma16(wf0, as_frag(in[m]), zero), r1 = mfma16(wf1, as_frag(in[m]), zero);
#pragma unroll
    for (int j = 0; j < 4; ++j) { x[m][j] = r0[j]; x[m][4 + j] = r1[j]; }
  }
#pragma unroll
  for (int m = 0; m < 2; ++m) {
    mx[m] = fmx(fmx(fmx(x[m][0], x[m][1]), x[m][2]), fmx(fmx(x[m][3], x[m][4]), fmx(fmx(x[m][5], x[m][6]), x[m][7])));
    mn[m] = -fmn(fmn(fmn(x[m][0], x[m][1]), x[m][2]), fmn(fmn(x[m][3], x[m][4]), fmn(fmn(x[m][5], x[m][6]), x[m][7])));
  }
  xred4<true>(mx[0], mn[0], mx[1], mn[1]);
  if (PR == 3) { asm volatile("" : "+v"(mx[0]), "+v"(mx[1])); SCHED0; PRIO(2); SCHED0; }
  f32x2 e[2][8];
  float sp[2], sn[2];
#pragma unroll
  for (int m = 0; m < 2; ++m) {
    const float sl = rs[m] * LOG2E;
    const f32x2 l2 = {sl, -sl};
    const f32x2 off = f32x2{mx[m], mn[m]} * (-sl);
#pragma unroll
    for (int j = 0; j < 8; ++j) {
      f32x2 a = __builtin_elementwise_fma(f32x2{x[m][j], x[m][j]}, l2, off);
      e[m][j] = f32x2{__builtin_amdgcn_exp2f(a[0]), __builtin_amdgcn_exp2f(a[1])};
    }
    f32x2 sm = ((e[m][0] + e[m][1]) + (e[m][2] + e[m][3])) + ((e[m][4] + e[m][5]) + (e[m][6] + e[m][7]));
    sp[m] = sm[0]; sn[m] = sm[1];
    if (PR && m == (PR & 1)) { asm volatile("" : "+v"(sp[m]), "+v"(mx[1])); SCHED0; PRIO(PR == 3 ? 1 : 2); SCHED0; }
    if (PR == 2 && m == 1) { asm volatile("" : "+v"(sp[m])); SCHED0; PRIO(1); SCHED0; }
    if (POS == 2 + m) { asm volatile("" : "+v"(sp[m]), "+v"(mx[1])); SCHED0; mid(); SCHED0; }
  }
  xred4<false, true>(sp[0], sn[0], sp[1], sn[1]);
  if (POS == 4) { asm volatile("" : "+v"(sp[0]), "+v"(sp[1])); SCHED0; mid(); SCHED0; }
#pragma unroll
  for (int m = 0; m < 2; ++m) {
    const f32x2 inv = {sp[m], sn[m]};
#pragma unroll
    for (int j = 0; j < 8; ++j) e[m][j] *= inv;
    out[m][0] = u32x4{pk2(e[m][0][0], e[m][1][0]), pk2(e[m][2][0], e[m][3][0]), pk2(e[m][4][0], e[m][5][0]), pk2(e[m][6][0], e[m][7][0])};
    out[m][1] = u32x4{pk2(e[m][0][1], e[m][1][1]), pk2(e[m][2][1], e[m][3][1]), pk2(e[m][4][1], e[m][5][1]), pk2(e[m][6][1], e[m][7][1])};
  }
}

#define SLP_X0 8
#define SLP_X1 14
#define SLP_XD0 3
#define SLP_XD1 9
#define SLP_O 22
#define SLP_OD 12
#define SLP_O0 8
#define SLP_O0D 2
#define SLP_K 4
#define SLP_R 4
#define SLP_HX 1
#define SLP_Q 4
#define SLP_P 40
#define SLP_E 8
#define WA 1
#define WB 4
#define SLP_N 0
#define SLP_4 13
#define KPOS 4
#define WP0 0
#define WP2 2
#define QP 0
#define SLP_G 16
#define RSP 0
#define SLP_A 7
#define NB1 0
#define SLP_1 40
#define LPP 1
#define NCK 3
#define SLP_2 0
#define LP5 0
#define SLP_W 0
#define NI 4
#define NJ NI
#define SPR0 2
#define SW0 0
#define LANEID __builtin_amdgcn_mbcnt_hi(~0u, __builtin_amdgcn_mbcnt_lo(~0u, 0u))
#define STAMP(i) do {} while (0)
#define STAMP_DUMP do {} while (0)
#define COUNT(k) do {} while (0)
#define PSTAMP(l) do { const int layer = l; (void)layer; STAMP(15); } while (0)
constexpr unsigned MBY = 1u << 20;
constexpr unsigned WS_WQ = 0;
constexpr unsigned WS_WO = 12 * MBY;
constexpr unsigned WS_AR = 16 * MBY, ARENA = 12 * MBY;
constexpr unsigned AR_X = 0, AR_ST = 4 * MBY, AR_O = 5 * MBY, AR_KV = 9 * MBY;
constexpr unsigned AR_L2 = AR_ST + 256 * 1024, AR_FL = AR_ST + 320 * 1024;
constexpr unsigned KVREC = 9 * 1024;
constexpr unsigned WS_CS = WS_AR + 4 * ARENA;
constexpr unsigned WS_F1 = WS_CS + 512 * 1024;
constexpr unsigned WS_F2 = WS_F1 + 64 * 1024;
constexpr unsigned WS_EP = WS_F2 + 2048;
constexpr unsigned WS_END = WS_EP + 128;
constexpr unsigned POISON = 0xffffffffu;
constexpr unsigned OOB = 0x80000000u;

typedef __amdgpu_buffer_rsrc_t rsrc_t;
DI rsrc_t mk_rsrc(const void* p, unsigned bytes) { return __builtin_amdgcn_make_buffer_rsrc((void*)p, 0, bytes, 0x00020000); }
DI void st16(rsrc_t r, unsigned off, u32x4 v) { __builtin_amdgcn_raw_buffer_store_b128(v, r, off, 0, 16); }
DI void st16s(rsrc_t r, unsigned voff, unsigned soff, u32x4 v) { __builtin_amdgcn_raw_buffer_store_b128(v, r, voff, soff, 16); }
DI u32x4 ld16(rsrc_t r, unsigned off) { return __builtin_amdgcn_raw_buffer_load_b128(r, off, 0, 16); }
DI u32x4 ld16s(rsrc_t r, unsigned voff, unsigned soff) { return __builtin_amdgcn_raw_buffer_load_b128(r, voff, soff, 16); }
DI u32x4 clean(u32x4 v) { return __builtin_elementwise_min(v, u32x4{POISON - 1, POISON - 1, POISON - 1, POISON - 1}); }
DI unsigned umax(unsigned a, unsigned b) { return a > b ? a : b; }
DI unsigned umin(unsigned a, unsigned b) { return a < b ? a : b; }
DI unsigned umax4(u32x4 v) { return umax(umax(umax(v[0], v[1]), v[2]), v[3]); }
DI unsigned umax4(unsigned m, u32x4 v) { return umax(umax(umax(umax(m, v[0]), v[1]), v[2]), v[3]); }
DI bool poisoned(unsigned m) { return __builtin_amdgcn_ballot_w64(m == POISON) != 0; }
#define MEMFENCE asm volatile("" ::: "memory")

#define BL(i, o) "buffer_load_dwordx4 %1, %2, %" #i " offen" o " nt lds\n\t"
#define BL_OPS(...) : "=&s"(keep) : "v"(voff), "s"(r), "s"(lds), __VA_ARGS__ : "memory"
template <int N>
DI void blds_run(rsrc_t r, unsigned voff, unsigned lds, unsigned s0, unsigned s1 = 0, unsigned s2 = 0, unsigned s3 = 0) {
  unsigned keep;
  if constexpr (N == 2) asm volatile(GLDS_PRE BL(4, "") BL(5, " offset:1024") GLDS_POST BL_OPS("s"(s0), "s"(s1)));
  else asm volatile(GLDS_PRE BL(4, "") BL(5, " offset:1024") BL(6, " offset:2048") BL(7, " offset:3072") GLDS_POST BL_OPS("s"(s0), "s"(s1), "s"(s2), "s"(s3)));
}

#define CODEB 35600
constexpr int MEGA_CODE_BYTES = CODEB;
DI unsigned code_prefetch(int i) {
  unsigned long long pc; int rel; unsigned d;
  asm volatile("s_getpc_b64 %0\n\ts_mov_b32 %1, _Z11mega_kernel10MegaParams@rel32@lo+4" : "=s"(pc), "=s"(rel));
  const unsigned long long a = pc + (long long)rel + (i * 192 + 192 <= MEGA_CODE_BYTES ? i * 192 : 0);
  asm volatile("s_load_dword %0, %1, 0x0\n\ts_load_dword %0, %1, 0x40\n\ts_load_dword %0, %1, 0x80" : "=&s"(d) : "s"(a) : "memory");
  return d;
}

struct MegaParams {
  char* ws;
  const u16* x;
  const u16* wqkvA;
  const u16* woA;
  const u16* wqkvB;
  const u16* woB;
  const u16* wphi;
  u16* out;
};

constexpr int QSLOT = 6 * 1024, QGRP = 4 * QSLOT;
constexpr int L_STG = QGRP;
constexpr int L_A2 = 16 * 1024, L_Z = 21 * 1024;
constexpr int L_RT = 4 * QGRP;
constexpr int L_PB = 0;
constexpr int L_RED = L_STG + 128 * 1024;
constexpr int L_WP = L_RED + 3072;
constexpr int L_STAMP = L_WP + 2048;
constexpr int MEGA_LDS = L_STAMP + 1536;
static_assert(MEGA_LDS <= 160 * 1024, "LDS map");

template <int I, int N, typename F> DI void static_for_(F&& f) {
  if constexpr (I < N) { f(IC<I>{}); static_for_<I + 1, N>(f); }
}
template <int N, typename F> DI void static_for(F&& f) { static_for_<0, N>(f); }

constexpr int NCHK = NCK;
constexpr int RO = 8;
constexpr int SPIN_BUDGET = 1 << 15;

__global__ __launch_bounds__(768, 3) void mega_kernel(MegaParams p) {
  extern __shared__ __attribute__((aligned(16))) char smem[];
  const int tid = threadIdx.x;
  const int wave = __builtin_amdgcn_readfirstlane(tid >> 6);
  const int xcd = blockIdx.x & 7, idx = blockIdx.x >> 3;
  int bm = (xcd >> 2) * 4 + (idx >> 3), head = ((xcd & 1) * 2 + ((xcd >> 1) & 1)) * 8 + (idx & 7);
  int cm = (xcd >> 1) & 1, cn = (xcd & 1) * 8 + (idx & 7);
  const int bm0 = bm, head0 = head, cm0 = cm, cn0 = cn;
  char* ws = p.ws;
  const u16 *px = p.x, *pq = p.wqkvA;
  asm volatile("" : "+s"(ws), "+s"(px), "+s"(pq));
  const int swv = wave == SW0 ? 0 : wave == 4 ? 1 : wave == 11 ? 2 : -1; (void)swv;
  float* lmean = (float*)(smem + L_RED);
  float* lrstd = lmean + 272;
  float* lcs = lrstd + 272;
  unsigned* lflag = (unsigned*)lmean;
  const rsrc_t wsr = mk_rsrc(ws, WS_END);
  int budget = SPIN_BUDGET;
  unsigned tag0 = 0, tag1 = 0;
  auto ld_ep = [&]() AI { MEMFENCE; return __builtin_amdgcn_raw_buffer_load_b64(wsr, 0, WS_EP, 16); };
  auto mk_tag = [&](u32x2 ep) AI {
    tag0 = __builtin_amdgcn_readfirstlane(ep[0]) + 0x9e3779b9u;
    tag1 = __builtin_amdgcn_readfirstlane(ep[1]) ^ 0x85ebca6bu;
  };
  const unsigned sbase = (unsigned)(size_t)smem;
  int L = wave, cw = wave - 4;
#define OPAQUE_S asm volatile("" : "+s"(L), "+s"(bm), "+s"(head), "+s"(cm), "+s"(cn));
#define LANE_VARS asm volatile("" : "+s"(cw), "+s"(bm), "+s"(head), "+s"(cm), "+s"(cn)); int lane_ = __builtin_amdgcn_mbcnt_hi(~0u, __builtin_amdgcn_mbcnt_lo(~0u, 0u)); asm volatile("" : "+v"(lane_)); \
  const int lane = lane_, fr = lane & 15, fq = lane >> 4; const unsigned v16 = lane * 16; (void)fr; (void)fq; (void)v16;
  auto put_x = [&](u32x4* xr, unsigned v16, unsigned dst) AI {
    float sm = 0.f, sq = 0.f;
#pragma unroll
    for (int j = 0; j < 2; ++j) {
      xr[j][0] = umin(xr[j][0], POISON - 1); xr[j][2] = umin(xr[j][2], POISON - 1);
      st16s(wsr, v16, dst + j * 16384, xr[j]);
    }
    SCHED0;
#pragma unroll
    for (int j = 0; j < 2; ++j)
#pragma unroll
      for (int e = 0; e < 4; ++e) { sm = dot2(xr[j][e], 0x3f803f80u, sm); sq = dot2(xr[j][e], xr[j][e], sq); }
    xred2<false>(sm, sq);
    return u32x2{__float_as_uint(sm), __float_as_uint(sq)};
  };

#define RT_VARS const bool tq = L < 3; const int tsg = tq ? (bm * 2 + (head >> 4)) * 3 + L : bm * 2 + (head >> 4), ks = (head & 15) + 16 * h;
  auto ld_rt = [&](const u16* w, int h, unsigned lds) AI {
    RT_VARS
    LANE_VARS
    const unsigned rs = tq ? 6144 : 2048, D = 8 * rs - 1024, o = ks * 32 * rs + tsg * 128;
    blds_run<4>(mk_rsrc(w, rs * 1024), (lane >> 3) * rs + ((lane & 7) ^ ((lane >> 3) & 6)) * 16, sbase + lds, o, o + D, o + 2 * D, o + 3 * D);
  };
  auto retile = [&](int side, int h, unsigned lds, f32x4* rcs) AI {
    RT_VARS
    LANE_VARS
    const unsigned dst = tq ? WS_WQ + side * (6 * MBY) + (unsigned)((((tsg & 15) * 2) * 32 + ks) * 6 + (tsg >> 4) * 2) * 1024
                            : WS_WO + side * (2 * MBY) + (unsigned)((tsg * 32 + ks) * 4) * 1024;
    const unsigned gstr = tq ? 32 * 6 * 1024 : 2048;
    const unsigned a0 = (lane >> 2) * 128 + ((lane >> 3) & 3) * 32 + (lane & 3) * 8;
#pragma unroll
    for (int pc = 0; pc < 4; ++pc) {
      const char* aa = smem + lds + (a0 ^ (pc * 32));
      u32x2 r0_ = trread(aa), r1_ = trread(aa + 2048);
      u32x4 v = u32x4{r0_[0], r0_[1], r1_[0], r1_[1]};
      st16s(wsr, v16 + (pc & 1) * 1024, dst + (pc >> 1) * gstr, v);
      if (rcs) rcs[pc] = mfma16(as_frag(u32x4{0x3f803f80u, 0x3f803f80u, 0x3f803f80u, 0x3f803f80u}), as_frag(v), rcs[pc]);
    }
    if (rcs && h == 1) {
      const f32x4 c0 = rcs[0], c1 = rcs[1], c2 = rcs[2], c3 = rcs[3];
      const float ca = fq & 1 ? c1[0] : c0[0], cb = fq & 1 ? c3[0] : c2[0], cc = fq & 2 ? cb : ca;
      if (tq) __builtin_amdgcn_raw_buffer_store_b32(__float_as_uint(cc), wsr, ((fq >> 1) * 1536 + (fq & 1) * 16 + fr) * 4,
                                                    WS_CS + ((side * 32 + (tsg & 15) * 2) * 1536 + (tsg >> 4) * 512 + (head & 15) * 32) * 4, 16);
    }
  };
  auto flag = [&](unsigned table) AI {
    if (LANEID == 0) __builtin_amdgcn_raw_buffer_store_b64(u32x2{tag0, tag1}, wsr, 0, table + (bm * 32 + head) * 8, 16);
  };
  auto poll = [&](unsigned table, auto between, int rows = 0) AI {
    LANE_VARS
    const int q = (umin(fq, 2) * 16 + (head >> 1)) * 43 >> 7;
    const unsigned vo = rows ? bm * 256 + (fr & rows) * 16 : fq == 3 ? bm * 256 + fr * 16 : q * 128 + (fr & 7) * 16;
    auto miss = [&](u32x4 f) AI { return __builtin_amdgcn_ballot_w64(((f[0] ^ tag0) | (f[1] ^ tag1) | (f[2] ^ tag0) | (f[3] ^ tag1)) != 0) != 0; };
    MEMFENCE;
    const u32x4 f0 = ld16s(wsr, vo, table);
    MEMFENCE;
    between();
    MEMFENCE;
    bool wait = miss(f0);
#pragma unroll 1
    while (wait && --budget > 0) {
      SLEEP(SLP_Q);
      MEMFENCE;
      wait = miss(ld16s(wsr, vo, table));
      asm volatile("" : "+s"(budget) :: "memory");
    }
    MEMFENCE;
  };
  auto ld_x = [&](int cx, unsigned lds) AI {
    LANE_VARS
    const unsigned o = (bm * 16 + (head >> 1)) * 32768 + cx * 128;
    blds_run<2>(mk_rsrc(px, 4 * MBY), (lane >> 3) * 2048 + ((lane & 7) ^ (lane >> 3)) * 16, sbase + lds, o, o + 15360);
  };
  auto st_x = [&](int cx, unsigned lds, int w) AI {
    LANE_VARS
    const int mt = bm * 16 + (head >> 1);
    u32x4 xr[2];
#pragma unroll
    for (int j = 0; j < 2; ++j) {
      const char* a = smem + lds + fr * 128 + (fq & 1) * 8;
      const u32x2 lo = *(const u32x2*)(a + ((j * 4 + (fq >> 1)) ^ (fr & 7)) * 16), hi = *(const u32x2*)(a + ((j * 4 + 2 + (fq >> 1)) ^ (fr & 7)) * 16);
      xr[j] = u32x4{lo[0], lo[1], hi[0], hi[1]};
    }
    const u32x2 stat = put_x(xr, v16, WS_AR + AR_X + xt_off(mt, cx * 2));
    if (fq == 0) *(u32x2*)(lmean + 16 + fr * 4 + w) = stat;
  };

  {
    const int lane = LANEID;
    const bool tq = wave < 4;
    const unsigned rs = tq ? 6144 : 2048, D = 8 * rs - 1024;
    const unsigned o = (tq ? (head & 15) * 32 : (bm * 16 + (head >> 1)) * 16) * rs + (tq ? (bm * 2 + (head >> 4)) * 3 + wave : (head & 1) * 4 + cw) * 128;
    const unsigned lds = sbase + L_PB + (tq ? 8192 + wave * 4096 : cw * 2048);
    const unsigned vo = (lane >> 3) * rs + ((lane & 7) ^ ((lane >> 3) & (tq ? 6 : 7))) * 16;
    const rsrc_t r = mk_rsrc(tq ? pq : px, tq ? 6 * MBY : 4 * MBY);
    if ((wave & 7) != 3 && wave < 8) blds_run<2>(r, vo, lds, o, o + D);
    if (wave < 3) blds_run<2>(r, vo, lds + 2048, o + 2 * D + 2048, o + 3 * D + 2048);
  }
  if (wave >= 8) {
    const unsigned cp = code_prefetch(idx * 9 + cw), cq = code_prefetch(idx * 9 + cw - 4);
    L = wave - 8;
    bar();
    ld_x(8 + (head & 1) * 4 + L, L_PB + 32768 + L * 2048);
    if (L < 3) ld_rt(pq, 1, L_PB + 20480 + L * 4096);
    sync_lds();
    asm volatile("; pi" :: "s"(cp), "s"(cq) : "memory");
    wait_vm<0>();
    PSTAMP(1);
    st_x(8 + (head & 1) * 4 + L, L_PB + 32768 + L * 2048, L * 64 + 2);
    if (L < 3) {
      const f32x4 t = *(const f32x4*)(smem + L_PB + 40960 + L * 1024 + LANEID * 16);
      f32x4 rca[4] = {{t[0], 0.f, 0.f, 0.f}, {t[1], 0.f, 0.f, 0.f}, {t[2], 0.f, 0.f, 0.f}, {t[3], 0.f, 0.f, 0.f}};
      retile(0, 1, L_PB + 20480 + L * 4096, rca);
    }
    asm volatile("s_waitcnt lgkmcnt(0)" ::: "memory");
    PSTAMP(2);
  } else if (wave >= 4) {
    wait_vm<0>();
    PSTAMP(1);
    st_x((head & 1) * 4 + cw, L_PB + cw * 2048, cw * 64);
    bar();
    wait_vm_lgkm0<0>();
    PSTAMP(2);
    bar();
    asm volatile("; pc" ::: "memory");
    SLEEP(SLP_E);
    L = 3;
    if (cw < 2) ld_rt(p.woA, cw, L_RT + 3 * 14336 + cw * 4096);
    if (cw == 2) ld_rt(p.woB, 0, L_RT + 3 * 14336 + 8192);
  } else if (wave < 3) {
    const u32x2 ep = ld_ep();
    wait_vm<1>();
    PSTAMP(1);
    f32x4 rca[4] = {};
    retile(0, 0, L_PB + 8192 + L * 4096, rca);
    *(f32x4*)(smem + L_PB + 40960 + L * 1024 + LANEID * 16) = f32x4{rca[0][0], rca[1][0], rca[2][0], rca[3][0]};
    bar();
    drain();
    mk_tag(ep);
    PSTAMP(2);
    sync_lds();
    asm volatile("; pl" ::: "memory");
    SLEEP(SLP_E);
    ld_rt(p.wqkvB, 0, L_RT + L * 14336);
    ld_rt(p.wqkvB, 1, L_RT + L * 14336 + 4096);
  } else {
    const u32x2 ep = ld_ep();
    const unsigned cp = code_prefetch(idx * 9 + 8);
    if (LANEID < 16) lflag[LANEID] = 0;
    wait_vm_lgkm0<0>();
    asm volatile("" :: "s"(cp));
    mk_tag(ep);
    bar();
    bar();
    flag(WS_F1);
    if (LANEID == 0) *(u32x2*)(lflag + 10) = u32x2{tag0, tag1};
    PSTAMP(2);
    SLEEP(SLP_P);
    poll(WS_F1, []() {});
    asm volatile("; pp" ::: "memory");
  }
  bar();
  PSTAMP(3);
  int role = wave;
  asm volatile("" : "+s"(role));

  if (__builtin_expect(role < 4, 1)) {
    const unsigned v16 = __builtin_amdgcn_mbcnt_hi(~0u, __builtin_amdgcn_mbcnt_lo(~0u, 0u)) * 16;
#define OPAQUE_LANE int lane = v16 >> 4; asm volatile("" : "+v"(lane));
    auto q_groupW = [&](int layer, int g, int pos) AI {
      const char* gW = ws + WS_WQ + (layer & 1) * (6 * MBY) + head * (192 * 1024) + (g * 4 + L) * 6144;
      const unsigned d = sbase + (pos * 4 + L) * QSLOT;
      glds_run<3, true>(v16, gW, d);
      glds_run<3, true>(v16, gW + 3072, d + 3072);
    };
    u32x4 wn[4][6];
    auto ld_wn = [&](int layer, int g0, int g1) AI {
      const unsigned so = WS_WQ + (layer & 1) * (6 * MBY) + head * (192 * 1024) + L * 6144;
#pragma unroll
      for (int g = g0; g < g1; ++g)
#pragma unroll
        for (int n = 0; n < 6; ++n) wn[g][n] = ld16s(wsr, v16 + (n % 3) * 1024, so + g * 24576 + (n / 3) * 3072);
      MEMFENCE;
    };
    auto wr_wn = [&](int g) AI {
#pragma unroll
      for (int n = 0; n < 6; ++n) *(u32x4*)(smem + (g * 4 + L) * QSLOT + n * 1024 + v16) = wn[g][n];
    };
    const u32x4 pz = u32x4{POISON, POISON, POISON, POISON};
    auto poison = [&](unsigned so) AI { st16s(wsr, v16, so, pz); };
    auto poison_o = [&]() AI {
      const unsigned o = WS_AR + AR_O + xt_off(bm * 16 + L * 4, head);
#pragma unroll
      for (int i = 0; i < 4; ++i) poison(o + i * 1024);
      STPAD;
    };
    auto poison_x = [&](int l) AI {
      if (L < NCHK) {
        const unsigned o = WS_AR + l * ARENA + AR_X + bm * (512 * 1024) + head * (NCHK * 2048) + L * 2048;
        poison(o); poison(o + 1024);
        STPAD;
      }
    };
    auto poison_s = [&](int l) AI {
      if (L == 3) {
        const unsigned o = WS_AR + l * ARENA + AR_KV + (unsigned)(bm * 32 + head) * KVREC;
        poison(o); poison(o + 1024);
        STPAD;
      }
    };
    auto poison_kv = [&](int l) AI {
      const unsigned kvo = WS_AR + l * ARENA + AR_KV + (unsigned)(bm * 32 + head) * KVREC;
      poison(kvo + L * 1024);
      if (L == (l ? 0 : 1)) st16s(wsr, v16 < 256 ? v16 : OOB, kvo + 4096, pz);
      STPAD;
    };
    q_groupW(0, 0, 0);
    q_groupW(0, 1, 1);
    auto put_l2 = [&](float sm, float sq, unsigned vo, unsigned so) AI {
      xred2<false>(sm, sq);
      if (v16 < 256) st16s(wsr, vo, so, u32x4{__float_as_uint(sm), tag0, __float_as_uint(sq), tag1});
      MEMFENCE;
    };
    if (L == 3) {
      const f32x4 t = *(const f32x4*)((const char*)(lmean + 16) + v16);
      put_l2(t[0] + t[2], t[1] + t[3], v16, WS_AR + AR_L2 + (head & 1) * 32768 + (bm * 16 + (head >> 1)) * 256);
    }
    if (L == 3) wait_vm<7>(); else wait_vm<6>();
    auto body = [&](auto kind, const int layer) AI {
      constexpr bool isB = decltype(kind)::value == 1, is0 = decltype(kind)::value == 0;
      const unsigned ar = WS_AR + layer * ARENA;
      f32x4 rcs[4] = {};
      unsigned tch = 0;
      auto sW = [&]() AI { return WS_WO + (layer & 1) * (2 * MBY) + cn * (128 * 1024); };
      auto stg = [&](unsigned off, auto n) AI { glds_run<decltype(n)::value, true>(v16, ws + sW() + off, sbase + L_STG + off); };
      auto stg7 = [&](unsigned o) AI { stg(o, IC<4>{}); stg(o + 4096, IC<3>{}); };
      auto stg6 = [&](unsigned o) AI { stg(o, IC<3>{}); stg(o + 3072, IC<3>{}); };

      auto extra = [&](int b) AI {
        if (!is0) return;
        const unsigned lu = L_RT + L * 14336;
        if (b < 7) { SCHED0; PRIO(3); SCHED0; }
        if (b == 1) retile(L < 3, 0, lu, rcs);
        if (b == 2 && L == 3) retile(0, 1, lu + 4096, nullptr);
        if (b == 6 && L == 3) ld_rt(p.woB, 1, lu + 4096);
        if (b == 8) {
          if (L < 3) retile(1, 1, lu + 4096, rcs);
          if (L == 3) retile(1, 0, lu + 8192, nullptr);
          if (L == 3) retile(1, 1, lu + 4096, nullptr);
        }
        if (b < 7) { SCHED0; PRIO(0); SCHED0; }
      };
      auto extra2 = [&](int b) AI {
        if (!is0) return;
        if (b < 7) { SCHED0; PRIO(3); SCHED0; }
        if (b == 1) poison_o();
        if (b == 2) poison_kv(0);
        if (b == 5) poison_kv(2);
        if (b == 8) poison_s(1);
        if (b == 9) poison_x(1);
        if (b == 10) { poison_x(2); poison_s(3); }
        if (b < 7) { SCHED0; PRIO(0); SCHED0; }
      };
      if (is0) sync_lds();
      STAMP(0);
      {
        OPAQUE_S
        OPAQUE_LANE
        u32x4 c0, c1;
        asm volatile("" : "=v"(c0), "=v"(c1));
        auto ldcs = [&]() AI {
          if (L > 0) {
            const unsigned so = WS_CS + (((layer & 1) * 32 + head) * 1536 + (L - 1) * 512) * 4;
            c0 = ld16s(wsr, v16, so); c1 = ld16s(wsr, v16 + 1024, so);
          }
          MEMFENCE;
        };
        auto wrcs = [&]() AI {
          if (L > 0) {
            f32x4 t = add4(__builtin_bit_cast(f32x4, c0), __builtin_bit_cast(f32x4, c1));
#pragma unroll
            for (int e = 0; e < 4; ++e)
              t[e] += __uint_as_float(__builtin_amdgcn_update_dpp(0u, __float_as_uint(t[e]), 0x128, 0xf, 0xf, false));
            float t0 = t[0], t1 = t[1], t2 = t[2], t3 = t[3];
            xred4<false>(t0, t1, t2, t3);
            *(f32x4*)(lcs + (isB ? 96 : 0) + (L - 1) * 32 + (lane & 7) * 4) = f32x4{t0, t1, t2, t3};
          }
        };
        if (isB && layer == 1) ldcs();
        if constexpr (is0) {
          if (L == 3) flag(WS_F1 + 2048);
          q_groupW(0, 2, 2); q_groupW(0, 3, 3);
          wait_vm<12>();
          if (L >= 2) glds_run<1>(v16, (const char*)p.wphi + head * 2048 + (L - 2) * 1024, sbase + L_WP + (L - 2) * 1024);
        }
        STAMP(1);
        if (!is0 && LPP) PRIO(3);
        if (is0 || !NB1) sync_lds();
        else SLEEP(SLP_1);
        STAMP(2);
        OPAQUE_S
        u32x4 s0 = u32x4{0u, 0u, 0u, 0u}, s1 = s0;
        auto ldst = [&]() AI {
          const unsigned so = ar + AR_L2 + (bm * 16 + L * 4) * 256;
          s0 = ld16s(wsr, v16, so);
          if (is0) s1 = ld16s(wsr, v16, so + 32768);
          if (isB && L == 0) s1 = ld16s(wsr, v16 & 255, bm > 0 ? so - 256 : so);
          MEMFENCE;
        };
        auto fin = [&](float sm, float sq, int r, bool own) AI {
          float mean = sm * (1.0f / 1024.0f);
          float var = fmaxf(__builtin_fmaf(sq, 1.0f / 1024.0f, -mul1(mean, mean)), 0.f);
          if (own) lmean[r] = -mean;
          lrstd[r] = __builtin_amdgcn_rsqf(var + 1e-5f);
        };
        auto wrst = [&]() AI {
          while (true) {
            unsigned d = (s0[1] ^ tag0) | (s0[3] ^ tag1);
            if (is0 || (isB && L == 0)) d |= (s1[1] ^ tag0) | (s1[3] ^ tag1);
            if (__builtin_expect(__builtin_amdgcn_ballot_w64(d != 0) == 0, 1) || --budget <= 0) break;
            COUNT(1);
            ldst();
          }
          MEMFENCE;
          float sm = __uint_as_float(s0[0]), sq = __uint_as_float(s0[2]);
          if (is0) { sm += __uint_as_float(s1[0]); sq += __uint_as_float(s1[2]); }
          fin(sm, sq, 16 + L * 64 + lane, true);
          if (isB && L == 0) fin(__uint_as_float(s1[0]), __uint_as_float(s1[2]), lane & 15, false);
        };
        auto q_group = [&](int g) AI {
          if (is0) {
            if (L & 1) SLEEP(SPR0);
            if (L & 2) SLEEP(2 * SPR0);
            q_groupW(layer, g, g & 3);
          } else if (g >= 5) q_groupW(layer, g, g < 6 ? g : g - 4);
        };
        auto poll2 = [&]() AI {
        if (!is0 && L == 0) {
          poll(ar + AR_FL, []() {}, 15);
          if (head < 16) {
            const unsigned so = ar + AR_ST + (bm * 16 + head) * 2048;
            s0 = ld16s(wsr, v16, so); s1 = ld16s(wsr, v16 + 1024, so);
            MEMFENCE;
          }
        }
        };
        if (NCK == 3) poll2();
        if (is0) {
          drain();
          poll(WS_F1 + 2048, [&]() AI { extra(1); });
          ldcs();
          extra2(1);
        }
        if (isB && layer == 1) wrcs();
        MEMFENCE;
        STAMP(3);
        if (!is0 && LPP) PRIO(0);
        sync_lds();
        STAMP(4);
        OPAQUE_S
        if (!is0) drain();
        auto sum2 = [&]() AI {
        if (!is0 && L == 0 && head < 16) {
          f32x4 t = add4(__builtin_bit_cast(f32x4, s0), __builtin_bit_cast(f32x4, s1));
#pragma unroll
          for (int e = 0; e < 4; ++e)
            t[e] += __uint_as_float(__builtin_amdgcn_update_dpp(0u, __float_as_uint(t[e]), 0x128, 0xf, 0xf, false));
          const bool hi = lane & 8;
          put_l2(hi ? t[2] : t[0], hi ? t[3] : t[1], (lane & 7) * 32 + (lane & 8) * 2, ar + AR_L2 + (bm * 16 + head) * 256);
        }
        };
        if (NCK == 3) sum2();
        q_group(4);
        extra(2); extra2(2);
        if (NCK == 4 && !is0) { if (LPP) PRIO(3); SLEEP(SLP_2); poll2(); if (LPP) PRIO(0); }
        STAMP(5);
        sync_lds();
        STAMP(6);
        OPAQUE_S
        drain();
        if (NCK == 4 && !is0) { if (LPP) PRIO(3); sum2(); if (LPP) PRIO(0); }
        q_group(5);
        if (is0) wrcs();
        STAMP(7);
        bar();
        OPAQUE_S
        drain();
        if (is0 && L == 0 && v16 == 0) __builtin_amdgcn_raw_buffer_store_b64(u32x2{tag0, tag1}, wsr, 0, WS_F2 + (bm * 32 + head) * 8, 16);
        q_group(6);
        if (!is0 && LP5) SLEEP(LP5);
        ldst();
        STAMP(8);
        bar();
        STAMP(9);
        OPAQUE_S
        drain();
        q_group(7);
        extra2(5);
        SCHED0;
        wrst();
        STAMP(10);
        bar();
        STAMP(11);
        OPAQUE_S
        drain();
        if (isB && layer == 3 && L == 3) {
          asm volatile("global_load_dword %0, %1, %2" : "=v"(tch) : "v"((unsigned)((bm * 256 + cm * 16 + (lane & 7) * 32) * 2048 + cn * 128)), "s"(p.out) : "memory");
        }
        extra(6);
        if (isB) stg6(96 * 1024 + L * 6144);
        SCHED0;
      }
      STAMP(12);
      sync_lds();
      STAMP(13);
      OPAQUE_S
      u32x4 fl[2];
      fl[0] = fl[1] = u32x4{tag0, tag1, tag0, tag1};
      auto ldfl = [&]() AI { fl[0] = ld16s(wsr, v16, WS_F2); fl[1] = ld16s(wsr, v16 + 1024, WS_F2); };
      if (is0) ldfl();
      bar();
      constexpr int NR = isB ? 6 : 9;
      const unsigned ro = isB ? 48 * 1024 + L * 6144 : L * 9216;
      u32x4 r[NR];
      bool pend = false;
      auto flags = [&]() AI {
        const u32x4 d = (fl[0] ^ u32x4{tag0, tag1, tag0, tag1}) | (fl[1] ^ u32x4{tag0, tag1, tag0, tag1});
        pend = __builtin_amdgcn_ballot_w64((d[0] | d[1] | d[2] | d[3]) != 0) != 0;
      };
      if (isB) {
        stg6(L * 6144); stg(120 * 1024 + L * 2048, IC<2>{});
        stg6(24 * 1024 + L * 6144);
        stg6(72 * 1024 + L * 6144);
      } else {
        drain();
        if (is0) flags();
        MEMFENCE;
        extra(8); extra2(8);
        if (!is0) poison_x(3);
        while (is0 && __builtin_expect(pend, 0) && --budget > 0) {
          ldfl();
          flags();
          MEMFENCE;
        }
        stg(36 * 1024 + L * 3072, IC<3>{}); stg7(72 * 1024 + L * 14336); stg7(72 * 1024 + L * 14336 + 7168);
      }
#pragma unroll
      for (int i = 0; i < NR; ++i) r[i] = ld16s(wsr, v16 + (i & 3) * 1024, sW() + ro + (i >> 2) * 4096);
      MEMFENCE;
      if (is0 && bm + head + L == 0 && v16 == 0) __builtin_amdgcn_raw_buffer_store_b32(tag0, wsr, 0, WS_EP, 16);
      auto wr_r = [&]() AI {
        unsigned vv = v16;
        asm volatile("" : "+v"(vv));
#pragma unroll
        for (int i = 0; i < NR; ++i) *(u32x4*)(smem + L_STG + ro + i * 1024 + vv) = r[i];
      };
      auto left = [&]() AI {
        OPAQUE_LANE
        for (;;) {
          MEMFENCE;
          if (__builtin_amdgcn_ballot_w64(lflag[lane & 7] != (unsigned)layer) == 0 || --budget <= 0) break;
          SLEEP(2);
        }
        MEMFENCE;
      };
      if (isB) {
#pragma unroll
        for (int i = 0; i < 24; ++i) asm volatile("" : "=v"(wn[i / 6][i % 6]));
        if (layer == 1) ld_wn(layer + 1, 0, 2);
        STAMP(14);
        left();
        wr_r();
        MEMFENCE;
        if (layer == 1) ld_wn(layer + 1, 2, 4);
        wait_vm_lgkm0<12>();
        if (layer == 3) wait_vm<0>();
        asm volatile("" :: "v"(tch));
      } else {
        constexpr int W0 = is0 ? WP0 : WP2, W1 = W0 > WA ? W0 : WA;
        ld_wn(layer + 1, 0, W0);
        if (is0) wait_vm<26 + 6 * W0>();
        STAMP(14);
        bar();
        stg6(48 * 1024 + L * 6144);
        ld_wn(layer + 1, W0, W1);
        bar();
        extra2(9);
        if (SLP_N) { wr_r(); MEMFENCE; SLEEP(SLP_N); }
        ld_wn(layer + 1, W1, WB);
        if (!SLP_N) wr_r();
        wait_vm<6 * (WB - W1)>();
        bar();
        if (WB < 4) SLEEP(SLP_4);
        extra2(10);
        ld_wn(layer + 1, WB, 4);
        wait_vm_lgkm0<6 * (4 - WB)>();
      }
      bar();
      if (isB && layer == 3) { STAMP_DUMP; __builtin_amdgcn_endpgm(); }
      OPAQUE_S
      SLEEP(SLP_W);
      wr_wn(0);
      drain();
      sync_lds();
      OPAQUE_S
      SCHED0;
      if (!SLP_G) q_groupW(layer + 1, 4, 4);
#pragma unroll
      for (int g = 1; g < 4; ++g) wr_wn(g);
      if (NB1) { MEMFENCE; lflag[12 + L] = layer + 1; MEMFENCE; }
      if (SLP_G) { MEMFENCE; SLEEP(SLP_G); q_groupW(layer + 1, 4, 4); }
    };
    body(IC<0>{}, 0);
#pragma unroll 1
    for (int layer = 1;; layer += 2) {
      body(IC<1>{}, layer);
      body(IC<2>{}, 2);
    }
  }

  const f32x4 zero = f32x4{0.f, 0.f, 0.f, 0.f};
  u32x4 xr[2];
  asm volatile("" : "=v"(xr[0]), "=v"(xr[1]));
#pragma unroll 1
  for (int layer = 0; layer < 4; ++layer) {
    bm = bm0; head = head0; cm = cm0; cn = cn0; cw = wave - 4;
    const bool isB = layer & 1;
    const unsigned ar = WS_AR + layer * ARENA;
    {
      f32x4 acc[2][6];
      f32x4 ck[2], mv[2], xv[2];
      float mean[2], rs[2], cv[2];
      u32x2 wt[4];
      const float* lcl = lcs + (isB ? 96 : 0);
      {
        LANE_VARS
        const unsigned va = v16 + cw * 2048;
        const unsigned sa0 = ar + AR_X + bm * (512 * 1024);
        auto lda = [&](unsigned so, u32x4* a) AI {
          a[0] = ld16s(wsr, va, so);
          a[1] = ld16s(wsr, va + 1024, so);
        };
        bf16x8 f[2][6];
        u32x4 a[4][2];
        auto unset = [&](const u32x4* e) AI { return poisoned(umax(umax(umax(e[0][0], e[0][2]), e[1][0]), e[1][2])); };
        auto refill = [&](auto ic, unsigned cur) AI {
          constexpr int I = decltype(ic)::value;
          do {
            COUNT(0);
            SLEEP(SLP_R);
            MEMFENCE;
            lda(cur, a[I]);
            bool u[4];
#pragma unroll
            for (int j = 1; j < 4; ++j) u[j] = unset(a[(I + j) % 4]);
            SCHED0;
#pragma unroll
            for (int j = 1; j < 4; ++j)
              if (u[j]) lda(cur + j * 16384, a[(I + j) % 4]);
            __builtin_amdgcn_s_waitcnt(0x0f70);
            MEMFENCE;
          } while (unset(a[I]) && --budget > 0);
        };
        STAMP(0);
        if (layer > 0) {
          bar();
          if (cn < 8) { if (cw < 4) SLEEP(SLP_X0); else SLEEP(SLP_X1); }
          else { if (cw < 4) SLEEP(SLP_XD0); else SLEEP(SLP_XD1); }
        }
#pragma unroll
        for (int j = 0; j < 4; ++j) {
          if (RSP && j == RSP && cw < 4) SLEEP(SLP_A);
          lda(sa0 + j * 16384, a[j]); SCHED0;
        }
#pragma unroll
        for (int m = 0; m < 2; ++m)
#pragma unroll
          for (int n = 0; n < 6; ++n) acc[m][n] = zero;
        asm volatile("" : "+v"(acc[0][0]), "+v"(acc[0][1]), "+v"(acc[0][2]), "+v"(acc[0][3]), "+v"(acc[0][4]), "+v"(acc[0][5]));
        asm volatile("" : "+v"(acc[1][0]), "+v"(acc[1][1]), "+v"(acc[1][2]), "+v"(acc[1][3]), "+v"(acc[1][4]), "+v"(acc[1][5]));
        if (layer == 0) { wait_vm<8>(); sync_lds(); }
#pragma unroll
        for (int nn = 0; nn < 6; ++nn) { const int n = nn < 4 ? nn + 2 : nn - 4; f[0][n] = *(const bf16x8*)(smem + v16 + n * 1024); }
        SCHED0;
        if (layer > 0) {
          const u32x2 tg = *(const u32x2*)(lflag + 10);
          wait_vm<8>();
          unsigned old;
          asm volatile("s_mov_b64 exec, 1\n\tds_add_rtn_u32 %0, %1, %2\n\ts_mov_b64 exec, -1\n\ts_waitcnt lgkmcnt(0)" : "=v"(old) : "v"(sbase + L_RED + 32), "v"(1) : "memory");
          if (__builtin_amdgcn_readfirstlane(old) == 8u * layer - 1)
            __builtin_amdgcn_raw_buffer_store_b64(tg, wsr, lane ? OOB : 0, ar + AR_FL + bm * 256 + (cm * 16 + cn) * 8, 16);
          MEMFENCE;
        }
        SCHED0;
        bool bad = layer > 0 && unset(a[0]);
        const int wrap = layer > 0 ? 6 : 4;
        unsigned ps[4] = {sbase + L_RED + 64 + cw * 128, sbase + L_RED + 2176 + (layer & 1) * 384, 0, sbase + L_WP};
        ps[2] = ps[0] + 1088 + (layer & 1) * (2176 + 384 - 1088 - 64 - cw * 128);
        static_assert(L_WP % 1024 == 0, "");
        int early = (layer & 1) * cw;
        asm volatile("" : "+s"(ps[0]), "+s"(ps[1]), "+s"(ps[2]), "+s"(ps[3]), "+s"(early));
        STAMP(1);
        auto group = [&](auto modec, int g) AI {
          constexpr int MODE = decltype(modec)::value;
          constexpr bool LAST = MODE == 3, CHK = MODE == 0 || MODE == 4, NOB = MODE == 4;
          unsigned fv = 0;
          bool nf = false;
          const unsigned gs = (g < wrap ? g : g - 4) * QGRP, gn = (g + 1 < wrap ? g + 1 : g - 3) * QGRP;
          const unsigned sc = sa0 + g * 65536, so = sc + 4 * 16384;
          unsigned gl = sbase + gs + v16;
          if constexpr (LAST) asm volatile("" : "+v"(gl));
          unsigned pa[5];
          static_for<4>([&](auto ic) AI {
            constexpr int i = decltype(ic)::value;
            constexpr bool END = LAST && i == 3;
            SCHED0;
            if (i == 3 && !LAST && !NOB) { sync_lds(); SCHED0; }
            if constexpr (i == 3 && NOB) {
              while (__builtin_expect(nf, 0) && --budget > 0) { MEMFENCE; nf = __builtin_amdgcn_ballot_w64(lflag[12 + (lane & 3)] != (unsigned)layer) != 0; MEMFENCE; }
              SCHED0;
            }
            if (i == 0 && LAST) {
              PRIO(0);
              if (early >= 4) PRIO(3);
              SCHED0;
              bar();
              SCHED0;
            }
            if constexpr (NOB) { if (i == 0) PRIO(3); if (i == 3) PRIO(2); }
            else if constexpr (!LAST) PRIO(i == 3 ? 3 : 2 - i);
            if constexpr (CHK) { if (__builtin_expect(bad, 0)) refill(ic, sc + i * 16384); }
            SCHED0;
            const bf16x8* fc = f[i & 1];
            bf16x8* fn = f[(i + 1) & 1];
            const u32x4* an = a[(i + 1) % 4];
            const unsigned slot = i < 3 ? gs + (i + 1) * QSLOT : gn;
            const char* pb = LAST ? (const char*)((const LDS(char)*)gl + (i + 1) * QSLOT) : smem + slot + v16;
            unsigned mx = 0;
            static_for<12>([&](auto jc) AI {
              constexpr int j = decltype(jc)::value, nn = j >> 1, m = j & 1, n = nn < 4 ? nn + 2 : nn - 4;
              if constexpr (n < 4) mfma_acc(acc[m][n], fc[n], as_frag(a[i][m]));
              else mfma_acc(acc[m][n], as_frag(a[i][m]), fc[n]);
              SCHED0;
              if constexpr (!END && j < 6) { constexpr int r = j < 4 ? j + 2 : j - 4; fn[r] = *(const bf16x8*)(pb + r * 1024); }
              if constexpr (LAST && i == 0) {
                if constexpr (j == 7) { pa[4] = v16 >> 1; asm volatile("" : "+v"(pa[4])); }
                if constexpr (j == 9) { pa[4] = (pa[4] & 24) | ps[3]; asm volatile("" : "+v"(pa[4])); }
                if constexpr (j == 11) { pa[4] |= v16 & ~63u; asm volatile("" : "+v"(pa[4])); }
              }
              if constexpr (LAST && i == 1) {
                if constexpr (j == 6) { pa[0] = lane & 48; asm volatile("" : "+v"(pa[0])); }
                if constexpr (j == 7) { pa[1] = v16 >> 2; asm volatile("" : "+v"(pa[1])); }
                if constexpr (j == 8) { pa[1] &= 60; asm volatile("" : "+v"(pa[1])); }
                if constexpr (j == 9) { pa[2] = pa[1] + ps[0]; asm volatile("" : "+v"(pa[2])); }
                if constexpr (j == 10) { pa[1] += ps[1]; asm volatile("" : "+v"(pa[1])); }
                if constexpr (j == 11) { pa[3] = pa[0] + ps[1]; asm volatile("" : "+v"(pa[3])); }
              }
              if constexpr (LAST && i == 2) {
                if constexpr (j == 6) ck[0] = ((const LDS(f32x4)*)pa[3])[8];
                if constexpr (j == 7) ck[1] = ((const LDS(f32x4)*)pa[3])[12];
                if constexpr (j == 8) { mean[0] = ((const LDS(float)*)pa[2])[0]; asm volatile("" : "+v"(pa[2])); }
                if constexpr (j == 9) { mean[1] = ((const LDS(float)*)pa[2])[16]; asm volatile("" : "+v"(pa[2])); }
                if constexpr (j == 10) { cv[0] = ((const LDS(float)*)pa[1])[64]; cv[1] = ((const LDS(float)*)pa[1])[80]; }
                if constexpr (j == 11) { pa[1] = pa[0] + ps[0]; asm volatile("" : "+v"(pa[1])); }
              }
              if constexpr (LAST && i == 3) {
                if constexpr (j == 0) mv[0] = ((const LDS(f32x4)*)pa[1])[0];
                if constexpr (j == 1) mv[1] = ((const LDS(f32x4)*)pa[1])[4];
                if constexpr (j == 2) { pa[0] += ps[2]; asm volatile("" : "+v"(pa[0])); }
                if constexpr (j == 3) { rs[0] = ((const LDS(float)*)pa[2])[272]; asm volatile("" : "+v"(pa[2])); }
                if constexpr (j == 4) xv[0] = ((const LDS(f32x4)*)pa[0])[0];
                if constexpr (j == 5) xv[1] = ((const LDS(f32x4)*)pa[0])[4];
                if constexpr (j == 6) rs[1] = ((const LDS(float)*)pa[2])[288];
                if constexpr (j == 8) wt[0] = trl(pa[4]);
                if constexpr (j == 9) wt[1] = trl(pa[4] + 1024);
                if constexpr (j == 10) wt[2] = trl(pa[4] + 32);
                if constexpr (j == 11) wt[3] = trl(pa[4] + 1056);
              }
              if constexpr (NOB && i == 1 && j == 6) { MEMFENCE; fv = lflag[12 + (lane & 3)]; MEMFENCE; }
              if constexpr (NOB && i == 2 && j == 6) nf = __builtin_amdgcn_ballot_w64(fv != (unsigned)layer) != 0;
              if constexpr (CHK) {
                if constexpr (j == 8) mx = umax(umax(umax(an[0][0], an[0][2]), an[1][0]), an[1][2]);
                if constexpr (j == 10) bad = poisoned(mx);
              }
              if constexpr (j == 10 && !LAST) a[i][0] = ld16s(wsr, va, so + i * 16384);
              if constexpr (j == 11 && !LAST) a[i][1] = ld16s(wsr, va + 1024, so + i * 16384);
              SCHED0;
            });
          });
        };
        int g = 0;
        if (layer > 0) {
          if (NB1) { group(IC<4>{}, 0); g = 1; }
#pragma unroll 1
          for (; g < NCHK; ++g) group(IC<0>{}, g);
        }
#pragma unroll 1
        for (; g < 7; ++g) group(IC<1>{}, g);
        group(IC<3>{}, 7);
        if (cw < 4) PRIO(1); else if (!isB) PRIO(2);
        mfma_done();
      }
      STAMP(2);
      LANE_VARS
      char* const scr = smem;

      u32x4 qf[2], kf[2];
      u32x2 vf[2][2];
      auto fold_rows = [&](int part, u32x4* out, const f32x4* c) AI {
#pragma unroll
        for (int m = 0; m < 2; ++m) {
          f32x4 a0 = acc[m][part * 2] + c[0] * mean[m], a1 = acc[m][part * 2 + 1] + c[1] * mean[m];
          out[m] = u32x4{pk2(a0[0], a0[1]), pk2(a0[2], a0[3]), pk2(a1[0], a1[1]), pk2(a1[2], a1[3])};
        }
      };
      auto fold_v = [&](bool scaled) AI {
#pragma unroll
        for (int m = 0; m < 2; ++m) {
          f32x4 a0 = acc[m][4] + mv[m] * cv[0], a1 = acc[m][5] + mv[m] * cv[1];
          if (scaled) { a0 = mul4(a0, xv[m]); a1 = mul4(a1, xv[m]); }
          vf[m][0] = u32x2{pk2(a0[0], a0[1]), pk2(a0[2], a0[3])};
          vf[m][1] = u32x2{pk2(a1[0], a1[1]), pk2(a1[2], a1[3])};
        }
      };
      const unsigned otile = WS_AR + AR_O + xt_off(bm * 16 + cw * 2, head);
      auto put_o = [&](int m, f32x4 a, f32x4 b, unsigned opar) AI {
        const u32x4 v = u32x4{(pk2(a[0], a[1]) & ~1u) | opar, pk2(a[2], a[3]), (pk2(b[0], b[1]) & ~1u) | opar, pk2(b[2], b[3])};
        st16s(wsr, v16 + m * 1024, otile, v);
        return v;
      };
      auto hold = [&](u32x4 a, u32x4 b) AI { SCHED0; asm volatile("s_nop 1" :: "v"(a), "v"(b)); SCHED0; };

      if (!isB) {
        fold_rows(1, kf, ck); fold_v(true);
        char* PK = smem + L_STG;
        char* VF = scr;
        const bf16x8 wf0 = as_frag(u32x4{wt[0][0], wt[0][1], wt[1][0], wt[1][1]}), wf1 = as_frag(u32x4{wt[2][0], wt[2][1], wt[3][0], wt[3][1]});
#pragma unroll
        for (int t = 0; t < 2; ++t)
          *(u32x4*)(VF + (cw * 2 + t) * 1024 + lane * 16) = u32x4{vf[0][t][0], vf[0][t][1], vf[1][t][0], vf[1][t][1]};
        {
          u32x4 pk[2][2];
          phi_map2<0>(kf, rs, wf0, wf1, pk, []() {});
          char* d0 = PK + cw * 4096 + fr * 64 + ((fr >> 2) & 1) * 32 + ((fq ^ ((fr >> 3) * 2)) * 8);
          char* d1 = PK + cw * 4096 + fr * 64 + (((fr >> 2) & 1) ^ 1) * 32 + ((fq ^ ((fr >> 3) * 2)) * 8);
#pragma unroll
          for (int m = 0; m < 2; ++m) {
            *(u32x2*)(d0 + m * 2048) = u32x2{pk[m][0][0], pk[m][0][1]};
            *(u32x2*)(d0 + m * 2048 + 1024) = u32x2{pk[m][0][2], pk[m][0][3]};
            *(u32x2*)(d1 + m * 2048) = u32x2{pk[m][1][0], pk[m][1][1]};
            *(u32x2*)(d1 + m * 2048 + 1024) = u32x2{pk[m][1][2], pk[m][1][3]};
          }
        }
        PRIO(1);
        sync_lds();
        STAMP(3);
        const unsigned rec = ar + AR_KV + (unsigned)head * KVREC;
        {
          const u32x4 ones = u32x4{0x3f803f80u, 0x3f803f80u, 0x3f803f80u, 0x3f803f80u};
          f32x4 c0 = zero, c1 = zero;
          const char* ad = PK + (cw & 1) * 1024 + (fq * 4 + (fr >> 2)) * 64 + (((cw >> 1) ^ fq) & 1) * 32 + (((fr & 3) ^ (fq & 2)) * 8);
          const char* vd = VF + (cw >> 2) * 1024 + lane * 16;
          bf16x8 a[8], b0[8];
#pragma unroll
          for (int ks = 0; ks < 8; ++ks) {
            u32x2 a0 = trread(ad + ks * 4096), a1 = trread(ad + ks * 4096 + 2048);
            a[ks] = as_frag(u32x4{a0[0], a0[1], a1[0], a1[1]});
            b0[ks] = *(const bf16x8*)(vd + ks * 2048);
          }
          SCHED0;
#pragma unroll
          for (int ks = 0; ks < 8; ks += 2) {
            c0 = mfma16(a[ks], b0[ks], c0);
            c1 = mfma16(a[ks + 1], b0[ks + 1], c1);
          }
          c0 += c1;
          const unsigned dst = rec + (unsigned)bm * (32 * KVREC);
          const unsigned p0 = pk2(c0[0], c0[1]), p1 = pk2(c0[2], c0[3]);
          const auto q0 = __builtin_amdgcn_permlane16_swap(p0, p0, false, false), q1 = __builtin_amdgcn_permlane16_swap(p1, p1, false, false);
          st16(wsr, fq & 1 ? OOB : dst + cw * 512 + (fq >> 1) * 256 + fr * 16, clean(u32x4{q0[0], q1[0], q0[1], q1[1]}));
          SCHED0;
          if (cw < 4) {
            f32x4 cz = zero, cy = zero;
#pragma unroll
            for (int ks = 0; ks < 8; ks += 2) {
              cz = mfma16(a[ks], as_frag(ones), cz);
              cy = mfma16(a[ks + 1], as_frag(ones), cy);
            }
            cz += cy;
            st16(wsr, fr ? OOB : dst + 4096 + cw * 64 + fq * 16, clean(__builtin_bit_cast(u32x4, cz)));
          }
        }
        SCHED0;
        STAMP(4);
        bar();
        if (layer == 0) {
#pragma unroll
          for (int j = 0; j < 2; ++j) xr[j] = ld16s(wsr, v16, WS_AR + AR_X + xt_off(bm * 16 + cw * 2 + cm, cn * 2 + j));
          MEMFENCE;
        }
        if (QP) PRIO(3);
        const f32x4 cq[2] = {*(const f32x4*)(lcl + fq * 4), *(const f32x4*)(lcl + 16 + fq * 4)};
        fold_rows(0, qf, cq);
        u32x4 pq[2][2];
        u32x4 zp, zq;
        u32x4 part[4];
        const unsigned vk = (fq >> 1) * (32 * KVREC) + (fq & 1) * 256 + fr * 16;
        auto ldp = [&](int i) AI { part[i] = ld16s(wsr, vk, rec + i * (64 * KVREC) + cw * 512); };
        auto ldz = [&]() AI {
          zp = ld16(wsr, rec + fq * (32 * KVREC) + 4096 + fr * 16);
          zq = ld16(wsr, rec + (4 + fq) * (32 * KVREC) + 4096 + fr * 16);
        };
#pragma unroll
        for (int i = 0; i < 4; ++i) asm volatile("" : "=v"(part[i]));
        asm volatile("" : "=v"(zp), "=v"(zq));
        phi_map2<KPOS, QP>(qf, rs, wf0, wf1, pq, [&]() AI {
          if (QP || cw >= 4) {
            MEMFENCE;
            if (QP && cw == 0) ldz();
#pragma unroll
            for (int i = 0; i < 4; ++i) ldp(i);
            MEMFENCE;
          }
        });
        asm volatile("" : "+v"(pq[0][0]), "+v"(pq[0][1]), "+v"(pq[1][0]), "+v"(pq[1][1]));
        SCHED0;
        STAMP(5);
        char* kvf = smem + L_A2;
        if (!KPOS || (!QP && cw < 4)) {
          SLEEP(SLP_K);
          MEMFENCE;
          if (cw == 0) ldz();
#pragma unroll
          for (int i = 0; i < 4; ++i) ldp(i);
          MEMFENCE;
        }
        unsigned kw = sbase + L_A2 + v16 + (cw >> 1) * 1024 + (cw & 1) * 8;
        const unsigned one = (fq & 1) == (fr >> 3) ? (fr & 1 ? 0x3f800000u : 0x00003f80u) : 0u;
        u32x4 eye = u32x4{(fr & 6) == 0 ? one : 0u, (fr & 6) == 2 ? one : 0u, (fr & 6) == 4 ? one : 0u, (fr & 6) == 6 ? one : 0u};
        asm volatile("" : "+v"(kw), "+v"(eye));
        u32x2 kvs;
        auto nans = [&](f32x4 v) AI { return __builtin_amdgcn_ballot_w64(__builtin_isunordered(v[0], v[1])) | __builtin_amdgcn_ballot_w64(__builtin_isunordered(v[2], v[3])); };
        auto sums = [&]() AI {
          unsigned long long nan = 0;
          if (cw == 0) {
            f32x4 z = __builtin_bit_cast(f32x4, zp) + __builtin_bit_cast(f32x4, zq);
            nan = nans(z);
            float z0 = z[0], z1 = z[1], z2 = z[2], z3 = z[3];
            xred4<false>(z0, z1, z2, z3);
            *(u32x2*)(smem + L_Z + (fr >> 3) * 64 + (fr & 3) * 16 + ((fr >> 2) & 1) * 8) = u32x2{pk2(z0, z1), pk2(z2, z3)};
          }
          f32x4 s4 = zero;
#pragma unroll
          for (int i = 0; i < 4; ++i) { s4 = mfma16(as_frag(eye), as_frag(part[i]), s4); SCHED0; }
          kvs = u32x2{pk2(s4[0], s4[1]), pk2(s4[2], s4[3])};
          asm volatile("" : "+v"(kvs));
          return nan | __builtin_amdgcn_ballot_w64(s4[0] != s4[0]);
        };
        if (__builtin_expect(sums() != 0, 0)) {
          for (;;) {
            bool again = false;
            if (cw == 0 && poisoned(umax4(umax4(zp), zq))) { again = true; ldz(); }
#pragma unroll
            for (int i = 0; i < 4; ++i)
              if (poisoned(umax4(part[i]))) { again = true; ldp(i); }
            __builtin_amdgcn_s_waitcnt(0x0f70);
            MEMFENCE;
            if (!again || --budget <= 0) break;
            COUNT(2);
          }
          sums();
        }
        STAMP(6);
        *(LDS(u32x2)*)kw = kvs;
        sync_lds();
        STAMP(7);
        bf16x8 kvr[2][2], zf[2];
#pragma unroll
        for (int s = 0; s < 2; ++s) {
          kvr[0][s] = *(const bf16x8*)(kvf + (0 * 2 + s) * 1024 + lane * 16);
          kvr[1][s] = *(const bf16x8*)(kvf + (1 * 2 + s) * 1024 + lane * 16);
          zf[s] = *(const bf16x8*)(smem + L_Z + s * 64 + fq * 16);
        }
        u32x4 oo[2];
#pragma unroll
        for (int m = 0; m < 2; ++m) {
          f32x4 c0 = mfma16(kvr[0][1], as_frag(pq[m][1]), mfma16(kvr[0][0], as_frag(pq[m][0]), zero));
          f32x4 c1 = mfma16(kvr[1][1], as_frag(pq[m][1]), mfma16(kvr[1][0], as_frag(pq[m][0]), zero));
          f32x4 cd = mfma16(zf[1], as_frag(pq[m][1]), mfma16(zf[0], as_frag(pq[m][0]), zero));
          const float iden = __builtin_amdgcn_rcpf(cd[0] + 1e-6f);
          c0 *= iden; c1 *= iden;
          oo[m] = put_o(m, c0, c1, 0);
        }
        hold(oo[0], oo[1]);
        STAMP(8);
        asm volatile("" : "=v"(acc[0][0]), "=v"(acc[0][1]), "=v"(acc[0][2]), "=v"(acc[0][3]), "=v"(acc[0][4]), "=v"(acc[0][5]));
        asm volatile("" : "=v"(acc[1][0]), "=v"(acc[1][1]), "=v"(acc[1][2]), "=v"(acc[1][3]), "=v"(acc[1][4]), "=v"(acc[1][5]));
        asm volatile("" : "=v"(ck[0]), "=v"(ck[1]), "=v"(mv[0]), "=v"(mv[1]), "=v"(xv[0]), "=v"(xv[1]));
        asm volatile("" : "=v"(mean[0]), "=v"(mean[1]), "=v"(rs[0]), "=v"(rs[1]), "=v"(cv[0]), "=v"(cv[1]));
      }
      int kind = layer & 1;
      asm volatile("" : "+s"(kind));
      if (kind) {
        char* ex = scr;
        fold_rows(1, kf, ck); fold_v(false);
        const unsigned hrec = ar + AR_KV + (unsigned)(bm * 32 + head) * KVREC;
        {
          const u32x4 vl = u32x4{vf[1][0][0], vf[1][0][1], vf[1][1][0], vf[1][1][1]};
          if (cw == 7) {
            st16s(wsr, v16, hrec, clean(kf[1]));
            STPAD;
            st16s(wsr, v16 + 1024, hrec, clean(vl));
            STPAD;
          }
          char* e2 = ex + (cw + 1) * 2048;
          *(u32x4*)(e2 + lane * 16) = kf[1];
          *(u32x2*)(e2 + 1024 + lane * 8) = vf[1][0];
          *(u32x2*)(e2 + 1536 + lane * 8) = vf[1][1];
          MEMFENCE;
          lflag[cw] = layer;
          MEMFENCE;
          PRIO(1);
        }
        SCHED0;
        fold_rows(0, qf, xv);
        u32x4 kp;
        u32x4 va0 = __builtin_shufflevector(vf[0][0], vf[0][0], -1, -1, 0, 1), vb0 = __builtin_shufflevector(vf[0][1], vf[0][1], -1, -1, 0, 1);
        auto setp = [&](u32x4& d, u32x2 v) AI { d[0] = v[0]; d[1] = v[1]; };
        auto ldh = [&]() AI {
          MEMFENCE;
          kp = ld16s(wsr, v16, hrec - 32 * KVREC);
          setp(va0, __builtin_amdgcn_raw_buffer_load_b64(wsr, v16 + 1024, hrec - 32 * KVREC, 16));
          MEMFENCE;
          setp(vb0, __builtin_amdgcn_raw_buffer_load_b64(wsr, v16 + 1032, hrec - 32 * KVREC, 16));
          MEMFENCE;
        };
        unsigned fl = 0;
        auto rdl = [&]() AI {
          MEMFENCE;
          fl = lflag[cw - 1];
          MEMFENCE;
          kp = *(const u32x4*)(ex + cw * 2048 + lane * 16);
          setp(va0, *(const u32x2*)(ex + cw * 2048 + 1024 + lane * 8));
          MEMFENCE;
          setp(vb0, *(const u32x2*)(ex + cw * 2048 + 1536 + lane * 8));
          MEMFENCE;
        };
        auto prev = [&]() AI {
          if (cw > 0) {
            do { SLEEP(1); rdl(); __builtin_amdgcn_s_waitcnt(0xc07f); } while (__builtin_amdgcn_readfirstlane(fl) != (unsigned)layer && --budget > 0);
          } else if (bm > 0) {
            SLEEP(SLP_HX);
            do { ldh(); __builtin_amdgcn_s_waitcnt(0x0f70); }
            while (poisoned(umax4(umax4(kp), u32x4{va0[0], va0[1], vb0[0], vb0[1]})) && --budget > 0);
          } else {
            kp = u32x4{0u, 0u, 0u, 0u};
            setp(va0, u32x2{0u, 0u}); setp(vb0, u32x2{0u, 0u});
          }
          asm volatile("" : "+v"(kp), "+v"(va0), "+v"(vb0));
        };
        const float scale = 0.17677669529663687f * LOG2E;
        const bool odd = fr & 1;
        const bool own0 = fr >= fq * 4 + (fr & 1), own1 = fr >= fq * 4 + (fr & 1) + 2;
        const bool nopre = (bm == 0) && (cw == 0);
        float l[2][2], mx[2], rk[2][2], sum[2];
#pragma unroll
        for (int m = 0; m < 2; ++m) {
          const float* rb = lrstd + (cw * 2 + m) * 16 + fq * 4 + (fr & 1);
          rk[m][0] = rb[own0 ? 16 : 0];
          rk[m][1] = rb[own1 ? 18 : 2];
        }
        float qk[2][2];
#pragma unroll
        for (int m = 0; m < 2; ++m) { const float qs = rs[m] * scale; qk[m][0] = qs * rk[m][0]; qk[m][1] = qs * rk[m][1]; }
        const unsigned e0 = odd ? 0x01000c0cu : 0x0c0c0100u, e1 = odd ? 0x03020c0cu : 0x0c0c0302u, ez = 0x0c0c0c0cu;
        u32x4 sel = u32x4{own0 ? ez : e0, own1 ? ez : e1, own0 ? e0 : ez, own1 ? e1 : ez};
        u32x4 va1 = u32x4{vf[0][0][0], vf[0][0][1], vf[1][0][0], vf[1][0][1]}, vb1 = u32x4{vf[0][1][0], vf[0][1][1], vf[1][1][0], vf[1][1][1]};
        float off0 = nopre && !own0 ? -3.0e38f : 0.f, off1 = nopre && !own1 ? -3.0e38f : 0.f;
        auto score = [&](int m) AI {
          f32x4 s0 = mfma16(as_frag(m == 0 ? kp : kf[0]), as_frag(qf[m]), zero);
          f32x4 s1 = mfma16(as_frag(kf[m]), as_frag(qf[m]), zero);
          float a = own0 ? (odd ? s1[1] : s1[0]) : (odd ? s0[1] : s0[0]);
          float b = own1 ? (odd ? s1[3] : s1[2]) : (odd ? s0[3] : s0[2]);
          if (m == 0) { l[m][0] = __builtin_fmaf(a, qk[m][0], off0); l[m][1] = __builtin_fmaf(b, qk[m][1], off1); }
          else { l[m][0] = a * qk[m][0]; l[m][1] = b * qk[m][1]; }
          mx[m] = fmx(l[m][0], l[m][1]);
        };
        auto expo = [&](int m) AI {
          l[m][0] = __builtin_amdgcn_exp2f(l[m][0] - mx[m]);
          l[m][1] = __builtin_amdgcn_exp2f(l[m][1] - mx[m]);
          sum[m] = l[m][0] + l[m][1];
        };
        f32x4 ov[2][2];
        auto pv = [&](int m) AI {
          const unsigned pr = pk2(mul1(mul1(l[m][0], rk[m][0]), sum[m]), mul1(mul1(l[m][1], rk[m][1]), sum[m]));
          const u32x4 pf = u32x4{__builtin_amdgcn_perm(pr, pr, sel[0]), __builtin_amdgcn_perm(pr, pr, sel[1]),
                                 __builtin_amdgcn_perm(pr, pr, sel[2]), __builtin_amdgcn_perm(pr, pr, sel[3])};
          ov[m][0] = mfma16(as_frag(m ? va1 : va0), as_frag(pf), zero);
          ov[m][1] = mfma16(as_frag(m ? vb1 : vb0), as_frag(pf), zero);
        };
        asm volatile("" : "+v"(qf[0]), "+v"(qf[1]), "+v"(va0), "+v"(vb0));
        SCHED0;
        if (cw > 0) rdl();
        SCHED0;
        score(1);
        asm volatile("" : "+v"(qf[0]), "+v"(qk[0][0]), "+v"(qk[0][1]), "+v"(mx[1]), "+v"(l[1][0]), "+v"(l[1][1]));
        asm volatile("" : "+v"(sel), "+v"(va1), "+v"(vb1), "+v"(off0), "+v"(off1));
        SCHED0;
        STAMP(3);
        if (__builtin_expect(cw == 0 || __builtin_amdgcn_readfirstlane(fl) != (unsigned)layer, 0)) prev();
        score(0);
        xred2<true>(mx[0], mx[1]);
        expo(0); expo(1);
        xred2<false, true>(sum[0], sum[1]);
        pv(0); pv(1);
        SCHED0;
        const u32x4 o0 = put_o(0, ov[0][0], ov[0][1], 1), o1 = put_o(1, ov[1][0], ov[1][1], 1);
        hold(o0, o1);
        STAMP(4);
      }
    }
    {
      LANE_VARS
      const int mt = bm * 16 + cw * 2 + cm;
      const unsigned so0 = WS_AR + AR_O + xt_off(mt, 0);
      const unsigned opar = layer & 1;
      auto diff = [&](u32x4 v) AI { return ((v[0] ^ opar) | (v[2] ^ opar)) & 1u; };
      auto dif3 = [&](u32x4 v) AI { return __builtin_amdgcn_bitop3_b32(v[0], v[2], opar, 0x7e); };
      auto any = [&](unsigned d) AI { return __builtin_amdgcn_ballot_w64(d != 0) != 0; };
      const int g0 = cm;
      u32x4 a[RO];
      auto refill = [&](auto ic, auto wc, unsigned cur, unsigned nxt) AI {
        constexpr int I = decltype(ic)::value, W = decltype(wc)::value;
        do {
          COUNT(1);
          SLEEP(SLP_R);
          MEMFENCE;
          a[I] = ld16s(wsr, v16, cur + I * 16384);
          bool u[RO];
#pragma unroll
          for (int j = I + 1; j < I + W; ++j) u[j % RO] = any(diff(a[j % RO]));
          SCHED0;
#pragma unroll
          for (int j = I + 1; j < I + W; ++j)
            if (u[j % RO]) a[j % RO] = ld16s(wsr, v16, (j < RO ? cur : nxt) + (j % RO) * 16384);
          __builtin_amdgcn_s_waitcnt(0x0f70);
          MEMFENCE;
        } while (any(diff(a[I]) | diff(a[I + 1])) && --budget > 0);
      };
      const unsigned sf = so0 + g0 * (RO * 16384);
      f32x4 acc[4];
#pragma unroll
      for (int n = 0; n < 4; ++n)
        acc[n] = f32x4{bflo(xr[n >> 1][(n & 1) * 2]), bfhi(xr[n >> 1][(n & 1) * 2]), bflo(xr[n >> 1][(n & 1) * 2 + 1]), bfhi(xr[n >> 1][(n & 1) * 2 + 1])};
      asm volatile("" : "+v"(acc[0]), "+v"(acc[1]), "+v"(acc[2]), "+v"(acc[3]));
      SCHED0;
      const bool late = isB && cw == 0;
      if (late) bar();
      if (cn < 8) { if (late) SLEEP(SLP_O0); else SLEEP(SLP_O); }
      else { if (late) SLEEP(SLP_O0D); else SLEEP(SLP_OD); }
#pragma unroll
      for (int j = 0; j < NI; ++j) { a[j] = ld16s(wsr, v16, sf + j * 16384); SCHED0; }
      if (!late) bar();
#pragma unroll
      for (int j = NI; j < NJ; ++j) { a[j] = ld16s(wsr, v16, sf + j * 16384); SCHED0; }
      STAMP(9);
      bf16x8 wf[2][4];
      const char* pw0 = smem + L_STG + v16;
#pragma unroll
      for (int n = 0; n < 8; ++n) wf[n >> 2][n & 3] = *(const bf16x8*)(pw0 + g0 * (RO * 4096) + n * 1024);
      SCHED0;
      STAMP(10);
      if (any(diff(a[0]) | diff(a[1]))) refill(IC<0>{}, IC<NJ>{}, sf, sf);
      SCHED0;
#pragma unroll
      for (int j = NJ; j < RO; ++j) { a[j] = ld16s(wsr, v16, sf + j * 16384); SCHED0; }
      STAMP(11);
      bool bad = false;
      auto group = [&](auto lastc, int g) AI {
        constexpr bool LAST = decltype(lastc)::value;
        const int gc = g ^ g0, gn = (g + 1) ^ g0;
        const unsigned sc = so0 + gc * (RO * 16384), so = so0 + gn * (RO * 16384);
        const char* pc = pw0 + gc * (RO * 4096);
        const char* pn = pw0 + gn * (RO * 4096);
        static_for<RO>([&](auto ic) AI {
          constexpr int i = decltype(ic)::value;
          SCHED0;
          if constexpr (i % 2 == 0)
            if (__builtin_expect(bad, 0)) refill(ic, IC<(LAST && i + 4 > RO) ? RO - i : 4>{}, sc, so);
          SCHED0;
          unsigned mx = 0, my = 0;
          static_for<4>([&](auto nc) AI {
            constexpr int n = decltype(nc)::value;
            mfma_acc(acc[n], wf[i & 1][n], as_frag(a[i]));
            SCHED0;
            if constexpr (i + 2 < RO) wf[i & 1][n] = *(const bf16x8*)(pc + (i + 2) * 4096 + n * 1024);
            else if constexpr (!LAST) wf[i & 1][n] = *(const bf16x8*)(pn + (i + 2 - RO) * 4096 + n * 1024);
            if constexpr (!(LAST && i == RO - 1) && (i & 1)) {
              if constexpr (n == 0) mx = dif3(a[(i + 1) % RO]);
              if constexpr (n == 1) my = dif3(a[(i + 2) % RO]);
              if constexpr (n == 2) bad = any((mx | my) & 1u);
            }
            if constexpr (n == 3 && !LAST) a[i] = ld16s(wsr, v16, so + i * 16384);
            SCHED0;
          });
        });
      };
#pragma unroll 1
      for (int g = 0; g < 3; ++g) group(std::false_type{}, g);
      group(std::true_type{}, 3);
      mfma_done();
      STAMP(12);
#pragma unroll
      for (int j = 0; j < 2; ++j)
#pragma unroll
        for (int e = 0; e < 4; ++e) xr[j][e] = pk2(acc[j * 2 + (e >> 1)][(e & 1) * 2], acc[j * 2 + (e >> 1)][(e & 1) * 2 + 1]);
      if (layer == 3) {
        u32x4 yo[2];
#pragma unroll
        for (int j = 0; j < 2; ++j) {
          auto u = __builtin_amdgcn_permlane16_swap(xr[j][0], xr[j][2], false, false);
          auto v = __builtin_amdgcn_permlane16_swap(xr[j][1], xr[j][3], false, false);
          yo[j] = u32x4{u[0], v[0], u[1], v[1]};
        }
        const unsigned vo = fr * 2048 + (fq & 1) * 32 + (fq >> 1) * 16;
        __builtin_amdgcn_raw_buffer_store_b128(yo[0], mk_rsrc(p.out, 4 * MBY), vo, mt * 32768 + cn * 128, 2);
        __builtin_amdgcn_raw_buffer_store_b128(yo[1], mk_rsrc(p.out, 4 * MBY), vo + 64, mt * 32768 + cn * 128, 2);
        STAMP(13);
        STAMP(14);
        STAMP_DUMP;
        __builtin_amdgcn_endpgm();
      }
      const u32x2 stat = put_x(xr, v16, ar + ARENA + AR_X + xt_off(mt, cn * 2));
      __builtin_amdgcn_raw_buffer_store_b64(stat, wsr, fq ? OOB : fr * 8, ar + ARENA + AR_ST + (mt * 16 + cn) * 128, 16);
      MEMFENCE;
      STAMP(14);
    }
  }
}

extern "C" void kernel_launch(void* const* d_in, const int* in_sizes, int n_in, void* d_out, int out_size,
                              void* d_ws, size_t ws_size, hipStream_t stream) {
  (void)hipFuncSetAttribute((const void*)mega_kernel, hipFuncAttributeMaxDynamicSharedMemorySize, MEGA_LDS);
  MegaParams m;
  m.ws = (char*)d_ws; m.out = (u16*)d_out; m.x = (const u16*)d_in[0]; m.wphi = (const u16*)d_in[2];
  m.wqkvA = (const u16*)d_in[1]; m.wqkvB = (const u16*)d_in[5];
  m.woA = (const u16*)d_in[3]; m.woB = (const u16*)d_in[6];
  mega_kernel<<<256, 768, MEGA_LDS, stream>>>(m);
}
```

```cpp
#include <hip/hip_runtime.h>
#include <stdint.h>
#include <type_traits>

#define DI __device__ __forceinline__
#define AI __attribute__((always_inline))
typedef unsigned short u16;
typedef __attribute__((ext_vector_type(8))) short bf16x8;
typedef __attribute__((ext_vector_type(4))) short s16x4;
typedef __attribute__((ext_vector_type(4))) float f32x4;
typedef __attribute__((ext_vector_type(2))) float f32x2;
typedef __attribute__((ext_vector_type(2))) __bf16 bf2;
typedef __attribute__((ext_vector_type(4))) unsigned u32x4;
typedef __attribute__((ext_vector_type(2))) unsigned u32x2;
typedef __attribute__((address_space(3))) s16x4* lds_s16x4_ptr;
#define LDS(T) __attribute__((address_space(3))) T
template <int N> using IC = std::integral_constant<int, N>;

constexpr float LOG2E = 1.4426950408889634f;

DI unsigned pk2(float a, float b) {
  f32x2 v = {a, b};
  bf2 r = __builtin_convertvector(v, bf2);
  return __builtin_bit_cast(unsigned, r);
}
DI float bflo(unsigned u) { return __uint_as_float(u << 16); }
DI float bfhi(unsigned u) { return __uint_as_float(u & 0xffff0000u); }
DI f32x4 mfma16(bf16x8 a, bf16x8 b, f32x4 c) { return __builtin_amdgcn_mfma_f32_16x16x32_bf16(a, b, c, 0, 0, 0); }
DI void mfma_acc(f32x4& c, bf16x8 a, bf16x8 b) { asm volatile("v_mfma_f32_16x16x32_bf16 %0, %1, %2, %0" : "+v"(c) : "v"(a), "v"(b)); }
DI void mfma_done() { asm volatile("s_nop 7\n\ts_nop 1" ::: "memory"); __builtin_amdgcn_sched_barrier(0); }
DI bf16x8 as_frag(u32x4 v) { return __builtin_bit_cast(bf16x8, v); }
DI float dot2(unsigned a, unsigned b, float c) {
  return __builtin_amdgcn_fdot2_f32_bf16(__builtin_bit_cast(bf2, a), __builtin_bit_cast(bf2, b), c, false);
}
DI u32x2 trread(const char* p) { return __builtin_bit_cast(u32x2, __builtin_amdgcn_ds_read_tr16_b64_v4i16((lds_s16x4_ptr)p)); }
DI u32x2 trl(unsigned a) { return __builtin_bit_cast(u32x2, __builtin_amdgcn_ds_read_tr16_b64_v4i16((lds_s16x4_ptr)a)); }
DI float fmx(float a, float b) { return __builtin_elementwise_maximum(a, b); }
DI float fmn(float a, float b) { return __builtin_elementwise_minimum(a, b); }

DI float mul1(float a, float b) { float r; asm("v_mul_f32 %0, %1, %2" : "=v"(r) : "v"(a), "v"(b)); return r; }
DI float add1(float a, float b) { float r; asm("v_add_f32 %0, %1, %2" : "=v"(r) : "v"(a), "v"(b)); return r; }
DI f32x4 mul4(f32x4 a, f32x4 b) {
  float r0, r1, r2, r3;
  asm("v_mul_f32 %0, %4, %8\n\tv_mul_f32 %1, %5, %9\n\tv_mul_f32 %2, %6, %10\n\tv_mul_f32 %3, %7, %11"
      : "=&v"(r0), "=&v"(r1), "=&v"(r2), "=&v"(r3) : "v"(a[0]), "v"(a[1]), "v"(a[2]), "v"(a[3]), "v"(b[0]), "v"(b[1]), "v"(b[2]), "v"(b[3]));
  return f32x4{r0, r1, r2, r3};
}
DI f32x4 add4(f32x4 a, f32x4 b) { return f32x4{add1(a[0], b[0]), add1(a[1], b[1]), add1(a[2], b[2]), add1(a[3], b[3])}; }

template <bool IS_MAX, bool INV = false>
DI void xred2(float& a, float& b) {
  auto r = __builtin_amdgcn_permlane32_swap(__float_as_uint(a), __float_as_uint(b), false, false);
  float c = IS_MAX ? fmx(__uint_as_float(r[0]), __uint_as_float(r[1])) : __uint_as_float(r[0]) + __uint_as_float(r[1]);
  auto q = __builtin_amdgcn_permlane16_swap(__float_as_uint(c), __float_as_uint(c), false, false);
  c = IS_MAX ? fmx(__uint_as_float(q[0]), __uint_as_float(q[1])) : __uint_as_float(q[0]) + __uint_as_float(q[1]);
  if (INV) c = __builtin_amdgcn_rcpf(c);
  auto w = __builtin_amdgcn_permlane32_swap(__float_as_uint(c), __float_as_uint(c), false, false);
  a = __uint_as_float(w[0]); b = __uint_as_float(w[1]);
}
template <bool IS_MAX, bool INV = false>
DI void xred4(float& a, float& b, float& c, float& d) {
  auto op = [](unsigned x, unsigned y) { return IS_MAX ? fmx(__uint_as_float(x), __uint_as_float(y)) : __uint_as_float(x) + __uint_as_float(y); };
  auto r0 = __builtin_amdgcn_permlane32_swap(__float_as_uint(a), __float_as_uint(b), false, false);
  auto r1 = __builtin_amdgcn_permlane32_swap(__float_as_uint(c), __float_as_uint(d), false, false);
  float ab = op(r0[0], r0[1]), cd = op(r1[0], r1[1]);
  auto q = __builtin_amdgcn_permlane16_swap(__float_as_uint(ab), __float_as_uint(cd), false, false);
  float e = op(q[0], q[1]);
  if (INV) e = __builtin_amdgcn_rcpf(e);
  auto w = __builtin_amdgcn_permlane16_swap(__float_as_uint(e), __float_as_uint(e), false, false);
  auto u = __builtin_amdgcn_permlane32_swap(w[0], w[0], false, false);
  auto v = __builtin_amdgcn_permlane32_swap(w[1], w[1], false, false);
  a = __uint_as_float(u[0]); b = __uint_as_float(u[1]); c = __uint_as_float(v[0]); d = __uint_as_float(v[1]);
}

DI unsigned xt_off(int mt, int ks) { return (unsigned)((((mt >> 4) * 32 + ks) * 16 + (mt & 15)) << 10); }

#define GLDS_PRE "s_mov_b32 %0, m0\n\ts_mov_b32 m0, %3\n\ts_nop 2\n\t"
#define GLDS_POST "s_mov_b32 m0, %0"
#define GLDS_OPS : "=&s"(keep) : "v"(voff), "s"(sbase), "s"(lds) : "memory"
#define GL1(c) "global_load_lds_dwordx4 %1, %2" c "\n\t"
#define GL2(c) GL1(c) "global_load_lds_dwordx4 %1, %2 offset:1024" c "\n\t"
#define GL3(c) GL2(c) "global_load_lds_dwordx4 %1, %2 offset:2048" c "\n\t"
#define GL4(c) GL3(c) "global_load_lds_dwordx4 %1, %2 offset:3072" c "\n\t"
template <int N, bool SC1 = false>
DI void glds_run(unsigned voff, const void* sbase, unsigned lds) {
  unsigned keep;
  if constexpr (SC1) {
    if constexpr (N == 2) asm volatile(GLDS_PRE GL2(" sc1") GLDS_POST GLDS_OPS);
    else if constexpr (N == 3) asm volatile(GLDS_PRE GL3(" sc1") GLDS_POST GLDS_OPS);
    else asm volatile(GLDS_PRE GL4(" sc1") GLDS_POST GLDS_OPS);
  } else {
    static_assert(SC1 || N == 1, "");
    asm volatile(GLDS_PRE GL1("") GLDS_POST GLDS_OPS);
  }
}
template <int N> DI void wait_vm_lgkm0() { asm volatile("s_waitcnt vmcnt(%0) lgkmcnt(0)" ::"n"(N) : "memory"); }
template <int N> DI void wait_vm() { asm volatile("s_waitcnt vmcnt(%0)" ::"n"(N) : "memory"); }
DI void drain() { asm volatile("s_waitcnt vmcnt(0)" ::: "memory"); __builtin_amdgcn_s_waitcnt(0x0f70); }
DI void bar() { asm volatile("" ::: "memory"); __builtin_amdgcn_s_barrier(); asm volatile("" ::: "memory"); }
DI void sync_lds() { asm volatile("s_waitcnt lgkmcnt(0)" ::: "memory"); bar(); }
#define SCHED0 __builtin_amdgcn_sched_barrier(0)
#define STPAD do { SCHED0; asm volatile("s_nop 1"); SCHED0; } while (0)
#define SLEEP(n) do { if (n) __builtin_amdgcn_s_sleep(n); } while (0)
#define PRIO(n) __builtin_amdgcn_s_setprio(n)

template <typename F>
DI void phi_map2(const u32x4* in, const float* rs, bf16x8 wf0, bf16x8 wf1, u32x4 (*out)[2], F mid) {
  const f32x4 zero = f32x4{0.f, 0.f, 0.f, 0.f};
  float x[2][8], mx[2], mn[2];
#pragma unroll
  for (int m = 0; m < 2; ++m) {
    f32x4 r0 = mfma16(wf0, as_frag(in[m]), zero), r1 = mfma16(wf1, as_frag(in[m]), zero);
#pragma unroll
    for (int j = 0; j < 4; ++j) { x[m][j] = r0[j]; x[m][4 + j] = r1[j]; }
  }
#pragma unroll
  for (int m = 0; m < 2; ++m) {
    mx[m] = fmx(fmx(fmx(x[m][0], x[m][1]), x[m][2]), fmx(fmx(x[m][3], x[m][4]), fmx(fmx(x[m][5], x[m][6]), x[m][7])));
    mn[m] = -fmn(fmn(fmn(x[m][0], x[m][1]), x[m][2]), fmn(fmn(x[m][3], x[m][4]), fmn(fmn(x[m][5], x[m][6]), x[m][7])));
  }
  xred4<true>(mx[0], mn[0], mx[1], mn[1]);
  f32x2 e[2][8];
  float sp[2], sn[2];
#pragma unroll
  for (int m = 0; m < 2; ++m) {
    const float sl = rs[m] * LOG2E;
    const f32x2 l2 = {sl, -sl};
    const f32x2 off = f32x2{mx[m], mn[m]} * (-sl);
#pragma unroll
    for (int j = 0; j < 8; ++j) {
      f32x2 a = __builtin_elementwise_fma(f32x2{x[m][j], x[m][j]}, l2, off);
      e[m][j] = f32x2{__builtin_amdgcn_exp2f(a[0]), __builtin_amdgcn_exp2f(a[1])};
    }
    f32x2 sm = ((e[m][0] + e[m][1]) + (e[m][2] + e[m][3])) + ((e[m][4] + e[m][5]) + (e[m][6] + e[m][7]));
    sp[m] = sm[0]; sn[m] = sm[1];
  }
  xred4<false, true>(sp[0], sn[0], sp[1], sn[1]);
  mid(sp);
#pragma unroll
  for (int m = 0; m < 2; ++m) {
    const f32x2 inv = {sp[m], sn[m]};
#pragma unroll
    for (int j = 0; j < 8; ++j) e[m][j] *= inv;
    out[m][0] = u32x4{pk2(e[m][0][0], e[m][1][0]), pk2(e[m][2][0], e[m][3][0]), pk2(e[m][4][0], e[m][5][0]), pk2(e[m][6][0], e[m][7][0])};
    out[m][1] = u32x4{pk2(e[m][0][1], e[m][1][1]), pk2(e[m][2][1], e[m][3][1]), pk2(e[m][4][1], e[m][5][1]), pk2(e[m][6][1], e[m][7][1])};
  }
}

#define SLP_X0 6
#define SLP_X1 11
#define SLP_XD0 2
#define SLP_XD1 7
#define SLP_O 19
#define SLP_OD 10
#define SLP_O0 8
#define SLP_O0D 2
#define SLP_K 4
#define SLP_R 4
#define SLP_HX 1
#define SLP_Q 4
#define SLP_P 40
#define SLP_E 8
#define SLP_G 14
#define SPR0 2
#define SW0 0
#define LANEID __builtin_amdgcn_mbcnt_hi(~0u, __builtin_amdgcn_mbcnt_lo(~0u, 0u))
#define STAMP(i) do {} while (0)
#define STAMP_DUMP do {} while (0)
#define COUNT(k) do {} while (0)
#define PSTAMP(l) do { const int layer = l; (void)layer; STAMP(15); } while (0)
constexpr unsigned MBY = 1u << 20;
constexpr unsigned WS_WQ = 0;
constexpr unsigned WS_WO = 12 * MBY;
constexpr unsigned WS_AR = 16 * MBY, ARENA = 12 * MBY;
constexpr unsigned AR_X = 0, AR_ST = 4 * MBY, AR_O = 5 * MBY, AR_KV = 9 * MBY;
constexpr unsigned AR_L2 = AR_ST + 256 * 1024, AR_FL = AR_ST + 320 * 1024;
constexpr unsigned KVREC = 9 * 1024;
constexpr unsigned WS_CS = WS_AR + 4 * ARENA;
constexpr unsigned WS_F1 = WS_CS + 512 * 1024;
constexpr unsigned WS_F2 = WS_F1 + 64 * 1024;
constexpr unsigned WS_EP = WS_F2 + 2048;
constexpr unsigned WS_END = WS_EP + 128;
constexpr unsigned POISON = 0xffffffffu;
constexpr unsigned OOB = 0x80000000u;

typedef __amdgpu_buffer_rsrc_t rsrc_t;
DI rsrc_t mk_rsrc(const void* p, unsigned bytes) { return __builtin_amdgcn_make_buffer_rsrc((void*)p, 0, bytes, 0x00020000); }
DI void st16(rsrc_t r, unsigned off, u32x4 v) { __builtin_amdgcn_raw_buffer_store_b128(v, r, off, 0, 16); }
DI void st16s(rsrc_t r, unsigned voff, unsigned soff, u32x4 v) { __builtin_amdgcn_raw_buffer_store_b128(v, r, voff, soff, 16); }
DI u32x4 ld16(rsrc_t r, unsigned off) { return __builtin_amdgcn_raw_buffer_load_b128(r, off, 0, 16); }
DI u32x4 ld16s(rsrc_t r, unsigned voff, unsigned soff) { return __builtin_amdgcn_raw_buffer_load_b128(r, voff, soff, 16); }
DI u32x4 clean(u32x4 v) { return __builtin_elementwise_min(v, u32x4{POISON - 1, POISON - 1, POISON - 1, POISON - 1}); }
DI unsigned umax(unsigned a, unsigned b) { return a > b ? a : b; }
DI unsigned umin(unsigned a, unsigned b) { return a < b ? a : b; }
DI unsigned umax4(u32x4 v) { return umax(umax(umax(v[0], v[1]), v[2]), v[3]); }
DI unsigned umax4(unsigned m, u32x4 v) { return umax(umax(umax(umax(m, v[0]), v[1]), v[2]), v[3]); }
DI bool poisoned(unsigned m) { return __builtin_amdgcn_ballot_w64(m == POISON) != 0; }
#define MEMFENCE asm volatile("" ::: "memory")

#define BL(i, o) "buffer_load_dwordx4 %1, %2, %" #i " offen" o " nt lds\n\t"
#define BL_OPS(...) : "=&s"(keep) : "v"(voff), "s"(r), "s"(lds), __VA_ARGS__ : "memory"
template <int N>
DI void blds_run(rsrc_t r, unsigned voff, unsigned lds, unsigned s0, unsigned s1 = 0, unsigned s2 = 0, unsigned s3 = 0) {
  unsigned keep;
  if constexpr (N == 2) asm volatile(GLDS_PRE BL(4, "") BL(5, " offset:1024") GLDS_POST BL_OPS("s"(s0), "s"(s1)));
  else asm volatile(GLDS_PRE BL(4, "") BL(5, " offset:1024") BL(6, " offset:2048") BL(7, " offset:3072") GLDS_POST BL_OPS("s"(s0), "s"(s1), "s"(s2), "s"(s3)));
}

#define CODEB 35636
constexpr int MEGA_CODE_BYTES = CODEB;
DI unsigned code_prefetch(int i) {
  unsigned long long pc; int rel; unsigned d;
  asm volatile("s_getpc_b64 %0\n\ts_mov_b32 %1, _Z11mega_kernel10MegaParams@rel32@lo+4" : "=s"(pc), "=s"(rel));
  const unsigned long long a = pc + (long long)rel + (i * 192 + 192 <= MEGA_CODE_BYTES ? i * 192 : 0);
  asm volatile("s_load_dword %0, %1, 0x0\n\ts_load_dword %0, %1, 0x40\n\ts_load_dword %0, %1, 0x80" : "=&s"(d) : "s"(a) : "memory");
  return d;
}

struct MegaParams {
  char* ws;
  const u16* x;
  const u16* wqkvA;
  const u16* woA;
  const u16* wqkvB;
  const u16* woB;
  const u16* wphi;
  u16* out;
};

constexpr int QSLOT = 6 * 1024, QGRP = 4 * QSLOT;
constexpr int L_STG = QGRP;
constexpr int L_A2 = 16 * 1024, L_Z = 21 * 1024;
constexpr int L_RT = 4 * QGRP;
constexpr int L_PB = 0;
constexpr int L_RED = L_STG + 128 * 1024;
constexpr int L_WP = L_RED + 3072;
constexpr int L_STAMP = L_WP + 2048;
constexpr int MEGA_LDS = L_STAMP + 1536;
static_assert(MEGA_LDS <= 160 * 1024, "LDS map");

template <int I, int N, typename F> DI void static_for_(F&& f) {
  if constexpr (I < N) { f(IC<I>{}); static_for_<I + 1, N>(f); }
}
template <int N, typename F> DI void static_for(F&& f) { static_for_<0, N>(f); }

constexpr int NCHK = 3;
constexpr int RO = 8;
constexpr int SPIN_BUDGET = 1 << 15;

__global__ __launch_bounds__(768, 3) void mega_kernel(MegaParams p) {
  extern __shared__ __attribute__((aligned(16))) char smem[];
  const int tid = threadIdx.x;
  const int wave = __builtin_amdgcn_readfirstlane(tid >> 6);
  const int xcd = blockIdx.x & 7, idx = blockIdx.x >> 3;
  int bm = (xcd >> 2) * 4 + (idx >> 3), head = ((xcd & 1) * 2 + ((xcd >> 1) & 1)) * 8 + (idx & 7);
  int cm = (xcd >> 1) & 1, cn = (xcd & 1) * 8 + (idx & 7);
  const int bm0 = bm, head0 = head, cm0 = cm, cn0 = cn;
  char* ws = p.ws;
  const u16 *px = p.x, *pq = p.wqkvA;
  asm volatile("" : "+s"(ws), "+s"(px), "+s"(pq));
  const int swv = wave == SW0 ? 0 : wave == 4 ? 1 : wave == 11 ? 2 : -1; (void)swv;
  float* lmean = (float*)(smem + L_RED);
  float* lrstd = lmean + 272;
  float* lcs = lrstd + 272;
  unsigned* lflag = (unsigned*)lmean;
  const rsrc_t wsr = mk_rsrc(ws, WS_END);
  int budget = SPIN_BUDGET;
  unsigned tag0 = 0, tag1 = 0;
  auto ld_ep = [&]() AI { MEMFENCE; return __builtin_amdgcn_raw_buffer_load_b64(wsr, 0, WS_EP, 16); };
  auto mk_tag = [&](u32x2 ep) AI {
    tag0 = __builtin_amdgcn_readfirstlane(ep[0]) + 0x9e3779b9u;
    tag1 = __builtin_amdgcn_readfirstlane(ep[1]) ^ 0x85ebca6bu;
  };
  const unsigned sbase = (unsigned)(size_t)smem;
  int L = wave, cw = wave - 4;
#define OPAQUE_S asm volatile("" : "+s"(L), "+s"(bm), "+s"(head), "+s"(cm), "+s"(cn));
#define LANE_VARS asm volatile("" : "+s"(cw), "+s"(bm), "+s"(head), "+s"(cm), "+s"(cn)); int lane_ = __builtin_amdgcn_mbcnt_hi(~0u, __builtin_amdgcn_mbcnt_lo(~0u, 0u)); asm volatile("" : "+v"(lane_)); \
  const int lane = lane_, fr = lane & 15, fq = lane >> 4; const unsigned v16 = lane * 16; (void)fr; (void)fq; (void)v16;
  auto put_x = [&](u32x4* xr, unsigned v16, unsigned dst) AI {
    float sm = 0.f, sq = 0.f;
#pragma unroll
    for (int j = 0; j < 2; ++j) {
      xr[j][0] = umin(xr[j][0], POISON - 1); xr[j][2] = umin(xr[j][2], POISON - 1);
      st16s(wsr, v16, dst + j * 16384, xr[j]);
    }
    SCHED0;
#pragma unroll
    for (int j = 0; j < 2; ++j)
#pragma unroll
      for (int e = 0; e < 4; ++e) { sm = dot2(xr[j][e], 0x3f803f80u, sm); sq = dot2(xr[j][e], xr[j][e], sq); }
    xred2<false>(sm, sq);
    return u32x2{__float_as_uint(sm), __float_as_uint(sq)};
  };

#define RT_VARS const bool tq = L < 3; const int tsg = tq ? (bm * 2 + (head >> 4)) * 3 + L : bm * 2 + (head >> 4), ks = (head & 15) + 16 * h;
  auto ld_rt = [&](const u16* w, int h, unsigned lds) AI {
    RT_VARS
    LANE_VARS
    const unsigned rs = tq ? 6144 : 2048, D = 8 * rs - 1024, o = ks * 32 * rs + tsg * 128;
    blds_run<4>(mk_rsrc(w, rs * 1024), (lane >> 3) * rs + ((lane & 7) ^ ((lane >> 3) & 6)) * 16, sbase + lds, o, o + D, o + 2 * D, o + 3 * D);
  };
  auto retile = [&](int side, int h, unsigned lds, f32x4* rcs) AI {
    RT_VARS
    LANE_VARS
    const unsigned dst = tq ? WS_WQ + side * (6 * MBY) + (unsigned)((((tsg & 15) * 2) * 32 + ks) * 6 + (tsg >> 4) * 2) * 1024
                            : WS_WO + side * (2 * MBY) + (unsigned)((tsg * 32 + ks) * 4) * 1024;
    const unsigned gstr = tq ? 32 * 6 * 1024 : 2048;
    const unsigned a0 = (lane >> 2) * 128 + ((lane >> 3) & 3) * 32 + (lane & 3) * 8;
#pragma unroll
    for (int pc = 0; pc < 4; ++pc) {
      const char* aa = smem + lds + (a0 ^ (pc * 32));
      u32x2 r0_ = trread(aa), r1_ = trread(aa + 2048);
      u32x4 v = u32x4{r0_[0], r0_[1], r1_[0], r1_[1]};
      st16s(wsr, v16 + (pc & 1) * 1024, dst + (pc >> 1) * gstr, v);
      if (rcs) rcs[pc] = mfma16(as_frag(u32x4{0x3f803f80u, 0x3f803f80u, 0x3f803f80u, 0x3f803f80u}), as_frag(v), rcs[pc]);
    }
    if (rcs && h == 1) {
      const f32x4 c0 = rcs[0], c1 = rcs[1], c2 = rcs[2], c3 = rcs[3];
      const float ca = fq & 1 ? c1[0] : c0[0], cb = fq & 1 ? c3[0] : c2[0], cc = fq & 2 ? cb : ca;
      if (tq) __builtin_amdgcn_raw_buffer_store_b32(__float_as_uint(cc), wsr, ((fq >> 1) * 1536 + (fq & 1) * 16 + fr) * 4,
                                                    WS_CS + ((side * 32 + (tsg & 15) * 2) * 1536 + (tsg >> 4) * 512 + (head & 15) * 32) * 4, 16);
    }
  };
  auto flag = [&](unsigned table) AI {
    if (LANEID == 0) __builtin_amdgcn_raw_buffer_store_b64(u32x2{tag0, tag1}, wsr, 0, table + (bm * 32 + head) * 8, 16);
  };
  auto poll = [&](unsigned table, auto between, int rows = 0) AI {
    LANE_VARS
    const int q = (umin(fq, 2) * 16 + (head >> 1)) * 43 >> 7;
    const unsigned vo = rows ? bm * 256 + (fr & rows) * 16 : fq == 3 ? bm * 256 + fr * 16 : q * 128 + (fr & 7) * 16;
    auto miss = [&](u32x4 f) AI { return __builtin_amdgcn_ballot_w64(((f[0] ^ tag0) | (f[1] ^ tag1) | (f[2] ^ tag0) | (f[3] ^ tag1)) != 0) != 0; };
    MEMFENCE;
    const u32x4 f0 = ld16s(wsr, vo, table);
    MEMFENCE;
    between();
    MEMFENCE;
    bool wait = miss(f0);
#pragma unroll 1
    while (wait && --budget > 0) {
      SLEEP(SLP_Q);
      MEMFENCE;
      wait = miss(ld16s(wsr, vo, table));
      asm volatile("" : "+s"(budget) :: "memory");
    }
    MEMFENCE;
  };
  auto ld_x = [&](int cx, unsigned lds) AI {
    LANE_VARS
    const unsigned o = (bm * 16 + (head >> 1)) * 32768 + cx * 128;
    blds_run<2>(mk_rsrc(px, 4 * MBY), (lane >> 3) * 2048 + ((lane & 7) ^ (lane >> 3)) * 16, sbase + lds, o, o + 15360);
  };
  auto st_x = [&](int cx, unsigned lds, int w) AI {
    LANE_VARS
    const int mt = bm * 16 + (head >> 1);
    u32x4 xr[2];
#pragma unroll
    for (int j = 0; j < 2; ++j) {
      const char* a = smem + lds + fr * 128 + (fq & 1) * 8;
      const u32x2 lo = *(const u32x2*)(a + ((j * 4 + (fq >> 1)) ^ (fr & 7)) * 16), hi = *(const u32x2*)(a + ((j * 4 + 2 + (fq >> 1)) ^ (fr & 7)) * 16);
      xr[j] = u32x4{lo[0], lo[1], hi[0], hi[1]};
    }
    const u32x2 stat = put_x(xr, v16, WS_AR + AR_X + xt_off(mt, cx * 2));
    if (fq == 0) *(u32x2*)(lmean + 16 + fr * 4 + w) = stat;
  };

  {
    const int lane = LANEID;
    const bool tq = wave < 4;
    const unsigned rs = tq ? 6144 : 2048, D = 8 * rs - 1024;
    const unsigned o = (tq ? (head & 15) * 32 : (bm * 16 + (head >> 1)) * 16) * rs + (tq ? (bm * 2 + (head >> 4)) * 3 + wave : (head & 1) * 4 + cw) * 128;
    const unsigned lds = sbase + L_PB + (tq ? 8192 + wave * 4096 : cw * 2048);
    const unsigned vo = (lane >> 3) * rs + ((lane & 7) ^ ((lane >> 3) & (tq ? 6 : 7))) * 16;
    const rsrc_t r = mk_rsrc(tq ? pq : px, tq ? 6 * MBY : 4 * MBY);
    if ((wave & 7) != 3 && wave < 8) blds_run<2>(r, vo, lds, o, o + D);
    if (wave < 3) blds_run<2>(r, vo, lds + 2048, o + 2 * D + 2048, o + 3 * D + 2048);
  }
  if (wave >= 8) {
    const unsigned cp = code_prefetch(idx * 9 + cw), cq = code_prefetch(idx * 9 + cw - 4);
    L = wave - 8;
    bar();
    ld_x(8 + (head & 1) * 4 + L, L_PB + 32768 + L * 2048);
    if (L < 3) ld_rt(pq, 1, L_PB + 20480 + L * 4096);
    sync_lds();
    asm volatile("; pi" :: "s"(cp), "s"(cq) : "memory");
    wait_vm<0>();
    PSTAMP(1);
    st_x(8 + (head & 1) * 4 + L, L_PB + 32768 + L * 2048, L * 64 + 2);
    if (L < 3) {
      const f32x4 t = *(const f32x4*)(smem + L_PB + 40960 + L * 1024 + LANEID * 16);
      f32x4 rca[4] = {{t[0], 0.f, 0.f, 0.f}, {t[1], 0.f, 0.f, 0.f}, {t[2], 0.f, 0.f, 0.f}, {t[3], 0.f, 0.f, 0.f}};
      retile(0, 1, L_PB + 20480 + L * 4096, rca);
    }
    asm volatile("s_waitcnt lgkmcnt(0)" ::: "memory");
    PSTAMP(2);
  } else if (wave >= 4) {
    wait_vm<0>();
    PSTAMP(1);
    st_x((head & 1) * 4 + cw, L_PB + cw * 2048, cw * 64);
    bar();
    wait_vm_lgkm0<0>();
    PSTAMP(2);
    bar();
    asm volatile("; pc" ::: "memory");
    SLEEP(SLP_E);
    L = 3;
    if (cw < 2) ld_rt(p.woA, cw, L_RT + 3 * 14336 + cw * 4096);
    if (cw == 2) ld_rt(p.woB, 0, L_RT + 3 * 14336 + 8192);
  } else if (wave < 3) {
    const u32x2 ep = ld_ep();
    wait_vm<1>();
    PSTAMP(1);
    f32x4 rca[4] = {};
    retile(0, 0, L_PB + 8192 + L * 4096, rca);
    *(f32x4*)(smem + L_PB + 40960 + L * 1024 + LANEID * 16) = f32x4{rca[0][0], rca[1][0], rca[2][0], rca[3][0]};
    bar();
    drain();
    mk_tag(ep);
    PSTAMP(2);
    sync_lds();
    asm volatile("; pl" ::: "memory");
    SLEEP(SLP_E);
    ld_rt(p.wqkvB, 0, L_RT + L * 14336);
    ld_rt(p.wqkvB, 1, L_RT + L * 14336 + 4096);
  } else {
    const u32x2 ep = ld_ep();
    const unsigned cp = code_prefetch(idx * 9 + 8);
    if (LANEID < 9) lflag[LANEID] = 0;
    wait_vm_lgkm0<0>();
    asm volatile("" :: "s"(cp));
    mk_tag(ep);
    bar();
    bar();
    flag(WS_F1);
    if (LANEID == 0) *(u32x2*)(lflag + 10) = u32x2{tag0, tag1};
    PSTAMP(2);
    SLEEP(SLP_P);
    poll(WS_F1, []() {});
    asm volatile("; pp" ::: "memory");
  }
  bar();
  PSTAMP(3);
  int role = wave;
  asm volatile("" : "+s"(role));

  if (__builtin_expect(role < 4, 1)) {
    const unsigned v16 = __builtin_amdgcn_mbcnt_hi(~0u, __builtin_amdgcn_mbcnt_lo(~0u, 0u)) * 16;
#define OPAQUE_LANE int lane = v16 >> 4; asm volatile("" : "+v"(lane));
    auto q_groupW = [&](int layer, int g, int pos) AI {
      const char* gW = ws + WS_WQ + (layer & 1) * (6 * MBY) + head * (192 * 1024) + (g * 4 + L) * 6144;
      const unsigned d = sbase + (pos * 4 + L) * QSLOT;
      glds_run<3, true>(v16, gW, d);
      glds_run<3, true>(v16, gW + 3072, d + 3072);
    };
    u32x4 wn[4][6];
    auto ld_wn = [&](int layer, int g0, int g1) AI {
      const unsigned so = WS_WQ + (layer & 1) * (6 * MBY) + head * (192 * 1024) + L * 6144;
#pragma unroll
      for (int g = g0; g < g1; ++g)
#pragma unroll
        for (int n = 0; n < 6; ++n) wn[g][n] = ld16s(wsr, v16 + (n % 3) * 1024, so + g * 24576 + (n / 3) * 3072);
      MEMFENCE;
    };
    auto wr_wn = [&](int g) AI {
#pragma unroll
      for (int n = 0; n < 6; ++n) *(u32x4*)(smem + (g * 4 + L) * QSLOT + n * 1024 + v16) = wn[g][n];
    };
    const u32x4 pz = u32x4{POISON, POISON, POISON, POISON};
    auto poison = [&](unsigned so) AI { st16s(wsr, v16, so, pz); };
    auto poison_o = [&]() AI {
      const unsigned o = WS_AR + AR_O + xt_off(bm * 16 + L * 4, head);
#pragma unroll
      for (int i = 0; i < 4; ++i) poison(o + i * 1024);
      STPAD;
    };
    auto poison_x = [&](int l) AI {
      if (L < NCHK) {
        const unsigned o = WS_AR + l * ARENA + AR_X + bm * (512 * 1024) + head * (NCHK * 2048) + L * 2048;
        poison(o); poison(o + 1024);
        STPAD;
      }
    };
    auto poison_s = [&](int l) AI {
      if (L == 3) {
        const unsigned o = WS_AR + l * ARENA + AR_KV + (unsigned)(bm * 32 + head) * KVREC;
        poison(o); poison(o + 1024);
        STPAD;
      }
    };
    auto poison_kv = [&](int l) AI {
      const unsigned kvo = WS_AR + l * ARENA + AR_KV + (unsigned)(bm * 32 + head) * KVREC;
      poison(kvo + L * 1024);
      if (L == (l ? 0 : 1)) st16s(wsr, v16 < 256 ? v16 : OOB, kvo + 4096, pz);
      STPAD;
    };
    q_groupW(0, 0, 0);
    q_groupW(0, 1, 1);
    auto put_l2 = [&](float sm, float sq, unsigned vo, unsigned so) AI {
      xred2<false>(sm, sq);
      if (v16 < 256) st16s(wsr, vo, so, u32x4{__float_as_uint(sm), tag0, __float_as_uint(sq), tag1});
      MEMFENCE;
    };
    if (L == 3) {
      const f32x4 t = *(const f32x4*)((const char*)(lmean + 16) + v16);
      put_l2(t[0] + t[2], t[1] + t[3], v16, WS_AR + AR_L2 + (head & 1) * 32768 + (bm * 16 + (head >> 1)) * 256);
    }
    if (L == 3) wait_vm<7>(); else wait_vm<6>();
    auto body = [&](auto kind, const int layer) AI {
      constexpr bool isB = decltype(kind)::value == 1, is0 = decltype(kind)::value == 0;
      const unsigned ar = WS_AR + layer * ARENA;
      f32x4 rcs[4] = {};
      unsigned tch = 0;
      auto sW = [&]() AI { return WS_WO + (layer & 1) * (2 * MBY) + cn * (128 * 1024); };
      auto stg = [&](unsigned off, auto n) AI { glds_run<decltype(n)::value, true>(v16, ws + sW() + off, sbase + L_STG + off); };
      auto stg7 = [&](unsigned o) AI { stg(o, IC<4>{}); stg(o + 4096, IC<3>{}); };
      auto stg6 = [&](unsigned o) AI { stg(o, IC<3>{}); stg(o + 3072, IC<3>{}); };

      auto extra = [&](int b) AI {
        if (!is0) return;
        const unsigned lu = L_RT + L * 14336;
        if (b < 7) { SCHED0; PRIO(3); SCHED0; }
        if (b == 1) retile(L < 3, 0, lu, rcs);
        if (b == 2 && L == 3) retile(0, 1, lu + 4096, nullptr);
        if (b == 6 && L == 3) ld_rt(p.woB, 1, lu + 4096);
        if (b == 8) {
          if (L < 3) retile(1, 1, lu + 4096, rcs);
          if (L == 3) retile(1, 0, lu + 8192, nullptr);
          if (L == 3) retile(1, 1, lu + 4096, nullptr);
        }
        if (b < 7) { SCHED0; PRIO(0); SCHED0; }
      };
      auto extra2 = [&](int b) AI {
        if (!is0) return;
        if (b < 7) { SCHED0; PRIO(3); SCHED0; }
        if (b == 1) poison_o();
        if (b == 2) poison_kv(0);
        if (b == 5) poison_kv(2);
        if (b == 8) poison_s(1);
        if (b == 9) poison_x(1);
        if (b == 10) { poison_x(2); poison_s(3); }
        if (b < 7) { SCHED0; PRIO(0); SCHED0; }
      };
      if (is0) sync_lds();
      STAMP(0);
      {
        OPAQUE_S
        OPAQUE_LANE
        u32x4 c0, c1;
        asm volatile("" : "=v"(c0), "=v"(c1));
        auto ldcs = [&]() AI {
          if (L > 0) {
            const unsigned so = WS_CS + (((layer & 1) * 32 + head) * 1536 + (L - 1) * 512) * 4;
            c0 = ld16s(wsr, v16, so); c1 = ld16s(wsr, v16 + 1024, so);
          }
          MEMFENCE;
        };
        auto wrcs = [&]() AI {
          if (L > 0) {
            f32x4 t = add4(__builtin_bit_cast(f32x4, c0), __builtin_bit_cast(f32x4, c1));
#pragma unroll
            for (int e = 0; e < 4; ++e)
              t[e] += __uint_as_float(__builtin_amdgcn_update_dpp(0u, __float_as_uint(t[e]), 0x128, 0xf, 0xf, false));
            float t0 = t[0], t1 = t[1], t2 = t[2], t3 = t[3];
            xred4<false>(t0, t1, t2, t3);
            *(f32x4*)(lcs + (isB ? 96 : 0) + (L - 1) * 32 + (lane & 7) * 4) = f32x4{t0, t1, t2, t3};
          }
        };
        if (isB && layer == 1) ldcs();
        if constexpr (is0) {
          if (L == 3) flag(WS_F1 + 2048);
          q_groupW(0, 2, 2); q_groupW(0, 3, 3);
          wait_vm<12>();
          if (L >= 2) glds_run<1>(v16, (const char*)p.wphi + head * 2048 + (L - 2) * 1024, sbase + L_WP + (L - 2) * 1024);
        }
        STAMP(1);
        if (!is0) PRIO(3);
        sync_lds();
        STAMP(2);
        OPAQUE_S
        u32x4 s0 = u32x4{0u, 0u, 0u, 0u}, s1 = s0;
        auto ldst = [&]() AI {
          const unsigned so = ar + AR_L2 + (bm * 16 + L * 4) * 256;
          s0 = ld16s(wsr, v16, so);
          if (is0) s1 = ld16s(wsr, v16, so + 32768);
          if (isB && L == 0) s1 = ld16s(wsr, v16 & 255, bm > 0 ? so - 256 : so);
          MEMFENCE;
        };
        auto fin = [&](float sm, float sq, int r, bool own) AI {
          float mean = sm * (1.0f / 1024.0f);
          float var = fmaxf(__builtin_fmaf(sq, 1.0f / 1024.0f, -mul1(mean, mean)), 0.f);
          if (own) lmean[r] = -mean;
          lrstd[r] = __builtin_amdgcn_rsqf(var + 1e-5f);
        };
        auto wrst = [&]() AI {
          while (true) {
            unsigned d = (s0[1] ^ tag0) | (s0[3] ^ tag1);
            if (is0 || (isB && L == 0)) d |= (s1[1] ^ tag0) | (s1[3] ^ tag1);
            if (__builtin_expect(__builtin_amdgcn_ballot_w64(d != 0) == 0, 1) || --budget <= 0) break;
            COUNT(1);
            ldst();
          }
          MEMFENCE;
          float sm = __uint_as_float(s0[0]), sq = __uint_as_float(s0[2]);
          if (is0) { sm += __uint_as_float(s1[0]); sq += __uint_as_float(s1[2]); }
          fin(sm, sq, 16 + L * 64 + lane, true);
          if (isB && L == 0) fin(__uint_as_float(s1[0]), __uint_as_float(s1[2]), lane & 15, false);
        };
        auto q_group = [&](int g) AI {
          if (is0) {
            if (L & 1) SLEEP(SPR0);
            if (L & 2) SLEEP(2 * SPR0);
            q_groupW(layer, g, g & 3);
          } else if (g >= 5) q_groupW(layer, g, g < 6 ? g : g - 4);
        };
        if (!is0 && L == 0) {
          poll(ar + AR_FL, []() {}, 15);
          if (head < 16) {
            const unsigned so = ar + AR_ST + (bm * 16 + head) * 2048;
            s0 = ld16s(wsr, v16, so); s1 = ld16s(wsr, v16 + 1024, so);
            MEMFENCE;
          }
        }
        if (is0) {
          drain();
          poll(WS_F1 + 2048, [&]() AI { extra(1); });
          ldcs();
          extra2(1);
        }
        if (isB && layer == 1) wrcs();
        MEMFENCE;
        STAMP(3);
        if (!is0) PRIO(0);
        sync_lds();
        STAMP(4);
        OPAQUE_S
        if (!is0) drain();
        if (!is0 && L == 0 && head < 16) {
          f32x4 t = add4(__builtin_bit_cast(f32x4, s0), __builtin_bit_cast(f32x4, s1));
#pragma unroll
          for (int e = 0; e < 4; ++e)
            t[e] += __uint_as_float(__builtin_amdgcn_update_dpp(0u, __float_as_uint(t[e]), 0x128, 0xf, 0xf, false));
          const bool hi = lane & 8;
          put_l2(hi ? t[2] : t[0], hi ? t[3] : t[1], (lane & 7) * 32 + (lane & 8) * 2, ar + AR_L2 + (bm * 16 + head) * 256);
        }
        q_group(4);
        extra(2); extra2(2);
        STAMP(5);
        sync_lds();
        STAMP(6);
        OPAQUE_S
        drain();
        q_group(5);
        if (is0) wrcs();
        STAMP(7);
        bar();
        OPAQUE_S
        drain();
        if (is0 && L == 0 && v16 == 0) __builtin_amdgcn_raw_buffer_store_b64(u32x2{tag0, tag1}, wsr, 0, WS_F2 + (bm * 32 + head) * 8, 16);
        q_group(6);
        ldst();
        STAMP(8);
        bar();
        STAMP(9);
        OPAQUE_S
        drain();
        q_group(7);
        extra2(5);
        SCHED0;
        wrst();
        STAMP(10);
        bar();
        STAMP(11);
        OPAQUE_S
        drain();
        if (isB && layer == 3 && L == 3) {
          asm volatile("global_load_dword %0, %1, %2" : "=v"(tch) : "v"((unsigned)((bm * 256 + cm * 16 + (lane & 7) * 32) * 2048 + cn * 128)), "s"(p.out) : "memory");
        }
        extra(6);
        if (isB) stg6(96 * 1024 + L * 6144);
        SCHED0;
      }
      STAMP(12);
      sync_lds();
      STAMP(13);
      OPAQUE_S
      u32x4 fl[2];
      fl[0] = fl[1] = u32x4{tag0, tag1, tag0, tag1};
      auto ldfl = [&]() AI { fl[0] = ld16s(wsr, v16, WS_F2); fl[1] = ld16s(wsr, v16 + 1024, WS_F2); };
      if (is0) ldfl();
      bar();
      constexpr int NR = isB ? 6 : 9;
      const unsigned ro = isB ? 48 * 1024 + L * 6144 : L * 9216;
      u32x4 r[NR];
      bool pend = false;
      auto flags = [&]() AI {
        const u32x4 d = (fl[0] ^ u32x4{tag0, tag1, tag0, tag1}) | (fl[1] ^ u32x4{tag0, tag1, tag0, tag1});
        pend = __builtin_amdgcn_ballot_w64((d[0] | d[1] | d[2] | d[3]) != 0) != 0;
      };
      if (isB) {
        stg6(L * 6144); stg(120 * 1024 + L * 2048, IC<2>{});
        stg6(24 * 1024 + L * 6144);
        stg6(72 * 1024 + L * 6144);
      } else {
        drain();
        if (is0) flags();
        MEMFENCE;
        extra(8); extra2(8);
        if (!is0) poison_x(3);
        while (is0 && __builtin_expect(pend, 0) && --budget > 0) {
          ldfl();
          flags();
          MEMFENCE;
        }
        stg(36 * 1024 + L * 3072, IC<3>{}); stg7(72 * 1024 + L * 14336); stg7(72 * 1024 + L * 14336 + 7168);
      }
#pragma unroll
      for (int i = 0; i < NR; ++i) r[i] = ld16s(wsr, v16 + (i & 3) * 1024, sW() + ro + (i >> 2) * 4096);
      MEMFENCE;
      if (is0 && bm + head + L == 0 && v16 == 0) __builtin_amdgcn_raw_buffer_store_b32(tag0, wsr, 0, WS_EP, 16);
      auto wr_r = [&]() AI {
        unsigned vv = v16;
        asm volatile("" : "+v"(vv));
#pragma unroll
        for (int i = 0; i < NR; ++i) *(u32x4*)(smem + L_STG + ro + i * 1024 + vv) = r[i];
      };
      auto left = [&]() AI {
        OPAQUE_LANE
        for (;;) {
          MEMFENCE;
          if (__builtin_amdgcn_ballot_w64(lflag[lane & 7] != (unsigned)layer) == 0 || --budget <= 0) break;
          SLEEP(2);
        }
        MEMFENCE;
      };
      if (isB) {
#pragma unroll
        for (int i = 0; i < 24; ++i) asm volatile("" : "=v"(wn[i / 6][i % 6]));
        if (layer == 1) ld_wn(layer + 1, 0, 2);
        STAMP(14);
        left();
        wr_r();
        MEMFENCE;
        if (layer == 1) ld_wn(layer + 1, 2, 4);
        wait_vm_lgkm0<12>();
        if (layer == 3) wait_vm<0>();
        asm volatile("" :: "v"(tch));
      } else {
        constexpr int W0 = is0 ? 0 : 2, W1 = is0 ? 1 : 2;
        ld_wn(layer + 1, 0, W0);
        if (is0) wait_vm<26>();
        STAMP(14);
        bar();
        stg6(48 * 1024 + L * 6144);
        ld_wn(layer + 1, W0, W1);
        bar();
        extra2(9);
        ld_wn(layer + 1, W1, 4);
        wr_r();
        wait_vm<6 * (4 - W1)>();
        bar();
        extra2(10);
        wait_vm_lgkm0<0>();
      }
      bar();
      if (isB && layer == 3) { STAMP_DUMP; __builtin_amdgcn_endpgm(); }
      OPAQUE_S
      wr_wn(0);
      drain();
      sync_lds();
      OPAQUE_S
      SCHED0;
#pragma unroll
      for (int g = 1; g < 4; ++g) wr_wn(g);
      MEMFENCE;
      SLEEP(SLP_G);
      q_groupW(layer + 1, 4, 4);
    };
    body(IC<0>{}, 0);
#pragma unroll 1
    for (int layer = 1;; layer += 2) {
      body(IC<1>{}, layer);
      body(IC<2>{}, 2);
    }
  }

  const f32x4 zero = f32x4{0.f, 0.f, 0.f, 0.f};
  u32x4 xr[2];
  asm volatile("" : "=v"(xr[0]), "=v"(xr[1]));
  u32x4 sel;
  u32x4 eye;
  unsigned vk;
  unsigned ra0, ra1;
  float off0, off1;
  {
    const int lane = LANEID, fr = lane & 15, fq = lane >> 4;
    const bool odd = fr & 1, own0 = fr >= fq * 4 + (fr & 1), own1 = fr >= fq * 4 + (fr & 1) + 2, nopre = bm0 == 0 && wave == 4;
    const unsigned e0 = odd ? 0x01000c0cu : 0x0c0c0100u, e1 = odd ? 0x03020c0cu : 0x0c0c0302u, ez = 0x0c0c0c0cu;
    sel = u32x4{own0 ? ez : e0, own1 ? ez : e1, own0 ? e0 : ez, own1 ? e1 : ez};
    const unsigned rb = sbase + L_RED + 1088 + ((wave - 4) * 32 + fq * 4 + (fr & 1)) * 4;
    ra0 = rb + (own0 ? 64 : 0); ra1 = rb + (own1 ? 72 : 8);
    off0 = nopre && !own0 ? -3.0e38f : 0.f; off1 = nopre && !own1 ? -3.0e38f : 0.f;
    const unsigned one = (fq & 1) == (fr >> 3) ? (fr & 1 ? 0x3f800000u : 0x00003f80u) : 0u;
    eye = u32x4{(fr & 6) == 0 ? one : 0u, (fr & 6) == 2 ? one : 0u, (fr & 6) == 4 ? one : 0u, (fr & 6) == 6 ? one : 0u};
    vk = (fq >> 1) * (32 * KVREC) + (fq & 1) * 256 + fr * 16;
    asm volatile("" : "+v"(sel), "+v"(ra0), "+v"(ra1), "+v"(off0), "+v"(off1), "+v"(eye), "+v"(vk));
  }
#pragma unroll 1
  for (int layer = 0; layer < 4; ++layer) {
    bm = bm0; head = head0; cm = cm0; cn = cn0; cw = wave - 4;
    const bool isB = layer & 1;
    const unsigned ar = WS_AR + layer * ARENA;
    {
      f32x4 acc[2][6];
      f32x4 ck[2], mv[2], xv[2];
      float mean[2], rs[2], cv[2];
      u32x2 wt[4];
      const float* lcl = lcs + (isB ? 96 : 0);
      {
        LANE_VARS
        const unsigned va = v16 + cw * 2048;
        const unsigned sa0 = ar + AR_X + bm * (512 * 1024);
        auto lda = [&](unsigned so, u32x4* a) AI {
          a[0] = ld16s(wsr, va, so);
          a[1] = ld16s(wsr, va + 1024, so);
        };
        bf16x8 f[2][6];
        u32x4 a[4][2];
        auto unset = [&](const u32x4* e) AI { return poisoned(umax(umax(umax(e[0][0], e[0][2]), e[1][0]), e[1][2])); };
        auto refill = [&](auto ic, unsigned cur) AI {
          constexpr int I = decltype(ic)::value;
          do {
            COUNT(0);
            SLEEP(SLP_R);
            MEMFENCE;
            lda(cur, a[I]);
            bool u[4];
#pragma unroll
            for (int j = 1; j < 4; ++j) u[j] = unset(a[(I + j) % 4]);
            SCHED0;
#pragma unroll
            for (int j = 1; j < 4; ++j)
              if (u[j]) lda(cur + j * 16384, a[(I + j) % 4]);
            __builtin_amdgcn_s_waitcnt(0x0f70);
            MEMFENCE;
          } while (unset(a[I]) && --budget > 0);
        };
        STAMP(0);
        if (layer > 0) {
          bar();
          if (cn < 8) { if (cw < 4) SLEEP(SLP_X0); else SLEEP(SLP_X1); }
          else { if (cw < 4) SLEEP(SLP_XD0); else SLEEP(SLP_XD1); }
        }
#pragma unroll
        for (int j = 0; j < 4; ++j) { lda(sa0 + j * 16384, a[j]); SCHED0; }
#pragma unroll
        for (int m = 0; m < 2; ++m)
#pragma unroll
          for (int n = 0; n < 6; ++n) acc[m][n] = zero;
        asm volatile("" : "+v"(acc[0][0]), "+v"(acc[0][1]), "+v"(acc[0][2]), "+v"(acc[0][3]), "+v"(acc[0][4]), "+v"(acc[0][5]));
        asm volatile("" : "+v"(acc[1][0]), "+v"(acc[1][1]), "+v"(acc[1][2]), "+v"(acc[1][3]), "+v"(acc[1][4]), "+v"(acc[1][5]));
        if (layer == 0) { wait_vm<8>(); sync_lds(); }
#pragma unroll
        for (int nn = 0; nn < 6; ++nn) { const int n = nn < 4 ? nn + 2 : nn - 4; f[0][n] = *(const bf16x8*)(smem + v16 + n * 1024); }
        SCHED0;
        if (layer > 0) {
          const u32x2 tg = *(const u32x2*)(lflag + 10);
          wait_vm<8>();
          unsigned old;
          asm volatile("s_mov_b64 exec, 1\n\tds_add_rtn_u32 %0, %1, %2\n\ts_mov_b64 exec, -1\n\ts_waitcnt lgkmcnt(0)" : "=v"(old) : "v"(sbase + L_RED + 32), "v"(1) : "memory");
          if (__builtin_amdgcn_readfirstlane(old) == 8u * layer - 1)
            __builtin_amdgcn_raw_buffer_store_b64(tg, wsr, lane ? OOB : 0, ar + AR_FL + bm * 256 + (cm * 16 + cn) * 8, 16);
          MEMFENCE;
        }
        SCHED0;
        bool bad = layer > 0 && unset(a[0]);
        const int wrap = layer > 0 ? 6 : 4;
        unsigned ps[4] = {sbase + L_RED + 64 + cw * 128, sbase + L_RED + 2176 + (layer & 1) * 384, 0, sbase + L_WP};
        ps[2] = ps[0] + 1088 + (layer & 1) * (2176 + 384 - 1088 - 64 - cw * 128);
        static_assert(L_WP % 1024 == 0, "");
        int early = (layer & 1) * cw;
        asm volatile("" : "+s"(ps[0]), "+s"(ps[1]), "+s"(ps[2]), "+s"(ps[3]), "+s"(early));
        STAMP(1);
        auto group = [&](auto modec, int g) AI {
          constexpr int MODE = decltype(modec)::value;
          constexpr bool LAST = MODE == 3, CHK = MODE == 0;
          const unsigned gs = (g < wrap ? g : g - 4) * QGRP, gn = (g + 1 < wrap ? g + 1 : g - 3) * QGRP;
          const unsigned sc = sa0 + g * 65536, so = sc + 4 * 16384;
          unsigned gl = sbase + gs + v16;
          if constexpr (LAST) asm volatile("" : "+v"(gl));
          unsigned pa[5];
          static_for<4>([&](auto ic) AI {
            constexpr int i = decltype(ic)::value;
            constexpr bool END = LAST && i == 3;
            SCHED0;
            if (i == 3 && !LAST) { sync_lds(); SCHED0; }
            if (i == 0 && LAST) {
              PRIO(0);
              if (early >= 4) PRIO(3);
              SCHED0;
              bar();
              SCHED0;
            }
            if constexpr (!LAST) PRIO(i == 3 ? 3 : 2 - i);
            if constexpr (CHK) { if (__builtin_expect(bad, 0)) refill(ic, sc + i * 16384); }
            SCHED0;
            const bf16x8* fc = f[i & 1];
            bf16x8* fn = f[(i + 1) & 1];
            const u32x4* an = a[(i + 1) % 4];
            const unsigned slot = i < 3 ? gs + (i + 1) * QSLOT : gn;
            const char* pb = LAST ? (const char*)((const LDS(char)*)gl + (i + 1) * QSLOT) : smem + slot + v16;
            unsigned mx = 0;
            static_for<12>([&](auto jc) AI {
              constexpr int j = decltype(jc)::value, nn = j >> 1, m = j & 1, n = nn < 4 ? nn + 2 : nn - 4;
              if constexpr (n < 4) mfma_acc(acc[m][n], fc[n], as_frag(a[i][m]));
              else mfma_acc(acc[m][n], as_frag(a[i][m]), fc[n]);
              SCHED0;
              if constexpr (!END && j < 6) { constexpr int r = j < 4 ? j + 2 : j - 4; fn[r] = *(const bf16x8*)(pb + r * 1024); }
              if constexpr (LAST && i == 0) {
                if constexpr (j == 7) { pa[4] = v16 >> 1; asm volatile("" : "+v"(pa[4])); }
                if constexpr (j == 9) { pa[4] = (pa[4] & 24) | ps[3]; asm volatile("" : "+v"(pa[4])); }
                if constexpr (j == 11) { pa[4] |= v16 & ~63u; asm volatile("" : "+v"(pa[4])); }
              }
              if constexpr (LAST && i == 1) {
                if constexpr (j == 6) { pa[0] = lane & 48; asm volatile("" : "+v"(pa[0])); }
                if constexpr (j == 7) { pa[1] = v16 >> 2; asm volatile("" : "+v"(pa[1])); }
                if constexpr (j == 8) { pa[1] &= 60; asm volatile("" : "+v"(pa[1])); }
                if constexpr (j == 9) { pa[2] = pa[1] + ps[0]; asm volatile("" : "+v"(pa[2])); }
                if constexpr (j == 10) { pa[1] += ps[1]; asm volatile("" : "+v"(pa[1])); }
                if constexpr (j == 11) { pa[3] = pa[0] + ps[1]; asm volatile("" : "+v"(pa[3])); }
              }
              if constexpr (LAST && i == 2) {
                if constexpr (j == 6) ck[0] = ((const LDS(f32x4)*)pa[3])[8];
                if constexpr (j == 7) ck[1] = ((const LDS(f32x4)*)pa[3])[12];
                if constexpr (j == 8) { mean[0] = ((const LDS(float)*)pa[2])[0]; asm volatile("" : "+v"(pa[2])); }
                if constexpr (j == 9) { mean[1] = ((const LDS(float)*)pa[2])[16]; asm volatile("" : "+v"(pa[2])); }
                if constexpr (j == 10) { cv[0] = ((const LDS(float)*)pa[1])[64]; cv[1] = ((const LDS(float)*)pa[1])[80]; }
                if constexpr (j == 11) { pa[1] = pa[0] + ps[0]; asm volatile("" : "+v"(pa[1])); }
              }
              if constexpr (LAST && i == 3) {
                if constexpr (j == 0) mv[0] = ((const LDS(f32x4)*)pa[1])[0];
                if constexpr (j == 1) mv[1] = ((const LDS(f32x4)*)pa[1])[4];
                if constexpr (j == 2) { pa[0] += ps[2]; asm volatile("" : "+v"(pa[0])); }
                if constexpr (j == 3) { rs[0] = ((const LDS(float)*)pa[2])[272]; asm volatile("" : "+v"(pa[2])); }
                if constexpr (j == 4) xv[0] = ((const LDS(f32x4)*)pa[0])[0];
                if constexpr (j == 5) xv[1] = ((const LDS(f32x4)*)pa[0])[4];
                if constexpr (j == 6) rs[1] = ((const LDS(float)*)pa[2])[288];
                if constexpr (j == 8) wt[0] = trl(pa[4]);
                if constexpr (j == 9) wt[1] = trl(pa[4] + 1024);
                if constexpr (j == 10) wt[2] = trl(pa[4] + 32);
                if constexpr (j == 11) wt[3] = trl(pa[4] + 1056);
              }
              if constexpr (CHK) {
                if constexpr (j == 8) mx = umax(umax(umax(an[0][0], an[0][2]), an[1][0]), an[1][2]);
                if constexpr (j == 10) bad = poisoned(mx);
              }
              if constexpr (j == 10 && !LAST) a[i][0] = ld16s(wsr, va, so + i * 16384);
              if constexpr (j == 11 && !LAST) a[i][1] = ld16s(wsr, va + 1024, so + i * 16384);
              SCHED0;
            });
          });
        };
        int g = 0;
        if (layer > 0) {
#pragma unroll 1
          for (; g < NCHK; ++g) group(IC<0>{}, g);
        }
#pragma unroll 1
        for (; g < 7; ++g) group(IC<1>{}, g);
        group(IC<3>{}, 7);
        if (cw < 4) PRIO(1); else if (!isB) PRIO(2);
        mfma_done();
      }
      STAMP(2);
      LANE_VARS
      char* const scr = smem;

      u32x4 qf[2], kf[2];
      u32x2 vf[2][2];
      auto fold_rows = [&](int part, u32x4* out, const f32x4* c) AI {
#pragma unroll
        for (int m = 0; m < 2; ++m) {
          f32x4 a0 = acc[m][part * 2] + c[0] * mean[m], a1 = acc[m][part * 2 + 1] + c[1] * mean[m];
          out[m] = u32x4{pk2(a0[0], a0[1]), pk2(a0[2], a0[3]), pk2(a1[0], a1[1]), pk2(a1[2], a1[3])};
        }
      };
      auto fold_v = [&](bool scaled) AI {
#pragma unroll
        for (int m = 0; m < 2; ++m) {
          f32x4 a0 = acc[m][4] + mv[m] * cv[0], a1 = acc[m][5] + mv[m] * cv[1];
          if (scaled) { a0 = mul4(a0, xv[m]); a1 = mul4(a1, xv[m]); }
          vf[m][0] = u32x2{pk2(a0[0], a0[1]), pk2(a0[2], a0[3])};
          vf[m][1] = u32x2{pk2(a1[0], a1[1]), pk2(a1[2], a1[3])};
        }
      };
      const unsigned otile = WS_AR + AR_O + xt_off(bm * 16 + cw * 2, head);
      auto put_o = [&](int m, f32x4 a, f32x4 b, unsigned opar) AI {
        const u32x4 v = u32x4{(pk2(a[0], a[1]) & ~1u) | opar, pk2(a[2], a[3]), (pk2(b[0], b[1]) & ~1u) | opar, pk2(b[2], b[3])};
        st16s(wsr, v16 + m * 1024, otile, v);
        return v;
      };
      auto hold = [&](u32x4 a, u32x4 b) AI { SCHED0; asm volatile("s_nop 1" :: "v"(a), "v"(b)); SCHED0; };

      if (!isB) {
        fold_rows(1, kf, ck); fold_v(true);
        char* PK = smem + L_STG;
        char* VF = scr;
        const bf16x8 wf0 = as_frag(u32x4{wt[0][0], wt[0][1], wt[1][0], wt[1][1]}), wf1 = as_frag(u32x4{wt[2][0], wt[2][1], wt[3][0], wt[3][1]});
#pragma unroll
        for (int t = 0; t < 2; ++t)
          *(u32x4*)(VF + (cw * 2 + t) * 1024 + lane * 16) = u32x4{vf[0][t][0], vf[0][t][1], vf[1][t][0], vf[1][t][1]};
        {
          u32x4 pk[2][2];
          phi_map2(kf, rs, wf0, wf1, pk, [](float*) {});
          char* d0 = PK + cw * 4096 + fr * 64 + ((fr >> 2) & 1) * 32 + ((fq ^ ((fr >> 3) * 2)) * 8);
          char* d1 = PK + cw * 4096 + fr * 64 + (((fr >> 2) & 1) ^ 1) * 32 + ((fq ^ ((fr >> 3) * 2)) * 8);
#pragma unroll
          for (int m = 0; m < 2; ++m) {
            *(u32x2*)(d0 + m * 2048) = u32x2{pk[m][0][0], pk[m][0][1]};
            *(u32x2*)(d0 + m * 2048 + 1024) = u32x2{pk[m][0][2], pk[m][0][3]};
            *(u32x2*)(d1 + m * 2048) = u32x2{pk[m][1][0], pk[m][1][1]};
            *(u32x2*)(d1 + m * 2048 + 1024) = u32x2{pk[m][1][2], pk[m][1][3]};
          }
        }
        PRIO(1);
        const unsigned rec = ar + AR_KV + (unsigned)head * KVREC, dst = rec + (unsigned)bm * (32 * KVREC);
        unsigned ad = sbase + L_STG + (cw & 1) * 1024 + (fq * 4 + (fr >> 2)) * 64 + (((cw >> 1) ^ fq) & 1) * 32 + (((fr & 3) ^ (fq & 2)) * 8);
        unsigned vd = sbase + (cw >> 2) * 1024 + v16, kd = fq & 1 ? OOB : dst + cw * 512 + (fq >> 1) * 256 + fr * 16;
        asm volatile("" : "+v"(ad), "+v"(vd), "+v"(kd));
        sync_lds();
        STAMP(3);
        {
          const u32x4 ones = u32x4{0x3f803f80u, 0x3f803f80u, 0x3f803f80u, 0x3f803f80u};
          f32x4 c0 = zero;
          bf16x8 a[8], b0[8];
#pragma unroll
          for (int ks = 0; ks < 8; ++ks) {
            u32x2 a0 = trl(ad + ks * 4096), a1 = trl(ad + ks * 4096 + 2048);
            a[ks] = as_frag(u32x4{a0[0], a0[1], a1[0], a1[1]});
            b0[ks] = *(const LDS(bf16x8)*)(vd + ks * 2048);
            SCHED0;
          }
#pragma unroll
          for (int ks = 0; ks < 8; ++ks) c0 = mfma16(a[ks], b0[ks], c0);
          const unsigned p0 = umin(pk2(c0[0], c0[1]), POISON - 1), p1 = umin(pk2(c0[2], c0[3]), POISON - 1);
          const auto q0 = __builtin_amdgcn_permlane16_swap(p0, p0, false, false), q1 = __builtin_amdgcn_permlane16_swap(p1, p1, false, false);
          st16(wsr, kd, u32x4{q0[0], q1[0], q0[1], q1[1]});
          SCHED0;
          if (cw < 4) {
            f32x4 cz = zero;
#pragma unroll
            for (int ks = 0; ks < 8; ++ks) cz = mfma16(a[ks], as_frag(ones), cz);
            st16(wsr, fr ? OOB : dst + 4096 + cw * 64 + fq * 16, clean(__builtin_bit_cast(u32x4, cz)));
          }
        }
        SCHED0;
        STAMP(4);
        bar();
        if (layer == 0) {
#pragma unroll
          for (int j = 0; j < 2; ++j) xr[j] = ld16s(wsr, v16, WS_AR + AR_X + xt_off(bm * 16 + cw * 2 + cm, cn * 2 + j));
          MEMFENCE;
        }
        const f32x4 cq[2] = {*(const f32x4*)(lcl + fq * 4), *(const f32x4*)(lcl + 16 + fq * 4)};
        fold_rows(0, qf, cq);
        u32x4 pq[2][2];
        u32x4 zp, zq;
        u32x4 part[4];
        auto ldp = [&](int i) AI { part[i] = ld16s(wsr, vk, rec + i * (64 * KVREC) + cw * 512); };
        auto ldz = [&]() AI {
          zp = ld16(wsr, rec + fq * (32 * KVREC) + 4096 + fr * 16);
          zq = ld16(wsr, rec + (4 + fq) * (32 * KVREC) + 4096 + fr * 16);
        };
#pragma unroll
        for (int i = 0; i < 4; ++i) asm volatile("" : "=v"(part[i]));
        asm volatile("" : "=v"(zp), "=v"(zq));
        phi_map2(qf, rs, wf0, wf1, pq, [&](float* sp) AI {
          asm volatile("" : "+v"(sp[0]), "+v"(sp[1]));
          SCHED0;
          if (cw >= 4) {
            MEMFENCE;
#pragma unroll
            for (int i = 0; i < 4; ++i) ldp(i);
            MEMFENCE;
          }
          SCHED0;
        });
        asm volatile("" : "+v"(pq[0][0]), "+v"(pq[0][1]), "+v"(pq[1][0]), "+v"(pq[1][1]));
        SCHED0;
        STAMP(5);
        char* kvf = smem + L_A2;
        if (cw < 4) {
          SLEEP(SLP_K);
          MEMFENCE;
          if (cw == 0) ldz();
#pragma unroll
          for (int i = 0; i < 4; ++i) ldp(i);
          MEMFENCE;
        }
        unsigned kw = sbase + L_A2 + v16 + (cw >> 1) * 1024 + (cw & 1) * 8;
        unsigned ka = sbase + L_A2 + v16, za = sbase + L_Z + fq * 16;
        asm volatile("" : "+v"(kw), "+v"(ka), "+v"(za));
        u32x2 kvs;
        auto nans = [&](f32x4 v) AI { return __builtin_amdgcn_ballot_w64(__builtin_isunordered(v[0], v[1])) | __builtin_amdgcn_ballot_w64(__builtin_isunordered(v[2], v[3])); };
        auto sums = [&]() AI {
          unsigned long long nan = 0;
          if (cw == 0) {
            f32x4 z = __builtin_bit_cast(f32x4, zp) + __builtin_bit_cast(f32x4, zq);
            nan = nans(z);
            float z0 = z[0], z1 = z[1], z2 = z[2], z3 = z[3];
            xred4<false>(z0, z1, z2, z3);
            *(u32x2*)(smem + L_Z + (fr >> 3) * 64 + (fr & 3) * 16 + ((fr >> 2) & 1) * 8) = u32x2{pk2(z0, z1), pk2(z2, z3)};
          }
          f32x4 s4 = zero;
#pragma unroll
          for (int i = 0; i < 4; ++i) { s4 = mfma16(as_frag(eye), as_frag(part[i]), s4); SCHED0; }
          kvs = u32x2{pk2(s4[0], s4[1]), pk2(s4[2], s4[3])};
          asm volatile("" : "+v"(kvs));
          return nan | __builtin_amdgcn_ballot_w64(s4[0] != s4[0]);
        };
        if (__builtin_expect(sums() != 0, 0)) {
          for (;;) {
            bool again = false;
            if (cw == 0 && poisoned(umax4(umax4(zp), zq))) { again = true; ldz(); }
#pragma unroll
            for (int i = 0; i < 4; ++i)
              if (poisoned(umax4(part[i]))) { again = true; ldp(i); }
            __builtin_amdgcn_s_waitcnt(0x0f70);
            MEMFENCE;
            if (!again || --budget <= 0) break;
            COUNT(2);
          }
          sums();
        }
        STAMP(6);
        *(LDS(u32x2)*)kw = kvs;
        sync_lds();
        STAMP(7);
        bf16x8 kvr[2][2], zf[2];
#pragma unroll
        for (int s = 0; s < 2; ++s) { zf[s] = *(const LDS(bf16x8)*)(za + s * 64); SCHED0; }
#pragma unroll
        for (int s = 0; s < 2; ++s) {
          kvr[0][s] = *(const LDS(bf16x8)*)(ka + (0 * 2 + s) * 1024);
          kvr[1][s] = *(const LDS(bf16x8)*)(ka + (1 * 2 + s) * 1024);
          SCHED0;
        }
        f32x4 cd[2], c[2][2];
        u32x4 oo[2];
        auto den = [&](int m, int s) AI { cd[m] = mfma16(zf[s], as_frag(pq[m][s]), s ? cd[m] : zero); SCHED0; };
        auto num = [&](int m, int s) AI {
#pragma unroll
          for (int t = 0; t < 2; ++t) { c[m][t] = mfma16(kvr[t][s], as_frag(pq[m][s]), s ? c[m][t] : zero); SCHED0; }
        };
        auto fin = [&](int m, float id) AI { const f32x4 i4 = f32x4{id, id, id, id}; oo[m] = put_o(m, mul4(c[m][0], i4), mul4(c[m][1], i4), 0); SCHED0; };
        den(0, 0); den(1, 0); den(0, 1); den(1, 1);
        num(0, 0); num(0, 1); num(1, 0);
        const float i0 = __builtin_amdgcn_rcpf(cd[0][0] + 1e-6f), i1 = __builtin_amdgcn_rcpf(cd[1][0] + 1e-6f);
        SCHED0;
        num(1, 1);
        fin(0, i0); fin(1, i1);
        hold(oo[0], oo[1]);
        STAMP(8);
        asm volatile("" : "=v"(acc[0][0]), "=v"(acc[0][1]), "=v"(acc[0][2]), "=v"(acc[0][3]), "=v"(acc[0][4]), "=v"(acc[0][5]));
        asm volatile("" : "=v"(acc[1][0]), "=v"(acc[1][1]), "=v"(acc[1][2]), "=v"(acc[1][3]), "=v"(acc[1][4]), "=v"(acc[1][5]));
        asm volatile("" : "=v"(ck[0]), "=v"(ck[1]), "=v"(mv[0]), "=v"(mv[1]), "=v"(xv[0]), "=v"(xv[1]));
        asm volatile("" : "=v"(mean[0]), "=v"(mean[1]), "=v"(rs[0]), "=v"(rs[1]), "=v"(cv[0]), "=v"(cv[1]));
      }
      int kind = layer & 1;
      asm volatile("" : "+s"(kind));
      if (kind) {
        char* ex = scr;
        fold_rows(1, kf, ck); fold_v(false);
        const unsigned hrec = ar + AR_KV + (unsigned)(bm * 32 + head) * KVREC;
        {
          const u32x4 vl = u32x4{vf[1][0][0], vf[1][0][1], vf[1][1][0], vf[1][1][1]};
          if (cw == 7) {
            st16s(wsr, v16, hrec, clean(kf[1]));
            STPAD;
            st16s(wsr, v16 + 1024, hrec, clean(vl));
            STPAD;
          }
          char* e2 = ex + (cw + 1) * 2048;
          *(u32x4*)(e2 + lane * 16) = kf[1];
          *(u32x2*)(e2 + 1024 + lane * 8) = vf[1][0];
          *(u32x2*)(e2 + 1536 + lane * 8) = vf[1][1];
          MEMFENCE;
          lflag[cw] = layer;
          MEMFENCE;
          PRIO(1);
        }
        SCHED0;
        fold_rows(0, qf, xv);
        u32x4 kp;
        u32x4 va0 = __builtin_shufflevector(vf[0][0], vf[0][0], -1, -1, 0, 1), vb0 = __builtin_shufflevector(vf[0][1], vf[0][1], -1, -1, 0, 1);
        auto setp = [&](u32x4& d, u32x2 v) AI { d[0] = v[0]; d[1] = v[1]; };
        auto ldh = [&]() AI {
          MEMFENCE;
          kp = ld16s(wsr, v16, hrec - 32 * KVREC);
          setp(va0, __builtin_amdgcn_raw_buffer_load_b64(wsr, v16 + 1024, hrec - 32 * KVREC, 16));
          MEMFENCE;
          setp(vb0, __builtin_amdgcn_raw_buffer_load_b64(wsr, v16 + 1032, hrec - 32 * KVREC, 16));
          MEMFENCE;
        };
        unsigned fl = 0;
        auto rdl = [&]() AI {
          MEMFENCE;
          fl = lflag[cw - 1];
          MEMFENCE;
          kp = *(const u32x4*)(ex + cw * 2048 + lane * 16);
          setp(va0, *(const u32x2*)(ex + cw * 2048 + 1024 + lane * 8));
          MEMFENCE;
          setp(vb0, *(const u32x2*)(ex + cw * 2048 + 1536 + lane * 8));
          MEMFENCE;
        };
        auto prev = [&]() AI {
          if (cw > 0) {
            do { SLEEP(1); rdl(); __builtin_amdgcn_s_waitcnt(0xc07f); } while (__builtin_amdgcn_readfirstlane(fl) != (unsigned)layer && --budget > 0);
          } else if (bm > 0) {
            SLEEP(SLP_HX);
            do { ldh(); __builtin_amdgcn_s_waitcnt(0x0f70); }
            while (poisoned(umax4(umax4(kp), u32x4{va0[0], va0[1], vb0[0], vb0[1]})) && --budget > 0);
          } else {
            kp = u32x4{0u, 0u, 0u, 0u};
            setp(va0, u32x2{0u, 0u}); setp(vb0, u32x2{0u, 0u});
          }
          asm volatile("" : "+v"(kp), "+v"(va0), "+v"(vb0));
        };
        const float scale = 0.17677669529663687f * LOG2E;
        const bool odd = fr & 1;
        const bool own0 = sel[0] == 0x0c0c0c0cu, own1 = sel[1] == 0x0c0c0c0cu;
        float l[2][2], mx[2], rk[2][2], sum[2];
#pragma unroll
        for (int m = 0; m < 2; ++m) { rk[m][0] = ((const LDS(float)*)ra0)[m * 16]; rk[m][1] = ((const LDS(float)*)ra1)[m * 16]; }
        float qk[2][2];
#pragma unroll
        for (int m = 0; m < 2; ++m) { const float qs = rs[m] * scale; qk[m][0] = qs * rk[m][0]; qk[m][1] = qs * rk[m][1]; }
        u32x4 va1 = u32x4{vf[0][0][0], vf[0][0][1], vf[1][0][0], vf[1][0][1]}, vb1 = u32x4{vf[0][1][0], vf[0][1][1], vf[1][1][0], vf[1][1][1]};
        auto score = [&](int m) AI {
          f32x4 s0 = mfma16(as_frag(m == 0 ? kp : kf[0]), as_frag(qf[m]), zero);
          f32x4 s1 = mfma16(as_frag(kf[m]), as_frag(qf[m]), zero);
          float a = own0 ? (odd ? s1[1] : s1[0]) : (odd ? s0[1] : s0[0]);
          float b = own1 ? (odd ? s1[3] : s1[2]) : (odd ? s0[3] : s0[2]);
          if (m == 0) { l[m][0] = __builtin_fmaf(a, qk[m][0], off0); l[m][1] = __builtin_fmaf(b, qk[m][1], off1); }
          else { l[m][0] = a * qk[m][0]; l[m][1] = b * qk[m][1]; }
          mx[m] = fmx(l[m][0], l[m][1]);
        };
        auto expo = [&](int m) AI {
          l[m][0] = __builtin_amdgcn_exp2f(l[m][0] - mx[m]);
          l[m][1] = __builtin_amdgcn_exp2f(l[m][1] - mx[m]);
          sum[m] = l[m][0] + l[m][1];
        };
        f32x4 ov[2][2];
        auto pv = [&](int m) AI {
          const unsigned pr = pk2(mul1(mul1(l[m][0], rk[m][0]), sum[m]), mul1(mul1(l[m][1], rk[m][1]), sum[m]));
          const u32x4 pf = u32x4{__builtin_amdgcn_perm(pr, pr, sel[0]), __builtin_amdgcn_perm(pr, pr, sel[1]),
                                 __builtin_amdgcn_perm(pr, pr, sel[2]), __builtin_amdgcn_perm(pr, pr, sel[3])};
          ov[m][0] = mfma16(as_frag(m ? va1 : va0), as_frag(pf), zero);
          ov[m][1] = mfma16(as_frag(m ? vb1 : vb0), as_frag(pf), zero);
        };
        asm volatile("" : "+v"(qf[0]), "+v"(qf[1]), "+v"(va0), "+v"(vb0));
        SCHED0;
        if (cw > 0) rdl();
        SCHED0;
        score(1);
        asm volatile("" : "+v"(qf[0]), "+v"(qk[0][0]), "+v"(qk[0][1]), "+v"(mx[1]), "+v"(l[1][0]), "+v"(l[1][1]));
        asm volatile("" : "+v"(va1), "+v"(vb1));
        SCHED0;
        STAMP(3);
        if (__builtin_expect(cw == 0 || __builtin_amdgcn_readfirstlane(fl) != (unsigned)layer, 0)) prev();
        score(0);
        xred2<true>(mx[0], mx[1]);
        expo(0); expo(1);
        xred2<false, true>(sum[0], sum[1]);
        pv(0); pv(1);
        SCHED0;
        const u32x4 o0 = put_o(0, ov[0][0], ov[0][1], 1), o1 = put_o(1, ov[1][0], ov[1][1], 1);
        hold(o0, o1);
        STAMP(4);
      }
    }
    {
      LANE_VARS
      const int mt = bm * 16 + cw * 2 + cm;
      const unsigned so0 = WS_AR + AR_O + xt_off(mt, 0);
      const unsigned opar = layer & 1;
      auto diff = [&](u32x4 v) AI { return ((v[0] ^ opar) | (v[2] ^ opar)) & 1u; };
      auto dif3 = [&](u32x4 v) AI { return __builtin_amdgcn_bitop3_b32(v[0], v[2], opar, 0x7e); };
      auto any = [&](unsigned d) AI { return __builtin_amdgcn_ballot_w64(d != 0) != 0; };
      const int g0 = cm;
      u32x4 a[RO];
      auto refill = [&](auto ic, auto wc, unsigned cur, unsigned nxt) AI {
        constexpr int I = decltype(ic)::value, W = decltype(wc)::value;
        do {
          COUNT(1);
          SLEEP(SLP_R);
          MEMFENCE;
          a[I] = ld16s(wsr, v16, cur + I * 16384);
          bool u[RO];
#pragma unroll
          for (int j = I + 1; j < I + W; ++j) u[j % RO] = any(diff(a[j % RO]));
          SCHED0;
#pragma unroll
          for (int j = I + 1; j < I + W; ++j)
            if (u[j % RO]) a[j % RO] = ld16s(wsr, v16, (j < RO ? cur : nxt) + (j % RO) * 16384);
          __builtin_amdgcn_s_waitcnt(0x0f70);
          MEMFENCE;
        } while (any(diff(a[I]) | diff(a[I + 1])) && --budget > 0);
      };
      const unsigned sf = so0 + g0 * (RO * 16384);
      f32x4 acc[4];
#pragma unroll
      for (int n = 0; n < 4; ++n)
        acc[n] = f32x4{bflo(xr[n >> 1][(n & 1) * 2]), bfhi(xr[n >> 1][(n & 1) * 2]), bflo(xr[n >> 1][(n & 1) * 2 + 1]), bfhi(xr[n >> 1][(n & 1) * 2 + 1])};
      asm volatile("" : "+v"(acc[0]), "+v"(acc[1]), "+v"(acc[2]), "+v"(acc[3]));
      SCHED0;
      const bool late = isB && cw == 0;
      if (late) bar();
      if (cn < 8) { if (late) SLEEP(SLP_O0); else SLEEP(SLP_O); }
      else { if (late) SLEEP(SLP_O0D); else SLEEP(SLP_OD); }
#pragma unroll
      for (int j = 0; j < 4; ++j) { a[j] = ld16s(wsr, v16, sf + j * 16384); SCHED0; }
      if (!late) bar();
      STAMP(9);
      bf16x8 wf[2][4];
      const char* pw0 = smem + L_STG + v16;
#pragma unroll
      for (int n = 0; n < 8; ++n) wf[n >> 2][n & 3] = *(const bf16x8*)(pw0 + g0 * (RO * 4096) + n * 1024);
      SCHED0;
      STAMP(10);
      if (any(diff(a[0]) | diff(a[1]))) refill(IC<0>{}, IC<4>{}, sf, sf);
      SCHED0;
#pragma unroll
      for (int j = 4; j < RO; ++j) { a[j] = ld16s(wsr, v16, sf + j * 16384); SCHED0; }
      STAMP(11);
      bool bad = false;
      auto group = [&](auto lastc, int g) AI {
        constexpr bool LAST = decltype(lastc)::value;
        const int gc = g ^ g0, gn = (g + 1) ^ g0;
        const unsigned sc = so0 + gc * (RO * 16384), so = so0 + gn * (RO * 16384);
        const char* pc = pw0 + gc * (RO * 4096);
        const char* pn = pw0 + gn * (RO * 4096);
        static_for<RO>([&](auto ic) AI {
          constexpr int i = decltype(ic)::value;
          SCHED0;
          if constexpr (i % 2 == 0)
            if (__builtin_expect(bad, 0)) refill(ic, IC<(LAST && i + 4 > RO) ? RO - i : 4>{}, sc, so);
          SCHED0;
          unsigned mx = 0, my = 0;
          static_for<4>([&](auto nc) AI {
            constexpr int n = decltype(nc)::value;
            mfma_acc(acc[n], wf[i & 1][n], as_frag(a[i]));
            SCHED0;
            if constexpr (i + 2 < RO) wf[i & 1][n] = *(const bf16x8*)(pc + (i + 2) * 4096 + n * 1024);
            else if constexpr (!LAST) wf[i & 1][n] = *(const bf16x8*)(pn + (i + 2 - RO) * 4096 + n * 1024);
            if constexpr (!(LAST && i == RO - 1) && (i & 1)) {
              if constexpr (n == 0) mx = dif3(a[(i + 1) % RO]);
              if constexpr (n == 1) my = dif3(a[(i + 2) % RO]);
              if constexpr (n == 2) bad = any((mx | my) & 1u);
            }
            if constexpr (n == 3 && !LAST) a[i] = ld16s(wsr, v16, so + i * 16384);
            SCHED0;
          });
        });
      };
#pragma unroll 1
      for (int g = 0; g < 3; ++g) group(std::false_type{}, g);
      group(std::true_type{}, 3);
      mfma_done();
      STAMP(12);
#pragma unroll
      for (int j = 0; j < 2; ++j)
#pragma unroll
        for (int e = 0; e < 4; ++e) xr[j][e] = pk2(acc[j * 2 + (e >> 1)][(e & 1) * 2], acc[j * 2 + (e >> 1)][(e & 1) * 2 + 1]);
      if (layer == 3) {
        u32x4 yo[2];
#pragma unroll
        for (int j = 0; j < 2; ++j) {
          auto u = __builtin_amdgcn_permlane16_swap(xr[j][0], xr[j][2], false, false);
          auto v = __builtin_amdgcn_permlane16_swap(xr[j][1], xr[j][3], false, false);
          yo[j] = u32x4{u[0], v[0], u[1], v[1]};
        }
        const unsigned vo = fr * 2048 + (fq & 1) * 32 + (fq >> 1) * 16;
        __builtin_amdgcn_raw_buffer_store_b128(yo[0], mk_rsrc(p.out, 4 * MBY), vo, mt * 32768 + cn * 128, 2);
        __builtin_amdgcn_raw_buffer_store_b128(yo[1], mk_rsrc(p.out, 4 * MBY), vo + 64, mt * 32768 + cn * 128, 2);
        STAMP(13);
        STAMP(14);
        STAMP_DUMP;
        __builtin_amdgcn_endpgm();
      }
      const u32x2 stat = put_x(xr, v16, ar + ARENA + AR_X + xt_off(mt, cn * 2));
      __builtin_amdgcn_raw_buffer_store_b64(stat, wsr, fq ? OOB : fr * 8, ar + ARENA + AR_ST + (mt * 16 + cn) * 128, 16);
      MEMFENCE;
      STAMP(14);
    }
  }
}

extern "C" void kernel_launch(void* const* d_in, const int* in_sizes, int n_in, void* d_out, int out_size,
                              void* d_ws, size_t ws_size, hipStream_t stream) {
  (void)hipFuncSetAttribute((const void*)mega_kernel, hipFuncAttributeMaxDynamicSharedMemorySize, MEGA_LDS);
  MegaParams m;
  m.ws = (char*)d_ws; m.out = (u16*)d_out; m.x = (const u16*)d_in[0]; m.wphi = (const u16*)d_in[2];
  m.wqkvA = (const u16*)d_in[1]; m.wqkvB = (const u16*)d_in[5];
  m.woA = (const u16*)d_in[3]; m.woB = (const u16*)d_in[6];
  mega_kernel<<<256, 768, MEGA_LDS, stream>>>(m);
}
```

```cpp
#include <hip/hip_runtime.h>
#include <stdint.h>
#include <type_traits>

#define DI __device__ __forceinline__
#define AI __attribute__((always_inline))
typedef unsigned short u16;
typedef __attribute__((ext_vector_type(8))) short bf16x8;
typedef __attribute__((ext_vector_type(4))) short s16x4;
typedef __attribute__((ext_vector_type(4))) float f32x4;
typedef __attribute__((ext_vector_type(2))) float f32x2;
typedef __attribute__((ext_vector_type(2))) __bf16 bf2;
typedef __attribute__((ext_vector_type(4))) unsigned u32x4;
typedef __attribute__((ext_vector_type(2))) unsigned u32x2;
typedef __attribute__((address_space(3))) s16x4* lds_s16x4_ptr;
#define LDS(T) __attribute__((address_space(3))) T
template <int N> using IC = std::integral_constant<int, N>;

constexpr float LOG2E = 1.4426950408889634f;

DI unsigned pk2(float a, float b) {
  f32x2 v = {a, b};
  bf2 r = __builtin_convertvector(v, bf2);
  return __builtin_bit_cast(unsigned, r);
}
DI float bflo(unsigned u) { return __uint_as_float(u << 16); }
DI float bfhi(unsigned u) { return __uint_as_float(u & 0xffff0000u); }
DI f32x4 mfma16(bf16x8 a, bf16x8 b, f32x4 c) { return __builtin_amdgcn_mfma_f32_16x16x32_bf16(a, b, c, 0, 0, 0); }
DI void mfma_acc(f32x4& c, bf16x8 a, bf16x8 b) { asm volatile("v_mfma_f32_16x16x32_bf16 %0, %1, %2, %0" : "+v"(c) : "v"(a), "v"(b)); }
DI void mfma_done() { asm volatile("s_nop 7\n\ts_nop 1" ::: "memory"); __builtin_amdgcn_sched_barrier(0); }
DI bf16x8 as_frag(u32x4 v) { return __builtin_bit_cast(bf16x8, v); }
DI float dot2(unsigned a, unsigned b, float c) {
  return __builtin_amdgcn_fdot2_f32_bf16(__builtin_bit_cast(bf2, a), __builtin_bit_cast(bf2, b), c, false);
}
DI u32x2 trread(const char* p) { return __builtin_bit_cast(u32x2, __builtin_amdgcn_ds_read_tr16_b64_v4i16((lds_s16x4_ptr)p)); }
DI u32x2 trl(unsigned a) { return __builtin_bit_cast(u32x2, __builtin_amdgcn_ds_read_tr16_b64_v4i16((lds_s16x4_ptr)a)); }
DI float fmx(float a, float b) { return __builtin_elementwise_maximum(a, b); }
DI float fmn(float a, float b) { return __builtin_elementwise_minimum(a, b); }

DI float mul1(float a, float b) { float r; asm("v_mul_f32 %0, %1, %2" : "=v"(r) : "v"(a), "v"(b)); return r; }
DI float add1(float a, float b) { float r; asm("v_add_f32 %0, %1, %2" : "=v"(r) : "v"(a), "v"(b)); return r; }
DI f32x4 mul4(f32x4 a, f32x4 b) {
  float r0, r1, r2, r3;
  asm("v_mul_f32 %0, %4, %8\n\tv_mul_f32 %1, %5, %9\n\tv_mul_f32 %2, %6, %10\n\tv_mul_f32 %3, %7, %11"
      : "=&v"(r0), "=&v"(r1), "=&v"(r2), "=&v"(r3) : "v"(a[0]), "v"(a[1]), "v"(a[2]), "v"(a[3]), "v"(b[0]), "v"(b[1]), "v"(b[2]), "v"(b[3]));
  return f32x4{r0, r1, r2, r3};
}
DI f32x4 add4(f32x4 a, f32x4 b) { return f32x4{add1(a[0], b[0]), add1(a[1], b[1]), add1(a[2], b[2]), add1(a[3], b[3])}; }

template <bool IS_MAX, bool INV = false>
DI void xred2(float& a, float& b) {
  auto r = __builtin_amdgcn_permlane32_swap(__float_as_uint(a), __float_as_uint(b), false, false);
  float c = IS_MAX ? fmx(__uint_as_float(r[0]), __uint_as_float(r[1])) : __uint_as_float(r[0]) + __uint_as_float(r[1]);
  auto q = __builtin_amdgcn_permlane16_swap(__float_as_uint(c), __float_as_uint(c), false, false);
  c = IS_MAX ? fmx(__uint_as_float(q[0]), __uint_as_float(q[1])) : __uint_as_float(q[0]) + __uint_as_float(q[1]);
  if (INV) c = __builtin_amdgcn_rcpf(c);
  auto w = __builtin_amdgcn_permlane32_swap(__float_as_uint(c), __float_as_uint(c), false, false);
  a = __uint_as_float(w[0]); b = __uint_as_float(w[1]);
}
template <bool IS_MAX, bool INV = false>
DI void xred4(float& a, float& b, float& c, float& d) {
  auto op = [](unsigned x, unsigned y) { return IS_MAX ? fmx(__uint_as_float(x), __uint_as_float(y)) : __uint_as_float(x) + __uint_as_float(y); };
  auto r0 = __builtin_amdgcn_permlane32_swap(__float_as_uint(a), __float_as_uint(b), false, false);
  auto r1 = __builtin_amdgcn_permlane32_swap(__float_as_uint(c), __float_as_uint(d), false, false);
  float ab = op(r0[0], r0[1]), cd = op(r1[0], r1[1]);
  auto q = __builtin_amdgcn_permlane16_swap(__float_as_uint(ab), __float_as_uint(cd), false, false);
  float e = op(q[0], q[1]);
  if (INV) e = __builtin_amdgcn_rcpf(e);
  auto w = __builtin_amdgcn_permlane16_swap(__float_as_uint(e), __float_as_uint(e), false, false);
  auto u = __builtin_amdgcn_permlane32_swap(w[0], w[0], false, false);
  auto v = __builtin_amdgcn_permlane32_swap(w[1], w[1], false, false);
  a = __uint_as_float(u[0]); b = __uint_as_float(u[1]); c = __uint_as_float(v[0]); d = __uint_as_float(v[1]);
}

DI unsigned xt_off(int mt, int ks) { return (unsigned)((((mt >> 4) * 32 + ks) * 16 + (mt & 15)) << 10); }

#define GLDS_PRE "s_mov_b32 %0, m0\n\ts_mov_b32 m0, %3\n\ts_nop 2\n\t"
#define GLDS_POST "s_mov_b32 m0, %0"
#define GLDS_OPS : "=&s"(keep) : "v"(voff), "s"(sbase), "s"(lds) : "memory"
#define GL1(c) "global_load_lds_dwordx4 %1, %2" c "\n\t"
#define GL2(c) GL1(c) "global_load_lds_dwordx4 %1, %2 offset:1024" c "\n\t"
#define GL3(c) GL2(c) "global_load_lds_dwordx4 %1, %2 offset:2048" c "\n\t"
#define GL4(c) GL3(c) "global_load_lds_dwordx4 %1, %2 offset:3072" c "\n\t"
template <int N, bool SC1 = false>
DI void glds_run(unsigned voff, const void* sbase, unsigned lds) {
  unsigned keep;
  if constexpr (SC1) {
    if constexpr (N == 2) asm volatile(GLDS_PRE GL2(" sc1") GLDS_POST GLDS_OPS);
    else if constexpr (N == 3) asm volatile(GLDS_PRE GL3(" sc1") GLDS_POST GLDS_OPS);
    else asm volatile(GLDS_PRE GL4(" sc1") GLDS_POST GLDS_OPS);
  } else {
    static_assert(SC1 || N == 4, "");
    asm volatile(GLDS_PRE GL4("") GLDS_POST GLDS_OPS);
  }
}
template <int N> DI void wait_vm_lgkm0() { asm volatile("s_waitcnt vmcnt(%0) lgkmcnt(0)" ::"n"(N) : "memory"); }
template <int N> DI void wait_vm() { asm volatile("s_waitcnt vmcnt(%0)" ::"n"(N) : "memory"); }
DI void drain() { asm volatile("s_waitcnt vmcnt(0)" ::: "memory"); __builtin_amdgcn_s_waitcnt(0x0f70); }
DI void bar() { asm volatile("" ::: "memory"); __builtin_amdgcn_s_barrier(); asm volatile("" ::: "memory"); }
DI void sync_lds() { asm volatile("s_waitcnt lgkmcnt(0)" ::: "memory"); bar(); }
#define SCHED0 __builtin_amdgcn_sched_barrier(0)
#define STPAD do { SCHED0; asm volatile("s_nop 1"); SCHED0; } while (0)
#define SLEEP(n) do { if (n) __builtin_amdgcn_s_sleep(n); } while (0)
#define PRIO(n) __builtin_amdgcn_s_setprio(n)

template <typename F>
DI void phi_map2(const f32x2 (*x)[4], const float* rs, u32x4 (*out)[2], F mid) {
  auto top = [&](float sg, float* mx) AI {
#pragma unroll
    for (int m = 0; m < 2; ++m)
      mx[m] = fmx(fmx(fmx(sg * x[m][0][0], sg * x[m][0][1]), sg * x[m][1][0]), fmx(fmx(sg * x[m][1][1], sg * x[m][2][0]), fmx(fmx(sg * x[m][2][1], sg * x[m][3][0]), sg * x[m][3][1])));
    xred2<true>(mx[0], mx[1]);
  };
  auto sum8 = [](const f32x2* e) AI { const f32x2 t = (e[0] + e[1]) + (e[2] + e[3]); float lo = t[0]; asm("" : "+v"(lo)); return lo + t[1]; };
  float mx[2], sp[2], sn[2];
  top(1.f, mx);
  f32x2 ep[2][4], en[2][4];
#pragma unroll
  for (int m = 0; m < 2; ++m) {
    const float sl = rs[m] * LOG2E, off = mx[m] * -sl;
#pragma unroll
    for (int j = 0; j < 4; ++j) {
      const f32x2 a = __builtin_elementwise_fma(x[m][j], f32x2{sl, sl}, f32x2{off, off});
      ep[m][j] = f32x2{__builtin_amdgcn_exp2f(a[0]), __builtin_amdgcn_exp2f(a[1])};
    }
#pragma unroll
    for (int j = 0; j < 4; ++j) en[m][j] = f32x2{__builtin_amdgcn_rcpf(ep[m][j][0]), __builtin_amdgcn_rcpf(ep[m][j][1])};
    sp[m] = sum8(ep[m]); sn[m] = sum8(en[m]);
  }
  xred4<false, true>(sp[0], sn[0], sp[1], sn[1]);
  if (__builtin_expect(__builtin_amdgcn_ballot_w64(!(fmn(sn[0], sn[1]) > 1e-30f)) != 0, 0)) {
    top(-1.f, mx);
#pragma unroll
    for (int m = 0; m < 2; ++m) {
      const float sl = rs[m] * -LOG2E, off = mx[m] * sl;
#pragma unroll
      for (int j = 0; j < 4; ++j) {
        const f32x2 a = __builtin_elementwise_fma(x[m][j], f32x2{sl, sl}, f32x2{off, off});
        en[m][j] = f32x2{__builtin_amdgcn_exp2f(a[0]), __builtin_amdgcn_exp2f(a[1])};
      }
      sn[m] = sum8(en[m]);
    }
    xred2<false, true>(sn[0], sn[1]);
  }
  mid(sp);
#pragma unroll
  for (int m = 0; m < 2; ++m) {
#pragma unroll
    for (int j = 0; j < 4; j += 2) {
      f32x2 a0, a1, b0, b1;
      asm("v_pk_mul_f32 %0, %4, %5 op_sel_hi:[0,1]\n\tv_pk_mul_f32 %1, %4, %6 op_sel_hi:[0,1]\n\t"
          "v_pk_mul_f32 %2, %4, %7 op_sel:[1,0]\n\tv_pk_mul_f32 %3, %4, %8 op_sel:[1,0]"
          : "=&v"(a0), "=&v"(a1), "=&v"(b0), "=&v"(b1) : "v"(f32x2{sp[m], sn[m]}), "v"(ep[m][j]), "v"(ep[m][j + 1]), "v"(en[m][j]), "v"(en[m][j + 1]));
      out[m][0][j] = pk2(a0[0], a0[1]); out[m][0][j + 1] = pk2(a1[0], a1[1]);
      out[m][1][j] = pk2(b0[0], b0[1]); out[m][1][j + 1] = pk2(b1[0], b1[1]);
    }
  }
}

#define SLP_X0 6
#define SLP_X1 11
#define SLP_XD0 2
#define SLP_XD1 7
#define SLP_O 19
#define SLP_OD 10
#define SLP_O0 8
#define SLP_O0D 2
#define SLP_K 4
#define SLP_R 4
#define SLP_HX 1
#define SLP_Q 4
#define SLP_P 50
#define SLP_E 14
#define SLP_G 14
#define SPR0 2
#define SW0 0
#define LANEID __builtin_amdgcn_mbcnt_hi(~0u, __builtin_amdgcn_mbcnt_lo(~0u, 0u))
#define STAMP(i) do {} while (0)
#define STAMP_DUMP do {} while (0)
#define COUNT(k) do {} while (0)
#define PSTAMP(l) do { const int layer = l; (void)layer; STAMP(15); } while (0)
constexpr unsigned MBY = 1u << 20;
constexpr unsigned WS_WQ = 0;
constexpr unsigned WS_WO = 12 * MBY;
constexpr unsigned WS_AR = 16 * MBY, ARENA = 12 * MBY;
constexpr unsigned AR_X = 0, AR_ST = 4 * MBY, AR_O = 5 * MBY, AR_KV = 9 * MBY;
constexpr unsigned AR_L2 = AR_ST + 256 * 1024, AR_FL = AR_ST + 320 * 1024;
constexpr unsigned KVREC = 9 * 1024;
constexpr unsigned WS_CS = WS_AR + 4 * ARENA;
constexpr unsigned WS_F1 = WS_CS + 512 * 1024;
constexpr unsigned WS_F2 = WS_F1 + 64 * 1024;
constexpr unsigned WS_EP = WS_F2 + 2048;
constexpr unsigned WS_END = WS_EP + 128;
constexpr unsigned POISON = 0xffffffffu;
constexpr unsigned OOB = 0x80000000u;

typedef __amdgpu_buffer_rsrc_t rsrc_t;
DI rsrc_t mk_rsrc(const void* p, unsigned bytes) { return __builtin_amdgcn_make_buffer_rsrc((void*)p, 0, bytes, 0x00020000); }
DI void st16(rsrc_t r, unsigned off, u32x4 v) { __builtin_amdgcn_raw_buffer_store_b128(v, r, off, 0, 16); }
DI void st16s(rsrc_t r, unsigned voff, unsigned soff, u32x4 v) { __builtin_amdgcn_raw_buffer_store_b128(v, r, voff, soff, 16); }
DI u32x4 ld16(rsrc_t r, unsigned off) { return __builtin_amdgcn_raw_buffer_load_b128(r, off, 0, 16); }
DI u32x4 ld16s(rsrc_t r, unsigned voff, unsigned soff) { return __builtin_amdgcn_raw_buffer_load_b128(r, voff, soff, 16); }
DI u32x4 clean(u32x4 v) { return __builtin_elementwise_min(v, u32x4{POISON - 1, POISON - 1, POISON - 1, POISON - 1}); }
DI unsigned umax(unsigned a, unsigned b) { return a > b ? a : b; }
DI unsigned umin(unsigned a, unsigned b) { return a < b ? a : b; }
DI unsigned umax4(u32x4 v) { return umax(umax(umax(v[0], v[1]), v[2]), v[3]); }
DI unsigned umax4(unsigned m, u32x4 v) { return umax(umax(umax(umax(m, v[0]), v[1]), v[2]), v[3]); }
DI bool poisoned(unsigned m) { return __builtin_amdgcn_ballot_w64(m == POISON) != 0; }
#define MEMFENCE asm volatile("" ::: "memory")

#define BL(i, o) "buffer_load_dwordx4 %1, %2, %" #i " offen" o " nt lds\n\t"
#define BL_OPS(...) : "=&s"(keep) : "v"(voff), "s"(r), "s"(lds), __VA_ARGS__ : "memory"
template <int N>
DI void blds_run(rsrc_t r, unsigned voff, unsigned lds, unsigned s0, unsigned s1 = 0, unsigned s2 = 0, unsigned s3 = 0) {
  unsigned keep;
  if constexpr (N == 2) asm volatile(GLDS_PRE BL(4, "") BL(5, " offset:1024") GLDS_POST BL_OPS("s"(s0), "s"(s1)));
  else asm volatile(GLDS_PRE BL(4, "") BL(5, " offset:1024") BL(6, " offset:2048") BL(7, " offset:3072") GLDS_POST BL_OPS("s"(s0), "s"(s1), "s"(s2), "s"(s3)));
}

#define CODEB 36700
constexpr int MEGA_CODE_BYTES = CODEB;
DI unsigned code_prefetch(int i) {
  unsigned long long pc; int rel; unsigned d;
  asm volatile("s_getpc_b64 %0\n\ts_mov_b32 %1, _Z11mega_kernel10MegaParams@rel32@lo+4" : "=s"(pc), "=s"(rel));
  const unsigned long long a = pc + (long long)rel + (i * 192 + 192 <= MEGA_CODE_BYTES ? i * 192 : 0);
  asm volatile("s_load_dword %0, %1, 0x0\n\ts_load_dword %0, %1, 0x40\n\ts_load_dword %0, %1, 0x80" : "=&s"(d) : "s"(a) : "memory");
  return d;
}

struct MegaParams {
  char* ws;
  const u16* x;
  const u16* wqkvA;
  const u16* woA;
  const u16* wqkvB;
  const u16* woB;
  const u16* wphi;
  u16* out;
};

constexpr int QSLOT = 6 * 1024, QGRP = 4 * QSLOT;
constexpr int L_STG = QGRP;
constexpr int L_A2 = 16 * 1024, L_Z = 21 * 1024;
constexpr int L_RT = 4 * QGRP;
constexpr int L_PB = 0;
constexpr int L_RED = L_STG + 128 * 1024;
constexpr int L_STAMP = L_RED + 3072;
constexpr int MEGA_LDS = L_STAMP + 1536;
static_assert(MEGA_LDS <= 160 * 1024, "LDS map");

template <int I, int N, typename F> DI void static_for_(F&& f) {
  if constexpr (I < N) { f(IC<I>{}); static_for_<I + 1, N>(f); }
}
template <int N, typename F> DI void static_for(F&& f) { static_for_<0, N>(f); }

constexpr int NCHK = 3;
constexpr int RO = 8;
constexpr int SPIN_BUDGET = 1 << 15;

__global__ __launch_bounds__(768, 3) void mega_kernel(MegaParams p) {
  extern __shared__ __attribute__((aligned(16))) char smem[];
  const int tid = threadIdx.x;
  const int wave = __builtin_amdgcn_readfirstlane(tid >> 6);
  const int xcd = blockIdx.x & 7, idx = blockIdx.x >> 3;
  int bm = (xcd >> 2) * 4 + (idx >> 3), head = ((xcd & 1) * 2 + ((xcd >> 1) & 1)) * 8 + (idx & 7);
  int cm = (xcd >> 1) & 1, cn = (xcd & 1) * 8 + (idx & 7);
  const int bm0 = bm, head0 = head, cm0 = cm, cn0 = cn;
  char* ws = p.ws;
  const u16 *px = p.x, *pq = p.wqkvA;
  asm volatile("" : "+s"(ws), "+s"(px), "+s"(pq));
  const int swv = wave == SW0 ? 0 : wave == 4 ? 1 : wave == 11 ? 2 : -1; (void)swv;
  float* lmean = (float*)(smem + L_RED);
  float* lrstd = lmean + 272;
  float* lcs = lrstd + 272;
  unsigned* lflag = (unsigned*)lmean;
  const rsrc_t wsr = mk_rsrc(ws, WS_END);
  int budget = SPIN_BUDGET;
  unsigned tag0 = 0, tag1 = 0;
  auto ld_ep = [&]() AI { MEMFENCE; return __builtin_amdgcn_raw_buffer_load_b64(wsr, 0, WS_EP, 16); };
  auto mk_tag = [&](u32x2 ep) AI {
    tag0 = __builtin_amdgcn_readfirstlane(ep[0]) + 0x9e3779b9u;
    tag1 = __builtin_amdgcn_readfirstlane(ep[1]) ^ 0x85ebca6bu;
  };
  const unsigned sbase = (unsigned)(size_t)smem;
  int L = wave, cw = wave - 4;
#define OPAQUE_S asm volatile("" : "+s"(L), "+s"(bm), "+s"(head), "+s"(cm), "+s"(cn));
#define LANE_VARS asm volatile("" : "+s"(cw), "+s"(bm), "+s"(head), "+s"(cm), "+s"(cn)); int lane_ = __builtin_amdgcn_mbcnt_hi(~0u, __builtin_amdgcn_mbcnt_lo(~0u, 0u)); asm volatile("" : "+v"(lane_)); \
  const int lane = lane_, fr = lane & 15, fq = lane >> 4; const unsigned v16 = lane * 16; (void)fr; (void)fq; (void)v16;
  auto put_x = [&](u32x4* xr, unsigned v16, unsigned dst) AI {
    float sm = 0.f, sq = 0.f;
#pragma unroll
    for (int j = 0; j < 2; ++j) {
      xr[j][0] = umin(xr[j][0], POISON - 1); xr[j][2] = umin(xr[j][2], POISON - 1);
      st16s(wsr, v16, dst + j * 16384, xr[j]);
    }
    SCHED0;
#pragma unroll
    for (int j = 0; j < 2; ++j)
#pragma unroll
      for (int e = 0; e < 4; ++e) { sm = dot2(xr[j][e], 0x3f803f80u, sm); sq = dot2(xr[j][e], xr[j][e], sq); }
    xred2<false>(sm, sq);
    return u32x2{__float_as_uint(sm), __float_as_uint(sq)};
  };

#define RT_VARS const bool tq = L < 3; const int tsg = tq ? (bm * 2 + (head >> 4)) * 3 + L : bm * 2 + (head >> 4), ks = (head & 15) + 16 * h;
  auto ld_rt = [&](const u16* w, int h, unsigned lds) AI {
    RT_VARS
    LANE_VARS
    const unsigned rs = tq ? 6144 : 2048, D = 8 * rs - 1024, o = ks * 32 * rs + tsg * 128;
    blds_run<4>(mk_rsrc(w, rs * 1024), (lane >> 3) * rs + ((lane & 7) ^ ((lane >> 3) & 6)) * 16, sbase + lds, o, o + D, o + 2 * D, o + 3 * D);
  };
  auto retile = [&](int side, int h, unsigned lds, f32x4* rcs, int wp = -1) AI {
    RT_VARS
    LANE_VARS
    const unsigned dst = tq ? WS_WQ + side * (6 * MBY) + (unsigned)((((tsg & 15) * 2) * 32 + ks) * 6 + (tsg >> 4) * 2) * 1024
                            : WS_WO + side * (2 * MBY) + (unsigned)((tsg * 32 + ks) * 4) * 1024;
    const unsigned gstr = tq ? 32 * 6 * 1024 : 2048;
    const unsigned a0 = (lane >> 2) * 128 + ((lane >> 3) & 3) * 32 + (lane & 3) * 8;
    u32x4 fv[4];
    if (wp >= 0 && tsg < 32) {
      const f32x4 zero = f32x4{0.f, 0.f, 0.f, 0.f};
#pragma unroll
      for (int g = 0; g < 2; ++g) {
        const char* ra = smem + lds + fr * 128 + (((g * 4 + fq) ^ (fr & 6)) * 16);
        const bf16x8 w0 = *(const bf16x8*)ra, w1 = *(const bf16x8*)(ra + 2048);
#pragma unroll
        for (int u = 0; u < 2; ++u) {
          const char* pa = smem + wp + g * 2048 + (fq * 8 + (fr >> 2)) * 64 + u * 32 + (fr & 3) * 8;
          const u32x2 t0 = trread(pa), t1 = trread(pa + 256);
          const bf16x8 wf = as_frag(u32x4{t0[0], t0[1], t1[0], t1[1]});
          const f32x4 d0 = mfma16(w0, wf, zero), d1 = mfma16(w1, wf, zero);
          fv[g * 2 + u] = u32x4{pk2(d0[0], d0[1]), pk2(d0[2], d0[3]), pk2(d1[0], d1[1]), pk2(d1[2], d1[3])};
        }
      }
    } else {
#pragma unroll
      for (int pc = 0; pc < 4; ++pc) {
        const char* aa = smem + lds + (a0 ^ (pc * 32));
        const u32x2 r0_ = trread(aa), r1_ = trread(aa + 2048);
        fv[pc] = u32x4{r0_[0], r0_[1], r1_[0], r1_[1]};
      }
    }
#pragma unroll
    for (int pc = 0; pc < 4; ++pc) {
      const u32x4 v = fv[pc];
      st16s(wsr, v16 + (pc & 1) * 1024, dst + (pc >> 1) * gstr, v);
      if (rcs) rcs[pc] = mfma16(as_frag(u32x4{0x3f803f80u, 0x3f803f80u, 0x3f803f80u, 0x3f803f80u}), as_frag(v), rcs[pc]);
    }
    if (rcs && h == 1) {
      const f32x4 c0 = rcs[0], c1 = rcs[1], c2 = rcs[2], c3 = rcs[3];
      const float ca = fq & 1 ? c1[0] : c0[0], cb = fq & 1 ? c3[0] : c2[0], cc = fq & 2 ? cb : ca;
      if (tq) __builtin_amdgcn_raw_buffer_store_b32(__float_as_uint(cc), wsr, ((fq >> 1) * 1536 + (fq & 1) * 16 + fr) * 4,
                                                    WS_CS + ((side * 32 + (tsg & 15) * 2) * 1536 + (tsg >> 4) * 512 + (head & 15) * 32) * 4, 16);
    }
  };
  auto flag = [&](unsigned table) AI {
    if (LANEID == 0) __builtin_amdgcn_raw_buffer_store_b64(u32x2{tag0, tag1}, wsr, 0, table + (bm * 32 + head) * 8, 16);
  };
  auto poll = [&](unsigned table, auto between, int rows = 0) AI {
    LANE_VARS
    const int q = (umin(fq, 2) * 16 + (head >> 1)) * 43 >> 7;
    const unsigned vo = rows ? bm * 256 + (fr & rows) * 16 : fq == 3 ? bm * 256 + fr * 16 : q * 128 + (fr & 7) * 16;
    auto miss = [&](u32x4 f) AI { return __builtin_amdgcn_ballot_w64(((f[0] ^ tag0) | (f[1] ^ tag1) | (f[2] ^ tag0) | (f[3] ^ tag1)) != 0) != 0; };
    MEMFENCE;
    const u32x4 f0 = ld16s(wsr, vo, table);
    MEMFENCE;
    between();
    MEMFENCE;
    bool wait = miss(f0);
#pragma unroll 1
    while (wait && --budget > 0) {
      SLEEP(SLP_Q);
      MEMFENCE;
      wait = miss(ld16s(wsr, vo, table));
      asm volatile("" : "+s"(budget) :: "memory");
    }
    MEMFENCE;
  };
  auto ld_x = [&](int cx, unsigned lds) AI {
    LANE_VARS
    const unsigned o = (bm * 16 + (head >> 1)) * 32768 + cx * 128;
    blds_run<2>(mk_rsrc(px, 4 * MBY), (lane >> 3) * 2048 + ((lane & 7) ^ (lane >> 3)) * 16, sbase + lds, o, o + 15360);
  };
  auto st_x = [&](int cx, unsigned lds, int w) AI {
    LANE_VARS
    const int mt = bm * 16 + (head >> 1);
    u32x4 xr[2];
#pragma unroll
    for (int j = 0; j < 2; ++j) {
      const char* a = smem + lds + fr * 128 + (fq & 1) * 8;
      const u32x2 lo = *(const u32x2*)(a + ((j * 4 + (fq >> 1)) ^ (fr & 7)) * 16), hi = *(const u32x2*)(a + ((j * 4 + 2 + (fq >> 1)) ^ (fr & 7)) * 16);
      xr[j] = u32x4{lo[0], lo[1], hi[0], hi[1]};
    }
    const u32x2 stat = put_x(xr, v16, WS_AR + AR_X + xt_off(mt, cx * 2));
    if (fq == 0) *(u32x2*)(lmean + 16 + fr * 4 + w) = stat;
  };

  {
    const int lane = LANEID;
    const bool tq = wave < 4;
    const unsigned rs = tq ? 6144 : 2048, D = 8 * rs - 1024;
    const unsigned o = (tq ? (head & 15) * 32 : (bm * 16 + (head >> 1)) * 16) * rs + (tq ? (bm * 2 + (head >> 4)) * 3 + wave : (head & 1) * 4 + cw) * 128;
    const unsigned lds = sbase + L_PB + (tq ? 8192 + wave * 4096 : cw * 2048);
    const unsigned vo = (lane >> 3) * rs + ((lane & 7) ^ ((lane >> 3) & (tq ? 6 : 7))) * 16;
    const rsrc_t r = mk_rsrc(tq ? pq : px, tq ? 6 * MBY : 4 * MBY);
    if ((wave & 7) != 3 && wave < 8) blds_run<2>(r, vo, lds, o, o + D);
    if (wave < 3) {
      blds_run<2>(r, vo, lds + 2048, o + 2 * D + 2048, o + 3 * D + 2048);
      const int tsg = (bm * 2 + (head >> 4)) * 3 + wave;
      if (tsg < 32) glds_run<4>(lane * 16, (const char*)p.wphi + (tsg & 15) * 4096, sbase + L_PB + 45056 + wave * 4096);
    }
  }
  if (wave >= 8) {
    const unsigned cp = code_prefetch(idx * 9 + cw), cq = code_prefetch(idx * 9 + cw - 4);
    L = wave - 8;
    bar();
    ld_x(8 + (head & 1) * 4 + L, L_PB + 32768 + L * 2048);
    if (L < 3) ld_rt(pq, 1, L_PB + 20480 + L * 4096);
    sync_lds();
    asm volatile("; pi" :: "s"(cp), "s"(cq) : "memory");
    wait_vm<0>();
    PSTAMP(1);
    st_x(8 + (head & 1) * 4 + L, L_PB + 32768 + L * 2048, L * 64 + 2);
    if (L < 3) {
      const f32x4 t = *(const f32x4*)(smem + L_PB + 40960 + L * 1024 + LANEID * 16);
      f32x4 rca[4] = {{t[0], 0.f, 0.f, 0.f}, {t[1], 0.f, 0.f, 0.f}, {t[2], 0.f, 0.f, 0.f}, {t[3], 0.f, 0.f, 0.f}};
      retile(0, 1, L_PB + 20480 + L * 4096, rca, L_PB + 45056 + L * 4096);
    }
    asm volatile("s_waitcnt lgkmcnt(0)" ::: "memory");
    PSTAMP(2);
  } else if (wave >= 4) {
    wait_vm<0>();
    PSTAMP(1);
    st_x((head & 1) * 4 + cw, L_PB + cw * 2048, cw * 64);
    bar();
    wait_vm_lgkm0<0>();
    PSTAMP(2);
    bar();
    asm volatile("; pc" ::: "memory");
    SLEEP(SLP_E);
    L = 3;
    if (cw < 2) ld_rt(p.woA, cw, L_RT + 3 * 14336 + cw * 4096);
    if (cw == 2) ld_rt(p.woB, 0, L_RT + 3 * 14336 + 8192);
  } else if (wave < 3) {
    const u32x2 ep = ld_ep();
    wait_vm<1>();
    PSTAMP(1);
    f32x4 rca[4] = {};
    retile(0, 0, L_PB + 8192 + L * 4096, rca, L_PB + 45056 + L * 4096);
    *(f32x4*)(smem + L_PB + 40960 + L * 1024 + LANEID * 16) = f32x4{rca[0][0], rca[1][0], rca[2][0], rca[3][0]};
    bar();
    drain();
    mk_tag(ep);
    PSTAMP(2);
    sync_lds();
    asm volatile("; pl" ::: "memory");
    SLEEP(SLP_E);
    ld_rt(p.wqkvB, 0, L_RT + L * 14336);
    ld_rt(p.wqkvB, 1, L_RT + L * 14336 + 4096);
  } else {
    const u32x2 ep = ld_ep();
    const unsigned cp = code_prefetch(idx * 9 + 8);
    if (LANEID < 9) lflag[LANEID] = 0;
    wait_vm_lgkm0<0>();
    asm volatile("" :: "s"(cp));
    mk_tag(ep);
    bar();
    bar();
    flag(WS_F1);
    if (LANEID == 0) *(u32x2*)(lflag + 10) = u32x2{tag0, tag1};
    PSTAMP(2);
    SLEEP(SLP_P);
    poll(WS_F1, []() {});
    asm volatile("; pp" ::: "memory");
  }
  bar();
  PSTAMP(3);
  int role = wave;
  asm volatile("" : "+s"(role));

  if (__builtin_expect(role < 4, 1)) {
    const unsigned v16 = __builtin_amdgcn_mbcnt_hi(~0u, __builtin_amdgcn_mbcnt_lo(~0u, 0u)) * 16;
#define OPAQUE_LANE int lane = v16 >> 4; asm volatile("" : "+v"(lane));
    auto q_groupW = [&](int layer, int g, int pos) AI {
      const char* gW = ws + WS_WQ + (layer & 1) * (6 * MBY) + head * (192 * 1024) + (g * 4 + L) * 6144;
      const unsigned d = sbase + (pos * 4 + L) * QSLOT;
      glds_run<3, true>(v16, gW, d);
      glds_run<3, true>(v16, gW + 3072, d + 3072);
    };
    u32x4 wn[4][6];
    auto ld_wn = [&](int layer, int g0, int g1) AI {
      const unsigned so = WS_WQ + (layer & 1) * (6 * MBY) + head * (192 * 1024) + L * 6144;
#pragma unroll
      for (int g = g0; g < g1; ++g)
#pragma unroll
        for (int n = 0; n < 6; ++n) wn[g][n] = ld16s(wsr, v16 + (n % 3) * 1024, so + g * 24576 + (n / 3) * 3072);
      MEMFENCE;
    };
    auto wr_wn = [&](int g) AI {
#pragma unroll
      for (int n = 0; n < 6; ++n) *(u32x4*)(smem + (g * 4 + L) * QSLOT + n * 1024 + v16) = wn[g][n];
    };
    const u32x4 pz = u32x4{POISON, POISON, POISON, POISON};
    auto poison = [&](unsigned so) AI { st16s(wsr, v16, so, pz); };
    auto poison_o = [&]() AI {
      const unsigned o = WS_AR + AR_O + xt_off(bm * 16 + L * 4, head);
#pragma unroll
      for (int i = 0; i < 4; ++i) poison(o + i * 1024);
      STPAD;
    };
    auto poison_x = [&](int l) AI {
      if (L < NCHK) {
        const unsigned o = WS_AR + l * ARENA + AR_X + bm * (512 * 1024) + head * (NCHK * 2048) + L * 2048;
        poison(o); poison(o + 1024);
        STPAD;
      }
    };
    auto poison_s = [&](int l) AI {
      if (L == 3) {
        const unsigned o = WS_AR + l * ARENA + AR_KV + (unsigned)(bm * 32 + head) * KVREC;
        poison(o); poison(o + 1024);
        STPAD;
      }
    };
    auto poison_kv = [&](int l) AI {
      const unsigned kvo = WS_AR + l * ARENA + AR_KV + (unsigned)(bm * 32 + head) * KVREC;
      poison(kvo + L * 1024);
      if (L == (l ? 0 : 1)) st16s(wsr, v16 < 256 ? v16 : OOB, kvo + 4096, pz);
      STPAD;
    };
    q_groupW(0, 0, 0);
    q_groupW(0, 1, 1);
    auto put_l2 = [&](float sm, float sq, unsigned vo, unsigned so) AI {
      xred2<false>(sm, sq);
      if (v16 < 256) st16s(wsr, vo, so, u32x4{__float_as_uint(sm), tag0, __float_as_uint(sq), tag1});
      MEMFENCE;
    };
    auto put_st0 = [&]() AI {
      if (L == 0) {
        const f32x4 t = *(const f32x4*)((const char*)(lmean + 16) + v16);
        put_l2(t[0] + t[2], t[1] + t[3], v16, WS_AR + AR_L2 + (head & 1) * 32768 + (bm * 16 + (head >> 1)) * 256);
      }
    };
    wait_vm<6>();
    auto body = [&](auto kind, const int layer) AI {
      constexpr bool isB = decltype(kind)::value == 1, is0 = decltype(kind)::value == 0;
      const unsigned ar = WS_AR + layer * ARENA;
      f32x4 rcs[4] = {};
      auto sW = [&]() AI { return WS_WO + (layer & 1) * (2 * MBY) + cn * (128 * 1024); };
      auto stg = [&](unsigned off, auto n) AI { glds_run<decltype(n)::value, true>(v16, ws + sW() + off, sbase + L_STG + off); };
      auto stg7 = [&](unsigned o) AI { stg(o, IC<4>{}); stg(o + 4096, IC<3>{}); };
      auto stg6 = [&](unsigned o) AI { stg(o, IC<3>{}); stg(o + 3072, IC<3>{}); };

      auto extra = [&](int b) AI {
        if (!is0) return;
        const unsigned lu = L_RT + L * 14336;
        if (b < 7) { SCHED0; PRIO(3); SCHED0; }
        if (b == 1) retile(L < 3, 0, lu, rcs);
        if (b == 2 && L == 3) retile(0, 1, lu + 4096, nullptr);
        if (b == 6 && L == 3) ld_rt(p.woB, 1, lu + 4096);
        if (b == 8) {
          if (L < 3) retile(1, 1, lu + 4096, rcs);
          if (L == 3) retile(1, 0, lu + 8192, nullptr);
          if (L == 3) retile(1, 1, lu + 4096, nullptr);
        }
        if (b < 7) { SCHED0; PRIO(0); SCHED0; }
      };
      auto extra2 = [&](int b) AI {
        if (!is0) return;
        if (b < 7) { SCHED0; PRIO(3); SCHED0; }
        if (b == 2) put_st0();
        if (b == 1) poison_o();
        if (b == 2) poison_kv(0);
        if (b == 5) poison_kv(2);
        if (b == 8) poison_s(1);
        if (b == 9) poison_x(1);
        if (b == 10) { poison_x(2); poison_s(3); }
        if (b < 7) { SCHED0; PRIO(0); SCHED0; }
      };
      if (is0) sync_lds();
      STAMP(0);
      {
        OPAQUE_S
        OPAQUE_LANE
        u32x4 c0, c1;
        asm volatile("" : "=v"(c0), "=v"(c1));
        auto ldcs = [&]() AI {
          if (L > 0) {
            const unsigned so = WS_CS + (((layer & 1) * 32 + head) * 1536 + (L - 1) * 512) * 4;
            c0 = ld16s(wsr, v16, so); c1 = ld16s(wsr, v16 + 1024, so);
          }
          MEMFENCE;
        };
        auto wrcs = [&]() AI {
          if (L > 0) {
            f32x4 t = add4(__builtin_bit_cast(f32x4, c0), __builtin_bit_cast(f32x4, c1));
#pragma unroll
            for (int e = 0; e < 4; ++e)
              t[e] += __uint_as_float(__builtin_amdgcn_update_dpp(0u, __float_as_uint(t[e]), 0x128, 0xf, 0xf, false));
            float t0 = t[0], t1 = t[1], t2 = t[2], t3 = t[3];
            xred4<false>(t0, t1, t2, t3);
            *(f32x4*)(lcs + (isB ? 96 : 0) + (L - 1) * 32 + (lane & 7) * 4) = f32x4{t0, t1, t2, t3};
          }
        };
        if (isB && layer == 1) ldcs();
        if constexpr (is0) {
          if (L == 3) flag(WS_F1 + 2048);
          q_groupW(0, 2, 2); q_groupW(0, 3, 3);
          wait_vm<12>();
        }
        STAMP(1);
        if (!is0) PRIO(3);
        sync_lds();
        STAMP(2);
        OPAQUE_S
        u32x4 s0 = u32x4{0u, 0u, 0u, 0u}, s1 = s0;
        auto ldst = [&]() AI {
          const unsigned so = ar + AR_L2 + (bm * 16 + L * 4) * 256;
          s0 = ld16s(wsr, v16, so);
          if (is0) s1 = ld16s(wsr, v16, so + 32768);
          if (isB && L == 0) s1 = ld16s(wsr, v16 & 255, bm > 0 ? so - 256 : so);
          MEMFENCE;
        };
        auto fin = [&](float sm, float sq, int r, bool own) AI {
          float mean = sm * (1.0f / 1024.0f);
          float var = fmaxf(__builtin_fmaf(sq, 1.0f / 1024.0f, -mul1(mean, mean)), 0.f);
          if (own) lmean[r] = -mean;
          lrstd[r] = __builtin_amdgcn_rsqf(var + 1e-5f);
        };
        auto wrst = [&]() AI {
          while (true) {
            unsigned d = (s0[1] ^ tag0) | (s0[3] ^ tag1);
            if (is0 || (isB && L == 0)) d |= (s1[1] ^ tag0) | (s1[3] ^ tag1);
            if (__builtin_expect(__builtin_amdgcn_ballot_w64(d != 0) == 0, 1) || --budget <= 0) break;
            COUNT(1);
            ldst();
          }
          MEMFENCE;
          float sm = __uint_as_float(s0[0]), sq = __uint_as_float(s0[2]);
          if (is0) { sm += __uint_as_float(s1[0]); sq += __uint_as_float(s1[2]); }
          fin(sm, sq, 16 + L * 64 + lane, true);
          if (isB && L == 0) fin(__uint_as_float(s1[0]), __uint_as_float(s1[2]), lane & 15, false);
        };
        auto q_group = [&](int g) AI {
          if (is0) {
            if (L & 1) SLEEP(SPR0);
            if (L & 2) SLEEP(2 * SPR0);
            q_groupW(layer, g, g & 3);
          } else if (g >= 5) q_groupW(layer, g, g < 6 ? g : g - 4);
        };
        if (!is0 && L == 0) {
          poll(ar + AR_FL, []() {}, 15);
          if (head < 16) {
            const unsigned so = ar + AR_ST + (bm * 16 + head) * 2048;
            s0 = ld16s(wsr, v16, so); s1 = ld16s(wsr, v16 + 1024, so);
            MEMFENCE;
          }
        }
        if (is0) {
          drain();
          poll(WS_F1 + 2048, [&]() AI { extra(1); });
          ldcs();
          extra2(1);
        }
        if (isB && layer == 1) wrcs();
        MEMFENCE;
        STAMP(3);
        if (!is0) PRIO(0);
        sync_lds();
        STAMP(4);
        OPAQUE_S
        if (!is0) drain();
        if (!is0 && L == 0 && head < 16) {
          f32x4 t = add4(__builtin_bit_cast(f32x4, s0), __builtin_bit_cast(f32x4, s1));
#pragma unroll
          for (int e = 0; e < 4; ++e)
            t[e] += __uint_as_float(__builtin_amdgcn_update_dpp(0u, __float_as_uint(t[e]), 0x128, 0xf, 0xf, false));
          const bool hi = lane & 8;
          put_l2(hi ? t[2] : t[0], hi ? t[3] : t[1], (lane & 7) * 32 + (lane & 8) * 2, ar + AR_L2 + (bm * 16 + head) * 256);
        }
        q_group(4);
        extra(2); extra2(2);
        STAMP(5);
        sync_lds();
        STAMP(6);
        OPAQUE_S
        drain();
        q_group(5);
        if (is0) wrcs();
        STAMP(7);
        bar();
        OPAQUE_S
        drain();
        if (is0 && L == 0 && v16 == 0) __builtin_amdgcn_raw_buffer_store_b64(u32x2{tag0, tag1}, wsr, 0, WS_F2 + (bm * 32 + head) * 8, 16);
        q_group(6);
        ldst();
        STAMP(8);
        bar();
        STAMP(9);
        OPAQUE_S
        drain();
        q_group(7);
        extra2(5);
        SCHED0;
        wrst();
        STAMP(10);
        bar();
        STAMP(11);
        OPAQUE_S
        drain();
        extra(6);
        if (isB) stg6(96 * 1024 + L * 6144);
        SCHED0;
      }
      STAMP(12);
      sync_lds();
      STAMP(13);
      OPAQUE_S
      u32x4 fl[2];
      fl[0] = fl[1] = u32x4{tag0, tag1, tag0, tag1};
      auto ldfl = [&]() AI { fl[0] = ld16s(wsr, v16, WS_F2); fl[1] = ld16s(wsr, v16 + 1024, WS_F2); };
      if (is0) ldfl();
      bar();
      constexpr int NR = isB ? 6 : 9;
      const unsigned ro = isB ? 48 * 1024 + L * 6144 : L * 9216;
      u32x4 r[NR];
      bool pend = false;
      auto flags = [&]() AI {
        const u32x4 d = (fl[0] ^ u32x4{tag0, tag1, tag0, tag1}) | (fl[1] ^ u32x4{tag0, tag1, tag0, tag1});
        pend = __builtin_amdgcn_ballot_w64((d[0] | d[1] | d[2] | d[3]) != 0) != 0;
      };
      if (isB) {
        stg6(L * 6144); stg(120 * 1024 + L * 2048, IC<2>{});
        stg6(24 * 1024 + L * 6144);
        stg6(72 * 1024 + L * 6144);
      } else {
        drain();
        if (is0) flags();
        MEMFENCE;
        extra(8); extra2(8);
        if (!is0) poison_x(3);
        while (is0 && __builtin_expect(pend, 0) && --budget > 0) {
          ldfl();
          flags();
          MEMFENCE;
        }
        stg(36 * 1024 + L * 3072, IC<3>{}); stg7(72 * 1024 + L * 14336); stg7(72 * 1024 + L * 14336 + 7168);
      }
#pragma unroll
      for (int i = 0; i < NR; ++i) r[i] = ld16s(wsr, v16 + (i & 3) * 1024, sW() + ro + (i >> 2) * 4096);
      MEMFENCE;
      if (is0 && bm + head + L == 0 && v16 == 0) __builtin_amdgcn_raw_buffer_store_b32(tag0, wsr, 0, WS_EP, 16);
      auto wr_r = [&]() AI {
        unsigned vv = v16;
        asm volatile("" : "+v"(vv));
#pragma unroll
        for (int i = 0; i < NR; ++i) *(u32x4*)(smem + L_STG + ro + i * 1024 + vv) = r[i];
      };
      auto left = [&]() AI {
        OPAQUE_LANE
        for (;;) {
          MEMFENCE;
          if (__builtin_amdgcn_ballot_w64(lflag[lane & 7] != (unsigned)layer) == 0 || --budget <= 0) break;
          SLEEP(2);
        }
        MEMFENCE;
      };
      if (isB) {
#pragma unroll
        for (int i = 0; i < 24; ++i) asm volatile("" : "=v"(wn[i / 6][i % 6]));
        if (layer == 1) ld_wn(layer + 1, 0, 2);
        STAMP(14);
        left();
        wr_r();
        MEMFENCE;
        if (layer == 1) ld_wn(layer + 1, 2, 4);
        wait_vm_lgkm0<12>();
        if (layer == 3) wait_vm<0>();
      } else {
        constexpr int W0 = is0 ? 0 : 2, W1 = is0 ? 1 : 2;
        ld_wn(layer + 1, 0, W0);
        if (is0) wait_vm<26>();
        STAMP(14);
        bar();
        stg6(48 * 1024 + L * 6144);
        ld_wn(layer + 1, W0, W1);
        bar();
        extra2(9);
        ld_wn(layer + 1, W1, 4);
        wr_r();
        wait_vm<6 * (4 - W1)>();
        bar();
        extra2(10);
        wait_vm_lgkm0<0>();
      }
      bar();
      if (isB && layer == 3) { STAMP_DUMP; __builtin_amdgcn_endpgm(); }
      OPAQUE_S
      wr_wn(0);
      drain();
      sync_lds();
      OPAQUE_S
      SCHED0;
#pragma unroll
      for (int g = 1; g < 4; ++g) wr_wn(g);
      MEMFENCE;
      SLEEP(SLP_G);
      q_groupW(layer + 1, 4, 4);
    };
    body(IC<0>{}, 0);
#pragma unroll 1
    for (int layer = 1;; layer += 2) {
      body(IC<1>{}, layer);
      body(IC<2>{}, 2);
    }
  }

  const f32x4 zero = f32x4{0.f, 0.f, 0.f, 0.f};
  u32x4 xr[2];
  asm volatile("" : "=v"(xr[0]), "=v"(xr[1]));
  u32x4 sel;
  u32x4 eye;
  unsigned vk;
  unsigned ra0, ra1;
  float off0, off1;
  {
    const int lane = LANEID, fr = lane & 15, fq = lane >> 4;
    const bool odd = fr & 1, own0 = fr >= fq * 4 + (fr & 1), own1 = fr >= fq * 4 + (fr & 1) + 2, nopre = bm0 == 0 && wave == 4;
    const unsigned e0 = odd ? 0x01000c0cu : 0x0c0c0100u, e1 = odd ? 0x03020c0cu : 0x0c0c0302u, ez = 0x0c0c0c0cu;
    sel = u32x4{own0 ? ez : e0, own1 ? ez : e1, own0 ? e0 : ez, own1 ? e1 : ez};
    const unsigned rb = sbase + L_RED + 1088 + ((wave - 4) * 32 + fq * 4 + (fr & 1)) * 4;
    ra0 = rb + (own0 ? 64 : 0); ra1 = rb + (own1 ? 72 : 8);
    off0 = nopre && !own0 ? -3.0e38f : 0.f; off1 = nopre && !own1 ? -3.0e38f : 0.f;
    const unsigned one = (fq & 1) == (fr >> 3) ? (fr & 1 ? 0x3f800000u : 0x00003f80u) : 0u;
    eye = u32x4{(fr & 6) == 0 ? one : 0u, (fr & 6) == 2 ? one : 0u, (fr & 6) == 4 ? one : 0u, (fr & 6) == 6 ? one : 0u};
    vk = (fq >> 1) * (32 * KVREC) + (fq & 1) * 256 + fr * 16;
    asm volatile("" : "+v"(sel), "+v"(ra0), "+v"(ra1), "+v"(off0), "+v"(off1), "+v"(eye), "+v"(vk));
  }
#pragma unroll 1
  for (int layer = 0; layer < 4; ++layer) {
    bm = bm0; head = head0; cm = cm0; cn = cn0; cw = wave - 4;
    const bool isB = layer & 1;
    const unsigned ar = WS_AR + layer * ARENA;
    {
      f32x4 acc[2][6];
      f32x4 ck[2], mv[2], xv[2];
      float mean[2], rs[2], cv[2];
      const float* lcl = lcs + (isB ? 96 : 0);
      {
        LANE_VARS
        const unsigned va = v16 + cw * 2048;
        const unsigned sa0 = ar + AR_X + bm * (512 * 1024);
        auto lda = [&](unsigned so, u32x4* a) AI {
          a[0] = ld16s(wsr, va, so);
          a[1] = ld16s(wsr, va + 1024, so);
        };
        bf16x8 f[2][6];
        u32x4 a[4][2];
        auto unset = [&](const u32x4* e) AI { return poisoned(umax(umax(umax(e[0][0], e[0][2]), e[1][0]), e[1][2])); };
        auto refill = [&](auto ic, unsigned cur) AI {
          constexpr int I = decltype(ic)::value;
          do {
            COUNT(0);
            SLEEP(SLP_R);
            MEMFENCE;
            lda(cur, a[I]);
            bool u[4];
#pragma unroll
            for (int j = 1; j < 4; ++j) u[j] = unset(a[(I + j) % 4]);
            SCHED0;
#pragma unroll
            for (int j = 1; j < 4; ++j)
              if (u[j]) lda(cur + j * 16384, a[(I + j) % 4]);
            __builtin_amdgcn_s_waitcnt(0x0f70);
            MEMFENCE;
          } while (unset(a[I]) && --budget > 0);
        };
        STAMP(0);
        if (layer > 0) {
          bar();
          if (cn < 8) { if (cw < 4) SLEEP(SLP_X0); else SLEEP(SLP_X1); }
          else { if (cw < 4) SLEEP(SLP_XD0); else SLEEP(SLP_XD1); }
        }
#pragma unroll
        for (int j = 0; j < 4; ++j) { lda(sa0 + j * 16384, a[j]); SCHED0; }
#pragma unroll
        for (int m = 0; m < 2; ++m)
#pragma unroll
          for (int n = 0; n < 6; ++n) acc[m][n] = zero;
        asm volatile("" : "+v"(acc[0][0]), "+v"(acc[0][1]), "+v"(acc[0][2]), "+v"(acc[0][3]), "+v"(acc[0][4]), "+v"(acc[0][5]));
        asm volatile("" : "+v"(acc[1][0]), "+v"(acc[1][1]), "+v"(acc[1][2]), "+v"(acc[1][3]), "+v"(acc[1][4]), "+v"(acc[1][5]));
        if (layer == 0) { wait_vm<8>(); sync_lds(); }
#pragma unroll
        for (int nn = 0; nn < 6; ++nn) { const int n = nn < 4 ? nn + 2 : nn - 4; f[0][n] = *(const bf16x8*)(smem + v16 + n * 1024); }
        SCHED0;
        if (layer > 0) {
          const u32x2 tg = *(const u32x2*)(lflag + 10);
          wait_vm<8>();
          unsigned old;
          asm volatile("s_mov_b64 exec, 1\n\tds_add_rtn_u32 %0, %1, %2\n\ts_mov_b64 exec, -1\n\ts_waitcnt lgkmcnt(0)" : "=v"(old) : "v"(sbase + L_RED + 32), "v"(1) : "memory");
          if (__builtin_amdgcn_readfirstlane(old) == 8u * layer - 1)
            __builtin_amdgcn_raw_buffer_store_b64(tg, wsr, lane ? OOB : 0, ar + AR_FL + bm * 256 + (cm * 16 + cn) * 8, 16);
          MEMFENCE;
        }
        SCHED0;
        bool bad = layer > 0 && unset(a[0]);
        const int wrap = layer > 0 ? 6 : 4;
        unsigned ps[4] = {sbase + L_RED + 64 + cw * 128, sbase + L_RED + 2176 + (layer & 1) * 384, 0, WS_AR + AR_X + xt_off(bm * 16 + cw * 2 + cm, cn * 2)};
        ps[2] = ps[0] + 1088 + (layer & 1) * (2176 + 384 - 1088 - 64 - cw * 128);
        int early = (layer & 1) * cw;
        asm volatile("" : "+s"(ps[0]), "+s"(ps[1]), "+s"(ps[2]), "+s"(ps[3]), "+s"(early));
        STAMP(1);
        auto group = [&](auto modec, int g) AI {
          constexpr int MODE = decltype(modec)::value;
          constexpr bool LAST = MODE == 3, CHK = MODE == 0;
          const unsigned gs = (g < wrap ? g : g - 4) * QGRP, gn = (g + 1 < wrap ? g + 1 : g - 3) * QGRP;
          const unsigned sc = sa0 + g * 65536, so = sc + 4 * 16384;
          unsigned gl = sbase + gs + v16;
          if constexpr (LAST) asm volatile("" : "+v"(gl));
          unsigned pa[4];
          static_for<4>([&](auto ic) AI {
            constexpr int i = decltype(ic)::value;
            constexpr bool END = LAST && i == 3;
            SCHED0;
            if (i == 3 && !LAST) { sync_lds(); SCHED0; }
            if (i == 0 && LAST) {
              PRIO(0);
              if (early >= 4) PRIO(3);
              SCHED0;
              bar();
              SCHED0;
            }
            if constexpr (!LAST) PRIO(i == 3 ? 3 : 2 - i);
            if constexpr (CHK) { if (__builtin_expect(bad, 0)) refill(ic, sc + i * 16384); }
            SCHED0;
            const bf16x8* fc = f[i & 1];
            bf16x8* fn = f[(i + 1) & 1];
            const u32x4* an = a[(i + 1) % 4];
            const unsigned slot = i < 3 ? gs + (i + 1) * QSLOT : gn;
            const char* pb = LAST ? (const char*)((const LDS(char)*)gl + (i + 1) * QSLOT) : smem + slot + v16;
            unsigned mx = 0;
            static_for<12>([&](auto jc) AI {
              constexpr int j = decltype(jc)::value, nn = j >> 1, m = j & 1, n = nn < 4 ? nn + 2 : nn - 4;
              if constexpr (n < 4) mfma_acc(acc[m][n], fc[n], as_frag(a[i][m]));
              else mfma_acc(acc[m][n], as_frag(a[i][m]), fc[n]);
              SCHED0;
              if constexpr (!END && j < 6) { constexpr int r = j < 4 ? j + 2 : j - 4; fn[r] = *(const bf16x8*)(pb + r * 1024); }
              if constexpr (LAST && i == 0 && j == 6) {
                if (layer == 0) { xr[0] = ld16s(wsr, v16, ps[3]); xr[1] = ld16s(wsr, v16, ps[3] + 16384); }
              }
              if constexpr (LAST && i == 1) {
                if constexpr (j == 6) { pa[0] = lane & 48; asm volatile("" : "+v"(pa[0])); }
                if constexpr (j == 7) { pa[1] = v16 >> 2; asm volatile("" : "+v"(pa[1])); }
                if constexpr (j == 8) { pa[1] &= 60; asm volatile("" : "+v"(pa[1])); }
                if constexpr (j == 9) { pa[2] = pa[1] + ps[0]; asm volatile("" : "+v"(pa[2])); }
                if constexpr (j == 10) { pa[1] += ps[1]; asm volatile("" : "+v"(pa[1])); }
                if constexpr (j == 11) { pa[3] = pa[0] + ps[1]; asm volatile("" : "+v"(pa[3])); }
              }
              if constexpr (LAST && i == 2) {
                if constexpr (j == 6) ck[0] = ((const LDS(f32x4)*)pa[3])[8];
                if constexpr (j == 7) ck[1] = ((const LDS(f32x4)*)pa[3])[12];
                if constexpr (j == 8) { mean[0] = ((const LDS(float)*)pa[2])[0]; asm volatile("" : "+v"(pa[2])); }
                if constexpr (j == 9) { mean[1] = ((const LDS(float)*)pa[2])[16]; asm volatile("" : "+v"(pa[2])); }
                if constexpr (j == 10) { cv[0] = ((const LDS(float)*)pa[1])[64]; cv[1] = ((const LDS(float)*)pa[1])[80]; }
                if constexpr (j == 11) { pa[1] = pa[0] + ps[0]; asm volatile("" : "+v"(pa[1])); }
              }
              if constexpr (LAST && i == 3) {
                if constexpr (j == 0) mv[0] = ((const LDS(f32x4)*)pa[1])[0];
                if constexpr (j == 1) mv[1] = ((const LDS(f32x4)*)pa[1])[4];
                if constexpr (j == 2) { pa[0] += ps[2]; asm volatile("" : "+v"(pa[0])); }
                if constexpr (j == 3) { rs[0] = ((const LDS(float)*)pa[2])[272]; asm volatile("" : "+v"(pa[2])); }
                if constexpr (j == 4) xv[0] = ((const LDS(f32x4)*)pa[0])[0];
                if constexpr (j == 5) xv[1] = ((const LDS(f32x4)*)pa[0])[4];
                if constexpr (j == 6) rs[1] = ((const LDS(float)*)pa[2])[288];
              }
              if constexpr (CHK) {
                if constexpr (j == 8) mx = umax(umax(umax(an[0][0], an[0][2]), an[1][0]), an[1][2]);
                if constexpr (j == 10) bad = poisoned(mx);
              }
              if constexpr (j == 10 && !LAST) a[i][0] = ld16s(wsr, va, so + i * 16384);
              if constexpr (j == 11 && !LAST) a[i][1] = ld16s(wsr, va + 1024, so + i * 16384);
              SCHED0;
            });
          });
        };
        int g = 0;
        if (layer > 0) {
#pragma unroll 1
          for (; g < NCHK; ++g) group(IC<0>{}, g);
        }
#pragma unroll 1
        for (; g < 7; ++g) group(IC<1>{}, g);
        group(IC<3>{}, 7);
        if (cw < 4) PRIO(1); else if (!isB) PRIO(2);
        mfma_done();
      }
      STAMP(2);
      LANE_VARS
      char* const scr = smem;

      u32x4 qf[2], kf[2];
      u32x2 vf[2][2];
      auto fold_rows = [&](int part, u32x4* out, const f32x4* c) AI {
#pragma unroll
        for (int m = 0; m < 2; ++m) {
          f32x4 a0 = acc[m][part * 2] + c[0] * mean[m], a1 = acc[m][part * 2 + 1] + c[1] * mean[m];
          out[m] = u32x4{pk2(a0[0], a0[1]), pk2(a0[2], a0[3]), pk2(a1[0], a1[1]), pk2(a1[2], a1[3])};
        }
      };
      auto fold_x = [&](int part, f32x2 (*x)[4], const f32x4* c) AI {
#pragma unroll
        for (int m = 0; m < 2; ++m) {
          const f32x4 a0 = acc[m][part * 2] + c[0] * mean[m], a1 = acc[m][part * 2 + 1] + c[1] * mean[m];
          x[m][0] = f32x2{a0[0], a0[1]}; x[m][1] = f32x2{a0[2], a0[3]}; x[m][2] = f32x2{a1[0], a1[1]}; x[m][3] = f32x2{a1[2], a1[3]};
        }
      };
      auto fold_v = [&](bool scaled) AI {
#pragma unroll
        for (int m = 0; m < 2; ++m) {
          f32x4 a0 = acc[m][4] + mv[m] * cv[0], a1 = acc[m][5] + mv[m] * cv[1];
          if (scaled) { a0 = mul4(a0, xv[m]); a1 = mul4(a1, xv[m]); }
          vf[m][0] = u32x2{pk2(a0[0], a0[1]), pk2(a0[2], a0[3])};
          vf[m][1] = u32x2{pk2(a1[0], a1[1]), pk2(a1[2], a1[3])};
        }
      };
      const unsigned otile = WS_AR + AR_O + xt_off(bm * 16 + cw * 2, head);
      auto put_o = [&](int m, f32x4 a, f32x4 b, unsigned opar) AI {
        const u32x4 v = u32x4{(pk2(a[0], a[1]) & ~1u) | opar, pk2(a[2], a[3]), (pk2(b[0], b[1]) & ~1u) | opar, pk2(b[2], b[3])};
        st16s(wsr, v16 + m * 1024, otile, v);
        return v;
      };
      auto hold = [&](u32x4 a, u32x4 b) AI { SCHED0; asm volatile("s_nop 1" :: "v"(a), "v"(b)); SCHED0; };

      if (!isB) {
        f32x2 lx[2][4];
        fold_x(1, lx, ck); fold_v(true);
        char* PK = smem + L_STG;
        char* VF = scr;
#pragma unroll
        for (int t = 0; t < 2; ++t)
          *(u32x4*)(VF + (cw * 2 + t) * 1024 + lane * 16) = u32x4{vf[0][t][0], vf[0][t][1], vf[1][t][0], vf[1][t][1]};
        {
          u32x4 pk[2][2];
          phi_map2(lx, rs, pk, [](float*) {});
          char* d0 = PK + cw * 4096 + fr * 64 + ((fr >> 2) & 1) * 32 + ((fq ^ ((fr >> 3) * 2)) * 8);
          char* d1 = PK + cw * 4096 + fr * 64 + (((fr >> 2) & 1) ^ 1) * 32 + ((fq ^ ((fr >> 3) * 2)) * 8);
#pragma unroll
          for (int m = 0; m < 2; ++m) {
            *(u32x2*)(d0 + m * 2048) = u32x2{pk[m][0][0], pk[m][0][1]};
            *(u32x2*)(d0 + m * 2048 + 1024) = u32x2{pk[m][0][2], pk[m][0][3]};
            *(u32x2*)(d1 + m * 2048) = u32x2{pk[m][1][0], pk[m][1][1]};
            *(u32x2*)(d1 + m * 2048 + 1024) = u32x2{pk[m][1][2], pk[m][1][3]};
          }
        }
        PRIO(1);
        const unsigned rec = ar + AR_KV + (unsigned)head * KVREC, dst = rec + (unsigned)bm * (32 * KVREC);
        unsigned ad = sbase + L_STG + (cw & 1) * 1024 + (fq * 4 + (fr >> 2)) * 64 + (((cw >> 1) ^ fq) & 1) * 32 + (((fr & 3) ^ (fq & 2)) * 8);
        unsigned vd = sbase + (cw >> 2) * 1024 + v16, kd = fq & 1 ? OOB : dst + cw * 512 + (fq >> 1) * 256 + fr * 16;
        asm volatile("" : "+v"(ad), "+v"(vd), "+v"(kd));
        sync_lds();
        STAMP(3);
        {
          const u32x4 ones = u32x4{0x3f803f80u, 0x3f803f80u, 0x3f803f80u, 0x3f803f80u};
          f32x4 c0 = zero;
          bf16x8 a[8], b0[8];
#pragma unroll
          for (int ks = 0; ks < 8; ++ks) {
            u32x2 a0 = trl(ad + ks * 4096), a1 = trl(ad + ks * 4096 + 2048);
            a[ks] = as_frag(u32x4{a0[0], a0[1], a1[0], a1[1]});
            b0[ks] = *(const LDS(bf16x8)*)(vd + ks * 2048);
            SCHED0;
          }
#pragma unroll
          for (int ks = 0; ks < 8; ++ks) c0 = mfma16(a[ks], b0[ks], c0);
          const unsigned p0 = umin(pk2(c0[0], c0[1]), POISON - 1), p1 = umin(pk2(c0[2], c0[3]), POISON - 1);
          const auto q0 = __builtin_amdgcn_permlane16_swap(p0, p0, false, false), q1 = __builtin_amdgcn_permlane16_swap(p1, p1, false, false);
          st16(wsr, kd, u32x4{q0[0], q1[0], q0[1], q1[1]});
          SCHED0;
          if (cw < 4) {
            f32x4 cz = zero;
#pragma unroll
            for (int ks = 0; ks < 8; ++ks) cz = mfma16(a[ks], as_frag(ones), cz);
            st16(wsr, fr ? OOB : dst + 4096 + cw * 64 + fq * 16, clean(__builtin_bit_cast(u32x4, cz)));
          }
        }
        SCHED0;
        STAMP(4);
        bar();
        const f32x4 cq[2] = {*(const f32x4*)(lcl + fq * 4), *(const f32x4*)(lcl + 16 + fq * 4)};
        fold_x(0, lx, cq);
        u32x4 pq[2][2];
        u32x4 zp, zq;
        u32x4 part[4];
        auto ldp = [&](int i) AI { part[i] = ld16s(wsr, vk, rec + i * (64 * KVREC) + cw * 512); };
        auto ldz = [&]() AI {
          zp = ld16(wsr, rec + fq * (32 * KVREC) + 4096 + fr * 16);
          zq = ld16(wsr, rec + (4 + fq) * (32 * KVREC) + 4096 + fr * 16);
        };
#pragma unroll
        for (int i = 0; i < 4; ++i) asm volatile("" : "=v"(part[i]));
        asm volatile("" : "=v"(zp), "=v"(zq));
        phi_map2(lx, rs, pq, [&](float* sp) AI {
          asm volatile("" : "+v"(sp[0]), "+v"(sp[1]));
          SCHED0;
          if (cw >= 4) {
            MEMFENCE;
#pragma unroll
            for (int i = 0; i < 4; ++i) ldp(i);
            MEMFENCE;
          }
          SCHED0;
        });
        asm volatile("" : "+v"(pq[0][0]), "+v"(pq[0][1]), "+v"(pq[1][0]), "+v"(pq[1][1]));
        SCHED0;
        STAMP(5);
        char* kvf = smem + L_A2;
        if (cw < 4) {
          SLEEP(SLP_K);
          MEMFENCE;
          if (cw == 0) ldz();
#pragma unroll
          for (int i = 0; i < 4; ++i) ldp(i);
          MEMFENCE;
        }
        unsigned kw = sbase + L_A2 + v16 + (cw >> 1) * 1024 + (cw & 1) * 8;
        unsigned ka = sbase + L_A2 + v16, za = sbase + L_Z + fq * 16;
        asm volatile("" : "+v"(kw), "+v"(ka), "+v"(za));
        u32x2 kvs;
        auto nans = [&](f32x4 v) AI { return __builtin_amdgcn_ballot_w64(__builtin_isunordered(v[0], v[1])) | __builtin_amdgcn_ballot_w64(__builtin_isunordered(v[2], v[3])); };
        auto sums = [&]() AI {
          unsigned long long nan = 0;
          if (cw == 0) {
            f32x4 z = __builtin_bit_cast(f32x4, zp) + __builtin_bit_cast(f32x4, zq);
            nan = nans(z);
            float z0 = z[0], z1 = z[1], z2 = z[2], z3 = z[3];
            xred4<false>(z0, z1, z2, z3);
            *(u32x2*)(smem + L_Z + (fr >> 3) * 64 + (fr & 3) * 16 + ((fr >> 2) & 1) * 8) = u32x2{pk2(z0, z1), pk2(z2, z3)};
          }
          f32x4 s4 = zero;
#pragma unroll
          for (int i = 0; i < 4; ++i) { s4 = mfma16(as_frag(eye), as_frag(part[i]), s4); SCHED0; }
          kvs = u32x2{pk2(s4[0], s4[1]), pk2(s4[2], s4[3])};
          asm volatile("" : "+v"(kvs));
          return nan | __builtin_amdgcn_ballot_w64(s4[0] != s4[0]);
        };
        if (__builtin_expect(sums() != 0, 0)) {
          for (;;) {
            bool again = false;
            if (cw == 0 && poisoned(umax4(umax4(zp), zq))) { again = true; ldz(); }
#pragma unroll
            for (int i = 0; i < 4; ++i)
              if (poisoned(umax4(part[i]))) { again = true; ldp(i); }
            __builtin_amdgcn_s_waitcnt(0x0f70);
            MEMFENCE;
            if (!again || --budget <= 0) break;
            COUNT(2);
          }
          sums();
        }
        STAMP(6);
        *(LDS(u32x2)*)kw = kvs;
        sync_lds();
        STAMP(7);
        bf16x8 kvr[2][2], zf[2];
#pragma unroll
        for (int s = 0; s < 2; ++s) { zf[s] = *(const LDS(bf16x8)*)(za + s * 64); SCHED0; }
#pragma unroll
        for (int s = 0; s < 2; ++s) {
          kvr[0][s] = *(const LDS(bf16x8)*)(ka + (0 * 2 + s) * 1024);
          kvr[1][s] = *(const LDS(bf16x8)*)(ka + (1 * 2 + s) * 1024);
          SCHED0;
        }
        f32x4 cd[2], c[2][2];
        u32x4 oo[2];
        auto den = [&](int m, int s) AI { cd[m] = mfma16(zf[s], as_frag(pq[m][s]), s ? cd[m] : zero); SCHED0; };
        auto num = [&](int m, int s) AI {
#pragma unroll
          for (int t = 0; t < 2; ++t) { c[m][t] = mfma16(kvr[t][s], as_frag(pq[m][s]), s ? c[m][t] : zero); SCHED0; }
        };
        auto fin = [&](int m, float id) AI { const f32x4 i4 = f32x4{id, id, id, id}; oo[m] = put_o(m, mul4(c[m][0], i4), mul4(c[m][1], i4), 0); SCHED0; };
        den(0, 0); den(1, 0); den(0, 1); den(1, 1);
        num(0, 0); num(0, 1); num(1, 0);
        const float i0 = __builtin_amdgcn_rcpf(cd[0][0] + 1e-6f), i1 = __builtin_amdgcn_rcpf(cd[1][0] + 1e-6f);
        SCHED0;
        num(1, 1);
        fin(0, i0); fin(1, i1);
        hold(oo[0], oo[1]);
        STAMP(8);
        asm volatile("" : "=v"(acc[0][0]), "=v"(acc[0][1]), "=v"(acc[0][2]), "=v"(acc[0][3]), "=v"(acc[0][4]), "=v"(acc[0][5]));
        asm volatile("" : "=v"(acc[1][0]), "=v"(acc[1][1]), "=v"(acc[1][2]), "=v"(acc[1][3]), "=v"(acc[1][4]), "=v"(acc[1][5]));
        asm volatile("" : "=v"(ck[0]), "=v"(ck[1]), "=v"(mv[0]), "=v"(mv[1]), "=v"(xv[0]), "=v"(xv[1]));
        asm volatile("" : "=v"(mean[0]), "=v"(mean[1]), "=v"(rs[0]), "=v"(rs[1]), "=v"(cv[0]), "=v"(cv[1]));
      }
      int kind = layer & 1;
      asm volatile("" : "+s"(kind));
      if (kind) {
        char* ex = scr;
        fold_rows(1, kf, ck); fold_v(false);
        const unsigned hrec = ar + AR_KV + (unsigned)(bm * 32 + head) * KVREC;
        {
          const u32x4 vl = u32x4{vf[1][0][0], vf[1][0][1], vf[1][1][0], vf[1][1][1]};
          if (cw == 7) {
            st16s(wsr, v16, hrec, clean(kf[1]));
            STPAD;
            st16s(wsr, v16 + 1024, hrec, clean(vl));
            STPAD;
          }
          char* e2 = ex + (cw + 1) * 2048;
          *(u32x4*)(e2 + lane * 16) = kf[1];
          *(u32x2*)(e2 + 1024 + lane * 8) = vf[1][0];
          *(u32x2*)(e2 + 1536 + lane * 8) = vf[1][1];
          MEMFENCE;
          lflag[cw] = layer;
          MEMFENCE;
          PRIO(1);
        }
        SCHED0;
        fold_rows(0, qf, xv);
        u32x4 kp;
        u32x4 va0 = __builtin_shufflevector(vf[0][0], vf[0][0], -1, -1, 0, 1), vb0 = __builtin_shufflevector(vf[0][1], vf[0][1], -1, -1, 0, 1);
        auto setp = [&](u32x4& d, u32x2 v) AI { d[0] = v[0]; d[1] = v[1]; };
        auto ldh = [&]() AI {
          MEMFENCE;
          kp = ld16s(wsr, v16, hrec - 32 * KVREC);
          setp(va0, __builtin_amdgcn_raw_buffer_load_b64(wsr, v16 + 1024, hrec - 32 * KVREC, 16));
          MEMFENCE;
          setp(vb0, __builtin_amdgcn_raw_buffer_load_b64(wsr, v16 + 1032, hrec - 32 * KVREC, 16));
          MEMFENCE;
        };
        unsigned fl = 0;
        auto rdl = [&]() AI {
          MEMFENCE;
          fl = lflag[cw - 1];
          MEMFENCE;
          kp = *(const u32x4*)(ex + cw * 2048 + lane * 16);
          setp(va0, *(const u32x2*)(ex + cw * 2048 + 1024 + lane * 8));
          MEMFENCE;
          setp(vb0, *(const u32x2*)(ex + cw * 2048 + 1536 + lane * 8));
          MEMFENCE;
        };
        auto prev = [&]() AI {
          if (cw > 0) {
            do { SLEEP(1); rdl(); __builtin_amdgcn_s_waitcnt(0xc07f); } while (__builtin_amdgcn_readfirstlane(fl) != (unsigned)layer && --budget > 0);
          } else if (bm > 0) {
            SLEEP(SLP_HX);
            do { ldh(); __builtin_amdgcn_s_waitcnt(0x0f70); }
            while (poisoned(umax4(umax4(kp), u32x4{va0[0], va0[1], vb0[0], vb0[1]})) && --budget > 0);
          } else {
            kp = u32x4{0u, 0u, 0u, 0u};
            setp(va0, u32x2{0u, 0u}); setp(vb0, u32x2{0u, 0u});
          }
          asm volatile("" : "+v"(kp), "+v"(va0), "+v"(vb0));
        };
        const float scale = 0.17677669529663687f * LOG2E;
        const bool odd = fr & 1;
        const bool own0 = sel[0] == 0x0c0c0c0cu, own1 = sel[1] == 0x0c0c0c0cu;
        float l[2][2], mx[2], rk[2][2], sum[2];
#pragma unroll
        for (int m = 0; m < 2; ++m) { rk[m][0] = ((const LDS(float)*)ra0)[m * 16]; rk[m][1] = ((const LDS(float)*)ra1)[m * 16]; }
        float qk[2][2];
#pragma unroll
        for (int m = 0; m < 2; ++m) { const float qs = rs[m] * scale; qk[m][0] = qs * rk[m][0]; qk[m][1] = qs * rk[m][1]; }
        u32x4 va1 = u32x4{vf[0][0][0], vf[0][0][1], vf[1][0][0], vf[1][0][1]}, vb1 = u32x4{vf[0][1][0], vf[0][1][1], vf[1][1][0], vf[1][1][1]};
        auto score = [&](int m) AI {
          f32x4 s0 = mfma16(as_frag(m == 0 ? kp : kf[0]), as_frag(qf[m]), zero);
          f32x4 s1 = mfma16(as_frag(kf[m]), as_frag(qf[m]), zero);
          float a = own0 ? (odd ? s1[1] : s1[0]) : (odd ? s0[1] : s0[0]);
          float b = own1 ? (odd ? s1[3] : s1[2]) : (odd ? s0[3] : s0[2]);
          if (m == 0) { l[m][0] = __builtin_fmaf(a, qk[m][0], off0); l[m][1] = __builtin_fmaf(b, qk[m][1], off1); }
          else { l[m][0] = a * qk[m][0]; l[m][1] = b * qk[m][1]; }
          mx[m] = fmx(l[m][0], l[m][1]);
        };
        auto expo = [&](int m) AI {
          l[m][0] = __builtin_amdgcn_exp2f(l[m][0] - mx[m]);
          l[m][1] = __builtin_amdgcn_exp2f(l[m][1] - mx[m]);
          sum[m] = l[m][0] + l[m][1];
        };
        f32x4 ov[2][2];
        auto pv = [&](int m) AI {
          const unsigned pr = pk2(mul1(mul1(l[m][0], rk[m][0]), sum[m]), mul1(mul1(l[m][1], rk[m][1]), sum[m]));
          const u32x4 pf = u32x4{__builtin_amdgcn_perm(pr, pr, sel[0]), __builtin_amdgcn_perm(pr, pr, sel[1]),
                                 __builtin_amdgcn_perm(pr, pr, sel[2]), __builtin_amdgcn_perm(pr, pr, sel[3])};
          ov[m][0] = mfma16(as_frag(m ? va1 : va0), as_frag(pf), zero);
          ov[m][1] = mfma16(as_frag(m ? vb1 : vb0), as_frag(pf), zero);
        };
        asm volatile("" : "+v"(qf[0]), "+v"(qf[1]), "+v"(va0), "+v"(vb0));
        SCHED0;
        if (cw > 0) rdl();
        SCHED0;
        score(1);
        asm volatile("" : "+v"(qf[0]), "+v"(qk[0][0]), "+v"(qk[0][1]), "+v"(mx[1]), "+v"(l[1][0]), "+v"(l[1][1]));
        asm volatile("" : "+v"(va1), "+v"(vb1));
        SCHED0;
        STAMP(3);
        if (__builtin_expect(cw == 0 || __builtin_amdgcn_readfirstlane(fl) != (unsigned)layer, 0)) prev();
        score(0);
        xred2<true>(mx[0], mx[1]);
        expo(0); expo(1);
        xred2<false, true>(sum[0], sum[1]);
        pv(0); pv(1);
        SCHED0;
        const u32x4 o0 = put_o(0, ov[0][0], ov[0][1], 1), o1 = put_o(1, ov[1][0], ov[1][1], 1);
        hold(o0, o1);
        STAMP(4);
      }
    }
    {
      LANE_VARS
      const int mt = bm * 16 + cw * 2 + cm;
      const unsigned so0 = WS_AR + AR_O + xt_off(mt, 0);
      const unsigned opar = layer & 1;
      auto diff = [&](u32x4 v) AI { return ((v[0] ^ opar) | (v[2] ^ opar)) & 1u; };
      auto dif3 = [&](u32x4 v) AI { return __builtin_amdgcn_bitop3_b32(v[0], v[2], opar, 0x7e); };
      auto any = [&](unsigned d) AI { return __builtin_amdgcn_ballot_w64(d != 0) != 0; };
      const int g0 = cm;
      u32x4 a[RO];
      auto refill = [&](auto ic, auto wc, unsigned cur, unsigned nxt) AI {
        constexpr int I = decltype(ic)::value, W = decltype(wc)::value;
        do {
          COUNT(1);
          SLEEP(SLP_R);
          MEMFENCE;
          a[I] = ld16s(wsr, v16, cur + I * 16384);
          bool u[RO];
#pragma unroll
          for (int j = I + 1; j < I + W; ++j) u[j % RO] = any(diff(a[j % RO]));
          SCHED0;
#pragma unroll
          for (int j = I + 1; j < I + W; ++j)
            if (u[j % RO]) a[j % RO] = ld16s(wsr, v16, (j < RO ? cur : nxt) + (j % RO) * 16384);
          __builtin_amdgcn_s_waitcnt(0x0f70);
          MEMFENCE;
        } while (any(diff(a[I]) | diff(a[I + 1])) && --budget > 0);
      };
      const unsigned sf = so0 + g0 * (RO * 16384);
      f32x4 acc[4];
#pragma unroll
      for (int n = 0; n < 4; ++n)
        acc[n] = f32x4{bflo(xr[n >> 1][(n & 1) * 2]), bfhi(xr[n >> 1][(n & 1) * 2]), bflo(xr[n >> 1][(n & 1) * 2 + 1]), bfhi(xr[n >> 1][(n & 1) * 2 + 1])};
      asm volatile("" : "+v"(acc[0]), "+v"(acc[1]), "+v"(acc[2]), "+v"(acc[3]));
      SCHED0;
      const bool late = isB && cw == 0;
      if (late) bar();
      if (cn < 8) { if (late) SLEEP(SLP_O0); else SLEEP(SLP_O); }
      else { if (late) SLEEP(SLP_O0D); else SLEEP(SLP_OD); }
#pragma unroll
      for (int j = 0; j < 4; ++j) { a[j] = ld16s(wsr, v16, sf + j * 16384); SCHED0; }
      if (!late) bar();
      STAMP(9);
      bf16x8 wf[2][4];
      const char* pw0 = smem + L_STG + v16;
#pragma unroll
      for (int n = 0; n < 8; ++n) wf[n >> 2][n & 3] = *(const bf16x8*)(pw0 + g0 * (RO * 4096) + n * 1024);
      SCHED0;
      STAMP(10);
      if (any(diff(a[0]) | diff(a[1]))) refill(IC<0>{}, IC<4>{}, sf, sf);
      SCHED0;
#pragma unroll
      for (int j = 4; j < RO; ++j) { a[j] = ld16s(wsr, v16, sf + j * 16384); SCHED0; }
      STAMP(11);
      bool bad = false;
      auto group = [&](auto lastc, int g) AI {
        constexpr bool LAST = decltype(lastc)::value;
        const int gc = g ^ g0, gn = (g + 1) ^ g0;
        const unsigned sc = so0 + gc * (RO * 16384), so = so0 + gn * (RO * 16384);
        const char* pc = pw0 + gc * (RO * 4096);
        const char* pn = pw0 + gn * (RO * 4096);
        static_for<RO>([&](auto ic) AI {
          constexpr int i = decltype(ic)::value;
          SCHED0;
          if constexpr (i % 2 == 0)
            if (__builtin_expect(bad, 0)) refill(ic, IC<(LAST && i + 4 > RO) ? RO - i : 4>{}, sc, so);
          SCHED0;
          unsigned mx = 0, my = 0;
          static_for<4>([&](auto nc) AI {
            constexpr int n = decltype(nc)::value;
            mfma_acc(acc[n], wf[i & 1][n], as_frag(a[i]));
            SCHED0;
            if constexpr (i + 2 < RO) wf[i & 1][n] = *(const bf16x8*)(pc + (i + 2) * 4096 + n * 1024);
            else if constexpr (!LAST) wf[i & 1][n] = *(const bf16x8*)(pn + (i + 2 - RO) * 4096 + n * 1024);
            if constexpr (!(LAST && i == RO - 1) && (i & 1)) {
              if constexpr (n == 0) mx = dif3(a[(i + 1) % RO]);
              if constexpr (n == 1) my = dif3(a[(i + 2) % RO]);
              if constexpr (n == 2) bad = any((mx | my) & 1u);
            }
            if constexpr (n == 3 && !LAST) a[i] = ld16s(wsr, v16, so + i * 16384);
            SCHED0;
          });
        });
      };
#pragma unroll 1
      for (int g = 0; g < 3; ++g) group(std::false_type{}, g);
      group(std::true_type{}, 3);
      mfma_done();
      STAMP(12);
#pragma unroll
      for (int j = 0; j < 2; ++j)
#pragma unroll
        for (int e = 0; e < 4; ++e) xr[j][e] = pk2(acc[j * 2 + (e >> 1)][(e & 1) * 2], acc[j * 2 + (e >> 1)][(e & 1) * 2 + 1]);
      if (layer == 3) {
        u32x4 yo[2];
#pragma unroll
        for (int j = 0; j < 2; ++j) {
          auto u = __builtin_amdgcn_permlane16_swap(xr[j][0], xr[j][2], false, false);
          auto v = __builtin_amdgcn_permlane16_swap(xr[j][1], xr[j][3], false, false);
          yo[j] = u32x4{u[0], v[0], u[1], v[1]};
        }
        const unsigned vo = fr * 2048 + (fq & 1) * 32 + (fq >> 1) * 16;
        __builtin_amdgcn_raw_buffer_store_b128(yo[0], mk_rsrc(p.out, 4 * MBY), vo, mt * 32768 + cn * 128, 2);
        __builtin_amdgcn_raw_buffer_store_b128(yo[1], mk_rsrc(p.out, 4 * MBY), vo + 64, mt * 32768 + cn * 128, 2);
        STAMP(13);
        STAMP(14);
        STAMP_DUMP;
        __builtin_amdgcn_endpgm();
      }
      const u32x2 stat = put_x(xr, v16, ar + ARENA + AR_X + xt_off(mt, cn * 2));
      __builtin_amdgcn_raw_buffer_store_b64(stat, wsr, fq ? OOB : fr * 8, ar + ARENA + AR_ST + (mt * 16 + cn) * 128, 16);
      MEMFENCE;
      STAMP(14);
    }
  }
}

extern "C" void kernel_launch(void* const* d_in, const int* in_sizes, int n_in, void* d_out, int out_size,
                              void* d_ws, size_t ws_size, hipStream_t stream) {
  (void)hipFuncSetAttribute((const void*)mega_kernel, hipFuncAttributeMaxDynamicSharedMemorySize, MEGA_LDS);
  MegaParams m;
  m.ws = (char*)d_ws; m.out = (u16*)d_out; m.x = (const u16*)d_in[0]; m.wphi = (const u16*)d_in[2];
  m.wqkvA = (const u16*)d_in[1]; m.wqkvB = (const u16*)d_in[5];
  m.woA = (const u16*)d_in[3]; m.woB = (const u16*)d_in[6];
  mega_kernel<<<256, 768, MEGA_LDS, stream>>>(m);
}
```

```cpp
#include <hip/hip_runtime.h>
#include <stdint.h>
#include <type_traits>

#define DI __device__ __forceinline__
#define AI __attribute__((always_inline))
typedef unsigned short u16;
typedef __attribute__((ext_vector_type(8))) short bf16x8;
typedef __attribute__((ext_vector_type(4))) short s16x4;
typedef __attribute__((ext_vector_type(4))) float f32x4;
typedef __attribute__((ext_vector_type(2))) float f32x2;
typedef __attribute__((ext_vector_type(2))) __bf16 bf2;
typedef __attribute__((ext_vector_type(4))) unsigned u32x4;
typedef __attribute__((ext_vector_type(2))) unsigned u32x2;
typedef __attribute__((address_space(3))) s16x4* lds_s16x4_ptr;
#define LDS(T) __attribute__((address_space(3))) T
template <int N> using IC = std::integral_constant<int, N>;

constexpr float LOG2E = 1.4426950408889634f;

DI unsigned pk2(float a, float b) {
  f32x2 v = {a, b};
  bf2 r = __builtin_convertvector(v, bf2);
  return __builtin_bit_cast(unsigned, r);
}
DI float bflo(unsigned u) { return __uint_as_float(u << 16); }
DI float bfhi(unsigned u) { return __uint_as_float(u & 0xffff0000u); }
DI f32x4 mfma16(bf16x8 a, bf16x8 b, f32x4 c) { return __builtin_amdgcn_mfma_f32_16x16x32_bf16(a, b, c, 0, 0, 0); }
DI void mfma_acc(f32x4& c, bf16x8 a, bf16x8 b) { asm volatile("v_mfma_f32_16x16x32_bf16 %0, %1, %2, %0" : "+v"(c) : "v"(a), "v"(b)); }
DI void mfma_done() { asm volatile("s_nop 7\n\ts_nop 1" ::: "memory"); __builtin_amdgcn_sched_barrier(0); }
DI bf16x8 as_frag(u32x4 v) { return __builtin_bit_cast(bf16x8, v); }
DI float dot2(unsigned a, unsigned b, float c) {
  return __builtin_amdgcn_fdot2_f32_bf16(__builtin_bit_cast(bf2, a), __builtin_bit_cast(bf2, b), c, false);
}
DI u32x2 trread(const char* p) { return __builtin_bit_cast(u32x2, __builtin_amdgcn_ds_read_tr16_b64_v4i16((lds_s16x4_ptr)p)); }
DI u32x2 trl(unsigned a) { return __builtin_bit_cast(u32x2, __builtin_amdgcn_ds_read_tr16_b64_v4i16((lds_s16x4_ptr)a)); }
DI float fmx(float a, float b) { return __builtin_elementwise_maximum(a, b); }
DI float fmn(float a, float b) { return __builtin_elementwise_minimum(a, b); }

DI float mul1(float a, float b) { float r; asm("v_mul_f32 %0, %1, %2" : "=v"(r) : "v"(a), "v"(b)); return r; }
DI float add1(float a, float b) { float r; asm("v_add_f32 %0, %1, %2" : "=v"(r) : "v"(a), "v"(b)); return r; }
DI f32x4 mul4(f32x4 a, f32x4 b) {
  float r0, r1, r2, r3;
  asm("v_mul_f32 %0, %4, %8\n\tv_mul_f32 %1, %5, %9\n\tv_mul_f32 %2, %6, %10\n\tv_mul_f32 %3, %7, %11"
      : "=&v"(r0), "=&v"(r1), "=&v"(r2), "=&v"(r3) : "v"(a[0]), "v"(a[1]), "v"(a[2]), "v"(a[3]), "v"(b[0]), "v"(b[1]), "v"(b[2]), "v"(b[3]));
  return f32x4{r0, r1, r2, r3};
}
DI f32x4 add4(f32x4 a, f32x4 b) { return f32x4{add1(a[0], b[0]), add1(a[1], b[1]), add1(a[2], b[2]), add1(a[3], b[3])}; }

template <bool IS_MAX, bool INV = false>
DI void xred2(float& a, float& b) {
  auto r = __builtin_amdgcn_permlane32_swap(__float_as_uint(a), __float_as_uint(b), false, false);
  float c = IS_MAX ? fmx(__uint_as_float(r[0]), __uint_as_float(r[1])) : __uint_as_float(r[0]) + __uint_as_float(r[1]);
  auto q = __builtin_amdgcn_permlane16_swap(__float_as_uint(c), __float_as_uint(c), false, false);
  c = IS_MAX ? fmx(__uint_as_float(q[0]), __uint_as_float(q[1])) : __uint_as_float(q[0]) + __uint_as_float(q[1]);
  if (INV) c = __builtin_amdgcn_rcpf(c);
  auto w = __builtin_amdgcn_permlane32_swap(__float_as_uint(c), __float_as_uint(c), false, false);
  a = __uint_as_float(w[0]); b = __uint_as_float(w[1]);
}
template <bool IS_MAX, bool INV = false>
DI void xred4(float& a, float& b, float& c, float& d) {
  auto op = [](unsigned x, unsigned y) { return IS_MAX ? fmx(__uint_as_float(x), __uint_as_float(y)) : __uint_as_float(x) + __uint_as_float(y); };
  auto r0 = __builtin_amdgcn_permlane32_swap(__float_as_uint(a), __float_as_uint(b), false, false);
  auto r1 = __builtin_amdgcn_permlane32_swap(__float_as_uint(c), __float_as_uint(d), false, false);
  float ab = op(r0[0], r0[1]), cd = op(r1[0], r1[1]);
  auto q = __builtin_amdgcn_permlane16_swap(__float_as_uint(ab), __float_as_uint(cd), false, false);
  float e = op(q[0], q[1]);
  if (INV) e = __builtin_amdgcn_rcpf(e);
  auto w = __builtin_amdgcn_permlane16_swap(__float_as_uint(e), __float_as_uint(e), false, false);
  auto u = __builtin_amdgcn_permlane32_swap(w[0], w[0], false, false);
  auto v = __builtin_amdgcn_permlane32_swap(w[1], w[1], false, false);
  a = __uint_as_float(u[0]); b = __uint_as_float(u[1]); c = __uint_as_float(v[0]); d = __uint_as_float(v[1]);
}

DI unsigned xt_off(int mt, int ks) { return (unsigned)((((mt >> 4) * 32 + ks) * 16 + (mt & 15)) << 10); }

#define GLDS_PRE "s_mov_b32 %0, m0\n\ts_mov_b32 m0, %3\n\ts_nop 2\n\t"
#define GLDS_POST "s_mov_b32 m0, %0"
#define GLDS_OPS : "=&s"(keep) : "v"(voff), "s"(sbase), "s"(lds) : "memory"
#define GL1(c) "global_load_lds_dwordx4 %1, %2" c "\n\t"
#define GL2(c) GL1(c) "global_load_lds_dwordx4 %1, %2 offset:1024" c "\n\t"
#define GL3(c) GL2(c) "global_load_lds_dwordx4 %1, %2 offset:2048" c "\n\t"
#define GL4(c) GL3(c) "global_load_lds_dwordx4 %1, %2 offset:3072" c "\n\t"
template <int N, bool SC1 = false>
DI void glds_run(unsigned voff, const void* sbase, unsigned lds) {
  unsigned keep;
  if constexpr (SC1) {
    if constexpr (N == 2) asm volatile(GLDS_PRE GL2(" sc1") GLDS_POST GLDS_OPS);
    else if constexpr (N == 3) asm volatile(GLDS_PRE GL3(" sc1") GLDS_POST GLDS_OPS);
    else asm volatile(GLDS_PRE GL4(" sc1") GLDS_POST GLDS_OPS);
  } else {
    static_assert(SC1 || N == 4, "");
    asm volatile(GLDS_PRE GL4("") GLDS_POST GLDS_OPS);
  }
}
template <int N> DI void wait_vm_lgkm0() { asm volatile("s_waitcnt vmcnt(%0) lgkmcnt(0)" ::"n"(N) : "memory"); }
template <int N> DI void wait_vm() { asm volatile("s_waitcnt vmcnt(%0)" ::"n"(N) : "memory"); }
DI void drain() { asm volatile("s_waitcnt vmcnt(0)" ::: "memory"); __builtin_amdgcn_s_waitcnt(0x0f70); }
DI void bar() { asm volatile("" ::: "memory"); __builtin_amdgcn_s_barrier(); asm volatile("" ::: "memory"); }
DI void sync_lds() { asm volatile("s_waitcnt lgkmcnt(0)" ::: "memory"); bar(); }
#define SCHED0 __builtin_amdgcn_sched_barrier(0)
#define STPAD do { SCHED0; asm volatile("s_nop 1"); SCHED0; } while (0)
#define SLEEP(n) do { if (n) __builtin_amdgcn_s_sleep(n); } while (0)
#define PRIO(n) __builtin_amdgcn_s_setprio(n)

template <typename F>
DI void phi_map2(const f32x2 (*x)[4], const float* rs, u32x4 (*out)[2], F mid) {
  auto top = [&](float sg, float* mx) AI {
#pragma unroll
    for (int m = 0; m < 2; ++m)
      mx[m] = fmx(fmx(fmx(sg * x[m][0][0], sg * x[m][0][1]), sg * x[m][1][0]), fmx(fmx(sg * x[m][1][1], sg * x[m][2][0]), fmx(fmx(sg * x[m][2][1], sg * x[m][3][0]), sg * x[m][3][1])));
    xred2<true>(mx[0], mx[1]);
  };
  auto sum8 = [](const f32x2* e) AI { const f32x2 t = (e[0] + e[1]) + (e[2] + e[3]); float lo = t[0]; asm("" : "+v"(lo)); return lo + t[1]; };
  float mx[2], sp[2], sn[2];
  top(1.f, mx);
  f32x2 ep[2][4], en[2][4];
#pragma unroll
  for (int m = 0; m < 2; ++m) {
    const float sl = rs[m] * LOG2E, off = mx[m] * -sl;
#pragma unroll
    for (int j = 0; j < 4; ++j) {
      const f32x2 a = __builtin_elementwise_fma(x[m][j], f32x2{sl, sl}, f32x2{off, off});
      ep[m][j] = f32x2{__builtin_amdgcn_exp2f(a[0]), __builtin_amdgcn_exp2f(a[1])};
    }
#pragma unroll
    for (int j = 0; j < 4; ++j) en[m][j] = f32x2{__builtin_amdgcn_rcpf(ep[m][j][0]), __builtin_amdgcn_rcpf(ep[m][j][1])};
    sp[m] = sum8(ep[m]); sn[m] = sum8(en[m]);
  }
  xred4<false, true>(sp[0], sn[0], sp[1], sn[1]);
  if (__builtin_expect(__builtin_amdgcn_ballot_w64(!(fmn(sn[0], sn[1]) > 1e-30f)) != 0, 0)) {
    top(-1.f, mx);
#pragma unroll
    for (int m = 0; m < 2; ++m) {
      const float sl = rs[m] * -LOG2E, off = mx[m] * sl;
#pragma unroll
      for (int j = 0; j < 4; ++j) {
        const f32x2 a = __builtin_elementwise_fma(x[m][j], f32x2{sl, sl}, f32x2{off, off});
        en[m][j] = f32x2{__builtin_amdgcn_exp2f(a[0]), __builtin_amdgcn_exp2f(a[1])};
      }
      sn[m] = sum8(en[m]);
    }
    xred2<false, true>(sn[0], sn[1]);
  }
  mid(sp);
#pragma unroll
  for (int m = 0; m < 2; ++m) {
#pragma unroll
    for (int j = 0; j < 4; j += 2) {
      f32x2 a0, a1, b0, b1;
      asm("v_pk_mul_f32 %0, %4, %5 op_sel_hi:[0,1]\n\tv_pk_mul_f32 %1, %4, %6 op_sel_hi:[0,1]\n\t"
          "v_pk_mul_f32 %2, %4, %7 op_sel:[1,0]\n\tv_pk_mul_f32 %3, %4, %8 op_sel:[1,0]"
          : "=&v"(a0), "=&v"(a1), "=&v"(b0), "=&v"(b1) : "v"(f32x2{sp[m], sn[m]}), "v"(ep[m][j]), "v"(ep[m][j + 1]), "v"(en[m][j]), "v"(en[m][j + 1]));
      out[m][0][j] = pk2(a0[0], a0[1]); out[m][0][j + 1] = pk2(a1[0], a1[1]);
      out[m][1][j] = pk2(b0[0], b0[1]); out[m][1][j + 1] = pk2(b1[0], b1[1]);
    }
  }
}

#define SLP_X 8
#define SLP_XD 4
#define SLP_O 19
#define SLP_OD 10
#define SLP_O0 8
#define SLP_O0D 2
#define SLP_K 4
#define SLP_R 4
#define SLP_HX 1
#define SLP_Q 4
#define SLP_P 50
#define SLP_E 14
#define SLP_G 12
#define SPR0 2
#define SW0 0
#define LANEID __builtin_amdgcn_mbcnt_hi(~0u, __builtin_amdgcn_mbcnt_lo(~0u, 0u))
#define STAMP(i) do {} while (0)
#define STAMP_DUMP do {} while (0)
#define COUNT(k) do {} while (0)
#define PSTAMP(l) do { const int layer = l; (void)layer; STAMP(15); } while (0)
constexpr unsigned MBY = 1u << 20;
constexpr unsigned WS_WQ = 0;
constexpr unsigned WS_WO = 12 * MBY;
constexpr unsigned WS_AR = 16 * MBY, ARENA = 12 * MBY;
constexpr unsigned AR_X = 0, AR_ST = 4 * MBY, AR_O = 5 * MBY, AR_KV = 9 * MBY;
constexpr unsigned AR_L2 = AR_ST + 256 * 1024, AR_FL = AR_ST + 320 * 1024;
constexpr unsigned KVREC = 9 * 1024;
constexpr unsigned WS_CS = WS_AR + 4 * ARENA;
constexpr unsigned WS_F1 = WS_CS + 512 * 1024;
constexpr unsigned WS_F2 = WS_F1 + 64 * 1024;
constexpr unsigned WS_EP = WS_F2 + 2048;
constexpr unsigned WS_END = WS_EP + 128;
constexpr unsigned POISON = 0xffffffffu;
constexpr unsigned OOB = 0x80000000u;

typedef __amdgpu_buffer_rsrc_t rsrc_t;
DI rsrc_t mk_rsrc(const void* p, unsigned bytes) { return __builtin_amdgcn_make_buffer_rsrc((void*)p, 0, bytes, 0x00020000); }
DI void st16(rsrc_t r, unsigned off, u32x4 v) { __builtin_amdgcn_raw_buffer_store_b128(v, r, off, 0, 16); }
DI void st16s(rsrc_t r, unsigned voff, unsigned soff, u32x4 v) { __builtin_amdgcn_raw_buffer_store_b128(v, r, voff, soff, 16); }
DI u32x4 ld16(rsrc_t r, unsigned off) { return __builtin_amdgcn_raw_buffer_load_b128(r, off, 0, 16); }
DI u32x4 ld16s(rsrc_t r, unsigned voff, unsigned soff) { return __builtin_amdgcn_raw_buffer_load_b128(r, voff, soff, 16); }
DI u32x4 clean(u32x4 v) { return __builtin_elementwise_min(v, u32x4{POISON - 1, POISON - 1, POISON - 1, POISON - 1}); }
DI unsigned umax(unsigned a, unsigned b) { return a > b ? a : b; }
DI unsigned umin(unsigned a, unsigned b) { return a < b ? a : b; }
DI unsigned umax4(u32x4 v) { return umax(umax(umax(v[0], v[1]), v[2]), v[3]); }
DI unsigned umax4(unsigned m, u32x4 v) { return umax(umax(umax(umax(m, v[0]), v[1]), v[2]), v[3]); }
DI bool poisoned(unsigned m) { return __builtin_amdgcn_ballot_w64(m == POISON) != 0; }
#define MEMFENCE asm volatile("" ::: "memory")

#define BL(i, o) "buffer_load_dwordx4 %1, %2, %" #i " offen" o " nt lds\n\t"
#define BL_OPS(...) : "=&s"(keep) : "v"(voff), "s"(r), "s"(lds), __VA_ARGS__ : "memory"
template <int N>
DI void blds_run(rsrc_t r, unsigned voff, unsigned lds, unsigned s0, unsigned s1 = 0, unsigned s2 = 0, unsigned s3 = 0) {
  unsigned keep;
  if constexpr (N == 2) asm volatile(GLDS_PRE BL(4, "") BL(5, " offset:1024") GLDS_POST BL_OPS("s"(s0), "s"(s1)));
  else asm volatile(GLDS_PRE BL(4, "") BL(5, " offset:1024") BL(6, " offset:2048") BL(7, " offset:3072") GLDS_POST BL_OPS("s"(s0), "s"(s1), "s"(s2), "s"(s3)));
}

#define CODEB 36648
constexpr int MEGA_CODE_BYTES = CODEB;
DI unsigned code_prefetch(int i) {
  unsigned long long pc; int rel; unsigned d;
  asm volatile("s_getpc_b64 %0\n\ts_mov_b32 %1, _Z11mega_kernel10MegaParams@rel32@lo+4" : "=s"(pc), "=s"(rel));
  const unsigned long long a = pc + (long long)rel + (i * 192 + 192 <= MEGA_CODE_BYTES ? i * 192 : 0);
  asm volatile("s_load_dword %0, %1, 0x0\n\ts_load_dword %0, %1, 0x40\n\ts_load_dword %0, %1, 0x80" : "=&s"(d) : "s"(a) : "memory");
  return d;
}

struct MegaParams {
  char* ws;
  const u16* x;
  const u16* wqkvA;
  const u16* woA;
  const u16* wqkvB;
  const u16* woB;
  const u16* wphi;
  u16* out;
};

constexpr int QSLOT = 6 * 1024, QGRP = 4 * QSLOT;
constexpr int L_STG = QGRP;
constexpr int L_A2 = 16 * 1024, L_Z = 21 * 1024;
constexpr int L_RT = 4 * QGRP;
constexpr int L_PB = 0;
constexpr int L_RED = L_STG + 128 * 1024;
constexpr int L_STAMP = L_RED + 3072;
constexpr int MEGA_LDS = L_STAMP + 1536;
static_assert(MEGA_LDS <= 160 * 1024, "LDS map");

template <int I, int N, typename F> DI void static_for_(F&& f) {
  if constexpr (I < N) { f(IC<I>{}); static_for_<I + 1, N>(f); }
}
template <int N, typename F> DI void static_for(F&& f) { static_for_<0, N>(f); }

constexpr int NCHK = 3;
constexpr int RO = 8;
constexpr int SPIN_BUDGET = 1 << 15;

__global__ __launch_bounds__(768, 3) void mega_kernel(MegaParams p) {
  extern __shared__ __attribute__((aligned(16))) char smem[];
  const int tid = threadIdx.x;
  const int wave = __builtin_amdgcn_readfirstlane(tid >> 6);
  const int xcd = blockIdx.x & 7, idx = blockIdx.x >> 3;
  int bm = (xcd >> 2) * 4 + (idx >> 3), head = ((xcd & 1) * 2 + ((xcd >> 1) & 1)) * 8 + (idx & 7);
  int cm = (xcd >> 1) & 1, cn = (xcd & 1) * 8 + (idx & 7);
  const int bm0 = bm, head0 = head, cm0 = cm, cn0 = cn;
  char* ws = p.ws;
  const u16 *px = p.x, *pq = p.wqkvA;
  asm volatile("" : "+s"(ws), "+s"(px), "+s"(pq));
  const int swv = wave == SW0 ? 0 : wave == 4 ? 1 : wave == 11 ? 2 : -1; (void)swv;
  float* lmean = (float*)(smem + L_RED);
  float* lrstd = lmean + 272;
  float* lcs = lrstd + 272;
  unsigned* lflag = (unsigned*)lmean;
  const rsrc_t wsr = mk_rsrc(ws, WS_END);
  int budget = SPIN_BUDGET;
  unsigned tag0 = 0, tag1 = 0;
  auto ld_ep = [&]() AI { MEMFENCE; return __builtin_amdgcn_raw_buffer_load_b64(wsr, 0, WS_EP, 16); };
  auto mk_tag = [&](u32x2 ep) AI {
    tag0 = __builtin_amdgcn_readfirstlane(ep[0]) + 0x9e3779b9u;
    tag1 = __builtin_amdgcn_readfirstlane(ep[1]) ^ 0x85ebca6bu;
  };
  const unsigned sbase = (unsigned)(size_t)smem;
  int L = wave, cw = wave - 4;
#define OPAQUE_S asm volatile("" : "+s"(L), "+s"(bm), "+s"(head), "+s"(cm), "+s"(cn));
#define LANE_VARS asm volatile("" : "+s"(cw), "+s"(bm), "+s"(head), "+s"(cm), "+s"(cn)); int lane_ = __builtin_amdgcn_mbcnt_hi(~0u, __builtin_amdgcn_mbcnt_lo(~0u, 0u)); asm volatile("" : "+v"(lane_)); \
  const int lane = lane_, fr = lane & 15, fq = lane >> 4; const unsigned v16 = lane * 16; (void)fr; (void)fq; (void)v16;
  auto put_x = [&](u32x4* xr, unsigned v16, unsigned dst) AI {
    float sm = 0.f, sq = 0.f;
#pragma unroll
    for (int j = 0; j < 2; ++j) {
      xr[j][0] = umin(xr[j][0], POISON - 1); xr[j][2] = umin(xr[j][2], POISON - 1);
      st16s(wsr, v16, dst + j * 16384, xr[j]);
    }
    SCHED0;
#pragma unroll
    for (int j = 0; j < 2; ++j)
#pragma unroll
      for (int e = 0; e < 4; ++e) { sm = dot2(xr[j][e], 0x3f803f80u, sm); sq = dot2(xr[j][e], xr[j][e], sq); }
    xred2<false>(sm, sq);
    return u32x2{__float_as_uint(sm), __float_as_uint(sq)};
  };

#define RT_VARS const bool tq = L < 3; const int tsg = tq ? (bm * 2 + (head >> 4)) * 3 + L : bm * 2 + (head >> 4), ks = (head & 15) + 16 * h;
  auto ld_rt = [&](const u16* w, int h, unsigned lds) AI {
    RT_VARS
    LANE_VARS
    const unsigned rs = tq ? 6144 : 2048, D = 8 * rs - 1024, o = ks * 32 * rs + tsg * 128;
    blds_run<4>(mk_rsrc(w, rs * 1024), (lane >> 3) * rs + ((lane & 7) ^ ((lane >> 3) & 6)) * 16, sbase + lds, o, o + D, o + 2 * D, o + 3 * D);
  };
  auto retile = [&](int side, int h, unsigned lds, f32x4* rcs, int wp = -1) AI {
    RT_VARS
    LANE_VARS
    const unsigned dst = tq ? WS_WQ + side * (6 * MBY) + (unsigned)((((tsg & 15) * 2) * 32 + ks) * 6 + (tsg >> 4) * 2) * 1024
                            : WS_WO + side * (2 * MBY) + (unsigned)((tsg * 32 + ks) * 4) * 1024;
    const unsigned gstr = tq ? 32 * 6 * 1024 : 2048;
    const unsigned a0 = (lane >> 2) * 128 + ((lane >> 3) & 3) * 32 + (lane & 3) * 8;
    u32x4 fv[4];
    if (wp >= 0 && tsg < 32) {
      const f32x4 zero = f32x4{0.f, 0.f, 0.f, 0.f};
#pragma unroll
      for (int g = 0; g < 2; ++g) {
        const char* ra = smem + lds + fr * 128 + (((g * 4 + fq) ^ (fr & 6)) * 16);
        const bf16x8 w0 = *(const bf16x8*)ra, w1 = *(const bf16x8*)(ra + 2048);
#pragma unroll
        for (int u = 0; u < 2; ++u) {
          const char* pa = smem + wp + g * 2048 + (fq * 8 + (fr >> 2)) * 64 + u * 32 + (fr & 3) * 8;
          const u32x2 t0 = trread(pa), t1 = trread(pa + 256);
          const bf16x8 wf = as_frag(u32x4{t0[0], t0[1], t1[0], t1[1]});
          const f32x4 d0 = mfma16(w0, wf, zero), d1 = mfma16(w1, wf, zero);
          fv[g * 2 + u] = u32x4{pk2(d0[0], d0[1]), pk2(d0[2], d0[3]), pk2(d1[0], d1[1]), pk2(d1[2], d1[3])};
        }
      }
    } else {
#pragma unroll
      for (int pc = 0; pc < 4; ++pc) {
        const char* aa = smem + lds + (a0 ^ (pc * 32));
        const u32x2 r0_ = trread(aa), r1_ = trread(aa + 2048);
        fv[pc] = u32x4{r0_[0], r0_[1], r1_[0], r1_[1]};
      }
    }
#pragma unroll
    for (int pc = 0; pc < 4; ++pc) {
      const u32x4 v = fv[pc];
      st16s(wsr, v16 + (pc & 1) * 1024, dst + (pc >> 1) * gstr, v);
      if (rcs) rcs[pc] = mfma16(as_frag(u32x4{0x3f803f80u, 0x3f803f80u, 0x3f803f80u, 0x3f803f80u}), as_frag(v), rcs[pc]);
    }
    if (rcs && h == 1) {
      const f32x4 c0 = rcs[0], c1 = rcs[1], c2 = rcs[2], c3 = rcs[3];
      const float ca = fq & 1 ? c1[0] : c0[0], cb = fq & 1 ? c3[0] : c2[0], cc = fq & 2 ? cb : ca;
      if (tq) __builtin_amdgcn_raw_buffer_store_b32(__float_as_uint(cc), wsr, ((fq >> 1) * 1536 + (fq & 1) * 16 + fr) * 4,
                                                    WS_CS + ((side * 32 + (tsg & 15) * 2) * 1536 + (tsg >> 4) * 512 + (head & 15) * 32) * 4, 16);
    }
  };
  auto flag = [&](unsigned table) AI {
    if (LANEID == 0) __builtin_amdgcn_raw_buffer_store_b64(u32x2{tag0, tag1}, wsr, 0, table + (bm * 32 + head) * 8, 16);
  };
  auto poll = [&](unsigned table, auto between, int rows = 0) AI {
    LANE_VARS
    const int q = (umin(fq, 2) * 16 + (head >> 1)) * 43 >> 7;
    const unsigned vo = rows ? bm * 256 + (fr & rows) * 16 : fq == 3 ? bm * 256 + fr * 16 : q * 128 + (fr & 7) * 16;
    auto miss = [&](u32x4 f) AI { return __builtin_amdgcn_ballot_w64(((f[0] ^ tag0) | (f[1] ^ tag1) | (f[2] ^ tag0) | (f[3] ^ tag1)) != 0) != 0; };
    MEMFENCE;
    const u32x4 f0 = ld16s(wsr, vo, table);
    MEMFENCE;
    between();
    MEMFENCE;
    bool wait = miss(f0);
#pragma unroll 1
    while (wait && --budget > 0) {
      SLEEP(SLP_Q);
      MEMFENCE;
      wait = miss(ld16s(wsr, vo, table));
      asm volatile("" : "+s"(budget) :: "memory");
    }
    MEMFENCE;
  };
  auto ld_x = [&](int cx, unsigned lds) AI {
    LANE_VARS
    const unsigned o = (bm * 16 + (head >> 1)) * 32768 + cx * 128;
    blds_run<2>(mk_rsrc(px, 4 * MBY), (lane >> 3) * 2048 + ((lane & 7) ^ (lane >> 3)) * 16, sbase + lds, o, o + 15360);
  };
  auto st_x = [&](int cx, unsigned lds, int w) AI {
    LANE_VARS
    const int mt = bm * 16 + (head >> 1);
    u32x4 xr[2];
#pragma unroll
    for (int j = 0; j < 2; ++j) {
      const char* a = smem + lds + fr * 128 + (fq & 1) * 8;
      const u32x2 lo = *(const u32x2*)(a + ((j * 4 + (fq >> 1)) ^ (fr & 7)) * 16), hi = *(const u32x2*)(a + ((j * 4 + 2 + (fq >> 1)) ^ (fr & 7)) * 16);
      xr[j] = u32x4{lo[0], lo[1], hi[0], hi[1]};
    }
    const u32x2 stat = put_x(xr, v16, WS_AR + AR_X + xt_off(mt, cx * 2));
    if (fq == 0) *(u32x2*)(lmean + 16 + fr * 4 + w) = stat;
  };

  {
    const int lane = LANEID;
    const bool tq = wave < 4;
    const unsigned rs = tq ? 6144 : 2048, D = 8 * rs - 1024;
    const unsigned o = (tq ? (head & 15) * 32 : (bm * 16 + (head >> 1)) * 16) * rs + (tq ? (bm * 2 + (head >> 4)) * 3 + wave : (head & 1) * 4 + cw) * 128;
    const unsigned lds = sbase + L_PB + (tq ? 8192 + wave * 4096 : cw * 2048);
    const unsigned vo = (lane >> 3) * rs + ((lane & 7) ^ ((lane >> 3) & (tq ? 6 : 7))) * 16;
    const rsrc_t r = mk_rsrc(tq ? pq : px, tq ? 6 * MBY : 4 * MBY);
    if ((wave & 7) != 3 && wave < 8) blds_run<2>(r, vo, lds, o, o + D);
    if (wave < 3) {
      blds_run<2>(r, vo, lds + 2048, o + 2 * D + 2048, o + 3 * D + 2048);
      const int tsg = (bm * 2 + (head >> 4)) * 3 + wave;
      if (tsg < 32) glds_run<4>(lane * 16, (const char*)p.wphi + (tsg & 15) * 4096, sbase + L_PB + 45056 + wave * 4096);
    }
  }
  if (wave >= 8) {
    const unsigned cp = code_prefetch(idx * 9 + cw), cq = code_prefetch(idx * 9 + cw - 4);
    L = wave - 8;
    bar();
    ld_x(8 + (head & 1) * 4 + L, L_PB + 32768 + L * 2048);
    if (L < 3) ld_rt(pq, 1, L_PB + 20480 + L * 4096);
    sync_lds();
    asm volatile("; pi" :: "s"(cp), "s"(cq) : "memory");
    wait_vm<0>();
    PSTAMP(1);
    st_x(8 + (head & 1) * 4 + L, L_PB + 32768 + L * 2048, L * 64 + 2);
    if (L < 3) {
      const f32x4 t = *(const f32x4*)(smem + L_PB + 40960 + L * 1024 + LANEID * 16);
      f32x4 rca[4] = {{t[0], 0.f, 0.f, 0.f}, {t[1], 0.f, 0.f, 0.f}, {t[2], 0.f, 0.f, 0.f}, {t[3], 0.f, 0.f, 0.f}};
      retile(0, 1, L_PB + 20480 + L * 4096, rca, L_PB + 45056 + L * 4096);
    }
    asm volatile("s_waitcnt lgkmcnt(0)" ::: "memory");
    PSTAMP(2);
  } else if (wave >= 4) {
    wait_vm<0>();
    PSTAMP(1);
    st_x((head & 1) * 4 + cw, L_PB + cw * 2048, cw * 64);
    bar();
    wait_vm_lgkm0<0>();
    PSTAMP(2);
    bar();
    asm volatile("; pc" ::: "memory");
    SLEEP(SLP_E);
    L = 3;
    if (cw < 2) ld_rt(p.woA, cw, L_RT + 3 * 14336 + cw * 4096);
    if (cw == 2) ld_rt(p.woB, 0, L_RT + 3 * 14336 + 8192);
  } else if (wave < 3) {
    const u32x2 ep = ld_ep();
    wait_vm<1>();
    PSTAMP(1);
    f32x4 rca[4] = {};
    retile(0, 0, L_PB + 8192 + L * 4096, rca, L_PB + 45056 + L * 4096);
    *(f32x4*)(smem + L_PB + 40960 + L * 1024 + LANEID * 16) = f32x4{rca[0][0], rca[1][0], rca[2][0], rca[3][0]};
    bar();
    drain();
    mk_tag(ep);
    PSTAMP(2);
    sync_lds();
    asm volatile("; pl" ::: "memory");
    SLEEP(SLP_E);
    ld_rt(p.wqkvB, 0, L_RT + L * 14336);
    ld_rt(p.wqkvB, 1, L_RT + L * 14336 + 4096);
  } else {
    const u32x2 ep = ld_ep();
    const unsigned cp = code_prefetch(idx * 9 + 8);
    if (LANEID < 9) lflag[LANEID] = 0;
    wait_vm_lgkm0<0>();
    asm volatile("" :: "s"(cp));
    mk_tag(ep);
    bar();
    bar();
    flag(WS_F1);
    if (LANEID == 0) *(u32x2*)(lflag + 10) = u32x2{tag0, tag1};
    PSTAMP(2);
    SLEEP(SLP_P);
    poll(WS_F1, []() {});
    asm volatile("; pp" ::: "memory");
  }
  bar();
  PSTAMP(3);
  int role = wave;
  asm volatile("" : "+s"(role));

  if (__builtin_expect(role < 4, 1)) {
    const unsigned v16 = __builtin_amdgcn_mbcnt_hi(~0u, __builtin_amdgcn_mbcnt_lo(~0u, 0u)) * 16;
#define OPAQUE_LANE int lane = v16 >> 4; asm volatile("" : "+v"(lane));
    auto q_groupW = [&](int layer, int g, int pos) AI {
      const char* gW = ws + WS_WQ + (layer & 1) * (6 * MBY) + head * (192 * 1024) + (g * 4 + L) * 6144;
      const unsigned d = sbase + (pos * 4 + L) * QSLOT;
      glds_run<3, true>(v16, gW, d);
      glds_run<3, true>(v16, gW + 3072, d + 3072);
    };
    u32x4 wn[4][6];
    auto ld_wn = [&](int layer, int g0, int g1) AI {
      const unsigned so = WS_WQ + (layer & 1) * (6 * MBY) + head * (192 * 1024) + L * 6144;
#pragma unroll
      for (int g = g0; g < g1; ++g)
#pragma unroll
        for (int n = 0; n < 6; ++n) wn[g][n] = ld16s(wsr, v16 + (n % 3) * 1024, so + g * 24576 + (n / 3) * 3072);
      MEMFENCE;
    };
    auto wr_wn = [&](int g) AI {
#pragma unroll
      for (int n = 0; n < 6; ++n) *(u32x4*)(smem + (g * 4 + L) * QSLOT + n * 1024 + v16) = wn[g][n];
    };
    const u32x4 pz = u32x4{POISON, POISON, POISON, POISON};
    auto poison = [&](unsigned so) AI { st16s(wsr, v16, so, pz); };
    auto poison_o = [&]() AI {
      const unsigned o = WS_AR + AR_O + xt_off(bm * 16 + L * 4, head);
#pragma unroll
      for (int i = 0; i < 4; ++i) poison(o + i * 1024);
      STPAD;
    };
    auto poison_x = [&](int l) AI {
      if (L < NCHK) {
        const unsigned o = WS_AR + l * ARENA + AR_X + bm * (512 * 1024) + head * (NCHK * 2048) + L * 2048;
        poison(o); poison(o + 1024);
        STPAD;
      }
    };
    auto poison_s = [&](int l) AI {
      if (L == 3) {
        const unsigned o = WS_AR + l * ARENA + AR_KV + (unsigned)(bm * 32 + head) * KVREC;
        poison(o); poison(o + 1024);
        STPAD;
      }
    };
    auto poison_kv = [&](int l) AI {
      const unsigned kvo = WS_AR + l * ARENA + AR_KV + (unsigned)(bm * 32 + head) * KVREC;
      poison(kvo + L * 1024);
      if (L == (l ? 0 : 1)) st16s(wsr, v16 < 256 ? v16 : OOB, kvo + 4096, pz);
      STPAD;
    };
    q_groupW(0, 0, 0);
    q_groupW(0, 1, 1);
    auto put_l2 = [&](float sm, float sq, unsigned vo, unsigned so) AI {
      xred2<false>(sm, sq);
      if (v16 < 256) st16s(wsr, vo, so, u32x4{__float_as_uint(sm), tag0, __float_as_uint(sq), tag1});
      MEMFENCE;
    };
    auto put_st0 = [&]() AI {
      if (L == 0) {
        const f32x4 t = *(const f32x4*)((const char*)(lmean + 16) + v16);
        put_l2(t[0] + t[2], t[1] + t[3], v16, WS_AR + AR_L2 + (head & 1) * 32768 + (bm * 16 + (head >> 1)) * 256);
      }
    };
    wait_vm<6>();
    auto body = [&](auto kind, const int layer) AI {
      constexpr bool isB = decltype(kind)::value == 1, is0 = decltype(kind)::value == 0;
      const unsigned ar = WS_AR + layer * ARENA;
      f32x4 rcs[4] = {};
      auto sW = [&]() AI { return WS_WO + (layer & 1) * (2 * MBY) + cn * (128 * 1024); };
      auto stg = [&](unsigned off, auto n) AI { glds_run<decltype(n)::value, true>(v16, ws + sW() + off, sbase + L_STG + off); };
      auto stg7 = [&](unsigned o) AI { stg(o, IC<4>{}); stg(o + 4096, IC<3>{}); };
      auto stg6 = [&](unsigned o) AI { stg(o, IC<3>{}); stg(o + 3072, IC<3>{}); };

      auto extra = [&](int b) AI {
        if (!is0) return;
        const unsigned lu = L_RT + L * 14336;
        if (b < 7) { SCHED0; PRIO(3); SCHED0; }
        if (b == 1) retile(L < 3, 0, lu, rcs);
        if (b == 2 && L == 3) retile(0, 1, lu + 4096, nullptr);
        if (b == 6 && L == 3) ld_rt(p.woB, 1, lu + 4096);
        if (b == 8) {
          if (L < 3) retile(1, 1, lu + 4096, rcs);
          if (L == 3) retile(1, 0, lu + 8192, nullptr);
          if (L == 3) retile(1, 1, lu + 4096, nullptr);
        }
        if (b < 7) { SCHED0; PRIO(0); SCHED0; }
      };
      auto extra2 = [&](int b) AI {
        if (!is0) return;
        if (b < 7) { SCHED0; PRIO(3); SCHED0; }
        if (b == 2) put_st0();
        if (b == 1) poison_o();
        if (b == 2) poison_kv(0);
        if (b == 5) poison_kv(2);
        if (b == 8) poison_s(1);
        if (b == 9) poison_x(1);
        if (b == 10) { poison_x(2); poison_s(3); }
        if (b < 7) { SCHED0; PRIO(0); SCHED0; }
      };
      if (is0) sync_lds();
      STAMP(0);
      {
        OPAQUE_S
        OPAQUE_LANE
        u32x4 c0, c1;
        asm volatile("" : "=v"(c0), "=v"(c1));
        auto ldcs = [&]() AI {
          if (L > 0) {
            const unsigned so = WS_CS + (((layer & 1) * 32 + head) * 1536 + (L - 1) * 512) * 4;
            c0 = ld16s(wsr, v16, so); c1 = ld16s(wsr, v16 + 1024, so);
          }
          MEMFENCE;
        };
        auto wrcs = [&]() AI {
          if (L > 0) {
            f32x4 t = add4(__builtin_bit_cast(f32x4, c0), __builtin_bit_cast(f32x4, c1));
#pragma unroll
            for (int e = 0; e < 4; ++e)
              t[e] += __uint_as_float(__builtin_amdgcn_update_dpp(0u, __float_as_uint(t[e]), 0x128, 0xf, 0xf, false));
            float t0 = t[0], t1 = t[1], t2 = t[2], t3 = t[3];
            xred4<false>(t0, t1, t2, t3);
            *(f32x4*)(lcs + (isB ? 96 : 0) + (L - 1) * 32 + (lane & 7) * 4) = f32x4{t0, t1, t2, t3};
          }
        };
        if (isB && layer == 1) ldcs();
        if constexpr (is0) {
          if (L == 3) flag(WS_F1 + 2048);
          q_groupW(0, 2, 2); q_groupW(0, 3, 3);
          wait_vm<12>();
        }
        STAMP(1);
        if (!is0) PRIO(3);
        sync_lds();
        STAMP(2);
        OPAQUE_S
        u32x4 s0 = u32x4{0u, 0u, 0u, 0u}, s1 = s0;
        auto ldst = [&]() AI {
          const unsigned so = ar + AR_L2 + (bm * 16 + L * 4) * 256;
          s0 = ld16s(wsr, v16, so);
          if (is0) s1 = ld16s(wsr, v16, so + 32768);
          if (isB && L == 0) s1 = ld16s(wsr, v16 & 255, bm > 0 ? so - 256 : so);
          MEMFENCE;
        };
        auto fin = [&](float sm, float sq, int r, bool own) AI {
          float mean = sm * (1.0f / 1024.0f);
          float var = fmaxf(__builtin_fmaf(sq, 1.0f / 1024.0f, -mul1(mean, mean)), 0.f);
          if (own) lmean[r] = -mean;
          lrstd[r] = __builtin_amdgcn_rsqf(var + 1e-5f);
        };
        auto wrst = [&]() AI {
          while (true) {
            unsigned d = (s0[1] ^ tag0) | (s0[3] ^ tag1);
            if (is0 || (isB && L == 0)) d |= (s1[1] ^ tag0) | (s1[3] ^ tag1);
            if (__builtin_expect(__builtin_amdgcn_ballot_w64(d != 0) == 0, 1) || --budget <= 0) break;
            COUNT(1);
            ldst();
          }
          MEMFENCE;
          float sm = __uint_as_float(s0[0]), sq = __uint_as_float(s0[2]);
          if (is0) { sm += __uint_as_float(s1[0]); sq += __uint_as_float(s1[2]); }
          fin(sm, sq, 16 + L * 64 + lane, true);
          if (isB && L == 0) fin(__uint_as_float(s1[0]), __uint_as_float(s1[2]), lane & 15, false);
        };
        auto q_group = [&](int g) AI {
          if (is0) {
            if (L & 1) SLEEP(SPR0);
            if (L & 2) SLEEP(2 * SPR0);
            q_groupW(layer, g, g & 3);
          } else if (g >= 5) q_groupW(layer, g, g < 6 ? g : g - 4);
        };
        if (!is0 && L == 0) {
          poll(ar + AR_FL, []() {}, 15);
          if (head < 16) {
            const unsigned so = ar + AR_ST + (bm * 16 + head) * 2048;
            s0 = ld16s(wsr, v16, so); s1 = ld16s(wsr, v16 + 1024, so);
            MEMFENCE;
          }
        }
        if (is0) {
          drain();
          poll(WS_F1 + 2048, [&]() AI { extra(1); });
          ldcs();
          extra2(1);
        }
        if (isB && layer == 1) wrcs();
        MEMFENCE;
        STAMP(3);
        if (!is0) PRIO(0);
        sync_lds();
        STAMP(4);
        OPAQUE_S
        if (!is0) drain();
        if (!is0 && L == 0 && head < 16) {
          f32x4 t = add4(__builtin_bit_cast(f32x4, s0), __builtin_bit_cast(f32x4, s1));
#pragma unroll
          for (int e = 0; e < 4; ++e)
            t[e] += __uint_as_float(__builtin_amdgcn_update_dpp(0u, __float_as_uint(t[e]), 0x128, 0xf, 0xf, false));
          const bool hi = lane & 8;
          put_l2(hi ? t[2] : t[0], hi ? t[3] : t[1], (lane & 7) * 32 + (lane & 8) * 2, ar + AR_L2 + (bm * 16 + head) * 256);
        }
        q_group(4);
        extra(2); extra2(2);
        STAMP(5);
        sync_lds();
        STAMP(6);
        OPAQUE_S
        drain();
        q_group(5);
        if (is0) wrcs();
        STAMP(7);
        bar();
        OPAQUE_S
        drain();
        if (is0 && L == 0 && v16 == 0) __builtin_amdgcn_raw_buffer_store_b64(u32x2{tag0, tag1}, wsr, 0, WS_F2 + (bm * 32 + head) * 8, 16);
        q_group(6);
        ldst();
        STAMP(8);
        bar();
        STAMP(9);
        OPAQUE_S
        drain();
        q_group(7);
        extra2(5);
        SCHED0;
        wrst();
        STAMP(10);
        bar();
        STAMP(11);
        OPAQUE_S
        drain();
        extra(6);
        if (isB) stg6(96 * 1024 + L * 6144);
        SCHED0;
      }
      STAMP(12);
      sync_lds();
      STAMP(13);
      OPAQUE_S
      u32x4 fl[2];
      fl[0] = fl[1] = u32x4{tag0, tag1, tag0, tag1};
      auto ldfl = [&]() AI { fl[0] = ld16s(wsr, v16, WS_F2); fl[1] = ld16s(wsr, v16 + 1024, WS_F2); };
      if (is0) ldfl();
      bar();
      constexpr int NR = isB ? 6 : 9;
      const unsigned ro = isB ? 48 * 1024 + L * 6144 : L * 9216;
      u32x4 r[NR];
      bool pend = false;
      auto flags = [&]() AI {
        const u32x4 d = (fl[0] ^ u32x4{tag0, tag1, tag0, tag1}) | (fl[1] ^ u32x4{tag0, tag1, tag0, tag1});
        pend = __builtin_amdgcn_ballot_w64((d[0] | d[1] | d[2] | d[3]) != 0) != 0;
      };
      if (isB) {
        stg6(L * 6144); stg(120 * 1024 + L * 2048, IC<2>{});
        stg6(24 * 1024 + L * 6144);
        stg6(72 * 1024 + L * 6144);
      } else {
        drain();
        if (is0) flags();
        MEMFENCE;
        extra(8); extra2(8);
        if (!is0) poison_x(3);
        while (is0 && __builtin_expect(pend, 0) && --budget > 0) {
          ldfl();
          flags();
          MEMFENCE;
        }
        stg(36 * 1024 + L * 3072, IC<3>{}); stg7(72 * 1024 + L * 14336); stg7(72 * 1024 + L * 14336 + 7168);
      }
#pragma unroll
      for (int i = 0; i < NR; ++i) r[i] = ld16s(wsr, v16 + (i & 3) * 1024, sW() + ro + (i >> 2) * 4096);
      MEMFENCE;
      if (is0 && bm + head + L == 0 && v16 == 0) __builtin_amdgcn_raw_buffer_store_b32(tag0, wsr, 0, WS_EP, 16);
      auto wr_r = [&]() AI {
        unsigned vv = v16;
        asm volatile("" : "+v"(vv));
#pragma unroll
        for (int i = 0; i < NR; ++i) *(u32x4*)(smem + L_STG + ro + i * 1024 + vv) = r[i];
      };
      auto left = [&]() AI {
        OPAQUE_LANE
        for (;;) {
          MEMFENCE;
          if (__builtin_amdgcn_ballot_w64(lflag[lane & 7] != (unsigned)layer) == 0 || --budget <= 0) break;
          SLEEP(2);
        }
        MEMFENCE;
      };
      if (isB) {
#pragma unroll
        for (int i = 0; i < 24; ++i) asm volatile("" : "=v"(wn[i / 6][i % 6]));
        if (layer == 1) ld_wn(layer + 1, 0, 2);
        STAMP(14);
        left();
        wr_r();
        MEMFENCE;
        if (layer == 1) ld_wn(layer + 1, 2, 4);
        wait_vm_lgkm0<12>();
        if (layer == 3) wait_vm<0>();
      } else {
        constexpr int W0 = is0 ? 0 : 2, W1 = is0 ? 1 : 2;
        ld_wn(layer + 1, 0, W0);
        if (is0) wait_vm<26>();
        STAMP(14);
        bar();
        stg6(48 * 1024 + L * 6144);
        ld_wn(layer + 1, W0, W1);
        bar();
        extra2(9);
        ld_wn(layer + 1, W1, 4);
        wr_r();
        wait_vm<6 * (4 - W1)>();
        bar();
        extra2(10);
        wait_vm_lgkm0<0>();
      }
      bar();
      if (isB && layer == 3) { STAMP_DUMP; __builtin_amdgcn_endpgm(); }
      OPAQUE_S
      wr_wn(0);
      drain();
      sync_lds();
      OPAQUE_S
      SCHED0;
#pragma unroll
      for (int g = 1; g < 4; ++g) wr_wn(g);
      MEMFENCE;
      SLEEP(SLP_G);
      q_groupW(layer + 1, 4, 4);
    };
    body(IC<0>{}, 0);
#pragma unroll 1
    for (int layer = 1;; layer += 2) {
      body(IC<1>{}, layer);
      body(IC<2>{}, 2);
    }
  }

  const f32x4 zero = f32x4{0.f, 0.f, 0.f, 0.f};
  u32x4 xr[2];
  asm volatile("" : "=v"(xr[0]), "=v"(xr[1]));
  u32x4 sel;
  u32x4 eye;
  unsigned vk;
  unsigned ra0, ra1;
  float off0, off1;
  {
    const int lane = LANEID, fr = lane & 15, fq = lane >> 4;
    const bool odd = fr & 1, own0 = fr >= fq * 4 + (fr & 1), own1 = fr >= fq * 4 + (fr & 1) + 2, nopre = bm0 == 0 && wave == 4;
    const unsigned e0 = odd ? 0x01000c0cu : 0x0c0c0100u, e1 = odd ? 0x03020c0cu : 0x0c0c0302u, ez = 0x0c0c0c0cu;
    sel = u32x4{own0 ? ez : e0, own1 ? ez : e1, own0 ? e0 : ez, own1 ? e1 : ez};
    const unsigned rb = sbase + L_RED + 1088 + ((wave - 4) * 32 + fq * 4 + (fr & 1)) * 4;
    ra0 = rb + (own0 ? 64 : 0); ra1 = rb + (own1 ? 72 : 8);
    off0 = nopre && !own0 ? -3.0e38f : 0.f; off1 = nopre && !own1 ? -3.0e38f : 0.f;
    const unsigned one = (fq & 1) == (fr >> 3) ? (fr & 1 ? 0x3f800000u : 0x00003f80u) : 0u;
    eye = u32x4{(fr & 6) == 0 ? one : 0u, (fr & 6) == 2 ? one : 0u, (fr & 6) == 4 ? one : 0u, (fr & 6) == 6 ? one : 0u};
    vk = (fq >> 1) * (32 * KVREC) + (fq & 1) * 256 + fr * 16;
    asm volatile("" : "+v"(sel), "+v"(ra0), "+v"(ra1), "+v"(off0), "+v"(off1), "+v"(eye), "+v"(vk));
  }
#pragma unroll 1
  for (int layer = 0; layer < 4; ++layer) {
    bm = bm0; head = head0; cm = cm0; cn = cn0; cw = wave - 4;
    const bool isB = layer & 1;
    const unsigned ar = WS_AR + layer * ARENA;
    {
      f32x4 acc[2][6];
      f32x4 ck[2], mv[2], xv[2];
      float mean[2], rs[2], cv[2];
      const float* lcl = lcs + (isB ? 96 : 0);
      {
        LANE_VARS
        const unsigned va = v16 + cw * 2048;
        const unsigned sa0 = ar + AR_X + bm * (512 * 1024);
        auto lda = [&](unsigned so, u32x4* a) AI {
          a[0] = ld16s(wsr, va, so);
          a[1] = ld16s(wsr, va + 1024, so);
        };
        bf16x8 f[2][6];
        u32x4 a[4][2];
        auto unset = [&](const u32x4* e) AI { return poisoned(umax(umax(umax(e[0][0], e[0][2]), e[1][0]), e[1][2])); };
        auto refill = [&](auto ic, unsigned cur) AI {
          constexpr int I = decltype(ic)::value;
          do {
            COUNT(0);
            SLEEP(SLP_R);
            MEMFENCE;
            lda(cur, a[I]);
            bool u[4];
#pragma unroll
            for (int j = 1; j < 4; ++j) u[j] = unset(a[(I + j) % 4]);
            SCHED0;
#pragma unroll
            for (int j = 1; j < 4; ++j)
              if (u[j]) lda(cur + j * 16384, a[(I + j) % 4]);
            __builtin_amdgcn_s_waitcnt(0x0f70);
            MEMFENCE;
          } while (unset(a[I]) && --budget > 0);
        };
        STAMP(0);
        if (layer > 0) {
          bar();
          if (cn < 8) SLEEP(SLP_X); else SLEEP(SLP_XD);
        }
#pragma unroll
        for (int j = 0; j < 4; ++j) { lda(sa0 + j * 16384, a[j]); SCHED0; }
#pragma unroll
        for (int m = 0; m < 2; ++m)
#pragma unroll
          for (int n = 0; n < 6; ++n) acc[m][n] = zero;
        asm volatile("" : "+v"(acc[0][0]), "+v"(acc[0][1]), "+v"(acc[0][2]), "+v"(acc[0][3]), "+v"(acc[0][4]), "+v"(acc[0][5]));
        asm volatile("" : "+v"(acc[1][0]), "+v"(acc[1][1]), "+v"(acc[1][2]), "+v"(acc[1][3]), "+v"(acc[1][4]), "+v"(acc[1][5]));
        if (layer == 0) { wait_vm<8>(); sync_lds(); }
#pragma unroll
        for (int nn = 0; nn < 6; ++nn) { const int n = nn < 4 ? nn + 2 : nn - 4; f[0][n] = *(const bf16x8*)(smem + v16 + n * 1024); }
        SCHED0;
        if (layer > 0) {
          const u32x2 tg = *(const u32x2*)(lflag + 10);
          wait_vm<8>();
          unsigned old;
          asm volatile("s_mov_b64 exec, 1\n\tds_add_rtn_u32 %0, %1, %2\n\ts_mov_b64 exec, -1\n\ts_waitcnt lgkmcnt(0)" : "=v"(old) : "v"(sbase + L_RED + 32), "v"(1) : "memory");
          if (__builtin_amdgcn_readfirstlane(old) == 8u * layer - 1)
            __builtin_amdgcn_raw_buffer_store_b64(tg, wsr, lane ? OOB : 0, ar + AR_FL + bm * 256 + (cm * 16 + cn) * 8, 16);
          MEMFENCE;
        }
        SCHED0;
        bool bad = layer > 0 && unset(a[0]);
        const int wrap = layer > 0 ? 6 : 4;
        unsigned ps[4] = {sbase + L_RED + 64 + cw * 128, sbase + L_RED + 2176 + (layer & 1) * 384, 0, WS_AR + AR_X + xt_off(bm * 16 + cw * 2 + cm, cn * 2)};
        ps[2] = ps[0] + 1088 + (layer & 1) * (2176 + 384 - 1088 - 64 - cw * 128);
        int early = (layer & 1) * cw;
        asm volatile("" : "+s"(ps[0]), "+s"(ps[1]), "+s"(ps[2]), "+s"(ps[3]), "+s"(early));
        STAMP(1);
        auto group = [&](auto modec, int g) AI {
          constexpr int MODE = decltype(modec)::value;
          constexpr bool LAST = MODE == 3, CHK = MODE == 0;
          const unsigned gs = (g < wrap ? g : g - 4) * QGRP, gn = (g + 1 < wrap ? g + 1 : g - 3) * QGRP;
          const unsigned sc = sa0 + g * 65536, so = sc + 4 * 16384;
          unsigned gl = sbase + gs + v16;
          if constexpr (LAST) asm volatile("" : "+v"(gl));
          unsigned pa[4];
          static_for<4>([&](auto ic) AI {
            constexpr int i = decltype(ic)::value;
            constexpr bool END = LAST && i == 3;
            SCHED0;
            if (i == 3 && !LAST) { sync_lds(); SCHED0; }
            if (i == 0 && LAST) {
              PRIO(1);
              if (early >= 4) PRIO(3);
              SCHED0;
              bar();
              SCHED0;
            }
            if constexpr (!LAST) PRIO(i == 3 ? 3 : 2 - i);
            if constexpr (CHK) { if (__builtin_expect(bad, 0)) refill(ic, sc + i * 16384); }
            SCHED0;
            const bf16x8* fc = f[i & 1];
            bf16x8* fn = f[(i + 1) & 1];
            const u32x4* an = a[(i + 1) % 4];
            const unsigned slot = i < 3 ? gs + (i + 1) * QSLOT : gn;
            const char* pb = LAST ? (const char*)((const LDS(char)*)gl + (i + 1) * QSLOT) : smem + slot + v16;
            unsigned mx = 0;
            static_for<12>([&](auto jc) AI {
              constexpr int j = decltype(jc)::value, nn = j >> 1, m = j & 1, n = nn < 4 ? nn + 2 : nn - 4;
              if constexpr (n < 4) mfma_acc(acc[m][n], fc[n], as_frag(a[i][m]));
              else mfma_acc(acc[m][n], as_frag(a[i][m]), fc[n]);
              SCHED0;
              if constexpr (!END && j < 6) { constexpr int r = j < 4 ? j + 2 : j - 4; fn[r] = *(const bf16x8*)(pb + r * 1024); }
              if constexpr (LAST && i == 0 && j == 6) {
                if (layer == 0) { xr[0] = ld16s(wsr, v16, ps[3]); xr[1] = ld16s(wsr, v16, ps[3] + 16384); }
              }
              if constexpr (LAST && i == 1) {
                if constexpr (j == 6) { pa[0] = lane & 48; asm volatile("" : "+v"(pa[0])); }
                if constexpr (j == 7) { pa[1] = v16 >> 2; asm volatile("" : "+v"(pa[1])); }
                if constexpr (j == 8) { pa[1] &= 60; asm volatile("" : "+v"(pa[1])); }
                if constexpr (j == 9) { pa[2] = pa[1] + ps[0]; asm volatile("" : "+v"(pa[2])); }
                if constexpr (j == 10) { pa[1] += ps[1]; asm volatile("" : "+v"(pa[1])); }
                if constexpr (j == 11) { pa[3] = pa[0] + ps[1]; asm volatile("" : "+v"(pa[3])); }
              }
              if constexpr (LAST && i == 2) {
                if constexpr (j == 6) ck[0] = ((const LDS(f32x4)*)pa[3])[8];
                if constexpr (j == 7) ck[1] = ((const LDS(f32x4)*)pa[3])[12];
                if constexpr (j == 8) { mean[0] = ((const LDS(float)*)pa[2])[0]; asm volatile("" : "+v"(pa[2])); }
                if constexpr (j == 9) { mean[1] = ((const LDS(float)*)pa[2])[16]; asm volatile("" : "+v"(pa[2])); }
                if constexpr (j == 10) { cv[0] = ((const LDS(float)*)pa[1])[64]; cv[1] = ((const LDS(float)*)pa[1])[80]; }
                if constexpr (j == 11) { pa[1] = pa[0] + ps[0]; asm volatile("" : "+v"(pa[1])); }
              }
              if constexpr (LAST && i == 3) {
                if constexpr (j == 0) mv[0] = ((const LDS(f32x4)*)pa[1])[0];
                if constexpr (j == 1) mv[1] = ((const LDS(f32x4)*)pa[1])[4];
                if constexpr (j == 2) { pa[0] += ps[2]; asm volatile("" : "+v"(pa[0])); }
                if constexpr (j == 3) { rs[0] = ((const LDS(float)*)pa[2])[272]; asm volatile("" : "+v"(pa[2])); }
                if constexpr (j == 4) xv[0] = ((const LDS(f32x4)*)pa[0])[0];
                if constexpr (j == 5) xv[1] = ((const LDS(f32x4)*)pa[0])[4];
                if constexpr (j == 6) rs[1] = ((const LDS(float)*)pa[2])[288];
              }
              if constexpr (CHK) {
                if constexpr (j == 8) mx = umax(umax(umax(an[0][0], an[0][2]), an[1][0]), an[1][2]);
                if constexpr (j == 10) bad = poisoned(mx);
              }
              if constexpr (j == 10 && !LAST) a[i][0] = ld16s(wsr, va, so + i * 16384);
              if constexpr (j == 11 && !LAST) a[i][1] = ld16s(wsr, va + 1024, so + i * 16384);
              SCHED0;
            });
          });
        };
        int g = 0;
        if (layer > 0) {
#pragma unroll 1
          for (; g < NCHK; ++g) group(IC<0>{}, g);
        }
#pragma unroll 1
        for (; g < 7; ++g) group(IC<1>{}, g);
        group(IC<3>{}, 7);
        if (!isB && cw >= 4) PRIO(2);
        mfma_done();
      }
      STAMP(2);
      LANE_VARS
      char* const scr = smem;

      u32x4 qf[2], kf[2];
      u32x2 vf[2][2];
      auto fold_rows = [&](int part, u32x4* out, const f32x4* c) AI {
#pragma unroll
        for (int m = 0; m < 2; ++m) {
          f32x4 a0 = acc[m][part * 2] + c[0] * mean[m], a1 = acc[m][part * 2 + 1] + c[1] * mean[m];
          out[m] = u32x4{pk2(a0[0], a0[1]), pk2(a0[2], a0[3]), pk2(a1[0], a1[1]), pk2(a1[2], a1[3])};
        }
      };
      auto fold_x = [&](int part, f32x2 (*x)[4], const f32x4* c) AI {
#pragma unroll
        for (int m = 0; m < 2; ++m) {
          const f32x4 a0 = acc[m][part * 2] + c[0] * mean[m], a1 = acc[m][part * 2 + 1] + c[1] * mean[m];
          x[m][0] = f32x2{a0[0], a0[1]}; x[m][1] = f32x2{a0[2], a0[3]}; x[m][2] = f32x2{a1[0], a1[1]}; x[m][3] = f32x2{a1[2], a1[3]};
        }
      };
      auto fold_v = [&](bool scaled) AI {
#pragma unroll
        for (int m = 0; m < 2; ++m) {
          f32x4 a0 = acc[m][4] + mv[m] * cv[0], a1 = acc[m][5] + mv[m] * cv[1];
          if (scaled) { a0 = mul4(a0, xv[m]); a1 = mul4(a1, xv[m]); }
          vf[m][0] = u32x2{pk2(a0[0], a0[1]), pk2(a0[2], a0[3])};
          vf[m][1] = u32x2{pk2(a1[0], a1[1]), pk2(a1[2], a1[3])};
        }
      };
      const unsigned otile = WS_AR + AR_O + xt_off(bm * 16 + cw * 2, head);
      auto put_o = [&](int m, f32x4 a, f32x4 b, unsigned opar) AI {
        const u32x4 v = u32x4{(pk2(a[0], a[1]) & ~1u) | opar, pk2(a[2], a[3]), (pk2(b[0], b[1]) & ~1u) | opar, pk2(b[2], b[3])};
        st16s(wsr, v16 + m * 1024, otile, v);
        return v;
      };
      auto hold = [&](u32x4 a, u32x4 b) AI { SCHED0; asm volatile("s_nop 1" :: "v"(a), "v"(b)); SCHED0; };

      if (!isB) {
        f32x2 lx[2][4];
        fold_x(1, lx, ck); fold_v(true);
        char* PK = smem + L_STG;
        char* VF = scr;
#pragma unroll
        for (int t = 0; t < 2; ++t)
          *(u32x4*)(VF + (cw * 2 + t) * 1024 + lane * 16) = u32x4{vf[0][t][0], vf[0][t][1], vf[1][t][0], vf[1][t][1]};
        {
          u32x4 pk[2][2];
          phi_map2(lx, rs, pk, [](float*) {});
          char* d0 = PK + cw * 4096 + fr * 64 + ((fr >> 2) & 1) * 32 + ((fq ^ ((fr >> 3) * 2)) * 8);
          char* d1 = PK + cw * 4096 + fr * 64 + (((fr >> 2) & 1) ^ 1) * 32 + ((fq ^ ((fr >> 3) * 2)) * 8);
#pragma unroll
          for (int m = 0; m < 2; ++m) {
            *(u32x2*)(d0 + m * 2048) = u32x2{pk[m][0][0], pk[m][0][1]};
            *(u32x2*)(d0 + m * 2048 + 1024) = u32x2{pk[m][0][2], pk[m][0][3]};
            *(u32x2*)(d1 + m * 2048) = u32x2{pk[m][1][0], pk[m][1][1]};
            *(u32x2*)(d1 + m * 2048 + 1024) = u32x2{pk[m][1][2], pk[m][1][3]};
          }
        }
        PRIO(1);
        const unsigned rec = ar + AR_KV + (unsigned)head * KVREC, dst = rec + (unsigned)bm * (32 * KVREC);
        unsigned ad = sbase + L_STG + (cw & 1) * 1024 + (fq * 4 + (fr >> 2)) * 64 + (((cw >> 1) ^ fq) & 1) * 32 + (((fr & 3) ^ (fq & 2)) * 8);
        unsigned vd = sbase + (cw >> 2) * 1024 + v16, kd = fq & 1 ? OOB : dst + cw * 512 + (fq >> 1) * 256 + fr * 16;
        asm volatile("" : "+v"(ad), "+v"(vd), "+v"(kd));
        sync_lds();
        STAMP(3);
        {
          const u32x4 ones = u32x4{0x3f803f80u, 0x3f803f80u, 0x3f803f80u, 0x3f803f80u};
          f32x4 c0 = zero;
          bf16x8 a[8], b0[8];
#pragma unroll
          for (int ks = 0; ks < 8; ++ks) {
            u32x2 a0 = trl(ad + ks * 4096), a1 = trl(ad + ks * 4096 + 2048);
            a[ks] = as_frag(u32x4{a0[0], a0[1], a1[0], a1[1]});
            b0[ks] = *(const LDS(bf16x8)*)(vd + ks * 2048);
            SCHED0;
          }
#pragma unroll
          for (int ks = 0; ks < 8; ++ks) c0 = mfma16(a[ks], b0[ks], c0);
          const unsigned p0 = umin(pk2(c0[0], c0[1]), POISON - 1), p1 = umin(pk2(c0[2], c0[3]), POISON - 1);
          const auto q0 = __builtin_amdgcn_permlane16_swap(p0, p0, false, false), q1 = __builtin_amdgcn_permlane16_swap(p1, p1, false, false);
          st16(wsr, kd, u32x4{q0[0], q1[0], q0[1], q1[1]});
          SCHED0;
          if (cw < 4) {
            f32x4 cz = zero;
#pragma unroll
            for (int ks = 0; ks < 8; ++ks) cz = mfma16(a[ks], as_frag(ones), cz);
            st16(wsr, fr ? OOB : dst + 4096 + cw * 64 + fq * 16, clean(__builtin_bit_cast(u32x4, cz)));
          }
        }
        SCHED0;
        STAMP(4);
        bar();
        const f32x4 cq[2] = {*(const f32x4*)(lcl + fq * 4), *(const f32x4*)(lcl + 16 + fq * 4)};
        fold_x(0, lx, cq);
        u32x4 pq[2][2];
        u32x4 zp, zq;
        u32x4 part[4];
        auto ldp = [&](int i) AI { part[i] = ld16s(wsr, vk, rec + i * (64 * KVREC) + cw * 512); };
        auto ldz = [&]() AI {
          zp = ld16(wsr, rec + fq * (32 * KVREC) + 4096 + fr * 16);
          zq = ld16(wsr, rec + (4 + fq) * (32 * KVREC) + 4096 + fr * 16);
        };
#pragma unroll
        for (int i = 0; i < 4; ++i) asm volatile("" : "=v"(part[i]));
        asm volatile("" : "=v"(zp), "=v"(zq));
        phi_map2(lx, rs, pq, [&](float* sp) AI {
          asm volatile("" : "+v"(sp[0]), "+v"(sp[1]));
          SCHED0;
          if (cw >= 4) {
            MEMFENCE;
#pragma unroll
            for (int i = 0; i < 4; ++i) ldp(i);
            MEMFENCE;
          }
          SCHED0;
        });
        asm volatile("" : "+v"(pq[0][0]), "+v"(pq[0][1]), "+v"(pq[1][0]), "+v"(pq[1][1]));
        SCHED0;
        STAMP(5);
        char* kvf = smem + L_A2;
        if (cw < 4) {
          SLEEP(SLP_K);
          MEMFENCE;
          if (cw == 0) ldz();
#pragma unroll
          for (int i = 0; i < 4; ++i) ldp(i);
          MEMFENCE;
        }
        unsigned kw = sbase + L_A2 + v16 + (cw >> 1) * 1024 + (cw & 1) * 8;
        unsigned ka = sbase + L_A2 + v16, za = sbase + L_Z + fq * 16;
        asm volatile("" : "+v"(kw), "+v"(ka), "+v"(za));
        u32x2 kvs;
        auto nans = [&](f32x4 v) AI { return __builtin_amdgcn_ballot_w64(__builtin_isunordered(v[0], v[1])) | __builtin_amdgcn_ballot_w64(__builtin_isunordered(v[2], v[3])); };
        auto sums = [&]() AI {
          unsigned long long nan = 0;
          if (cw == 0) {
            f32x4 z = __builtin_bit_cast(f32x4, zp) + __builtin_bit_cast(f32x4, zq);
            nan = nans(z);
            float z0 = z[0], z1 = z[1], z2 = z[2], z3 = z[3];
            xred4<false>(z0, z1, z2, z3);
            *(u32x2*)(smem + L_Z + (fr >> 3) * 64 + (fr & 3) * 16 + ((fr >> 2) & 1) * 8) = u32x2{pk2(z0, z1), pk2(z2, z3)};
          }
          f32x4 s4 = zero;
#pragma unroll
          for (int i = 0; i < 4; ++i) { s4 = mfma16(as_frag(eye), as_frag(part[i]), s4); SCHED0; }
          kvs = u32x2{pk2(s4[0], s4[1]), pk2(s4[2], s4[3])};
          asm volatile("" : "+v"(kvs));
          return nan | __builtin_amdgcn_ballot_w64(s4[0] != s4[0]);
        };
        if (__builtin_expect(sums() != 0, 0)) {
          for (;;) {
            bool again = false;
            if (cw == 0 && poisoned(umax4(umax4(zp), zq))) { again = true; ldz(); }
#pragma unroll
            for (int i = 0; i < 4; ++i)
              if (poisoned(umax4(part[i]))) { again = true; ldp(i); }
            __builtin_amdgcn_s_waitcnt(0x0f70);
            MEMFENCE;
            if (!again || --budget <= 0) break;
            COUNT(2);
          }
          sums();
        }
        STAMP(6);
        *(LDS(u32x2)*)kw = kvs;
        sync_lds();
        STAMP(7);
        bf16x8 kvr[2][2], zf[2];
#pragma unroll
        for (int s = 0; s < 2; ++s) { zf[s] = *(const LDS(bf16x8)*)(za + s * 64); SCHED0; }
#pragma unroll
        for (int s = 0; s < 2; ++s) {
          kvr[0][s] = *(const LDS(bf16x8)*)(ka + (0 * 2 + s) * 1024);
          kvr[1][s] = *(const LDS(bf16x8)*)(ka + (1 * 2 + s) * 1024);
          SCHED0;
        }
        f32x4 cd[2], c[2][2];
        u32x4 oo[2];
        auto den = [&](int m, int s) AI { cd[m] = mfma16(zf[s], as_frag(pq[m][s]), s ? cd[m] : zero); SCHED0; };
        auto num = [&](int m, int s) AI {
#pragma unroll
          for (int t = 0; t < 2; ++t) { c[m][t] = mfma16(kvr[t][s], as_frag(pq[m][s]), s ? c[m][t] : zero); SCHED0; }
        };
        auto fin = [&](int m, float id) AI { const f32x4 i4 = f32x4{id, id, id, id}; oo[m] = put_o(m, mul4(c[m][0], i4), mul4(c[m][1], i4), 0); SCHED0; };
        den(0, 0); den(1, 0); den(0, 1); den(1, 1);
        num(0, 0); num(0, 1); num(1, 0);
        const float i0 = __builtin_amdgcn_rcpf(cd[0][0] + 1e-6f), i1 = __builtin_amdgcn_rcpf(cd[1][0] + 1e-6f);
        SCHED0;
        num(1, 1);
        fin(0, i0); fin(1, i1);
        hold(oo[0], oo[1]);
        STAMP(8);
        asm volatile("" : "=v"(acc[0][0]), "=v"(acc[0][1]), "=v"(acc[0][2]), "=v"(acc[0][3]), "=v"(acc[0][4]), "=v"(acc[0][5]));
        asm volatile("" : "=v"(acc[1][0]), "=v"(acc[1][1]), "=v"(acc[1][2]), "=v"(acc[1][3]), "=v"(acc[1][4]), "=v"(acc[1][5]));
        asm volatile("" : "=v"(ck[0]), "=v"(ck[1]), "=v"(mv[0]), "=v"(mv[1]), "=v"(xv[0]), "=v"(xv[1]));
        asm volatile("" : "=v"(mean[0]), "=v"(mean[1]), "=v"(rs[0]), "=v"(rs[1]), "=v"(cv[0]), "=v"(cv[1]));
      }
      int kind = layer & 1;
      asm volatile("" : "+s"(kind));
      if (kind) {
        char* ex = scr;
        fold_rows(1, kf, ck); fold_v(false);
        const unsigned hrec = ar + AR_KV + (unsigned)(bm * 32 + head) * KVREC;
        {
          const u32x4 vl = u32x4{vf[1][0][0], vf[1][0][1], vf[1][1][0], vf[1][1][1]};
          if (cw == 7) {
            st16s(wsr, v16, hrec, clean(kf[1]));
            STPAD;
            st16s(wsr, v16 + 1024, hrec, clean(vl));
            STPAD;
          }
          char* e2 = ex + (cw + 1) * 2048;
          *(u32x4*)(e2 + lane * 16) = kf[1];
          *(u32x2*)(e2 + 1024 + lane * 8) = vf[1][0];
          *(u32x2*)(e2 + 1536 + lane * 8) = vf[1][1];
          MEMFENCE;
          lflag[cw] = layer;
          MEMFENCE;
          PRIO(1);
        }
        SCHED0;
        fold_rows(0, qf, xv);
        u32x4 kp;
        u32x4 va0 = __builtin_shufflevector(vf[0][0], vf[0][0], -1, -1, 0, 1), vb0 = __builtin_shufflevector(vf[0][1], vf[0][1], -1, -1, 0, 1);
        auto setp = [&](u32x4& d, u32x2 v) AI { d[0] = v[0]; d[1] = v[1]; };
        auto ldh = [&]() AI {
          MEMFENCE;
          kp = ld16s(wsr, v16, hrec - 32 * KVREC);
          setp(va0, __builtin_amdgcn_raw_buffer_load_b64(wsr, v16 + 1024, hrec - 32 * KVREC, 16));
          MEMFENCE;
          setp(vb0, __builtin_amdgcn_raw_buffer_load_b64(wsr, v16 + 1032, hrec - 32 * KVREC, 16));
          MEMFENCE;
        };
        unsigned fl = 0;
        auto rdl = [&]() AI {
          MEMFENCE;
          fl = lflag[cw - 1];
          MEMFENCE;
          kp = *(const u32x4*)(ex + cw * 2048 + lane * 16);
          setp(va0, *(const u32x2*)(ex + cw * 2048 + 1024 + lane * 8));
          MEMFENCE;
          setp(vb0, *(const u32x2*)(ex + cw * 2048 + 1536 + lane * 8));
          MEMFENCE;
        };
        auto prev = [&]() AI {
          if (cw > 0) {
            do { SLEEP(1); rdl(); __builtin_amdgcn_s_waitcnt(0xc07f); } while (__builtin_amdgcn_readfirstlane(fl) != (unsigned)layer && --budget > 0);
          } else if (bm > 0) {
            SLEEP(SLP_HX);
            do { ldh(); __builtin_amdgcn_s_waitcnt(0x0f70); }
            while (poisoned(umax4(umax4(kp), u32x4{va0[0], va0[1], vb0[0], vb0[1]})) && --budget > 0);
          } else {
            kp = u32x4{0u, 0u, 0u, 0u};
            setp(va0, u32x2{0u, 0u}); setp(vb0, u32x2{0u, 0u});
          }
          asm volatile("" : "+v"(kp), "+v"(va0), "+v"(vb0));
        };
        const float scale = 0.17677669529663687f * LOG2E;
        const bool odd = fr & 1;
        const bool own0 = sel[0] == 0x0c0c0c0cu, own1 = sel[1] == 0x0c0c0c0cu;
        float l[2][2], mx[2], rk[2][2], sum[2];
#pragma unroll
        for (int m = 0; m < 2; ++m) { rk[m][0] = ((const LDS(float)*)ra0)[m * 16]; rk[m][1] = ((const LDS(float)*)ra1)[m * 16]; }
        float qk[2][2];
#pragma unroll
        for (int m = 0; m < 2; ++m) { const float qs = rs[m] * scale; qk[m][0] = qs * rk[m][0]; qk[m][1] = qs * rk[m][1]; }
        u32x4 va1 = u32x4{vf[0][0][0], vf[0][0][1], vf[1][0][0], vf[1][0][1]}, vb1 = u32x4{vf[0][1][0], vf[0][1][1], vf[1][1][0], vf[1][1][1]};
        auto score = [&](int m) AI {
          f32x4 s0 = mfma16(as_frag(m == 0 ? kp : kf[0]), as_frag(qf[m]), zero);
          f32x4 s1 = mfma16(as_frag(kf[m]), as_frag(qf[m]), zero);
          float a = own0 ? (odd ? s1[1] : s1[0]) : (odd ? s0[1] : s0[0]);
          float b = own1 ? (odd ? s1[3] : s1[2]) : (odd ? s0[3] : s0[2]);
          if (m == 0) { l[m][0] = __builtin_fmaf(a, qk[m][0], off0); l[m][1] = __builtin_fmaf(b, qk[m][1], off1); }
          else { l[m][0] = a * qk[m][0]; l[m][1] = b * qk[m][1]; }
          mx[m] = fmx(l[m][0], l[m][1]);
        };
        auto expo = [&](int m) AI {
          l[m][0] = __builtin_amdgcn_exp2f(l[m][0] - mx[m]);
          l[m][1] = __builtin_amdgcn_exp2f(l[m][1] - mx[m]);
          sum[m] = l[m][0] + l[m][1];
        };
        f32x4 ov[2][2];
        auto pv = [&](int m) AI {
          const unsigned pr = pk2(mul1(mul1(l[m][0], rk[m][0]), sum[m]), mul1(mul1(l[m][1], rk[m][1]), sum[m]));
          const u32x4 pf = u32x4{__builtin_amdgcn_perm(pr, pr, sel[0]), __builtin_amdgcn_perm(pr, pr, sel[1]),
                                 __builtin_amdgcn_perm(pr, pr, sel[2]), __builtin_amdgcn_perm(pr, pr, sel[3])};
          ov[m][0] = mfma16(as_frag(m ? va1 : va0), as_frag(pf), zero);
          ov[m][1] = mfma16(as_frag(m ? vb1 : vb0), as_frag(pf), zero);
        };
        asm volatile("" : "+v"(qf[0]), "+v"(qf[1]), "+v"(va0), "+v"(vb0));
        SCHED0;
        if (cw > 0) rdl();
        SCHED0;
        score(1);
        asm volatile("" : "+v"(qf[0]), "+v"(qk[0][0]), "+v"(qk[0][1]), "+v"(mx[1]), "+v"(l[1][0]), "+v"(l[1][1]));
        asm volatile("" : "+v"(va1), "+v"(vb1));
        SCHED0;
        STAMP(3);
        if (__builtin_expect(cw == 0 || __builtin_amdgcn_readfirstlane(fl) != (unsigned)layer, 0)) prev();
        score(0);
        xred2<true>(mx[0], mx[1]);
        expo(0); expo(1);
        xred2<false, true>(sum[0], sum[1]);
        pv(0); pv(1);
        SCHED0;
        const u32x4 o0 = put_o(0, ov[0][0], ov[0][1], 1), o1 = put_o(1, ov[1][0], ov[1][1], 1);
        hold(o0, o1);
        STAMP(4);
      }
    }
    {
      LANE_VARS
      const int mt = bm * 16 + cw * 2 + cm;
      const unsigned so0 = WS_AR + AR_O + xt_off(mt, 0);
      const unsigned opar = layer & 1;
      auto diff = [&](u32x4 v) AI { return ((v[0] ^ opar) | (v[2] ^ opar)) & 1u; };
      auto dif3 = [&](u32x4 v) AI { return __builtin_amdgcn_bitop3_b32(v[0], v[2], opar, 0x7e); };
      auto any = [&](unsigned d) AI { return __builtin_amdgcn_ballot_w64(d != 0) != 0; };
      const int g0 = cm;
      u32x4 a[RO];
      auto refill = [&](auto ic, auto wc, unsigned cur, unsigned nxt) AI {
        constexpr int I = decltype(ic)::value, W = decltype(wc)::value;
        do {
          COUNT(1);
          SLEEP(SLP_R);
          MEMFENCE;
          a[I] = ld16s(wsr, v16, cur + I * 16384);
          bool u[RO];
#pragma unroll
          for (int j = I + 1; j < I + W; ++j) u[j % RO] = any(diff(a[j % RO]));
          SCHED0;
#pragma unroll
          for (int j = I + 1; j < I + W; ++j)
            if (u[j % RO]) a[j % RO] = ld16s(wsr, v16, (j < RO ? cur : nxt) + (j % RO) * 16384);
          __builtin_amdgcn_s_waitcnt(0x0f70);
          MEMFENCE;
        } while (any(diff(a[I]) | diff(a[I + 1])) && --budget > 0);
      };
      const unsigned sf = so0 + g0 * (RO * 16384);
      f32x4 acc[4];
#pragma unroll
      for (int n = 0; n < 4; ++n)
        acc[n] = f32x4{bflo(xr[n >> 1][(n & 1) * 2]), bfhi(xr[n >> 1][(n & 1) * 2]), bflo(xr[n >> 1][(n & 1) * 2 + 1]), bfhi(xr[n >> 1][(n & 1) * 2 + 1])};
      asm volatile("" : "+v"(acc[0]), "+v"(acc[1]), "+v"(acc[2]), "+v"(acc[3]));
      SCHED0;
      const bool late = isB && cw == 0;
      if (late) bar();
      if (cn < 8) { if (late) SLEEP(SLP_O0); else SLEEP(SLP_O); }
      else { if (late) SLEEP(SLP_O0D); else SLEEP(SLP_OD); }
#pragma unroll
      for (int j = 0; j < 4; ++j) { a[j] = ld16s(wsr, v16, sf + j * 16384); SCHED0; }
      if (!late) bar();
      STAMP(9);
      bf16x8 wf[2][4];
      const char* pw0 = smem + L_STG + v16;
#pragma unroll
      for (int n = 0; n < 8; ++n) wf[n >> 2][n & 3] = *(const bf16x8*)(pw0 + g0 * (RO * 4096) + n * 1024);
      SCHED0;
      STAMP(10);
      if (any(diff(a[0]) | diff(a[1]))) refill(IC<0>{}, IC<4>{}, sf, sf);
      SCHED0;
#pragma unroll
      for (int j = 4; j < RO; ++j) { a[j] = ld16s(wsr, v16, sf + j * 16384); SCHED0; }
      STAMP(11);
      bool bad = false;
      auto group = [&](auto lastc, int g) AI {
        constexpr bool LAST = decltype(lastc)::value;
        const int gc = g ^ g0, gn = (g + 1) ^ g0;
        const unsigned sc = so0 + gc * (RO * 16384), so = so0 + gn * (RO * 16384);
        const char* pc = pw0 + gc * (RO * 4096);
        const char* pn = pw0 + gn * (RO * 4096);
        static_for<RO>([&](auto ic) AI {
          constexpr int i = decltype(ic)::value;
          SCHED0;
          if constexpr (!LAST && i == 0) { if (g == 0) PRIO(3); else if (g == 2) PRIO(2); }
          if constexpr (LAST && i == 3) PRIO(1);
          if constexpr (i % 2 == 0)
            if (__builtin_expect(bad, 0)) refill(ic, IC<(LAST && i + 4 > RO) ? RO - i : 4>{}, sc, so);
          SCHED0;
          unsigned mx = 0, my = 0;
          static_for<4>([&](auto nc) AI {
            constexpr int n = decltype(nc)::value;
            mfma_acc(acc[n], wf[i & 1][n], as_frag(a[i]));
            SCHED0;
            if constexpr (i + 2 < RO) wf[i & 1][n] = *(const bf16x8*)(pc + (i + 2) * 4096 + n * 1024);
            else if constexpr (!LAST) wf[i & 1][n] = *(const bf16x8*)(pn + (i + 2 - RO) * 4096 + n * 1024);
            if constexpr (!(LAST && i == RO - 1) && (i & 1)) {
              if constexpr (n == 0) mx = dif3(a[(i + 1) % RO]);
              if constexpr (n == 1) my = dif3(a[(i + 2) % RO]);
              if constexpr (n == 2) bad = any((mx | my) & 1u);
            }
            if constexpr (n == 3 && !LAST) a[i] = ld16s(wsr, v16, so + i * 16384);
            SCHED0;
          });
        });
      };
#pragma unroll 1
      for (int g = 0; g < 3; ++g) group(std::false_type{}, g);
      group(std::true_type{}, 3);
      mfma_done();
      STAMP(12);
#pragma unroll
      for (int j = 0; j < 2; ++j)
#pragma unroll
        for (int e = 0; e < 4; ++e) xr[j][e] = pk2(acc[j * 2 + (e >> 1)][(e & 1) * 2], acc[j * 2 + (e >> 1)][(e & 1) * 2 + 1]);
      if (layer == 3) {
        u32x4 yo[2];
#pragma unroll
        for (int j = 0; j < 2; ++j) {
          auto u = __builtin_amdgcn_permlane16_swap(xr[j][0], xr[j][2], false, false);
          auto v = __builtin_amdgcn_permlane16_swap(xr[j][1], xr[j][3], false, false);
          yo[j] = u32x4{u[0], v[0], u[1], v[1]};
        }
        const unsigned vo = fr * 2048 + (fq & 1) * 32 + (fq >> 1) * 16;
        __builtin_amdgcn_raw_buffer_store_b128(yo[0], mk_rsrc(p.out, 4 * MBY), vo, mt * 32768 + cn * 128, 2);
        __builtin_amdgcn_raw_buffer_store_b128(yo[1], mk_rsrc(p.out, 4 * MBY), vo + 64, mt * 32768 + cn * 128, 2);
        STAMP(13);
        STAMP(14);
        STAMP_DUMP;
        __builtin_amdgcn_endpgm();
      }
      const u32x2 stat = put_x(xr, v16, ar + ARENA + AR_X + xt_off(mt, cn * 2));
      __builtin_amdgcn_raw_buffer_store_b64(stat, wsr, fq ? OOB : fr * 8, ar + ARENA + AR_ST + (mt * 16 + cn) * 128, 16);
      MEMFENCE;
      STAMP(14);
    }
  }
}

extern "C" void kernel_launch(void* const* d_in, const int* in_sizes, int n_in, void* d_out, int out_size,
                              void* d_ws, size_t ws_size, hipStream_t stream) {
  (void)hipFuncSetAttribute((const void*)mega_kernel, hipFuncAttributeMaxDynamicSharedMemorySize, MEGA_LDS);
  MegaParams m;
  m.ws = (char*)d_ws; m.out = (u16*)d_out; m.x = (const u16*)d_in[0]; m.wphi = (const u16*)d_in[2];
  m.wqkvA = (const u16*)d_in[1]; m.wqkvB = (const u16*)d_in[5];
  m.woA = (const u16*)d_in[3]; m.woB = (const u16*)d_in[6];
  mega_kernel<<<256, 768, MEGA_LDS, stream>>>(m);
}
```

```cpp
#include <hip/hip_runtime.h>
#include <stdint.h>
#include <type_traits>

#define DI __device__ __forceinline__
#define AI __attribute__((always_inline))
typedef unsigned short u16;
typedef __attribute__((ext_vector_type(8))) short bf16x8;
typedef __attribute__((ext_vector_type(4))) short s16x4;
typedef __attribute__((ext_vector_type(4))) float f32x4;
typedef __attribute__((ext_vector_type(2))) float f32x2;
typedef __attribute__((ext_vector_type(2))) __bf16 bf2;
typedef __attribute__((ext_vector_type(4))) unsigned u32x4;
typedef __attribute__((ext_vector_type(2))) unsigned u32x2;
typedef __attribute__((address_space(3))) s16x4* lds_s16x4_ptr;
#define LDS(T) __attribute__((address_space(3))) T
template <int N> using IC = std::integral_constant<int, N>;

constexpr float LOG2E = 1.4426950408889634f;

DI unsigned pk2(float a, float b) {
  f32x2 v = {a, b};
  bf2 r = __builtin_convertvector(v, bf2);
  return __builtin_bit_cast(unsigned, r);
}
DI float bflo(unsigned u) { return __uint_as_float(u << 16); }
DI float bfhi(unsigned u) { return __uint_as_float(u & 0xffff0000u); }
DI f32x4 mfma16(bf16x8 a, bf16x8 b, f32x4 c) { return __builtin_amdgcn_mfma_f32_16x16x32_bf16(a, b, c, 0, 0, 0); }
DI void mfma_acc(f32x4& c, bf16x8 a, bf16x8 b) { asm volatile("v_mfma_f32_16x16x32_bf16 %0, %1, %2, %0" : "+v"(c) : "v"(a), "v"(b)); }
DI void mfma_done() { asm volatile("s_nop 7\n\ts_nop 1" ::: "memory"); __builtin_amdgcn_sched_barrier(0); }
DI bf16x8 as_frag(u32x4 v) { return __builtin_bit_cast(bf16x8, v); }
DI float dot2(unsigned a, unsigned b, float c) {
  return __builtin_amdgcn_fdot2_f32_bf16(__builtin_bit_cast(bf2, a), __builtin_bit_cast(bf2, b), c, false);
}
DI u32x2 trread(const char* p) { return __builtin_bit_cast(u32x2, __builtin_amdgcn_ds_read_tr16_b64_v4i16((lds_s16x4_ptr)p)); }
DI u32x2 trl(unsigned a) { return __builtin_bit_cast(u32x2, __builtin_amdgcn_ds_read_tr16_b64_v4i16((lds_s16x4_ptr)a)); }
DI float fmx(float a, float b) { return __builtin_elementwise_maximum(a, b); }
DI float fmn(float a, float b) { return __builtin_elementwise_minimum(a, b); }

DI float mul1(float a, float b) { float r; asm("v_mul_f32 %0, %1, %2" : "=v"(r) : "v"(a), "v"(b)); return r; }
DI float add1(float a, float b) { float r; asm("v_add_f32 %0, %1, %2" : "=v"(r) : "v"(a), "v"(b)); return r; }
DI f32x4 mul4(f32x4 a, f32x4 b) {
  float r0, r1, r2, r3;
  asm("v_mul_f32 %0, %4, %8\n\tv_mul_f32 %1, %5, %9\n\tv_mul_f32 %2, %6, %10\n\tv_mul_f32 %3, %7, %11"
      : "=&v"(r0), "=&v"(r1), "=&v"(r2), "=&v"(r3) : "v"(a[0]), "v"(a[1]), "v"(a[2]), "v"(a[3]), "v"(b[0]), "v"(b[1]), "v"(b[2]), "v"(b[3]));
  return f32x4{r0, r1, r2, r3};
}
DI f32x4 add4(f32x4 a, f32x4 b) { return f32x4{add1(a[0], b[0]), add1(a[1], b[1]), add1(a[2], b[2]), add1(a[3], b[3])}; }

template <bool IS_MAX, bool INV = false>
DI void xred2(float& a, float& b) {
  auto r = __builtin_amdgcn_permlane32_swap(__float_as_uint(a), __float_as_uint(b), false, false);
  float c = IS_MAX ? fmx(__uint_as_float(r[0]), __uint_as_float(r[1])) : __uint_as_float(r[0]) + __uint_as_float(r[1]);
  auto q = __builtin_amdgcn_permlane16_swap(__float_as_uint(c), __float_as_uint(c), false, false);
  c = IS_MAX ? fmx(__uint_as_float(q[0]), __uint_as_float(q[1])) : __uint_as_float(q[0]) + __uint_as_float(q[1]);
  if (INV) c = __builtin_amdgcn_rcpf(c);
  auto w = __builtin_amdgcn_permlane32_swap(__float_as_uint(c), __float_as_uint(c), false, false);
  a = __uint_as_float(w[0]); b = __uint_as_float(w[1]);
}
template <bool IS_MAX, bool INV = false>
DI void xred4(float& a, float& b, float& c, float& d) {
  auto op = [](unsigned x, unsigned y) { return IS_MAX ? fmx(__uint_as_float(x), __uint_as_float(y)) : __uint_as_float(x) + __uint_as_float(y); };
  auto r0 = __builtin_amdgcn_permlane32_swap(__float_as_uint(a), __float_as_uint(b), false, false);
  auto r1 = __builtin_amdgcn_permlane32_swap(__float_as_uint(c), __float_as_uint(d), false, false);
  float ab = op(r0[0], r0[1]), cd = op(r1[0], r1[1]);
  auto q = __builtin_amdgcn_permlane16_swap(__float_as_uint(ab), __float_as_uint(cd), false, false);
  float e = op(q[0], q[1]);
  if (INV) e = __builtin_amdgcn_rcpf(e);
  auto w = __builtin_amdgcn_permlane16_swap(__float_as_uint(e), __float_as_uint(e), false, false);
  auto u = __builtin_amdgcn_permlane32_swap(w[0], w[0], false, false);
  auto v = __builtin_amdgcn_permlane32_swap(w[1], w[1], false, false);
  a = __uint_as_float(u[0]); b = __uint_as_float(u[1]); c = __uint_as_float(v[0]); d = __uint_as_float(v[1]);
}

DI unsigned xt_off(int mt, int ks) { return (unsigned)((((mt >> 4) * 32 + ks) * 16 + (mt & 15)) << 10); }

#define GLDS_PRE "s_mov_b32 %0, m0\n\ts_mov_b32 m0, %3\n\ts_nop 2\n\t"
#define GLDS_POST "s_mov_b32 m0, %0"
#define GLDS_OPS : "=&s"(keep) : "v"(voff), "s"(sbase), "s"(lds) : "memory"
#define GL1(c) "global_load_lds_dwordx4 %1, %2" c "\n\t"
#define GL2(c) GL1(c) "global_load_lds_dwordx4 %1, %2 offset:1024" c "\n\t"
#define GL3(c) GL2(c) "global_load_lds_dwordx4 %1, %2 offset:2048" c "\n\t"
#define GL4(c) GL3(c) "global_load_lds_dwordx4 %1, %2 offset:3072" c "\n\t"
template <int N, bool SC1 = false>
DI void glds_run(unsigned voff, const void* sbase, unsigned lds) {
  unsigned keep;
  if constexpr (SC1) {
    if constexpr (N == 2) asm volatile(GLDS_PRE GL2(" sc1") GLDS_POST GLDS_OPS);
    else if constexpr (N == 3) asm volatile(GLDS_PRE GL3(" sc1") GLDS_POST GLDS_OPS);
    else asm volatile(GLDS_PRE GL4(" sc1") GLDS_POST GLDS_OPS);
  } else {
    static_assert(SC1 || N == 4, "");
    asm volatile(GLDS_PRE GL4("") GLDS_POST GLDS_OPS);
  }
}
template <int N> DI void wait_vm_lgkm0() { asm volatile("s_waitcnt vmcnt(%0) lgkmcnt(0)" ::"n"(N) : "memory"); }
template <int N> DI void wait_vm() { asm volatile("s_waitcnt vmcnt(%0)" ::"n"(N) : "memory"); }
DI void drain() { asm volatile("s_waitcnt vmcnt(0)" ::: "memory"); __builtin_amdgcn_s_waitcnt(0x0f70); }
DI void bar() { asm volatile("" ::: "memory"); __builtin_amdgcn_s_barrier(); asm volatile("" ::: "memory"); }
DI void sync_lds() { asm volatile("s_waitcnt lgkmcnt(0)" ::: "memory"); bar(); }
#define SCHED0 __builtin_amdgcn_sched_barrier(0)
#define STPAD do { SCHED0; asm volatile("s_nop 1"); SCHED0; } while (0)
#define SLEEP(n) do { if (n) __builtin_amdgcn_s_sleep(n); } while (0)
#define PRIO(n) __builtin_amdgcn_s_setprio(n)

template <typename F>
DI void phi_map2(const f32x2 (*x)[4], const float* rs, u32x4 (*out)[2], F mid) {
  auto top = [&](float sg, float* mx) AI {
#pragma unroll
    for (int m = 0; m < 2; ++m)
      mx[m] = fmx(fmx(fmx(sg * x[m][0][0], sg * x[m][0][1]), sg * x[m][1][0]), fmx(fmx(sg * x[m][1][1], sg * x[m][2][0]), fmx(fmx(sg * x[m][2][1], sg * x[m][3][0]), sg * x[m][3][1])));
    xred2<true>(mx[0], mx[1]);
  };
  auto sum8 = [](const f32x2* e) AI { const f32x2 t = (e[0] + e[1]) + (e[2] + e[3]); float lo = t[0]; asm("" : "+v"(lo)); return lo + t[1]; };
  float mx[2], sp[2], sn[2];
  top(1.f, mx);
  f32x2 ep[2][4], en[2][4];
#pragma unroll
  for (int m = 0; m < 2; ++m) {
    const float sl = rs[m] * LOG2E, off = mx[m] * -sl;
#pragma unroll
    for (int j = 0; j < 4; ++j) {
      const f32x2 a = __builtin_elementwise_fma(x[m][j], f32x2{sl, sl}, f32x2{off, off});
      ep[m][j] = f32x2{__builtin_amdgcn_exp2f(a[0]), __builtin_amdgcn_exp2f(a[1])};
    }
#pragma unroll
    for (int j = 0; j < 4; ++j) en[m][j] = f32x2{__builtin_amdgcn_rcpf(ep[m][j][0]), __builtin_amdgcn_rcpf(ep[m][j][1])};
    sp[m] = sum8(ep[m]); sn[m] = sum8(en[m]);
  }
  xred4<false, true>(sp[0], sn[0], sp[1], sn[1]);
  if (__builtin_expect(__builtin_amdgcn_ballot_w64(!(fmn(sn[0], sn[1]) > 1e-30f)) != 0, 0)) {
    top(-1.f, mx);
#pragma unroll
    for (int m = 0; m < 2; ++m) {
      const float sl = rs[m] * -LOG2E, off = mx[m] * sl;
#pragma unroll
      for (int j = 0; j < 4; ++j) {
        const f32x2 a = __builtin_elementwise_fma(x[m][j], f32x2{sl, sl}, f32x2{off, off});
        en[m][j] = f32x2{__builtin_amdgcn_exp2f(a[0]), __builtin_amdgcn_exp2f(a[1])};
      }
      sn[m] = sum8(en[m]);
    }
    xred2<false, true>(sn[0], sn[1]);
  }
  mid(sp);
#pragma unroll
  for (int m = 0; m < 2; ++m) {
#pragma unroll
    for (int j = 0; j < 4; j += 2) {
      f32x2 a0, a1, b0, b1;
      asm("v_pk_mul_f32 %0, %4, %5 op_sel_hi:[0,1]\n\tv_pk_mul_f32 %1, %4, %6 op_sel_hi:[0,1]\n\t"
          "v_pk_mul_f32 %2, %4, %7 op_sel:[1,0]\n\tv_pk_mul_f32 %3, %4, %8 op_sel:[1,0]"
          : "=&v"(a0), "=&v"(a1), "=&v"(b0), "=&v"(b1) : "v"(f32x2{sp[m], sn[m]}), "v"(ep[m][j]), "v"(ep[m][j + 1]), "v"(en[m][j]), "v"(en[m][j + 1]));
      out[m][0][j] = pk2(a0[0], a0[1]); out[m][0][j + 1] = pk2(a1[0], a1[1]);
      out[m][1][j] = pk2(b0[0], b0[1]); out[m][1][j + 1] = pk2(b1[0], b1[1]);
    }
  }
}

#define SLP_X 8
#define SLP_XD 4
#define SLP_O 19
#define SLP_OD 10
#define SLP_O0 8
#define SLP_O0D 2
#define SLP_K 4
#define SLP_R 4
#define SLP_HX 1
#define SLP_Q 4
#define SLP_P 50
#define SLP_E 14
#define SLP_G 12
#define SPR0 1
#define SW0 0
#define LANEID __builtin_amdgcn_mbcnt_hi(~0u, __builtin_amdgcn_mbcnt_lo(~0u, 0u))
#define STAMP(i) do {} while (0)
#define STAMP_DUMP do {} while (0)
#define COUNT(k) do {} while (0)
#define PSTAMP(l) do { const int layer = l; (void)layer; STAMP(15); } while (0)
constexpr unsigned MBY = 1u << 20;
constexpr unsigned WS_WQ = 0;
constexpr unsigned WS_WO = 12 * MBY;
constexpr unsigned WS_AR = 16 * MBY, ARENA = 12 * MBY;
constexpr unsigned AR_X = 0, AR_ST = 4 * MBY, AR_O = 5 * MBY, AR_KV = 9 * MBY;
constexpr unsigned AR_L2 = AR_ST + 256 * 1024, AR_FL = AR_ST + 320 * 1024;
constexpr unsigned KVREC = 9 * 1024;
constexpr unsigned WS_CS = WS_AR + 4 * ARENA;
constexpr unsigned WS_F1 = WS_CS + 512 * 1024;
constexpr unsigned WS_F2 = WS_F1 + 64 * 1024;
constexpr unsigned WS_EP = WS_F2 + 2048;
constexpr unsigned WS_END = WS_EP + 128;
constexpr unsigned POISON = 0xffffffffu;
constexpr unsigned OOB = 0x80000000u;

typedef __amdgpu_buffer_rsrc_t rsrc_t;
DI rsrc_t mk_rsrc(const void* p, unsigned bytes) { return __builtin_amdgcn_make_buffer_rsrc((void*)p, 0, bytes, 0x00020000); }
DI void st16(rsrc_t r, unsigned off, u32x4 v) { __builtin_amdgcn_raw_buffer_store_b128(v, r, off, 0, 16); }
DI void st16s(rsrc_t r, unsigned voff, unsigned soff, u32x4 v) { __builtin_amdgcn_raw_buffer_store_b128(v, r, voff, soff, 16); }
DI u32x4 ld16(rsrc_t r, unsigned off) { return __builtin_amdgcn_raw_buffer_load_b128(r, off, 0, 16); }
DI u32x4 ld16s(rsrc_t r, unsigned voff, unsigned soff) { return __builtin_amdgcn_raw_buffer_load_b128(r, voff, soff, 16); }
DI u32x4 clean(u32x4 v) { return __builtin_elementwise_min(v, u32x4{POISON - 1, POISON - 1, POISON - 1, POISON - 1}); }
DI unsigned umax(unsigned a, unsigned b) { return a > b ? a : b; }
DI unsigned umin(unsigned a, unsigned b) { return a < b ? a : b; }
DI unsigned umax4(u32x4 v) { return umax(umax(umax(v[0], v[1]), v[2]), v[3]); }
DI unsigned umax4(unsigned m, u32x4 v) { return umax(umax(umax(umax(m, v[0]), v[1]), v[2]), v[3]); }
DI bool poisoned(unsigned m) { return __builtin_amdgcn_ballot_w64(m == POISON) != 0; }
#define MEMFENCE asm volatile("" ::: "memory")

#define BL(i, o) "buffer_load_dwordx4 %1, %2, %" #i " offen" o " nt lds\n\t"
#define BL_OPS(...) : "=&s"(keep) : "v"(voff), "s"(r), "s"(lds), __VA_ARGS__ : "memory"
template <int N>
DI void blds_run(rsrc_t r, unsigned voff, unsigned lds, unsigned s0, unsigned s1 = 0, unsigned s2 = 0, unsigned s3 = 0) {
  unsigned keep;
  if constexpr (N == 2) asm volatile(GLDS_PRE BL(4, "") BL(5, " offset:1024") GLDS_POST BL_OPS("s"(s0), "s"(s1)));
  else asm volatile(GLDS_PRE BL(4, "") BL(5, " offset:1024") BL(6, " offset:2048") BL(7, " offset:3072") GLDS_POST BL_OPS("s"(s0), "s"(s1), "s"(s2), "s"(s3)));
}

#define CODEB 36608
constexpr int MEGA_CODE_BYTES = CODEB;
DI unsigned code_prefetch(int i) {
  unsigned long long pc; int rel; unsigned d;
  asm volatile("s_getpc_b64 %0\n\ts_mov_b32 %1, _Z11mega_kernel10MegaParams@rel32@lo+4" : "=s"(pc), "=s"(rel));
  const unsigned long long a = pc + (long long)rel + (i * 192 + 192 <= MEGA_CODE_BYTES ? i * 192 : 0);
  asm volatile("s_load_dword %0, %1, 0x0\n\ts_load_dword %0, %1, 0x40\n\ts_load_dword %0, %1, 0x80" : "=&s"(d) : "s"(a) : "memory");
  return d;
}

struct MegaParams {
  char* ws;
  const u16* x;
  const u16* wqkvA;
  const u16* woA;
  const u16* wqkvB;
  const u16* woB;
  const u16* wphi;
  u16* out;
};

constexpr int QSLOT = 6 * 1024, QGRP = 4 * QSLOT;
constexpr int L_STG = QGRP;
constexpr int L_A2 = 16 * 1024, L_Z = 21 * 1024;
constexpr int L_RT = 4 * QGRP;
constexpr int L_PB = 0;
constexpr int L_RED = L_STG + 128 * 1024;
constexpr int L_STAMP = L_RED + 3072;
constexpr int MEGA_LDS = L_STAMP + 1536;
static_assert(MEGA_LDS <= 160 * 1024, "LDS map");

template <int I, int N, typename F> DI void static_for_(F&& f) {
  if constexpr (I < N) { f(IC<I>{}); static_for_<I + 1, N>(f); }
}
template <int N, typename F> DI void static_for(F&& f) { static_for_<0, N>(f); }

constexpr int NCHK = 3;
constexpr int RO = 8;
constexpr int SPIN_BUDGET = 1 << 15;

__global__ __launch_bounds__(768, 3) void mega_kernel(MegaParams p) {
  extern __shared__ __attribute__((aligned(16))) char smem[];
  const int tid = threadIdx.x;
  const int wave = __builtin_amdgcn_readfirstlane(tid >> 6);
  const int xcd = blockIdx.x & 7, idx = blockIdx.x >> 3;
  int bm = (xcd >> 2) * 4 + (idx >> 3), head = ((xcd & 1) * 2 + ((xcd >> 1) & 1)) * 8 + (idx & 7);
  int cm = (xcd >> 1) & 1, cn = (xcd & 1) * 8 + (idx & 7);
  const int bm0 = bm, head0 = head, cm0 = cm, cn0 = cn;
  char* ws = p.ws;
  const u16 *px = p.x, *pq = p.wqkvA;
  asm volatile("" : "+s"(ws), "+s"(px), "+s"(pq));
  const int swv = wave == SW0 ? 0 : wave == 4 ? 1 : wave == 11 ? 2 : -1; (void)swv;
  float* lmean = (float*)(smem + L_RED);
  float* lrstd = lmean + 272;
  float* lcs = lrstd + 272;
  unsigned* lflag = (unsigned*)lmean;
  const rsrc_t wsr = mk_rsrc(ws, WS_END);
  int budget = SPIN_BUDGET;
  unsigned tag0 = 0, tag1 = 0;
  auto ld_ep = [&]() AI { MEMFENCE; return __builtin_amdgcn_raw_buffer_load_b64(wsr, 0, WS_EP, 16); };
  auto mk_tag = [&](u32x2 ep) AI {
    tag0 = __builtin_amdgcn_readfirstlane(ep[0]) + 0x9e3779b9u;
    tag1 = __builtin_amdgcn_readfirstlane(ep[1]) ^ 0x85ebca6bu;
  };
  const unsigned sbase = (unsigned)(size_t)smem;
  int L = wave, cw = wave - 4;
#define OPAQUE_S asm volatile("" : "+s"(L), "+s"(bm), "+s"(head), "+s"(cm), "+s"(cn));
#define LANE_VARS asm volatile("" : "+s"(cw), "+s"(bm), "+s"(head), "+s"(cm), "+s"(cn)); int lane_ = __builtin_amdgcn_mbcnt_hi(~0u, __builtin_amdgcn_mbcnt_lo(~0u, 0u)); asm volatile("" : "+v"(lane_)); \
  const int lane = lane_, fr = lane & 15, fq = lane >> 4; const unsigned v16 = lane * 16; (void)fr; (void)fq; (void)v16;
  auto put_x = [&](u32x4* xr, unsigned v16, unsigned dst) AI {
    float sm = 0.f, sq = 0.f;
#pragma unroll
    for (int j = 0; j < 2; ++j) {
      xr[j][0] = umin(xr[j][0], POISON - 1); xr[j][2] = umin(xr[j][2], POISON - 1);
      st16s(wsr, v16, dst + j * 16384, xr[j]);
    }
    SCHED0;
#pragma unroll
    for (int j = 0; j < 2; ++j)
#pragma unroll
      for (int e = 0; e < 4; ++e) { sm = dot2(xr[j][e], 0x3f803f80u, sm); sq = dot2(xr[j][e], xr[j][e], sq); }
    xred2<false>(sm, sq);
    return u32x2{__float_as_uint(sm), __float_as_uint(sq)};
  };

#define RT_VARS const bool tq = L < 3; const int tsg = tq ? (bm * 2 + (head >> 4)) * 3 + L : bm * 2 + (head >> 4), ks = (head & 15) + 16 * h;
  auto ld_rt = [&](const u16* w, int h, unsigned lds) AI {
    RT_VARS
    LANE_VARS
    const unsigned rs = tq ? 6144 : 2048, D = 8 * rs - 1024, o = ks * 32 * rs + tsg * 128;
    blds_run<4>(mk_rsrc(w, rs * 1024), (lane >> 3) * rs + ((lane & 7) ^ ((lane >> 3) & 6)) * 16, sbase + lds, o, o + D, o + 2 * D, o + 3 * D);
  };
  auto retile = [&](int side, int h, unsigned lds, f32x4* rcs, int wp = -1) AI {
    RT_VARS
    LANE_VARS
    const unsigned dst = tq ? WS_WQ + side * (6 * MBY) + (unsigned)((((tsg & 15) * 2) * 32 + ks) * 6 + (tsg >> 4) * 2) * 1024
                            : WS_WO + side * (2 * MBY) + (unsigned)((tsg * 32 + ks) * 4) * 1024;
    const unsigned gstr = tq ? 32 * 6 * 1024 : 2048;
    const unsigned a0 = (lane >> 2) * 128 + ((lane >> 3) & 3) * 32 + (lane & 3) * 8;
    u32x4 fv[4];
    if (wp >= 0 && tsg < 32) {
      const f32x4 zero = f32x4{0.f, 0.f, 0.f, 0.f};
#pragma unroll
      for (int g = 0; g < 2; ++g) {
        const char* ra = smem + lds + fr * 128 + (((g * 4 + fq) ^ (fr & 6)) * 16);
        const bf16x8 w0 = *(const bf16x8*)ra, w1 = *(const bf16x8*)(ra + 2048);
#pragma unroll
        for (int u = 0; u < 2; ++u) {
          const char* pa = smem + wp + g * 2048 + (fq * 8 + (fr >> 2)) * 64 + u * 32 + (fr & 3) * 8;
          const u32x2 t0 = trread(pa), t1 = trread(pa + 256);
          const bf16x8 wf = as_frag(u32x4{t0[0], t0[1], t1[0], t1[1]});
          const f32x4 d0 = mfma16(w0, wf, zero), d1 = mfma16(w1, wf, zero);
          fv[g * 2 + u] = u32x4{pk2(d0[0], d0[1]), pk2(d0[2], d0[3]), pk2(d1[0], d1[1]), pk2(d1[2], d1[3])};
        }
      }
    } else {
#pragma unroll
      for (int pc = 0; pc < 4; ++pc) {
        const char* aa = smem + lds + (a0 ^ (pc * 32));
        const u32x2 r0_ = trread(aa), r1_ = trread(aa + 2048);
        fv[pc] = u32x4{r0_[0], r0_[1], r1_[0], r1_[1]};
      }
    }
#pragma unroll
    for (int pc = 0; pc < 4; ++pc) {
      const u32x4 v = fv[pc];
      st16s(wsr, v16 + (pc & 1) * 1024, dst + (pc >> 1) * gstr, v);
      if (rcs) rcs[pc] = mfma16(as_frag(u32x4{0x3f803f80u, 0x3f803f80u, 0x3f803f80u, 0x3f803f80u}), as_frag(v), rcs[pc]);
    }
    if (rcs && h == 1) {
      const f32x4 c0 = rcs[0], c1 = rcs[1], c2 = rcs[2], c3 = rcs[3];
      const float ca = fq & 1 ? c1[0] : c0[0], cb = fq & 1 ? c3[0] : c2[0], cc = fq & 2 ? cb : ca;
      if (tq) __builtin_amdgcn_raw_buffer_store_b32(__float_as_uint(cc), wsr, ((fq >> 1) * 1536 + (fq & 1) * 16 + fr) * 4,
                                                    WS_CS + ((side * 32 + (tsg & 15) * 2) * 1536 + (tsg >> 4) * 512 + (head & 15) * 32) * 4, 16);
    }
  };
  auto flag = [&](unsigned table) AI {
    if (LANEID == 0) __builtin_amdgcn_raw_buffer_store_b64(u32x2{tag0, tag1}, wsr, 0, table + (bm * 32 + head) * 8, 16);
  };
  auto poll = [&](unsigned table, auto between, int rows = 0) AI {
    LANE_VARS
    const int q = (umin(fq, 2) * 16 + (head >> 1)) * 43 >> 7;
    const unsigned vo = rows ? bm * 256 + (fr & rows) * 16 : fq == 3 ? bm * 256 + fr * 16 : q * 128 + (fr & 7) * 16;
    auto miss = [&](u32x4 f) AI { return __builtin_amdgcn_ballot_w64(((f[0] ^ tag0) | (f[1] ^ tag1) | (f[2] ^ tag0) | (f[3] ^ tag1)) != 0) != 0; };
    MEMFENCE;
    const u32x4 f0 = ld16s(wsr, vo, table);
    MEMFENCE;
    between();
    MEMFENCE;
    bool wait = miss(f0);
#pragma unroll 1
    while (wait && --budget > 0) {
      SLEEP(SLP_Q);
      MEMFENCE;
      wait = miss(ld16s(wsr, vo, table));
      asm volatile("" : "+s"(budget) :: "memory");
    }
    MEMFENCE;
  };
  auto ld_x = [&](int cx, unsigned lds) AI {
    LANE_VARS
    const unsigned o = (bm * 16 + (head >> 1)) * 32768 + cx * 128;
    blds_run<2>(mk_rsrc(px, 4 * MBY), (lane >> 3) * 2048 + ((lane & 7) ^ (lane >> 3)) * 16, sbase + lds, o, o + 15360);
  };
  auto st_x = [&](int cx, unsigned lds, int w) AI {
    LANE_VARS
    const int mt = bm * 16 + (head >> 1);
    u32x4 xr[2];
#pragma unroll
    for (int j = 0; j < 2; ++j) {
      const char* a = smem + lds + fr * 128 + (fq & 1) * 8;
      const u32x2 lo = *(const u32x2*)(a + ((j * 4 + (fq >> 1)) ^ (fr & 7)) * 16), hi = *(const u32x2*)(a + ((j * 4 + 2 + (fq >> 1)) ^ (fr & 7)) * 16);
      xr[j] = u32x4{lo[0], lo[1], hi[0], hi[1]};
    }
    const u32x2 stat = put_x(xr, v16, WS_AR + AR_X + xt_off(mt, cx * 2));
    if (fq == 0) *(u32x2*)(lmean + 16 + fr * 4 + w) = stat;
  };

  {
    const int lane = LANEID;
    const bool tq = wave < 4;
    const unsigned rs = tq ? 6144 : 2048, D = 8 * rs - 1024;
    const unsigned o = (tq ? (head & 15) * 32 : (bm * 16 + (head >> 1)) * 16) * rs + (tq ? (bm * 2 + (head >> 4)) * 3 + wave : (head & 1) * 4 + cw) * 128;
    const unsigned lds = sbase + L_PB + (tq ? 8192 + wave * 4096 : cw * 2048);
    const unsigned vo = (lane >> 3) * rs + ((lane & 7) ^ ((lane >> 3) & (tq ? 6 : 7))) * 16;
    const rsrc_t r = mk_rsrc(tq ? pq : px, tq ? 6 * MBY : 4 * MBY);
    if ((wave & 7) != 3 && wave < 8) blds_run<2>(r, vo, lds, o, o + D);
    if (wave < 3) {
      blds_run<2>(r, vo, lds + 2048, o + 2 * D + 2048, o + 3 * D + 2048);
      const int tsg = (bm * 2 + (head >> 4)) * 3 + wave;
      if (tsg < 32) glds_run<4>(lane * 16, (const char*)p.wphi + (tsg & 15) * 4096, sbase + L_PB + 45056 + wave * 4096);
    }
  }
  if (wave >= 8) {
    const unsigned cp = code_prefetch(idx * 9 + cw), cq = code_prefetch(idx * 9 + cw - 4);
    L = wave - 8;
    bar();
    ld_x(8 + (head & 1) * 4 + L, L_PB + 32768 + L * 2048);
    if (L < 3) ld_rt(pq, 1, L_PB + 20480 + L * 4096);
    sync_lds();
    asm volatile("; pi" :: "s"(cp), "s"(cq) : "memory");
    wait_vm<0>();
    PSTAMP(1);
    st_x(8 + (head & 1) * 4 + L, L_PB + 32768 + L * 2048, L * 64 + 2);
    if (L < 3) {
      const f32x4 t = *(const f32x4*)(smem + L_PB + 40960 + L * 1024 + LANEID * 16);
      f32x4 rca[4] = {{t[0], 0.f, 0.f, 0.f}, {t[1], 0.f, 0.f, 0.f}, {t[2], 0.f, 0.f, 0.f}, {t[3], 0.f, 0.f, 0.f}};
      retile(0, 1, L_PB + 20480 + L * 4096, rca, L_PB + 45056 + L * 4096);
    }
    asm volatile("s_waitcnt lgkmcnt(0)" ::: "memory");
    PSTAMP(2);
  } else if (wave >= 4) {
    wait_vm<0>();
    PSTAMP(1);
    st_x((head & 1) * 4 + cw, L_PB + cw * 2048, cw * 64);
    bar();
    wait_vm_lgkm0<0>();
    PSTAMP(2);
    bar();
    asm volatile("; pc" ::: "memory");
    SLEEP(SLP_E);
    L = 3;
    if (cw < 2) ld_rt(p.woA, cw, L_RT + 3 * 14336 + cw * 4096);
    if (cw == 2) ld_rt(p.woB, 0, L_RT + 3 * 14336 + 8192);
  } else if (wave < 3) {
    const u32x2 ep = ld_ep();
    wait_vm<1>();
    PSTAMP(1);
    f32x4 rca[4] = {};
    retile(0, 0, L_PB + 8192 + L * 4096, rca, L_PB + 45056 + L * 4096);
    *(f32x4*)(smem + L_PB + 40960 + L * 1024 + LANEID * 16) = f32x4{rca[0][0], rca[1][0], rca[2][0], rca[3][0]};
    bar();
    drain();
    mk_tag(ep);
    PSTAMP(2);
    sync_lds();
    asm volatile("; pl" ::: "memory");
    SLEEP(SLP_E);
    ld_rt(p.wqkvB, 0, L_RT + L * 14336);
    ld_rt(p.wqkvB, 1, L_RT + L * 14336 + 4096);
  } else {
    const u32x2 ep = ld_ep();
    const unsigned cp = code_prefetch(idx * 9 + 8);
    if (LANEID < 9) lflag[LANEID] = 0;
    wait_vm_lgkm0<0>();
    asm volatile("" :: "s"(cp));
    mk_tag(ep);
    bar();
    bar();
    flag(WS_F1);
    if (LANEID == 0) *(u32x2*)(lflag + 10) = u32x2{tag0, tag1};
    PSTAMP(2);
    SLEEP(SLP_P);
    poll(WS_F1, []() {});
    asm volatile("; pp" ::: "memory");
  }
  bar();
  PSTAMP(3);
  int role = wave;
  asm volatile("" : "+s"(role));

  if (__builtin_expect(role < 4, 1)) {
    const unsigned v16 = __builtin_amdgcn_mbcnt_hi(~0u, __builtin_amdgcn_mbcnt_lo(~0u, 0u)) * 16;
#define OPAQUE_LANE int lane = v16 >> 4; asm volatile("" : "+v"(lane));
    auto q_groupW = [&](int layer, int g, int pos) AI {
      const char* gW = ws + WS_WQ + (layer & 1) * (6 * MBY) + head * (192 * 1024) + (g * 4 + L) * 6144;
      const unsigned d = sbase + (pos * 4 + L) * QSLOT;
      glds_run<3, true>(v16, gW, d);
      glds_run<3, true>(v16, gW + 3072, d + 3072);
    };
    u32x4 wn[4][6];
    auto ld_wn = [&](int layer, int g0, int g1) AI {
      const unsigned so = WS_WQ + (layer & 1) * (6 * MBY) + head * (192 * 1024) + L * 6144;
#pragma unroll
      for (int g = g0; g < g1; ++g)
#pragma unroll
        for (int n = 0; n < 6; ++n) wn[g][n] = ld16s(wsr, v16 + (n % 3) * 1024, so + g * 24576 + (n / 3) * 3072);
      MEMFENCE;
    };
    auto wr_wn = [&](int g) AI {
#pragma unroll
      for (int n = 0; n < 6; ++n) *(u32x4*)(smem + (g * 4 + L) * QSLOT + n * 1024 + v16) = wn[g][n];
    };
    const u32x4 pz = u32x4{POISON, POISON, POISON, POISON};
    auto poison = [&](unsigned so) AI { st16s(wsr, v16, so, pz); };
    auto poison_o = [&]() AI {
      const unsigned o = WS_AR + AR_O + xt_off(bm * 16 + L * 4, head);
#pragma unroll
      for (int i = 0; i < 4; ++i) poison(o + i * 1024);
      STPAD;
    };
    auto poison_x = [&](int l) AI {
      if (L < NCHK) {
        const unsigned o = WS_AR + l * ARENA + AR_X + bm * (512 * 1024) + head * (NCHK * 2048) + L * 2048;
        poison(o); poison(o + 1024);
        STPAD;
      }
    };
    auto poison_s = [&](int l) AI {
      if (L == 3) {
        const unsigned o = WS_AR + l * ARENA + AR_KV + (unsigned)(bm * 32 + head) * KVREC;
        poison(o); poison(o + 1024);
        STPAD;
      }
    };
    auto poison_kv = [&](int l) AI {
      const unsigned kvo = WS_AR + l * ARENA + AR_KV + (unsigned)(bm * 32 + head) * KVREC;
      poison(kvo + L * 1024);
      if (L == (l ? 0 : 1)) st16s(wsr, v16 < 256 ? v16 : OOB, kvo + 4096, pz);
      STPAD;
    };
    q_groupW(0, 0, 0);
    q_groupW(0, 1, 1);
    auto put_l2 = [&](float sm, float sq, unsigned vo, unsigned so) AI {
      xred2<false>(sm, sq);
      if (v16 < 256) st16s(wsr, vo, so, u32x4{__float_as_uint(sm), tag0, __float_as_uint(sq), tag1});
      MEMFENCE;
    };
    auto put_st0 = [&]() AI {
      if (L == 0) {
        const f32x4 t = *(const f32x4*)((const char*)(lmean + 16) + v16);
        put_l2(t[0] + t[2], t[1] + t[3], v16, WS_AR + AR_L2 + (head & 1) * 32768 + (bm * 16 + (head >> 1)) * 256);
      }
    };
    wait_vm<6>();
    auto body = [&](auto kind, const int layer) AI {
      constexpr bool isB = decltype(kind)::value == 1, is0 = decltype(kind)::value == 0;
      const unsigned ar = WS_AR + layer * ARENA;
      f32x4 rcs[4] = {};
      auto sW = [&]() AI { return WS_WO + (layer & 1) * (2 * MBY) + cn * (128 * 1024); };
      auto stg = [&](unsigned off, auto n) AI { glds_run<decltype(n)::value, true>(v16, ws + sW() + off, sbase + L_STG + off); };
      auto stg7 = [&](unsigned o) AI { stg(o, IC<4>{}); stg(o + 4096, IC<3>{}); };
      auto stg6 = [&](unsigned o) AI { stg(o, IC<3>{}); stg(o + 3072, IC<3>{}); };

      auto extra = [&](int b) AI {
        if (!is0) return;
        const unsigned lu = L_RT + L * 14336;
        if (b < 7) { SCHED0; PRIO(3); SCHED0; }
        if (b == 1) retile(L < 3, 0, lu, rcs);
        if (b == 2 && L == 3) retile(0, 1, lu + 4096, nullptr);
        if (b == 6) { if (L < 3) retile(1, 1, lu + 4096, rcs); else ld_rt(p.woB, 1, lu + 4096); }
        if (b == 8 && L == 3) { retile(1, 0, lu + 8192, nullptr); retile(1, 1, lu + 4096, nullptr); }
        if (b < 7) { SCHED0; PRIO(0); SCHED0; }
      };
      auto extra2 = [&](int b) AI {
        if (!is0) return;
        if (b < 7) { SCHED0; PRIO(3); SCHED0; }
        if (b == 2) put_st0();
        if (b == 1) poison_o();
        if (b == 2) poison_kv(0);
        if (b == 5) poison_kv(2);
        if (b == 8) { poison_s(1); poison_x(1); }
        if (b == 10) { poison_x(2); poison_s(3); }
        if (b < 7) { SCHED0; PRIO(0); SCHED0; }
      };
      if (is0) sync_lds();
      STAMP(0);
      {
        OPAQUE_S
        OPAQUE_LANE
        u32x4 c0, c1;
        asm volatile("" : "=v"(c0), "=v"(c1));
        auto ldcs = [&]() AI {
          if (L > 0) {
            const unsigned so = WS_CS + (((layer & 1) * 32 + head) * 1536 + (L - 1) * 512) * 4;
            c0 = ld16s(wsr, v16, so); c1 = ld16s(wsr, v16 + 1024, so);
          }
          MEMFENCE;
        };
        auto wrcs = [&]() AI {
          if (L > 0) {
            f32x4 t = add4(__builtin_bit_cast(f32x4, c0), __builtin_bit_cast(f32x4, c1));
#pragma unroll
            for (int e = 0; e < 4; ++e)
              t[e] += __uint_as_float(__builtin_amdgcn_update_dpp(0u, __float_as_uint(t[e]), 0x128, 0xf, 0xf, false));
            float t0 = t[0], t1 = t[1], t2 = t[2], t3 = t[3];
            xred4<false>(t0, t1, t2, t3);
            *(f32x4*)(lcs + (isB ? 96 : 0) + (L - 1) * 32 + (lane & 7) * 4) = f32x4{t0, t1, t2, t3};
          }
        };
        if (isB && layer == 1) ldcs();
        if constexpr (is0) {
          if (L == 3) flag(WS_F1 + 2048);
          q_groupW(0, 2, 2); q_groupW(0, 3, 3);
          wait_vm<12>();
        }
        STAMP(1);
        if (!is0) PRIO(3);
        sync_lds();
        STAMP(2);
        OPAQUE_S
        u32x4 s0 = u32x4{0u, 0u, 0u, 0u}, s1 = s0;
        auto ldst = [&]() AI {
          const unsigned so = ar + AR_L2 + (bm * 16 + L * 4) * 256;
          s0 = ld16s(wsr, v16, so);
          if (is0) s1 = ld16s(wsr, v16, so + 32768);
          if (isB && L == 0) s1 = ld16s(wsr, v16 & 255, bm > 0 ? so - 256 : so);
          MEMFENCE;
        };
        auto fin = [&](float sm, float sq, int r, bool own) AI {
          float mean = sm * (1.0f / 1024.0f);
          float var = fmaxf(__builtin_fmaf(sq, 1.0f / 1024.0f, -mul1(mean, mean)), 0.f);
          if (own) lmean[r] = -mean;
          lrstd[r] = __builtin_amdgcn_rsqf(var + 1e-5f);
        };
        auto wrst = [&]() AI {
          while (true) {
            unsigned d = (s0[1] ^ tag0) | (s0[3] ^ tag1);
            if (is0 || (isB && L == 0)) d |= (s1[1] ^ tag0) | (s1[3] ^ tag1);
            if (__builtin_expect(__builtin_amdgcn_ballot_w64(d != 0) == 0, 1) || --budget <= 0) break;
            COUNT(1);
            ldst();
          }
          MEMFENCE;
          float sm = __uint_as_float(s0[0]), sq = __uint_as_float(s0[2]);
          if (is0) { sm += __uint_as_float(s1[0]); sq += __uint_as_float(s1[2]); }
          fin(sm, sq, 16 + L * 64 + lane, true);
          if (isB && L == 0) fin(__uint_as_float(s1[0]), __uint_as_float(s1[2]), lane & 15, false);
        };
        auto q_group = [&](int g) AI {
          if (is0) {
            if (L & 1) SLEEP(SPR0);
            if (L & 2) SLEEP(2 * SPR0);
            q_groupW(layer, g, g & 3);
          } else if (g >= 5) q_groupW(layer, g, g < 6 ? g : g - 4);
        };
        if (!is0 && L == 0) {
          poll(ar + AR_FL, []() {}, 15);
          if (head < 16) {
            const unsigned so = ar + AR_ST + (bm * 16 + head) * 2048;
            s0 = ld16s(wsr, v16, so); s1 = ld16s(wsr, v16 + 1024, so);
            MEMFENCE;
          }
        }
        if (is0) {
          drain();
          poll(WS_F1 + 2048, [&]() AI { extra(1); });
          ldcs();
          extra2(1);
        }
        if (isB && layer == 1) wrcs();
        MEMFENCE;
        STAMP(3);
        if (!is0) PRIO(0);
        sync_lds();
        STAMP(4);
        OPAQUE_S
        if (!is0) drain();
        if (!is0 && L == 0 && head < 16) {
          f32x4 t = add4(__builtin_bit_cast(f32x4, s0), __builtin_bit_cast(f32x4, s1));
#pragma unroll
          for (int e = 0; e < 4; ++e)
            t[e] += __uint_as_float(__builtin_amdgcn_update_dpp(0u, __float_as_uint(t[e]), 0x128, 0xf, 0xf, false));
          const bool hi = lane & 8;
          put_l2(hi ? t[2] : t[0], hi ? t[3] : t[1], (lane & 7) * 32 + (lane & 8) * 2, ar + AR_L2 + (bm * 16 + head) * 256);
        }
        q_group(4);
        extra(2); extra2(2);
        STAMP(5);
        sync_lds();
        STAMP(6);
        OPAQUE_S
        drain();
        q_group(5);
        if (is0) wrcs();
        STAMP(7);
        bar();
        OPAQUE_S
        drain();
        if (is0 && L == 0 && v16 == 0) __builtin_amdgcn_raw_buffer_store_b64(u32x2{tag0, tag1}, wsr, 0, WS_F2 + (bm * 32 + head) * 8, 16);
        q_group(6);
        ldst();
        STAMP(8);
        bar();
        STAMP(9);
        OPAQUE_S
        drain();
        q_group(7);
        extra2(5);
        SCHED0;
        wrst();
        STAMP(10);
        bar();
        STAMP(11);
        OPAQUE_S
        drain();
        extra(6);
        if (isB) stg6(96 * 1024 + L * 6144);
        SCHED0;
      }
      STAMP(12);
      sync_lds();
      STAMP(13);
      OPAQUE_S
      u32x4 fl[2];
      fl[0] = fl[1] = u32x4{tag0, tag1, tag0, tag1};
      auto ldfl = [&]() AI { fl[0] = ld16s(wsr, v16, WS_F2); fl[1] = ld16s(wsr, v16 + 1024, WS_F2); };
      if (is0) ldfl();
      bar();
      constexpr int NR = isB ? 6 : 9;
      const unsigned ro = isB ? 48 * 1024 + L * 6144 : L * 9216;
      u32x4 r[NR];
      bool pend = false;
      auto flags = [&]() AI {
        const u32x4 d = (fl[0] ^ u32x4{tag0, tag1, tag0, tag1}) | (fl[1] ^ u32x4{tag0, tag1, tag0, tag1});
        pend = __builtin_amdgcn_ballot_w64((d[0] | d[1] | d[2] | d[3]) != 0) != 0;
      };
      if (isB) {
        stg6(L * 6144); stg(120 * 1024 + L * 2048, IC<2>{});
        stg6(24 * 1024 + L * 6144);
        stg6(72 * 1024 + L * 6144);
      } else {
        drain();
        if (is0) flags();
        MEMFENCE;
        extra(8); extra2(8);
        if (!is0) poison_x(3);
        while (is0 && __builtin_expect(pend, 0) && --budget > 0) {
          ldfl();
          flags();
          MEMFENCE;
        }
        stg(36 * 1024 + L * 3072, IC<3>{}); stg7(72 * 1024 + L * 14336); stg7(72 * 1024 + L * 14336 + 7168);
      }
#pragma unroll
      for (int i = 0; i < NR; ++i) r[i] = ld16s(wsr, v16 + (i & 3) * 1024, sW() + ro + (i >> 2) * 4096);
      MEMFENCE;
      if (is0 && bm + head + L == 0 && v16 == 0) __builtin_amdgcn_raw_buffer_store_b32(tag0, wsr, 0, WS_EP, 16);
      auto wr_r = [&]() AI {
        unsigned vv = v16;
        asm volatile("" : "+v"(vv));
#pragma unroll
        for (int i = 0; i < NR; ++i) *(u32x4*)(smem + L_STG + ro + i * 1024 + vv) = r[i];
      };
      auto left = [&]() AI {
        OPAQUE_LANE
        for (;;) {
          MEMFENCE;
          if (__builtin_amdgcn_ballot_w64(lflag[lane & 7] != (unsigned)layer) == 0 || --budget <= 0) break;
          SLEEP(2);
        }
        MEMFENCE;
      };
      if (isB) {
#pragma unroll
        for (int i = 0; i < 24; ++i) asm volatile("" : "=v"(wn[i / 6][i % 6]));
        if (layer == 1) ld_wn(layer + 1, 0, 2);
        STAMP(14);
        left();
        wr_r();
        MEMFENCE;
        if (layer == 1) ld_wn(layer + 1, 2, 4);
        wait_vm_lgkm0<12>();
        if (layer == 3) wait_vm<0>();
      } else {
        ld_wn(layer + 1, 0, is0 ? 1 : 2);
        if (is0) wait_vm<32>();
        STAMP(14);
        bar();
        stg6(48 * 1024 + L * 6144);
        if (is0) ld_wn(layer + 1, 1, 2);
        bar();
        ld_wn(layer + 1, 2, 4);
        wr_r();
        wait_vm<12>();
        bar();
        extra2(10);
        wait_vm_lgkm0<0>();
      }
      bar();
      if (isB && layer == 3) { STAMP_DUMP; __builtin_amdgcn_endpgm(); }
      OPAQUE_S
      wr_wn(0);
      drain();
      sync_lds();
      OPAQUE_S
      SCHED0;
#pragma unroll
      for (int g = 1; g < 4; ++g) wr_wn(g);
      MEMFENCE;
      SLEEP(SLP_G);
      q_groupW(layer + 1, 4, 4);
    };
    body(IC<0>{}, 0);
#pragma unroll 1
    for (int layer = 1;; layer += 2) {
      body(IC<1>{}, layer);
      body(IC<2>{}, 2);
    }
  }

  const f32x4 zero = f32x4{0.f, 0.f, 0.f, 0.f};
  u32x4 xr[2];
  asm volatile("" : "=v"(xr[0]), "=v"(xr[1]));
  u32x4 sel;
  u32x4 eye;
  unsigned vk;
  unsigned ra0, ra1;
  float off0, off1;
  {
    const int lane = LANEID, fr = lane & 15, fq = lane >> 4;
    const bool odd = fr & 1, own0 = fr >= fq * 4 + (fr & 1), own1 = fr >= fq * 4 + (fr & 1) + 2, nopre = bm0 == 0 && wave == 4;
    const unsigned e0 = odd ? 0x01000c0cu : 0x0c0c0100u, e1 = odd ? 0x03020c0cu : 0x0c0c0302u, ez = 0x0c0c0c0cu;
    sel = u32x4{own0 ? ez : e0, own1 ? ez : e1, own0 ? e0 : ez, own1 ? e1 : ez};
    const unsigned rb = sbase + L_RED + 1088 + ((wave - 4) * 32 + fq * 4 + (fr & 1)) * 4;
    ra0 = rb + (own0 ? 64 : 0); ra1 = rb + (own1 ? 72 : 8);
    off0 = nopre && !own0 ? -3.0e38f : 0.f; off1 = nopre && !own1 ? -3.0e38f : 0.f;
    const unsigned one = (fq & 1) == (fr >> 3) ? (fr & 1 ? 0x3f800000u : 0x00003f80u) : 0u;
    eye = u32x4{(fr & 6) == 0 ? one : 0u, (fr & 6) == 2 ? one : 0u, (fr & 6) == 4 ? one : 0u, (fr & 6) == 6 ? one : 0u};
    vk = (fq >> 1) * (32 * KVREC) + (fq & 1) * 256 + fr * 16;
    asm volatile("" : "+v"(sel), "+v"(ra0), "+v"(ra1), "+v"(off0), "+v"(off1), "+v"(eye), "+v"(vk));
  }
#pragma unroll 1
  for (int layer = 0; layer < 4; ++layer) {
    bm = bm0; head = head0; cm = cm0; cn = cn0; cw = wave - 4;
    const bool isB = layer & 1;
    const unsigned ar = WS_AR + layer * ARENA;
    {
      f32x4 acc[2][6];
      f32x4 ck[2], mv[2], xv[2];
      float mean[2], rs[2], cv[2];
      const float* lcl = lcs + (isB ? 96 : 0);
      {
        LANE_VARS
        const unsigned va = v16 + cw * 2048;
        const unsigned sa0 = ar + AR_X + bm * (512 * 1024);
        auto lda = [&](unsigned so, u32x4* a) AI {
          a[0] = ld16s(wsr, va, so);
          a[1] = ld16s(wsr, va + 1024, so);
        };
        bf16x8 f[2][6];
        u32x4 a[4][2];
        auto unset = [&](const u32x4* e) AI { return poisoned(umax(umax(umax(e[0][0], e[0][2]), e[1][0]), e[1][2])); };
        auto refill = [&](auto ic, unsigned cur) AI {
          constexpr int I = decltype(ic)::value;
          do {
            COUNT(0);
            SLEEP(SLP_R);
            MEMFENCE;
            lda(cur, a[I]);
            bool u[4];
#pragma unroll
            for (int j = 1; j < 4; ++j) u[j] = unset(a[(I + j) % 4]);
            SCHED0;
#pragma unroll
            for (int j = 1; j < 4; ++j)
              if (u[j]) lda(cur + j * 16384, a[(I + j) % 4]);
            __builtin_amdgcn_s_waitcnt(0x0f70);
            MEMFENCE;
          } while (unset(a[I]) && --budget > 0);
        };
        STAMP(0);
        if (layer > 0) {
          bar();
          if (cn < 8) SLEEP(SLP_X); else SLEEP(SLP_XD);
        }
#pragma unroll
        for (int j = 0; j < 4; ++j) { lda(sa0 + j * 16384, a[j]); SCHED0; }
#pragma unroll
        for (int m = 0; m < 2; ++m)
#pragma unroll
          for (int n = 0; n < 6; ++n) acc[m][n] = zero;
        asm volatile("" : "+v"(acc[0][0]), "+v"(acc[0][1]), "+v"(acc[0][2]), "+v"(acc[0][3]), "+v"(acc[0][4]), "+v"(acc[0][5]));
        asm volatile("" : "+v"(acc[1][0]), "+v"(acc[1][1]), "+v"(acc[1][2]), "+v"(acc[1][3]), "+v"(acc[1][4]), "+v"(acc[1][5]));
        if (layer == 0) { wait_vm<8>(); sync_lds(); }
#pragma unroll
        for (int nn = 0; nn < 6; ++nn) { const int n = nn < 4 ? nn + 2 : nn - 4; f[0][n] = *(const bf16x8*)(smem + v16 + n * 1024); }
        SCHED0;
        if (layer > 0) {
          const u32x2 tg = *(const u32x2*)(lflag + 10);
          wait_vm<8>();
          unsigned old;
          asm volatile("s_mov_b64 exec, 1\n\tds_add_rtn_u32 %0, %1, %2\n\ts_mov_b64 exec, -1\n\ts_waitcnt lgkmcnt(0)" : "=v"(old) : "v"(sbase + L_RED + 32), "v"(1) : "memory");
          if (__builtin_amdgcn_readfirstlane(old) == 8u * layer - 1)
            __builtin_amdgcn_raw_buffer_store_b64(tg, wsr, lane ? OOB : 0, ar + AR_FL + bm * 256 + (cm * 16 + cn) * 8, 16);
          MEMFENCE;
        }
        SCHED0;
        bool bad = layer > 0 && unset(a[0]);
        const int wrap = layer > 0 ? 6 : 4;
        unsigned ps[4] = {sbase + L_RED + 64 + cw * 128, sbase + L_RED + 2176 + (layer & 1) * 384, 0, WS_AR + AR_X + xt_off(bm * 16 + cw * 2 + cm, cn * 2)};
        ps[2] = ps[0] + 1088 + (layer & 1) * (2176 + 384 - 1088 - 64 - cw * 128);
        int early = (layer & 1) * cw;
        asm volatile("" : "+s"(ps[0]), "+s"(ps[1]), "+s"(ps[2]), "+s"(ps[3]), "+s"(early));
        STAMP(1);
        auto group = [&](auto modec, int g) AI {
          constexpr int MODE = decltype(modec)::value;
          constexpr bool LAST = MODE == 3, CHK = MODE == 0;
          const unsigned gs = (g < wrap ? g : g - 4) * QGRP, gn = (g + 1 < wrap ? g + 1 : g - 3) * QGRP;
          const unsigned sc = sa0 + g * 65536, so = sc + 4 * 16384;
          unsigned gl = sbase + gs + v16;
          if constexpr (LAST) asm volatile("" : "+v"(gl));
          unsigned pa[4];
          static_for<4>([&](auto ic) AI {
            constexpr int i = decltype(ic)::value;
            constexpr bool END = LAST && i == 3;
            SCHED0;
            if (i == 3 && !LAST) { sync_lds(); SCHED0; }
            if (i == 0 && LAST) {
              PRIO(1);
              if (early >= 4) PRIO(3);
              SCHED0;
              bar();
              SCHED0;
            }
            if constexpr (!LAST) PRIO(i == 3 ? 3 : 2 - i);
            if constexpr (CHK) { if (__builtin_expect(bad, 0)) refill(ic, sc + i * 16384); }
            SCHED0;
            const bf16x8* fc = f[i & 1];
            bf16x8* fn = f[(i + 1) & 1];
            const u32x4* an = a[(i + 1) % 4];
            const unsigned slot = i < 3 ? gs + (i + 1) * QSLOT : gn;
            const char* pb = LAST ? (const char*)((const LDS(char)*)gl + (i + 1) * QSLOT) : smem + slot + v16;
            unsigned mx = 0;
            static_for<12>([&](auto jc) AI {
              constexpr int j = decltype(jc)::value, nn = j >> 1, m = j & 1, n = nn < 4 ? nn + 2 : nn - 4;
              if constexpr (n < 4) mfma_acc(acc[m][n], fc[n], as_frag(a[i][m]));
              else mfma_acc(acc[m][n], as_frag(a[i][m]), fc[n]);
              SCHED0;
              if constexpr (!END && j < 6) { constexpr int r = j < 4 ? j + 2 : j - 4; fn[r] = *(const bf16x8*)(pb + r * 1024); }
              if constexpr (LAST && i == 0 && j == 6) {
                if (layer == 0) { xr[0] = ld16s(wsr, v16, ps[3]); xr[1] = ld16s(wsr, v16, ps[3] + 16384); }
              }
              if constexpr (LAST && i == 1) {
                if constexpr (j == 6) { pa[0] = lane & 48; asm volatile("" : "+v"(pa[0])); }
                if constexpr (j == 7) { pa[1] = v16 >> 2; asm volatile("" : "+v"(pa[1])); }
                if constexpr (j == 8) { pa[1] &= 60; asm volatile("" : "+v"(pa[1])); }
                if constexpr (j == 9) { pa[2] = pa[1] + ps[0]; asm volatile("" : "+v"(pa[2])); }
                if constexpr (j == 10) { pa[1] += ps[1]; asm volatile("" : "+v"(pa[1])); }
                if constexpr (j == 11) { pa[3] = pa[0] + ps[1]; asm volatile("" : "+v"(pa[3])); }
              }
              if constexpr (LAST && i == 2) {
                if constexpr (j == 6) ck[0] = ((const LDS(f32x4)*)pa[3])[8];
                if constexpr (j == 7) ck[1] = ((const LDS(f32x4)*)pa[3])[12];
                if constexpr (j == 8) { mean[0] = ((const LDS(float)*)pa[2])[0]; asm volatile("" : "+v"(pa[2])); }
                if constexpr (j == 9) { mean[1] = ((const LDS(float)*)pa[2])[16]; asm volatile("" : "+v"(pa[2])); }
                if constexpr (j == 10) { cv[0] = ((const LDS(float)*)pa[1])[64]; cv[1] = ((const LDS(float)*)pa[1])[80]; }
                if constexpr (j == 11) { pa[1] = pa[0] + ps[0]; asm volatile("" : "+v"(pa[1])); }
              }
              if constexpr (LAST && i == 3) {
                if constexpr (j == 0) mv[0] = ((const LDS(f32x4)*)pa[1])[0];
                if constexpr (j == 1) mv[1] = ((const LDS(f32x4)*)pa[1])[4];
                if constexpr (j == 2) { pa[0] += ps[2]; asm volatile("" : "+v"(pa[0])); }
                if constexpr (j == 3) { rs[0] = ((const LDS(float)*)pa[2])[272]; asm volatile("" : "+v"(pa[2])); }
                if constexpr (j == 4) xv[0] = ((const LDS(f32x4)*)pa[0])[0];
                if constexpr (j == 5) xv[1] = ((const LDS(f32x4)*)pa[0])[4];
                if constexpr (j == 6) rs[1] = ((const LDS(float)*)pa[2])[288];
              }
              if constexpr (CHK) {
                if constexpr (j == 8) mx = umax(umax(umax(an[0][0], an[0][2]), an[1][0]), an[1][2]);
                if constexpr (j == 10) bad = poisoned(mx);
              }
              if constexpr (j == 10 && !LAST) a[i][0] = ld16s(wsr, va, so + i * 16384);
              if constexpr (j == 11 && !LAST) a[i][1] = ld16s(wsr, va + 1024, so + i * 16384);
              SCHED0;
            });
          });
        };
        int g = 0;
        if (layer > 0) {
#pragma unroll 1
          for (; g < NCHK; ++g) group(IC<0>{}, g);
        }
#pragma unroll 1
        for (; g < 7; ++g) group(IC<1>{}, g);
        group(IC<3>{}, 7);
        if (!isB && cw >= 4) PRIO(2);
        mfma_done();
      }
      STAMP(2);
      LANE_VARS
      char* const scr = smem;

      u32x4 qf[2], kf[2];
      u32x2 vf[2][2];
      auto fold_rows = [&](int part, u32x4* out, const f32x4* c) AI {
#pragma unroll
        for (int m = 0; m < 2; ++m) {
          f32x4 a0 = acc[m][part * 2] + c[0] * mean[m], a1 = acc[m][part * 2 + 1] + c[1] * mean[m];
          out[m] = u32x4{pk2(a0[0], a0[1]), pk2(a0[2], a0[3]), pk2(a1[0], a1[1]), pk2(a1[2], a1[3])};
        }
      };
      auto fold_x = [&](int part, f32x2 (*x)[4], const f32x4* c) AI {
#pragma unroll
        for (int m = 0; m < 2; ++m) {
          const f32x4 a0 = acc[m][part * 2] + c[0] * mean[m], a1 = acc[m][part * 2 + 1] + c[1] * mean[m];
          x[m][0] = f32x2{a0[0], a0[1]}; x[m][1] = f32x2{a0[2], a0[3]}; x[m][2] = f32x2{a1[0], a1[1]}; x[m][3] = f32x2{a1[2], a1[3]};
        }
      };
      auto fold_v = [&](bool scaled) AI {
#pragma unroll
        for (int m = 0; m < 2; ++m) {
          f32x4 a0 = acc[m][4] + mv[m] * cv[0], a1 = acc[m][5] + mv[m] * cv[1];
          if (scaled) { a0 = mul4(a0, xv[m]); a1 = mul4(a1, xv[m]); }
          vf[m][0] = u32x2{pk2(a0[0], a0[1]), pk2(a0[2], a0[3])};
          vf[m][1] = u32x2{pk2(a1[0], a1[1]), pk2(a1[2], a1[3])};
        }
      };
      const unsigned otile = WS_AR + AR_O + xt_off(bm * 16 + cw * 2, head);
      auto put_o = [&](int m, f32x4 a, f32x4 b, unsigned opar) AI {
        const u32x4 v = u32x4{(pk2(a[0], a[1]) & ~1u) | opar, pk2(a[2], a[3]), (pk2(b[0], b[1]) & ~1u) | opar, pk2(b[2], b[3])};
        st16s(wsr, v16 + m * 1024, otile, v);
        return v;
      };
      auto hold = [&](u32x4 a, u32x4 b) AI { SCHED0; asm volatile("s_nop 1" :: "v"(a), "v"(b)); SCHED0; };

      if (!isB) {
        f32x2 lx[2][4];
        fold_x(1, lx, ck); fold_v(true);
        char* PK = smem + L_STG;
        char* VF = scr;
#pragma unroll
        for (int t = 0; t < 2; ++t)
          *(u32x4*)(VF + (cw * 2 + t) * 1024 + lane * 16) = u32x4{vf[0][t][0], vf[0][t][1], vf[1][t][0], vf[1][t][1]};
        {
          u32x4 pk[2][2];
          phi_map2(lx, rs, pk, [](float*) {});
          char* d0 = PK + cw * 4096 + fr * 64 + ((fr >> 2) & 1) * 32 + ((fq ^ ((fr >> 3) * 2)) * 8);
          char* d1 = PK + cw * 4096 + fr * 64 + (((fr >> 2) & 1) ^ 1) * 32 + ((fq ^ ((fr >> 3) * 2)) * 8);
#pragma unroll
          for (int m = 0; m < 2; ++m) {
            *(u32x2*)(d0 + m * 2048) = u32x2{pk[m][0][0], pk[m][0][1]};
            *(u32x2*)(d0 + m * 2048 + 1024) = u32x2{pk[m][0][2], pk[m][0][3]};
            *(u32x2*)(d1 + m * 2048) = u32x2{pk[m][1][0], pk[m][1][1]};
            *(u32x2*)(d1 + m * 2048 + 1024) = u32x2{pk[m][1][2], pk[m][1][3]};
          }
        }
        PRIO(1);
        const unsigned rec = ar + AR_KV + (unsigned)head * KVREC, dst = rec + (unsigned)bm * (32 * KVREC);
        unsigned ad = sbase + L_STG + (cw & 1) * 1024 + (fq * 4 + (fr >> 2)) * 64 + (((cw >> 1) ^ fq) & 1) * 32 + (((fr & 3) ^ (fq & 2)) * 8);
        unsigned vd = sbase + (cw >> 2) * 1024 + v16, kd = fq & 1 ? OOB : dst + cw * 512 + (fq >> 1) * 256 + fr * 16;
        asm volatile("" : "+v"(ad), "+v"(vd), "+v"(kd));
        sync_lds();
        STAMP(3);
        {
          const u32x4 ones = u32x4{0x3f803f80u, 0x3f803f80u, 0x3f803f80u, 0x3f803f80u};
          f32x4 c0 = zero;
          bf16x8 a[8], b0[8];
#pragma unroll
          for (int ks = 0; ks < 8; ++ks) {
            u32x2 a0 = trl(ad + ks * 4096), a1 = trl(ad + ks * 4096 + 2048);
            a[ks] = as_frag(u32x4{a0[0], a0[1], a1[0], a1[1]});
            b0[ks] = *(const LDS(bf16x8)*)(vd + ks * 2048);
            SCHED0;
          }
#pragma unroll
          for (int ks = 0; ks < 8; ++ks) c0 = mfma16(a[ks], b0[ks], c0);
          const unsigned p0 = umin(pk2(c0[0], c0[1]), POISON - 1), p1 = umin(pk2(c0[2], c0[3]), POISON - 1);
          const auto q0 = __builtin_amdgcn_permlane16_swap(p0, p0, false, false), q1 = __builtin_amdgcn_permlane16_swap(p1, p1, false, false);
          st16(wsr, kd, u32x4{q0[0], q1[0], q0[1], q1[1]});
          SCHED0;
          if (cw < 4) {
            f32x4 cz = zero;
#pragma unroll
            for (int ks = 0; ks < 8; ++ks) cz = mfma16(a[ks], as_frag(ones), cz);
            st16(wsr, fr ? OOB : dst + 4096 + cw * 64 + fq * 16, clean(__builtin_bit_cast(u32x4, cz)));
          }
        }
        SCHED0;
        STAMP(4);
        bar();
        const f32x4 cq[2] = {*(const f32x4*)(lcl + fq * 4), *(const f32x4*)(lcl + 16 + fq * 4)};
        fold_x(0, lx, cq);
        u32x4 pq[2][2];
        u32x4 zp, zq;
        u32x4 part[4];
        auto ldp = [&](int i) AI { part[i] = ld16s(wsr, vk, rec + i * (64 * KVREC) + cw * 512); };
        auto ldz = [&]() AI {
          zp = ld16(wsr, rec + fq * (32 * KVREC) + 4096 + fr * 16);
          zq = ld16(wsr, rec + (4 + fq) * (32 * KVREC) + 4096 + fr * 16);
        };
#pragma unroll
        for (int i = 0; i < 4; ++i) asm volatile("" : "=v"(part[i]));
        asm volatile("" : "=v"(zp), "=v"(zq));
        phi_map2(lx, rs, pq, [&](float* sp) AI {
          asm volatile("" : "+v"(sp[0]), "+v"(sp[1]));
          SCHED0;
          if (cw >= 4) {
            MEMFENCE;
#pragma unroll
            for (int i = 0; i < 4; ++i) ldp(i);
            MEMFENCE;
          }
          SCHED0;
        });
        asm volatile("" : "+v"(pq[0][0]), "+v"(pq[0][1]), "+v"(pq[1][0]), "+v"(pq[1][1]));
        SCHED0;
        STAMP(5);
        char* kvf = smem + L_A2;
        if (cw < 4) {
          SLEEP(SLP_K);
          MEMFENCE;
          if (cw == 0) ldz();
#pragma unroll
          for (int i = 0; i < 4; ++i) ldp(i);
          MEMFENCE;
        }
        unsigned kw = sbase + L_A2 + v16 + (cw >> 1) * 1024 + (cw & 1) * 8;
        unsigned ka = sbase + L_A2 + v16, za = sbase + L_Z + fq * 16;
        asm volatile("" : "+v"(kw), "+v"(ka), "+v"(za));
        u32x2 kvs;
        auto nans = [&](f32x4 v) AI { return __builtin_amdgcn_ballot_w64(__builtin_isunordered(v[0], v[1])) | __builtin_amdgcn_ballot_w64(__builtin_isunordered(v[2], v[3])); };
        auto sums = [&]() AI {
          unsigned long long nan = 0;
          if (cw == 0) {
            f32x4 z = __builtin_bit_cast(f32x4, zp) + __builtin_bit_cast(f32x4, zq);
            nan = nans(z);
            float z0 = z[0], z1 = z[1], z2 = z[2], z3 = z[3];
            xred4<false>(z0, z1, z2, z3);
            *(u32x2*)(smem + L_Z + (fr >> 3) * 64 + (fr & 3) * 16 + ((fr >> 2) & 1) * 8) = u32x2{pk2(z0, z1), pk2(z2, z3)};
          }
          f32x4 s4 = zero;
#pragma unroll
          for (int i = 0; i < 4; ++i) { s4 = mfma16(as_frag(eye), as_frag(part[i]), s4); SCHED0; }
          kvs = u32x2{pk2(s4[0], s4[1]), pk2(s4[2], s4[3])};
          asm volatile("" : "+v"(kvs));
          return nan | __builtin_amdgcn_ballot_w64(s4[0] != s4[0]);
        };
        if (__builtin_expect(sums() != 0, 0)) {
          for (;;) {
            bool again = false;
            if (cw == 0 && poisoned(umax4(umax4(zp), zq))) { again = true; ldz(); }
#pragma unroll
            for (int i = 0; i < 4; ++i)
              if (poisoned(umax4(part[i]))) { again = true; ldp(i); }
            __builtin_amdgcn_s_waitcnt(0x0f70);
            MEMFENCE;
            if (!again || --budget <= 0) break;
            COUNT(2);
          }
          sums();
        }
        STAMP(6);
        *(LDS(u32x2)*)kw = kvs;
        sync_lds();
        STAMP(7);
        bf16x8 kvr[2][2], zf[2];
#pragma unroll
        for (int s = 0; s < 2; ++s) { zf[s] = *(const LDS(bf16x8)*)(za + s * 64); SCHED0; }
#pragma unroll
        for (int s = 0; s < 2; ++s) {
          kvr[0][s] = *(const LDS(bf16x8)*)(ka + (0 * 2 + s) * 1024);
          kvr[1][s] = *(const LDS(bf16x8)*)(ka + (1 * 2 + s) * 1024);
          SCHED0;
        }
        f32x4 cd[2], c[2][2];
        u32x4 oo[2];
        auto den = [&](int m, int s) AI { cd[m] = mfma16(zf[s], as_frag(pq[m][s]), s ? cd[m] : zero); SCHED0; };
        auto num = [&](int m, int s) AI {
#pragma unroll
          for (int t = 0; t < 2; ++t) { c[m][t] = mfma16(kvr[t][s], as_frag(pq[m][s]), s ? c[m][t] : zero); SCHED0; }
        };
        auto fin = [&](int m, float id) AI { const f32x4 i4 = f32x4{id, id, id, id}; oo[m] = put_o(m, mul4(c[m][0], i4), mul4(c[m][1], i4), 0); SCHED0; };
        den(0, 0); den(1, 0); den(0, 1); den(1, 1);
        num(0, 0); num(0, 1); num(1, 0);
        const float i0 = __builtin_amdgcn_rcpf(cd[0][0] + 1e-6f), i1 = __builtin_amdgcn_rcpf(cd[1][0] + 1e-6f);
        SCHED0;
        num(1, 1);
        fin(0, i0); fin(1, i1);
        hold(oo[0], oo[1]);
        STAMP(8);
        asm volatile("" : "=v"(acc[0][0]), "=v"(acc[0][1]), "=v"(acc[0][2]), "=v"(acc[0][3]), "=v"(acc[0][4]), "=v"(acc[0][5]));
        asm volatile("" : "=v"(acc[1][0]), "=v"(acc[1][1]), "=v"(acc[1][2]), "=v"(acc[1][3]), "=v"(acc[1][4]), "=v"(acc[1][5]));
        asm volatile("" : "=v"(ck[0]), "=v"(ck[1]), "=v"(mv[0]), "=v"(mv[1]), "=v"(xv[0]), "=v"(xv[1]));
        asm volatile("" : "=v"(mean[0]), "=v"(mean[1]), "=v"(rs[0]), "=v"(rs[1]), "=v"(cv[0]), "=v"(cv[1]));
      }
      int kind = layer & 1;
      asm volatile("" : "+s"(kind));
      if (kind) {
        char* ex = scr;
        fold_rows(1, kf, ck); fold_v(false);
        const unsigned hrec = ar + AR_KV + (unsigned)(bm * 32 + head) * KVREC;
        {
          const u32x4 vl = u32x4{vf[1][0][0], vf[1][0][1], vf[1][1][0], vf[1][1][1]};
          if (cw == 7) {
            st16s(wsr, v16, hrec, clean(kf[1]));
            STPAD;
            st16s(wsr, v16 + 1024, hrec, clean(vl));
            STPAD;
          }
          char* e2 = ex + (cw + 1) * 2048;
          *(u32x4*)(e2 + lane * 16) = kf[1];
          *(u32x2*)(e2 + 1024 + lane * 8) = vf[1][0];
          *(u32x2*)(e2 + 1536 + lane * 8) = vf[1][1];
          MEMFENCE;
          lflag[cw] = layer;
          MEMFENCE;
          PRIO(1);
        }
        SCHED0;
        fold_rows(0, qf, xv);
        u32x4 kp;
        u32x4 va0 = __builtin_shufflevector(vf[0][0], vf[0][0], -1, -1, 0, 1), vb0 = __builtin_shufflevector(vf[0][1], vf[0][1], -1, -1, 0, 1);
        auto setp = [&](u32x4& d, u32x2 v) AI { d[0] = v[0]; d[1] = v[1]; };
        auto ldh = [&]() AI {
          MEMFENCE;
          kp = ld16s(wsr, v16, hrec - 32 * KVREC);
          setp(va0, __builtin_amdgcn_raw_buffer_load_b64(wsr, v16 + 1024, hrec - 32 * KVREC, 16));
          MEMFENCE;
          setp(vb0, __builtin_amdgcn_raw_buffer_load_b64(wsr, v16 + 1032, hrec - 32 * KVREC, 16));
          MEMFENCE;
        };
        unsigned fl = 0;
        auto rdl = [&]() AI {
          MEMFENCE;
          fl = lflag[cw - 1];
          MEMFENCE;
          kp = *(const u32x4*)(ex + cw * 2048 + lane * 16);
          setp(va0, *(const u32x2*)(ex + cw * 2048 + 1024 + lane * 8));
          MEMFENCE;
          setp(vb0, *(const u32x2*)(ex + cw * 2048 + 1536 + lane * 8));
          MEMFENCE;
        };
        auto prev = [&]() AI {
          if (cw > 0) {
            do { SLEEP(1); rdl(); __builtin_amdgcn_s_waitcnt(0xc07f); } while (__builtin_amdgcn_readfirstlane(fl) != (unsigned)layer && --budget > 0);
          } else if (bm > 0) {
            SLEEP(SLP_HX);
            do { ldh(); __builtin_amdgcn_s_waitcnt(0x0f70); }
            while (poisoned(umax4(umax4(kp), u32x4{va0[0], va0[1], vb0[0], vb0[1]})) && --budget > 0);
          } else {
            kp = u32x4{0u, 0u, 0u, 0u};
            setp(va0, u32x2{0u, 0u}); setp(vb0, u32x2{0u, 0u});
          }
          asm volatile("" : "+v"(kp), "+v"(va0), "+v"(vb0));
        };
        const float scale = 0.17677669529663687f * LOG2E;
        const bool odd = fr & 1;
        const bool own0 = sel[0] == 0x0c0c0c0cu, own1 = sel[1] == 0x0c0c0c0cu;
        float l[2][2], mx[2], rk[2][2], sum[2];
#pragma unroll
        for (int m = 0; m < 2; ++m) { rk[m][0] = ((const LDS(float)*)ra0)[m * 16]; rk[m][1] = ((const LDS(float)*)ra1)[m * 16]; }
        float qk[2][2];
#pragma unroll
        for (int m = 0; m < 2; ++m) { const float qs = rs[m] * scale; qk[m][0] = qs * rk[m][0]; qk[m][1] = qs * rk[m][1]; }
        u32x4 va1 = u32x4{vf[0][0][0], vf[0][0][1], vf[1][0][0], vf[1][0][1]}, vb1 = u32x4{vf[0][1][0], vf[0][1][1], vf[1][1][0], vf[1][1][1]};
        auto score = [&](int m) AI {
          f32x4 s0 = mfma16(as_frag(m == 0 ? kp : kf[0]), as_frag(qf[m]), zero);
          f32x4 s1 = mfma16(as_frag(kf[m]), as_frag(qf[m]), zero);
          float a = own0 ? (odd ? s1[1] : s1[0]) : (odd ? s0[1] : s0[0]);
          float b = own1 ? (odd ? s1[3] : s1[2]) : (odd ? s0[3] : s0[2]);
          if (m == 0) { l[m][0] = __builtin_fmaf(a, qk[m][0], off0); l[m][1] = __builtin_fmaf(b, qk[m][1], off1); }
          else { l[m][0] = a * qk[m][0]; l[m][1] = b * qk[m][1]; }
          mx[m] = fmx(l[m][0], l[m][1]);
        };
        auto expo = [&](int m) AI {
          l[m][0] = __builtin_amdgcn_exp2f(l[m][0] - mx[m]);
          l[m][1] = __builtin_amdgcn_exp2f(l[m][1] - mx[m]);
          sum[m] = l[m][0] + l[m][1];
        };
        f32x4 ov[2][2];
        auto pv = [&](int m) AI {
          const unsigned pr = pk2(mul1(mul1(l[m][0], rk[m][0]), sum[m]), mul1(mul1(l[m][1], rk[m][1]), sum[m]));
          const u32x4 pf = u32x4{__builtin_amdgcn_perm(pr, pr, sel[0]), __builtin_amdgcn_perm(pr, pr, sel[1]),
                                 __builtin_amdgcn_perm(pr, pr, sel[2]), __builtin_amdgcn_perm(pr, pr, sel[3])};
          ov[m][0] = mfma16(as_frag(m ? va1 : va0), as_frag(pf), zero);
          ov[m][1] = mfma16(as_frag(m ? vb1 : vb0), as_frag(pf), zero);
        };
        asm volatile("" : "+v"(qf[0]), "+v"(qf[1]), "+v"(va0), "+v"(vb0));
        SCHED0;
        if (cw > 0) rdl();
        SCHED0;
        score(1);
        asm volatile("" : "+v"(qf[0]), "+v"(qk[0][0]), "+v"(qk[0][1]), "+v"(mx[1]), "+v"(l[1][0]), "+v"(l[1][1]));
        asm volatile("" : "+v"(va1), "+v"(vb1));
        SCHED0;
        STAMP(3);
        if (__builtin_expect(cw == 0 || __builtin_amdgcn_readfirstlane(fl) != (unsigned)layer, 0)) prev();
        score(0);
        xred2<true>(mx[0], mx[1]);
        expo(0); expo(1);
        xred2<false, true>(sum[0], sum[1]);
        pv(0); pv(1);
        SCHED0;
        const u32x4 o0 = put_o(0, ov[0][0], ov[0][1], 1), o1 = put_o(1, ov[1][0], ov[1][1], 1);
        hold(o0, o1);
        STAMP(4);
      }
    }
    {
      LANE_VARS
      const int mt = bm * 16 + cw * 2 + cm;
      const unsigned so0 = WS_AR + AR_O + xt_off(mt, 0);
      const unsigned opar = layer & 1;
      auto diff = [&](u32x4 v) AI { return ((v[0] ^ opar) | (v[2] ^ opar)) & 1u; };
      auto dif3 = [&](u32x4 v) AI { return __builtin_amdgcn_bitop3_b32(v[0], v[2], opar, 0x7e); };
      auto any = [&](unsigned d) AI { return __builtin_amdgcn_ballot_w64(d != 0) != 0; };
      const int g0 = cm;
      u32x4 a[RO];
      auto refill = [&](auto ic, auto wc, unsigned cur, unsigned nxt) AI {
        constexpr int I = decltype(ic)::value, W = decltype(wc)::value;
        do {
          COUNT(1);
          SLEEP(SLP_R);
          MEMFENCE;
          a[I] = ld16s(wsr, v16, cur + I * 16384);
          bool u[RO];
#pragma unroll
          for (int j = I + 1; j < I + W; ++j) u[j % RO] = any(diff(a[j % RO]));
          SCHED0;
#pragma unroll
          for (int j = I + 1; j < I + W; ++j)
            if (u[j % RO]) a[j % RO] = ld16s(wsr, v16, (j < RO ? cur : nxt) + (j % RO) * 16384);
          __builtin_amdgcn_s_waitcnt(0x0f70);
          MEMFENCE;
        } while (any(diff(a[I]) | diff(a[I + 1])) && --budget > 0);
      };
      const unsigned sf = so0 + g0 * (RO * 16384);
      f32x4 acc[4];
#pragma unroll
      for (int n = 0; n < 4; ++n)
        acc[n] = f32x4{bflo(xr[n >> 1][(n & 1) * 2]), bfhi(xr[n >> 1][(n & 1) * 2]), bflo(xr[n >> 1][(n & 1) * 2 + 1]), bfhi(xr[n >> 1][(n & 1) * 2 + 1])};
      asm volatile("" : "+v"(acc[0]), "+v"(acc[1]), "+v"(acc[2]), "+v"(acc[3]));
      SCHED0;
      const bool late = isB && cw == 0;
      if (late) bar();
      if (cn < 8) { if (late) SLEEP(SLP_O0); else SLEEP(SLP_O); }
      else { if (late) SLEEP(SLP_O0D); else SLEEP(SLP_OD); }
#pragma unroll
      for (int j = 0; j < 4; ++j) { a[j] = ld16s(wsr, v16, sf + j * 16384); SCHED0; }
      if (!late) bar();
      STAMP(9);
      bf16x8 wf[2][4];
      const char* pw0 = smem + L_STG + v16;
#pragma unroll
      for (int n = 0; n < 8; ++n) wf[n >> 2][n & 3] = *(const bf16x8*)(pw0 + g0 * (RO * 4096) + n * 1024);
      SCHED0;
      STAMP(10);
      if (any(diff(a[0]) | diff(a[1]))) refill(IC<0>{}, IC<4>{}, sf, sf);
      SCHED0;
#pragma unroll
      for (int j = 4; j < RO; ++j) { a[j] = ld16s(wsr, v16, sf + j * 16384); SCHED0; }
      STAMP(11);
      bool bad = false;
      auto group = [&](auto lastc, int g) AI {
        constexpr bool LAST = decltype(lastc)::value;
        const int gc = g ^ g0, gn = (g + 1) ^ g0;
        const unsigned sc = so0 + gc * (RO * 16384), so = so0 + gn * (RO * 16384);
        const char* pc = pw0 + gc * (RO * 4096);
        const char* pn = pw0 + gn * (RO * 4096);
        static_for<RO>([&](auto ic) AI {
          constexpr int i = decltype(ic)::value;
          SCHED0;
          if constexpr (!LAST && i == 0) { if (g == 0) PRIO(3); else if (g == 2) PRIO(2); }
          if constexpr (LAST && i == 3) PRIO(1);
          if constexpr (i % 2 == 0)
            if (__builtin_expect(bad, 0)) refill(ic, IC<(LAST && i + 4 > RO) ? RO - i : 4>{}, sc, so);
          SCHED0;
          unsigned mx = 0, my = 0;
          static_for<4>([&](auto nc) AI {
            constexpr int n = decltype(nc)::value;
            mfma_acc(acc[n], wf[i & 1][n], as_frag(a[i]));
            SCHED0;
            if constexpr (i + 2 < RO) wf[i & 1][n] = *(const bf16x8*)(pc + (i + 2) * 4096 + n * 1024);
            else if constexpr (!LAST) wf[i & 1][n] = *(const bf16x8*)(pn + (i + 2 - RO) * 4096 + n * 1024);
            if constexpr (!(LAST && i == RO - 1) && (i & 1)) {
              if constexpr (n == 0) mx = dif3(a[(i + 1) % RO]);
              if constexpr (n == 1) my = dif3(a[(i + 2) % RO]);
              if constexpr (n == 2) bad = any((mx | my) & 1u);
            }
            if constexpr (n == 3 && !LAST) a[i] = ld16s(wsr, v16, so + i * 16384);
            SCHED0;
          });
        });
      };
#pragma unroll 1
      for (int g = 0; g < 3; ++g) group(std::false_type{}, g);
      group(std::true_type{}, 3);
      mfma_done();
      STAMP(12);
#pragma unroll
      for (int j = 0; j < 2; ++j)
#pragma unroll
        for (int e = 0; e < 4; ++e) xr[j][e] = pk2(acc[j * 2 + (e >> 1)][(e & 1) * 2], acc[j * 2 + (e >> 1)][(e & 1) * 2 + 1]);
      if (layer == 3) {
        u32x4 yo[2];
#pragma unroll
        for (int j = 0; j < 2; ++j) {
          auto u = __builtin_amdgcn_permlane16_swap(xr[j][0], xr[j][2], false, false);
          auto v = __builtin_amdgcn_permlane16_swap(xr[j][1], xr[j][3], false, false);
          yo[j] = u32x4{u[0], v[0], u[1], v[1]};
        }
        const unsigned vo = fr * 2048 + (fq & 1) * 32 + (fq >> 1) * 16;
        __builtin_amdgcn_raw_buffer_store_b128(yo[0], mk_rsrc(p.out, 4 * MBY), vo, mt * 32768 + cn * 128, 18);
        __builtin_amdgcn_raw_buffer_store_b128(yo[1], mk_rsrc(p.out, 4 * MBY), vo + 64, mt * 32768 + cn * 128, 18);
        STAMP(13);
        STAMP(14);
        STAMP_DUMP;
        __builtin_amdgcn_endpgm();
      }
      const u32x2 stat = put_x(xr, v16, ar + ARENA + AR_X + xt_off(mt, cn * 2));
      __builtin_amdgcn_raw_buffer_store_b64(stat, wsr, fq ? OOB : fr * 8, ar + ARENA + AR_ST + (mt * 16 + cn) * 128, 16);
      MEMFENCE;
      STAMP(14);
    }
  }
}

extern "C" void kernel_launch(void* const* d_in, const int* in_sizes, int n_in, void* d_out, int out_size,
                              void* d_ws, size_t ws_size, hipStream_t stream) {
  (void)hipFuncSetAttribute((const void*)mega_kernel, hipFuncAttributeMaxDynamicSharedMemorySize, MEGA_LDS);
  MegaParams m;
  m.ws = (char*)d_ws; m.out = (u16*)d_out; m.x = (const u16*)d_in[0]; m.wphi = (const u16*)d_in[2];
  m.wqkvA = (const u16*)d_in[1]; m.wqkvB = (const u16*)d_in[5];
  m.woA = (const u16*)d_in[3]; m.woB = (const u16*)d_in[6];
  mega_kernel<<<256, 768, MEGA_LDS, stream>>>(m);
}
```

```cpp
#include <hip/hip_runtime.h>
#include <stdint.h>
#include <type_traits>

#define DI __device__ __forceinline__
#define AI __attribute__((always_inline))
typedef unsigned short u16;
typedef __attribute__((ext_vector_type(8))) short bf16x8;
typedef __attribute__((ext_vector_type(4))) short s16x4;
typedef __attribute__((ext_vector_type(4))) float f32x4;
typedef __attribute__((ext_vector_type(2))) float f32x2;
typedef __attribute__((ext_vector_type(2))) __bf16 bf2;
typedef __attribute__((ext_vector_type(4))) unsigned u32x4;
typedef __attribute__((ext_vector_type(2))) unsigned u32x2;
typedef __attribute__((address_space(3))) s16x4* lds_s16x4_ptr;
#define LDS(T) __attribute__((address_space(3))) T
template <int N> using IC = std::integral_constant<int, N>;

constexpr float LOG2E = 1.4426950408889634f;

DI unsigned pk2(float a, float b) {
  f32x2 v = {a, b};
  bf2 r = __builtin_convertvector(v, bf2);
  return __builtin_bit_cast(unsigned, r);
}
DI float bflo(unsigned u) { return __uint_as_float(u << 16); }
DI float bfhi(unsigned u) { return __uint_as_float(u & 0xffff0000u); }
DI f32x4 mfma16(bf16x8 a, bf16x8 b, f32x4 c) { return __builtin_amdgcn_mfma_f32_16x16x32_bf16(a, b, c, 0, 0, 0); }
DI void mfma_acc(f32x4& c, bf16x8 a, bf16x8 b) { asm volatile("v_mfma_f32_16x16x32_bf16 %0, %1, %2, %0" : "+v"(c) : "v"(a), "v"(b)); }
DI void mfma_done() { asm volatile("s_nop 7\n\ts_nop 1" ::: "memory"); __builtin_amdgcn_sched_barrier(0); }
DI bf16x8 as_frag(u32x4 v) { return __builtin_bit_cast(bf16x8, v); }
DI float dot2(unsigned a, unsigned b, float c) {
  return __builtin_amdgcn_fdot2_f32_bf16(__builtin_bit_cast(bf2, a), __builtin_bit_cast(bf2, b), c, false);
}
DI u32x2 trread(const char* p) { return __builtin_bit_cast(u32x2, __builtin_amdgcn_ds_read_tr16_b64_v4i16((lds_s16x4_ptr)p)); }
DI u32x2 trl(unsigned a) { return __builtin_bit_cast(u32x2, __builtin_amdgcn_ds_read_tr16_b64_v4i16((lds_s16x4_ptr)a)); }
DI float fmx(float a, float b) { return __builtin_elementwise_maximum(a, b); }
DI float fmn(float a, float b) { return __builtin_elementwise_minimum(a, b); }

DI float mul1(float a, float b) { float r; asm("v_mul_f32 %0, %1, %2" : "=v"(r) : "v"(a), "v"(b)); return r; }
DI float add1(float a, float b) { float r; asm("v_add_f32 %0, %1, %2" : "=v"(r) : "v"(a), "v"(b)); return r; }
DI f32x4 mul4(f32x4 a, f32x4 b) {
  float r0, r1, r2, r3;
  asm("v_mul_f32 %0, %4, %8\n\tv_mul_f32 %1, %5, %9\n\tv_mul_f32 %2, %6, %10\n\tv_mul_f32 %3, %7, %11"
      : "=&v"(r0), "=&v"(r1), "=&v"(r2), "=&v"(r3) : "v"(a[0]), "v"(a[1]), "v"(a[2]), "v"(a[3]), "v"(b[0]), "v"(b[1]), "v"(b[2]), "v"(b[3]));
  return f32x4{r0, r1, r2, r3};
}
DI f32x4 add4(f32x4 a, f32x4 b) { return f32x4{add1(a[0], b[0]), add1(a[1], b[1]), add1(a[2], b[2]), add1(a[3], b[3])}; }

template <bool IS_MAX, bool INV = false>
DI void xred2(float& a, float& b) {
  auto r = __builtin_amdgcn_permlane32_swap(__float_as_uint(a), __float_as_uint(b), false, false);
  float c = IS_MAX ? fmx(__uint_as_float(r[0]), __uint_as_float(r[1])) : __uint_as_float(r[0]) + __uint_as_float(r[1]);
  auto q = __builtin_amdgcn_permlane16_swap(__float_as_uint(c), __float_as_uint(c), false, false);
  c = IS_MAX ? fmx(__uint_as_float(q[0]), __uint_as_float(q[1])) : __uint_as_float(q[0]) + __uint_as_float(q[1]);
  if (INV) c = __builtin_amdgcn_rcpf(c);
  auto w = __builtin_amdgcn_permlane32_swap(__float_as_uint(c), __float_as_uint(c), false, false);
  a = __uint_as_float(w[0]); b = __uint_as_float(w[1]);
}
template <bool IS_MAX, bool INV = false>
DI void xred4(float& a, float& b, float& c, float& d) {
  auto op = [](unsigned x, unsigned y) { return IS_MAX ? fmx(__uint_as_float(x), __uint_as_float(y)) : __uint_as_float(x) + __uint_as_float(y); };
  auto r0 = __builtin_amdgcn_permlane32_swap(__float_as_uint(a), __float_as_uint(b), false, false);
  auto r1 = __builtin_amdgcn_permlane32_swap(__float_as_uint(c), __float_as_uint(d), false, false);
  float ab = op(r0[0], r0[1]), cd = op(r1[0], r1[1]);
  auto q = __builtin_amdgcn_permlane16_swap(__float_as_uint(ab), __float_as_uint(cd), false, false);
  float e = op(q[0], q[1]);
  if (INV) e = __builtin_amdgcn_rcpf(e);
  auto w = __builtin_amdgcn_permlane16_swap(__float_as_uint(e), __float_as_uint(e), false, false);
  auto u = __builtin_amdgcn_permlane32_swap(w[0], w[0], false, false);
  auto v = __builtin_amdgcn_permlane32_swap(w[1], w[1], false, false);
  a = __uint_as_float(u[0]); b = __uint_as_float(u[1]); c = __uint_as_float(v[0]); d = __uint_as_float(v[1]);
}

DI unsigned xt_off(int mt, int ks) { return (unsigned)((((mt >> 4) * 32 + ks) * 16 + (mt & 15)) << 10); }

#define GLDS_PRE "s_mov_b32 %0, m0\n\ts_mov_b32 m0, %3\n\ts_nop 2\n\t"
#define GLDS_POST "s_mov_b32 m0, %0"
#define GLDS_OPS : "=&s"(keep) : "v"(voff), "s"(sbase), "s"(lds) : "memory"
#define GL1(c) "global_load_lds_dwordx4 %1, %2" c "\n\t"
#define GL2(c) GL1(c) "global_load_lds_dwordx4 %1, %2 offset:1024" c "\n\t"
#define GL3(c) GL2(c) "global_load_lds_dwordx4 %1, %2 offset:2048" c "\n\t"
#define GL4(c) GL3(c) "global_load_lds_dwordx4 %1, %2 offset:3072" c "\n\t"
template <int N, bool SC1 = false>
DI void glds_run(unsigned voff, const void* sbase, unsigned lds) {
  unsigned keep;
  if constexpr (SC1) {
    if constexpr (N == 2) asm volatile(GLDS_PRE GL2(" sc1") GLDS_POST GLDS_OPS);
    else if constexpr (N == 3) asm volatile(GLDS_PRE GL3(" sc1") GLDS_POST GLDS_OPS);
    else asm volatile(GLDS_PRE GL4(" sc1") GLDS_POST GLDS_OPS);
  } else {
    static_assert(SC1 || N == 4, "");
    asm volatile(GLDS_PRE GL4("") GLDS_POST GLDS_OPS);
  }
}
template <int N> DI void wait_vm_lgkm0() { asm volatile("s_waitcnt vmcnt(%0) lgkmcnt(0)" ::"n"(N) : "memory"); }
template <int N> DI void wait_vm() { asm volatile("s_waitcnt vmcnt(%0)" ::"n"(N) : "memory"); }
DI void drain() { asm volatile("s_waitcnt vmcnt(0)" ::: "memory"); __builtin_amdgcn_s_waitcnt(0x0f70); }
DI void bar() { asm volatile("" ::: "memory"); __builtin_amdgcn_s_barrier(); asm volatile("" ::: "memory"); }
DI void sync_lds() { asm volatile("s_waitcnt lgkmcnt(0)" ::: "memory"); bar(); }
#define SCHED0 __builtin_amdgcn_sched_barrier(0)
#define STPAD do { SCHED0; asm volatile("s_nop 1"); SCHED0; } while (0)
#define SLEEP(n) do { if (n) __builtin_amdgcn_s_sleep(n); } while (0)
#define PRIO(n) __builtin_amdgcn_s_setprio(n)

template <typename F>
DI void phi_map2(const f32x2 (*x)[4], const float* rs, u32x4 (*out)[2], F mid) {
  auto top = [&](float sg, float* mx) AI {
#pragma unroll
    for (int m = 0; m < 2; ++m)
      mx[m] = fmx(fmx(fmx(sg * x[m][0][0], sg * x[m][0][1]), sg * x[m][1][0]), fmx(fmx(sg * x[m][1][1], sg * x[m][2][0]), fmx(fmx(sg * x[m][2][1], sg * x[m][3][0]), sg * x[m][3][1])));
    xred2<true>(mx[0], mx[1]);
  };
  auto sum8 = [](const f32x2* e) AI { const f32x2 t = (e[0] + e[1]) + (e[2] + e[3]); float lo = t[0]; asm("" : "+v"(lo)); return lo + t[1]; };
  float mx[2], sp[2], sn[2];
  top(1.f, mx);
  f32x2 ep[2][4], en[2][4];
#pragma unroll
  for (int m = 0; m < 2; ++m) {
    const float sl = rs[m] * LOG2E, off = mx[m] * -sl;
#pragma unroll
    for (int j = 0; j < 4; ++j) {
      const f32x2 a = __builtin_elementwise_fma(x[m][j], f32x2{sl, sl}, f32x2{off, off});
      ep[m][j] = f32x2{__builtin_amdgcn_exp2f(a[0]), __builtin_amdgcn_exp2f(a[1])};
    }
#pragma unroll
    for (int j = 0; j < 4; ++j) en[m][j] = f32x2{__builtin_amdgcn_rcpf(ep[m][j][0]), __builtin_amdgcn_rcpf(ep[m][j][1])};
    sp[m] = sum8(ep[m]); sn[m] = sum8(en[m]);
  }
  xred4<false, true>(sp[0], sn[0], sp[1], sn[1]);
  if (__builtin_expect(__builtin_amdgcn_ballot_w64(!(fmn(sn[0], sn[1]) > 1e-30f)) != 0, 0)) {
    top(-1.f, mx);
#pragma unroll
    for (int m = 0; m < 2; ++m) {
      const float sl = rs[m] * -LOG2E, off = mx[m] * sl;
#pragma unroll
      for (int j = 0; j < 4; ++j) {
        const f32x2 a = __builtin_elementwise_fma(x[m][j], f32x2{sl, sl}, f32x2{off, off});
        en[m][j] = f32x2{__builtin_amdgcn_exp2f(a[0]), __builtin_amdgcn_exp2f(a[1])};
      }
      sn[m] = sum8(en[m]);
    }
    xred2<false, true>(sn[0], sn[1]);
  }
  mid(sp);
#pragma unroll
  for (int m = 0; m < 2; ++m) {
#pragma unroll
    for (int j = 0; j < 4; j += 2) {
      f32x2 a0, a1, b0, b1;
      asm("v_pk_mul_f32 %0, %4, %5 op_sel_hi:[0,1]\n\tv_pk_mul_f32 %1, %4, %6 op_sel_hi:[0,1]\n\t"
          "v_pk_mul_f32 %2, %4, %7 op_sel:[1,0]\n\tv_pk_mul_f32 %3, %4, %8 op_sel:[1,0]"
          : "=&v"(a0), "=&v"(a1), "=&v"(b0), "=&v"(b1) : "v"(f32x2{sp[m], sn[m]}), "v"(ep[m][j]), "v"(ep[m][j + 1]), "v"(en[m][j]), "v"(en[m][j + 1]));
      out[m][0][j] = pk2(a0[0], a0[1]); out[m][0][j + 1] = pk2(a1[0], a1[1]);
      out[m][1][j] = pk2(b0[0], b0[1]); out[m][1][j + 1] = pk2(b1[0], b1[1]);
    }
  }
}

#define SLP_X 8
#define SLP_XD 4
#define SLP_O 19
#define SLP_OD 10
#define SLP_O0 8
#define SLP_O0D 2
#define SLP_K 4
#define SLP_R 4
#define SLP_HX 1
#define SLP_Q 4
#define SLP_P 40
#define SLP_E 14
#define SLP_G 12
#define SPR0 1
#define SW0 0
#define LANEID __builtin_amdgcn_mbcnt_hi(~0u, __builtin_amdgcn_mbcnt_lo(~0u, 0u))
#define STAMP(i) do {} while (0)
#define STAMP_DUMP do {} while (0)
#define COUNT(k) do {} while (0)
#define PSTAMP(l) do { const int layer = l; (void)layer; STAMP(15); } while (0)
constexpr unsigned MBY = 1u << 20;
constexpr unsigned WS_WQ = 0;
constexpr unsigned WS_WO = 12 * MBY;
constexpr unsigned WS_AR = 16 * MBY, ARENA = 12 * MBY;
constexpr unsigned AR_X = 0, AR_ST = 4 * MBY, AR_O = 5 * MBY, AR_KV = 9 * MBY;
constexpr unsigned AR_L2 = AR_ST + 256 * 1024, AR_FL = AR_ST + 320 * 1024;
constexpr unsigned KVREC = 9 * 1024;
constexpr unsigned WS_CS = WS_AR + 4 * ARENA;
constexpr unsigned WS_F1 = WS_CS + 512 * 1024;
constexpr unsigned WS_F2 = WS_F1 + 64 * 1024;
constexpr unsigned WS_EP = WS_F2 + 2048;
constexpr unsigned WS_END = WS_EP + 128;
constexpr unsigned POISON = 0xffffffffu;
constexpr unsigned OOB = 0x80000000u;

typedef __amdgpu_buffer_rsrc_t rsrc_t;
DI rsrc_t mk_rsrc(const void* p, unsigned bytes) { return __builtin_amdgcn_make_buffer_rsrc((void*)p, 0, bytes, 0x00020000); }
DI void st16(rsrc_t r, unsigned off, u32x4 v) { __builtin_amdgcn_raw_buffer_store_b128(v, r, off, 0, 16); }
DI void st16s(rsrc_t r, unsigned voff, unsigned soff, u32x4 v) { __builtin_amdgcn_raw_buffer_store_b128(v, r, voff, soff, 16); }
DI u32x4 ld16(rsrc_t r, unsigned off) { return __builtin_amdgcn_raw_buffer_load_b128(r, off, 0, 16); }
DI u32x4 ld16s(rsrc_t r, unsigned voff, unsigned soff) { return __builtin_amdgcn_raw_buffer_load_b128(r, voff, soff, 16); }
DI u32x4 clean(u32x4 v) { return __builtin_elementwise_min(v, u32x4{POISON - 1, POISON - 1, POISON - 1, POISON - 1}); }
DI unsigned umax(unsigned a, unsigned b) { return a > b ? a : b; }
DI unsigned umin(unsigned a, unsigned b) { return a < b ? a : b; }
DI unsigned umax4(u32x4 v) { return umax(umax(umax(v[0], v[1]), v[2]), v[3]); }
DI unsigned umax4(unsigned m, u32x4 v) { return umax(umax(umax(umax(m, v[0]), v[1]), v[2]), v[3]); }
DI bool poisoned(unsigned m) { return __builtin_amdgcn_ballot_w64(m == POISON) != 0; }
#define MEMFENCE asm volatile("" ::: "memory")

#define BL(i, o) "buffer_load_dwordx4 %1, %2, %" #i " offen" o " nt lds\n\t"
#define BL_OPS(...) : "=&s"(keep) : "v"(voff), "s"(r), "s"(lds), __VA_ARGS__ : "memory"
template <int N>
DI void blds_run(rsrc_t r, unsigned voff, unsigned lds, unsigned s0, unsigned s1 = 0, unsigned s2 = 0, unsigned s3 = 0) {
  unsigned keep;
  if constexpr (N == 2) asm volatile(GLDS_PRE BL(4, "") BL(5, " offset:1024") GLDS_POST BL_OPS("s"(s0), "s"(s1)));
  else asm volatile(GLDS_PRE BL(4, "") BL(5, " offset:1024") BL(6, " offset:2048") BL(7, " offset:3072") GLDS_POST BL_OPS("s"(s0), "s"(s1), "s"(s2), "s"(s3)));
}

#define CODEB 36416
constexpr int MEGA_CODE_BYTES = CODEB;
DI unsigned code_prefetch(int i) {
  unsigned long long pc; int rel; unsigned d;
  asm volatile("s_getpc_b64 %0\n\ts_mov_b32 %1, _Z11mega_kernel10MegaParams@rel32@lo+4" : "=s"(pc), "=s"(rel));
  const unsigned long long a = pc + (long long)rel + (i * 192 + 192 <= MEGA_CODE_BYTES ? i * 192 : 0);
  asm volatile("s_load_dword %0, %1, 0x0\n\ts_load_dword %0, %1, 0x40\n\ts_load_dword %0, %1, 0x80" : "=&s"(d) : "s"(a) : "memory");
  return d;
}

struct MegaParams {
  char* ws;
  const u16* x;
  const u16* wqkvA;
  const u16* woA;
  const u16* wqkvB;
  const u16* woB;
  const u16* wphi;
  u16* out;
};

constexpr int QSLOT = 6 * 1024, QGRP = 4 * QSLOT;
constexpr int L_STG = QGRP;
constexpr int L_A2 = 16 * 1024, L_Z = 21 * 1024;
constexpr int L_RT = 4 * QGRP;
constexpr int L_PB = 0;
constexpr int L_RED = L_STG + 128 * 1024;
constexpr int L_STAMP = L_RED + 3072;
constexpr int MEGA_LDS = L_STAMP + 1536;
static_assert(MEGA_LDS <= 160 * 1024, "LDS map");

template <int I, int N, typename F> DI void static_for_(F&& f) {
  if constexpr (I < N) { f(IC<I>{}); static_for_<I + 1, N>(f); }
}
template <int N, typename F> DI void static_for(F&& f) { static_for_<0, N>(f); }

constexpr int NCHK = 3;
constexpr int RO = 8;
constexpr int SPIN_BUDGET = 1 << 15;

__global__ __launch_bounds__(768, 3) void mega_kernel(MegaParams p) {
  extern __shared__ __attribute__((aligned(16))) char smem[];
  const int tid = threadIdx.x;
  const int wave = __builtin_amdgcn_readfirstlane(tid >> 6);
  const int xcd = blockIdx.x & 7, idx = blockIdx.x >> 3;
  int bm = (xcd >> 2) * 4 + (idx >> 3), head = ((xcd & 1) * 2 + ((xcd >> 1) & 1)) * 8 + (idx & 7);
  int cm = (xcd >> 1) & 1, cn = (xcd & 1) * 8 + (idx & 7);
  const int bm0 = bm, head0 = head, cm0 = cm, cn0 = cn;
  char* ws = p.ws;
  const u16 *px = p.x, *pq = p.wqkvA;
  asm volatile("" : "+s"(ws), "+s"(px), "+s"(pq));
  const int swv = wave == SW0 ? 0 : wave == 4 ? 1 : wave == 11 ? 2 : -1; (void)swv;
  float* lmean = (float*)(smem + L_RED);
  float* lrstd = lmean + 272;
  float* lcs = lrstd + 272;
  unsigned* lflag = (unsigned*)lmean;
  const rsrc_t wsr = mk_rsrc(ws, WS_END);
  int budget = SPIN_BUDGET;
  unsigned tag0 = 0, tag1 = 0;
  auto ld_ep = [&]() AI { MEMFENCE; return __builtin_amdgcn_raw_buffer_load_b64(wsr, 0, WS_EP, 16); };
  auto mk_tag = [&](u32x2 ep) AI {
    tag0 = __builtin_amdgcn_readfirstlane(ep[0]) + 0x9e3779b9u;
    tag1 = __builtin_amdgcn_readfirstlane(ep[1]) ^ 0x85ebca6bu;
  };
  const unsigned sbase = (unsigned)(size_t)smem;
  int L = wave, cw = wave - 4;
#define OPAQUE_S asm volatile("" : "+s"(L), "+s"(bm), "+s"(head), "+s"(cm), "+s"(cn));
#define LANE_VARS asm volatile("" : "+s"(cw), "+s"(bm), "+s"(head), "+s"(cm), "+s"(cn)); int lane_ = __builtin_amdgcn_mbcnt_hi(~0u, __builtin_amdgcn_mbcnt_lo(~0u, 0u)); asm volatile("" : "+v"(lane_)); \
  const int lane = lane_, fr = lane & 15, fq = lane >> 4; const unsigned v16 = lane * 16; (void)fr; (void)fq; (void)v16;
  auto put_x = [&](u32x4* xr, unsigned v16, unsigned dst) AI {
    float sm = 0.f, sq = 0.f;
#pragma unroll
    for (int j = 0; j < 2; ++j) {
      xr[j][0] = umin(xr[j][0], POISON - 1); xr[j][2] = umin(xr[j][2], POISON - 1);
      st16s(wsr, v16, dst + j * 16384, xr[j]);
    }
    SCHED0;
#pragma unroll
    for (int j = 0; j < 2; ++j)
#pragma unroll
      for (int e = 0; e < 4; ++e) { sm = dot2(xr[j][e], 0x3f803f80u, sm); sq = dot2(xr[j][e], xr[j][e], sq); }
    xred2<false>(sm, sq);
    return u32x2{__float_as_uint(sm), __float_as_uint(sq)};
  };

#define RT_VARS const bool tq = L < 3; const int tsg = tq ? (bm * 2 + (head >> 4)) * 3 + L : bm * 2 + (head >> 4), ks = (head & 15) + 16 * h;
  auto ld_rt = [&](const u16* w, int h, unsigned lds) AI {
    RT_VARS
    LANE_VARS
    const unsigned rs = tq ? 6144 : 2048, D = 8 * rs - 1024, o = ks * 32 * rs + tsg * 128;
    blds_run<4>(mk_rsrc(w, rs * 1024), (lane >> 3) * rs + ((lane & 7) ^ ((lane >> 3) & 6)) * 16, sbase + lds, o, o + D, o + 2 * D, o + 3 * D);
  };
  auto retile = [&](int side, int h, unsigned lds, f32x4* rcs, int wp = -1) AI {
    RT_VARS
    LANE_VARS
    const unsigned dst = tq ? WS_WQ + side * (6 * MBY) + (unsigned)((((tsg & 15) * 2) * 32 + ks) * 6 + (tsg >> 4) * 2) * 1024
                            : WS_WO + side * (2 * MBY) + (unsigned)((tsg * 32 + ks) * 4) * 1024;
    const unsigned gstr = tq ? 32 * 6 * 1024 : 2048;
    const unsigned a0 = (lane >> 2) * 128 + ((lane >> 3) & 3) * 32 + (lane & 3) * 8;
    u32x4 fv[4];
    if (wp >= 0 && tsg < 32) {
      const f32x4 zero = f32x4{0.f, 0.f, 0.f, 0.f};
#pragma unroll
      for (int g = 0; g < 2; ++g) {
        const char* ra = smem + lds + fr * 128 + (((g * 4 + fq) ^ (fr & 6)) * 16);
        const bf16x8 w0 = *(const bf16x8*)ra, w1 = *(const bf16x8*)(ra + 2048);
#pragma unroll
        for (int u = 0; u < 2; ++u) {
          const char* pa = smem + wp + g * 2048 + (fq * 8 + (fr >> 2)) * 64 + u * 32 + (fr & 3) * 8;
          const u32x2 t0 = trread(pa), t1 = trread(pa + 256);
          const bf16x8 wf = as_frag(u32x4{t0[0], t0[1], t1[0], t1[1]});
          const f32x4 d0 = mfma16(w0, wf, zero), d1 = mfma16(w1, wf, zero);
          fv[g * 2 + u] = u32x4{pk2(d0[0], d0[1]), pk2(d0[2], d0[3]), pk2(d1[0], d1[1]), pk2(d1[2], d1[3])};
        }
      }
    } else {
#pragma unroll
      for (int pc = 0; pc < 4; ++pc) {
        const char* aa = smem + lds + (a0 ^ (pc * 32));
        const u32x2 r0_ = trread(aa), r1_ = trread(aa + 2048);
        fv[pc] = u32x4{r0_[0], r0_[1], r1_[0], r1_[1]};
      }
    }
#pragma unroll
    for (int pc = 0; pc < 4; ++pc) {
      const u32x4 v = fv[pc];
      st16s(wsr, v16 + (pc & 1) * 1024, dst + (pc >> 1) * gstr, v);
      if (rcs) rcs[pc] = mfma16(as_frag(u32x4{0x3f803f80u, 0x3f803f80u, 0x3f803f80u, 0x3f803f80u}), as_frag(v), rcs[pc]);
    }
    if (rcs && h == 1) {
      const f32x4 c0 = rcs[0], c1 = rcs[1], c2 = rcs[2], c3 = rcs[3];
      const float ca = fq & 1 ? c1[0] : c0[0], cb = fq & 1 ? c3[0] : c2[0], cc = fq & 2 ? cb : ca;
      if (tq) __builtin_amdgcn_raw_buffer_store_b32(__float_as_uint(cc), wsr, ((fq >> 1) * 1536 + (fq & 1) * 16 + fr) * 4,
                                                    WS_CS + ((side * 32 + (tsg & 15) * 2) * 1536 + (tsg >> 4) * 512 + (head & 15) * 32) * 4, 16);
    }
  };
  auto flag = [&](unsigned table) AI {
    if (LANEID == 0) __builtin_amdgcn_raw_buffer_store_b64(u32x2{tag0, tag1}, wsr, 0, table + (bm * 32 + head) * 8, 16);
  };
  auto poll = [&](unsigned table, auto between, int rows = 0) AI {
    LANE_VARS
    const int q = (umin(fq, 2) * 16 + (head >> 1)) * 43 >> 7;
    const unsigned vo = rows ? bm * 256 + (fr & rows) * 16 : fq == 3 ? bm * 256 + fr * 16 : q * 128 + (fr & 7) * 16;
    auto miss = [&](u32x4 f) AI { return __builtin_amdgcn_ballot_w64(((f[0] ^ tag0) | (f[1] ^ tag1) | (f[2] ^ tag0) | (f[3] ^ tag1)) != 0) != 0; };
    MEMFENCE;
    const u32x4 f0 = ld16s(wsr, vo, table);
    MEMFENCE;
    between();
    MEMFENCE;
    bool wait = miss(f0);
#pragma unroll 1
    while (wait && --budget > 0) {
      SLEEP(SLP_Q);
      MEMFENCE;
      wait = miss(ld16s(wsr, vo, table));
      asm volatile("" : "+s"(budget) :: "memory");
    }
    MEMFENCE;
  };
  auto ld_x = [&](int cx, unsigned lds) AI {
    LANE_VARS
    const unsigned o = (bm * 16 + (head >> 1)) * 32768 + cx * 128;
    blds_run<2>(mk_rsrc(px, 4 * MBY), (lane >> 3) * 2048 + ((lane & 7) ^ (lane >> 3)) * 16, sbase + lds, o, o + 15360);
  };
  auto st_x = [&](int cx, unsigned lds, int w) AI {
    LANE_VARS
    const int mt = bm * 16 + (head >> 1);
    u32x4 xr[2];
#pragma unroll
    for (int j = 0; j < 2; ++j) {
      const char* a = smem + lds + fr * 128 + (fq & 1) * 8;
      const u32x2 lo = *(const u32x2*)(a + ((j * 4 + (fq >> 1)) ^ (fr & 7)) * 16), hi = *(const u32x2*)(a + ((j * 4 + 2 + (fq >> 1)) ^ (fr & 7)) * 16);
      xr[j] = u32x4{lo[0], lo[1], hi[0], hi[1]};
    }
    const u32x2 stat = put_x(xr, v16, WS_AR + AR_X + xt_off(mt, cx * 2));
    if (fq == 0) *(u32x2*)(lmean + 16 + fr * 4 + w) = stat;
  };

  {
    const int lane = LANEID;
    const bool tq = wave < 4;
    const unsigned rs = tq ? 6144 : 2048, D = 8 * rs - 1024;
    const unsigned o = (tq ? (head & 15) * 32 : (bm * 16 + (head >> 1)) * 16) * rs + (tq ? (bm * 2 + (head >> 4)) * 3 + wave : (head & 1) * 4 + cw) * 128;
    const unsigned lds = sbase + L_PB + (tq ? 8192 + wave * 4096 : cw * 2048);
    const unsigned vo = (lane >> 3) * rs + ((lane & 7) ^ ((lane >> 3) & (tq ? 6 : 7))) * 16;
    const rsrc_t r = mk_rsrc(tq ? pq : px, tq ? 6 * MBY : 4 * MBY);
    if ((wave & 7) != 3 && wave < 8) blds_run<2>(r, vo, lds, o, o + D);
    if (wave < 3) {
      blds_run<2>(r, vo, lds + 2048, o + 2 * D + 2048, o + 3 * D + 2048);
      const int tsg = (bm * 2 + (head >> 4)) * 3 + wave;
      if (tsg < 32) glds_run<4>(lane * 16, (const char*)p.wphi + (tsg & 15) * 4096, sbase + L_PB + 45056 + wave * 4096);
    }
  }
  if (wave >= 8) {
    const unsigned cp = code_prefetch(idx * 9 + cw), cq = code_prefetch(idx * 9 + cw - 4);
    L = wave - 8;
    bar();
    ld_x(8 + (head & 1) * 4 + L, L_PB + 32768 + L * 2048);
    if (L < 3) ld_rt(pq, 1, L_PB + 20480 + L * 4096);
    sync_lds();
    asm volatile("; pi" :: "s"(cp), "s"(cq) : "memory");
    wait_vm<0>();
    PSTAMP(1);
    st_x(8 + (head & 1) * 4 + L, L_PB + 32768 + L * 2048, L * 64 + 2);
    if (L < 3) {
      const f32x4 t = *(const f32x4*)(smem + L_PB + 40960 + L * 1024 + LANEID * 16);
      f32x4 rca[4] = {{t[0], 0.f, 0.f, 0.f}, {t[1], 0.f, 0.f, 0.f}, {t[2], 0.f, 0.f, 0.f}, {t[3], 0.f, 0.f, 0.f}};
      retile(0, 1, L_PB + 20480 + L * 4096, rca, L_PB + 45056 + L * 4096);
    }
    asm volatile("s_waitcnt lgkmcnt(0)" ::: "memory");
    PSTAMP(2);
  } else if (wave >= 4) {
    wait_vm<0>();
    PSTAMP(1);
    st_x((head & 1) * 4 + cw, L_PB + cw * 2048, cw * 64);
    bar();
    wait_vm_lgkm0<0>();
    PSTAMP(2);
    bar();
    asm volatile("; pc" ::: "memory");
    SLEEP(SLP_E);
    L = 3;
    if (cw < 2) ld_rt(p.woA, cw, L_RT + 3 * 14336 + cw * 4096);
  } else if (wave < 3) {
    const u32x2 ep = ld_ep();
    wait_vm<1>();
    PSTAMP(1);
    f32x4 rca[4] = {};
    retile(0, 0, L_PB + 8192 + L * 4096, rca, L_PB + 45056 + L * 4096);
    *(f32x4*)(smem + L_PB + 40960 + L * 1024 + LANEID * 16) = f32x4{rca[0][0], rca[1][0], rca[2][0], rca[3][0]};
    bar();
    drain();
    mk_tag(ep);
    PSTAMP(2);
    sync_lds();
    asm volatile("; pl" ::: "memory");
    SLEEP(SLP_E);
    ld_rt(p.wqkvB, 0, L_RT + L * 14336);
  } else {
    const u32x2 ep = ld_ep();
    const unsigned cp = code_prefetch(idx * 9 + 8);
    if (LANEID < 9) lflag[LANEID] = 0;
    wait_vm_lgkm0<0>();
    asm volatile("" :: "s"(cp));
    mk_tag(ep);
    bar();
    bar();
    flag(WS_F1);
    if (LANEID == 0) *(u32x2*)(lflag + 10) = u32x2{tag0, tag1};
    PSTAMP(2);
    SLEEP(SLP_P);
    poll(WS_F1, []() {});
    asm volatile("; pp" ::: "memory");
  }
  bar();
  PSTAMP(3);
  int role = wave;
  asm volatile("" : "+s"(role));

  if (__builtin_expect(role < 4, 1)) {
    const unsigned v16 = __builtin_amdgcn_mbcnt_hi(~0u, __builtin_amdgcn_mbcnt_lo(~0u, 0u)) * 16;
#define OPAQUE_LANE int lane = v16 >> 4; asm volatile("" : "+v"(lane));
    auto q_groupW = [&](int layer, int g, int pos) AI {
      const char* gW = ws + WS_WQ + (layer & 1) * (6 * MBY) + head * (192 * 1024) + (g * 4 + L) * 6144;
      const unsigned d = sbase + (pos * 4 + L) * QSLOT;
      glds_run<3, true>(v16, gW, d);
      glds_run<3, true>(v16, gW + 3072, d + 3072);
    };
    u32x4 wn[4][6];
    auto ld_wn = [&](int layer, int g0, int g1) AI {
      const unsigned so = WS_WQ + (layer & 1) * (6 * MBY) + head * (192 * 1024) + L * 6144;
#pragma unroll
      for (int g = g0; g < g1; ++g)
#pragma unroll
        for (int n = 0; n < 6; ++n) wn[g][n] = ld16s(wsr, v16 + (n % 3) * 1024, so + g * 24576 + (n / 3) * 3072);
      MEMFENCE;
    };
    auto wr_wn = [&](int g) AI {
#pragma unroll
      for (int n = 0; n < 6; ++n) *(u32x4*)(smem + (g * 4 + L) * QSLOT + n * 1024 + v16) = wn[g][n];
    };
    const u32x4 pz = u32x4{POISON, POISON, POISON, POISON};
    auto poison = [&](unsigned so) AI { st16s(wsr, v16, so, pz); };
    auto poison_o = [&]() AI {
      const unsigned o = WS_AR + AR_O + xt_off(bm * 16 + L * 4, head);
#pragma unroll
      for (int i = 0; i < 4; ++i) poison(o + i * 1024);
      STPAD;
    };
    auto poison_x = [&](int l) AI {
      if (L < NCHK) {
        const unsigned o = WS_AR + l * ARENA + AR_X + bm * (512 * 1024) + head * (NCHK * 2048) + L * 2048;
        poison(o); poison(o + 1024);
        STPAD;
      }
    };
    auto poison_s = [&](int l) AI {
      if (L == 3) {
        const unsigned o = WS_AR + l * ARENA + AR_KV + (unsigned)(bm * 32 + head) * KVREC;
        poison(o); poison(o + 1024);
        STPAD;
      }
    };
    auto poison_kv = [&](int l) AI {
      const unsigned kvo = WS_AR + l * ARENA + AR_KV + (unsigned)(bm * 32 + head) * KVREC;
      poison(kvo + L * 1024);
      if (L == (l ? 0 : 1)) st16s(wsr, v16 < 256 ? v16 : OOB, kvo + 4096, pz);
      STPAD;
    };
    q_groupW(0, 0, 0);
    q_groupW(0, 1, 1);
    auto put_l2 = [&](float sm, float sq, unsigned vo, unsigned so) AI {
      xred2<false>(sm, sq);
      if (v16 < 256) st16s(wsr, vo, so, u32x4{__float_as_uint(sm), tag0, __float_as_uint(sq), tag1});
      MEMFENCE;
    };
    auto put_st0 = [&]() AI {
      if (L == 0) {
        const f32x4 t = *(const f32x4*)((const char*)(lmean + 16) + v16);
        put_l2(t[0] + t[2], t[1] + t[3], v16, WS_AR + AR_L2 + (head & 1) * 32768 + (bm * 16 + (head >> 1)) * 256);
      }
    };
    wait_vm<6>();
    auto body = [&](auto kind, const int layer) AI {
      constexpr bool isB = decltype(kind)::value == 1, is0 = decltype(kind)::value == 0;
      const unsigned ar = WS_AR + layer * ARENA;
      f32x4 rcs[4] = {};
      auto sW = [&]() AI { return WS_WO + (layer & 1) * (2 * MBY) + cn * (128 * 1024); };
      auto stg = [&](unsigned off, auto n) AI { glds_run<decltype(n)::value, true>(v16, ws + sW() + off, sbase + L_STG + off); };
      auto stg7 = [&](unsigned o) AI { stg(o, IC<4>{}); stg(o + 4096, IC<3>{}); };
      auto stg6 = [&](unsigned o) AI { stg(o, IC<3>{}); stg(o + 3072, IC<3>{}); };

      auto extra = [&](int b) AI {
        if (!is0) return;
        const unsigned lu = L_RT + L * 14336;
        if (b < 7) { SCHED0; PRIO(3); SCHED0; }
        if (b == 1) retile(L < 3, 0, lu, rcs);
        if (b == 2 && L == 3) retile(0, 1, lu + 4096, nullptr);
        if (b == 3) {
          ld_rt(L < 3 ? p.wqkvB : p.woB, 1, lu + 4096);
          if (L == 3) ld_rt(p.woB, 0, lu + 8192);
        }
        if (b == 6 && L < 3) retile(1, 1, lu + 4096, rcs);
        if (b == 8 && L == 3) { retile(1, 0, lu + 8192, nullptr); retile(1, 1, lu + 4096, nullptr); }
        if (b < 7) { SCHED0; PRIO(0); SCHED0; }
      };
      auto extra2 = [&](int b) AI {
        if (!is0) return;
        if (b < 7) { SCHED0; PRIO(3); SCHED0; }
        if (b == 2) put_st0();
        if (b == 1) poison_o();
        if (b == 2) poison_kv(0);
        if (b == 5) poison_kv(2);
        if (b == 8) { poison_s(1); poison_x(1); }
        if (b == 10) { poison_x(2); poison_s(3); }
        if (b < 7) { SCHED0; PRIO(0); SCHED0; }
      };
      if (is0) sync_lds();
      STAMP(0);
      {
        OPAQUE_S
        OPAQUE_LANE
        u32x4 c0, c1;
        asm volatile("" : "=v"(c0), "=v"(c1));
        auto ldcs = [&]() AI {
          if (L > 0) {
            const unsigned so = WS_CS + (((layer & 1) * 32 + head) * 1536 + (L - 1) * 512) * 4;
            c0 = ld16s(wsr, v16, so); c1 = ld16s(wsr, v16 + 1024, so);
          }
          MEMFENCE;
        };
        auto wrcs = [&]() AI {
          if (L > 0) {
            f32x4 t = add4(__builtin_bit_cast(f32x4, c0), __builtin_bit_cast(f32x4, c1));
#pragma unroll
            for (int e = 0; e < 4; ++e)
              t[e] += __uint_as_float(__builtin_amdgcn_update_dpp(0u, __float_as_uint(t[e]), 0x128, 0xf, 0xf, false));
            float t0 = t[0], t1 = t[1], t2 = t[2], t3 = t[3];
            xred4<false>(t0, t1, t2, t3);
            *(f32x4*)(lcs + (isB ? 96 : 0) + (L - 1) * 32 + (lane & 7) * 4) = f32x4{t0, t1, t2, t3};
          }
        };
        if (isB && layer == 1) ldcs();
        if constexpr (is0) {
          if (L == 3) flag(WS_F1 + 2048);
          q_groupW(0, 2, 2); q_groupW(0, 3, 3);
          wait_vm<12>();
        }
        STAMP(1);
        if (!is0) PRIO(3);
        sync_lds();
        STAMP(2);
        OPAQUE_S
        u32x4 s0 = u32x4{0u, 0u, 0u, 0u}, s1 = s0;
        auto ldst = [&]() AI {
          const unsigned so = ar + AR_L2 + (bm * 16 + L * 4) * 256;
          s0 = ld16s(wsr, v16, so);
          if (is0) s1 = ld16s(wsr, v16, so + 32768);
          if (isB && L == 0) s1 = ld16s(wsr, v16 & 255, bm > 0 ? so - 256 : so);
          MEMFENCE;
        };
        auto fin = [&](float sm, float sq, int r, bool own) AI {
          float mean = sm * (1.0f / 1024.0f);
          float var = fmaxf(__builtin_fmaf(sq, 1.0f / 1024.0f, -mul1(mean, mean)), 0.f);
          if (own) lmean[r] = -mean;
          lrstd[r] = __builtin_amdgcn_rsqf(var + 1e-5f);
        };
        auto wrst = [&]() AI {
          while (true) {
            unsigned d = (s0[1] ^ tag0) | (s0[3] ^ tag1);
            if (is0 || (isB && L == 0)) d |= (s1[1] ^ tag0) | (s1[3] ^ tag1);
            if (__builtin_expect(__builtin_amdgcn_ballot_w64(d != 0) == 0, 1) || --budget <= 0) break;
            COUNT(1);
            ldst();
          }
          MEMFENCE;
          float sm = __uint_as_float(s0[0]), sq = __uint_as_float(s0[2]);
          if (is0) { sm += __uint_as_float(s1[0]); sq += __uint_as_float(s1[2]); }
          fin(sm, sq, 16 + L * 64 + lane, true);
          if (isB && L == 0) fin(__uint_as_float(s1[0]), __uint_as_float(s1[2]), lane & 15, false);
        };
        auto q_group = [&](int g) AI {
          if (is0) {
            if (L & 1) SLEEP(SPR0);
            if (L & 2) SLEEP(2 * SPR0);
            q_groupW(layer, g, g & 3);
          } else if (g >= 5) q_groupW(layer, g, g < 6 ? g : g - 4);
        };
        if (!is0 && L == 0) {
          poll(ar + AR_FL, []() {}, 15);
          if (head < 16) {
            const unsigned so = ar + AR_ST + (bm * 16 + head) * 2048;
            s0 = ld16s(wsr, v16, so); s1 = ld16s(wsr, v16 + 1024, so);
            MEMFENCE;
          }
        }
        if (is0) {
          drain();
          poll(WS_F1 + 2048, [&]() AI { extra(1); });
          ldcs();
          extra2(1);
        }
        if (isB && layer == 1) wrcs();
        MEMFENCE;
        STAMP(3);
        if (!is0) PRIO(0);
        sync_lds();
        STAMP(4);
        OPAQUE_S
        if (!is0) drain();
        if (!is0 && L == 0 && head < 16) {
          f32x4 t = add4(__builtin_bit_cast(f32x4, s0), __builtin_bit_cast(f32x4, s1));
#pragma unroll
          for (int e = 0; e < 4; ++e)
            t[e] += __uint_as_float(__builtin_amdgcn_update_dpp(0u, __float_as_uint(t[e]), 0x128, 0xf, 0xf, false));
          const bool hi = lane & 8;
          put_l2(hi ? t[2] : t[0], hi ? t[3] : t[1], (lane & 7) * 32 + (lane & 8) * 2, ar + AR_L2 + (bm * 16 + head) * 256);
        }
        q_group(4);
        extra(2); extra2(2);
        STAMP(5);
        sync_lds();
        STAMP(6);
        OPAQUE_S
        drain();
        q_group(5);
        extra(3);
        if (is0) wrcs();
        STAMP(7);
        bar();
        OPAQUE_S
        drain();
        if (is0 && L == 0 && v16 == 0) __builtin_amdgcn_raw_buffer_store_b64(u32x2{tag0, tag1}, wsr, 0, WS_F2 + (bm * 32 + head) * 8, 16);
        q_group(6);
        ldst();
        STAMP(8);
        bar();
        STAMP(9);
        OPAQUE_S
        drain();
        q_group(7);
        extra2(5);
        SCHED0;
        wrst();
        STAMP(10);
        bar();
        STAMP(11);
        OPAQUE_S
        drain();
        extra(6);
        if (isB) stg6(96 * 1024 + L * 6144);
        SCHED0;
      }
      STAMP(12);
      sync_lds();
      STAMP(13);
      OPAQUE_S
      u32x4 fl[2];
      fl[0] = fl[1] = u32x4{tag0, tag1, tag0, tag1};
      auto ldfl = [&]() AI { fl[0] = ld16s(wsr, v16, WS_F2); fl[1] = ld16s(wsr, v16 + 1024, WS_F2); };
      if (is0) ldfl();
      bar();
      constexpr int NR = isB ? 6 : 9;
      const unsigned ro = isB ? 48 * 1024 + L * 6144 : L * 9216;
      u32x4 r[NR];
      bool pend = false;
      auto flags = [&]() AI {
        const u32x4 d = (fl[0] ^ u32x4{tag0, tag1, tag0, tag1}) | (fl[1] ^ u32x4{tag0, tag1, tag0, tag1});
        pend = __builtin_amdgcn_ballot_w64((d[0] | d[1] | d[2] | d[3]) != 0) != 0;
      };
      if (isB) {
        stg6(L * 6144); stg(120 * 1024 + L * 2048, IC<2>{});
        stg6(24 * 1024 + L * 6144);
        stg6(72 * 1024 + L * 6144);
      } else {
        drain();
        if (is0) flags();
        MEMFENCE;
        extra(8); extra2(8);
        if (!is0) poison_x(3);
        while (is0 && __builtin_expect(pend, 0) && --budget > 0) {
          ldfl();
          flags();
          MEMFENCE;
        }
        stg(36 * 1024 + L * 3072, IC<3>{}); stg7(72 * 1024 + L * 14336); stg7(72 * 1024 + L * 14336 + 7168);
      }
#pragma unroll
      for (int i = 0; i < NR; ++i) r[i] = ld16s(wsr, v16 + (i & 3) * 1024, sW() + ro + (i >> 2) * 4096);
      MEMFENCE;
      if (is0 && bm + head + L == 0 && v16 == 0) __builtin_amdgcn_raw_buffer_store_b32(tag0, wsr, 0, WS_EP, 16);
      auto wr_r = [&]() AI {
        unsigned vv = v16;
        asm volatile("" : "+v"(vv));
#pragma unroll
        for (int i = 0; i < NR; ++i) *(u32x4*)(smem + L_STG + ro + i * 1024 + vv) = r[i];
      };
      auto left = [&]() AI {
        OPAQUE_LANE
        for (;;) {
          MEMFENCE;
          if (__builtin_amdgcn_ballot_w64(lflag[lane & 7] != (unsigned)layer) == 0 || --budget <= 0) break;
          SLEEP(2);
        }
        MEMFENCE;
      };
      if (isB) {
#pragma unroll
        for (int i = 0; i < 24; ++i) asm volatile("" : "=v"(wn[i / 6][i % 6]));
        if (layer == 1) ld_wn(layer + 1, 0, 2);
        STAMP(14);
        left();
        wr_r();
        MEMFENCE;
        if (layer == 1) ld_wn(layer + 1, 2, 4);
        wait_vm_lgkm0<12>();
        if (layer == 3) wait_vm<0>();
      } else {
        ld_wn(layer + 1, 0, is0 ? 1 : 2);
        if (is0) wait_vm<32>();
        STAMP(14);
        bar();
        stg6(48 * 1024 + L * 6144);
        if (is0) ld_wn(layer + 1, 1, 2);
        bar();
        ld_wn(layer + 1, 2, 4);
        wr_r();
        wait_vm<12>();
        bar();
        extra2(10);
        wait_vm_lgkm0<0>();
      }
      bar();
      if (isB && layer == 3) { STAMP_DUMP; __builtin_amdgcn_endpgm(); }
      OPAQUE_S
      wr_wn(0);
      drain();
      sync_lds();
      OPAQUE_S
      SCHED0;
#pragma unroll
      for (int g = 1; g < 4; ++g) wr_wn(g);
      MEMFENCE;
      SLEEP(SLP_G);
      q_groupW(layer + 1, 4, 4);
    };
    body(IC<0>{}, 0);
#pragma unroll 1
    for (int layer = 1;; layer += 2) {
      body(IC<1>{}, layer);
      body(IC<2>{}, 2);
    }
  }

  const f32x4 zero = f32x4{0.f, 0.f, 0.f, 0.f};
  u32x4 xr[2];
  asm volatile("" : "=v"(xr[0]), "=v"(xr[1]));
  u32x4 sel;
  u32x4 eye;
  unsigned vk;
  unsigned ra0, ra1;
  float off0, off1;
  {
    const int lane = LANEID, fr = lane & 15, fq = lane >> 4;
    const bool odd = fr & 1, own0 = fr >= fq * 4 + (fr & 1), own1 = fr >= fq * 4 + (fr & 1) + 2, nopre = bm0 == 0 && wave == 4;
    const unsigned e0 = odd ? 0x01000c0cu : 0x0c0c0100u, e1 = odd ? 0x03020c0cu : 0x0c0c0302u, ez = 0x0c0c0c0cu;
    sel = u32x4{own0 ? ez : e0, own1 ? ez : e1, own0 ? e0 : ez, own1 ? e1 : ez};
    const unsigned rb = sbase + L_RED + 1088 + ((wave - 4) * 32 + fq * 4 + (fr & 1)) * 4;
    ra0 = rb + (own0 ? 64 : 0); ra1 = rb + (own1 ? 72 : 8);
    off0 = nopre && !own0 ? -3.0e38f : 0.f; off1 = nopre && !own1 ? -3.0e38f : 0.f;
    const unsigned one = (fq & 1) == (fr >> 3) ? (fr & 1 ? 0x3f800000u : 0x00003f80u) : 0u;
    eye = u32x4{(fr & 6) == 0 ? one : 0u, (fr & 6) == 2 ? one : 0u, (fr & 6) == 4 ? one : 0u, (fr & 6) == 6 ? one : 0u};
    vk = (fq >> 1) * (32 * KVREC) + (fq & 1) * 256 + fr * 16;
    asm volatile("" : "+v"(sel), "+v"(ra0), "+v"(ra1), "+v"(off0), "+v"(off1), "+v"(eye), "+v"(vk));
  }
#pragma unroll 1
  for (int layer = 0; layer < 4; ++layer) {
    bm = bm0; head = head0; cm = cm0; cn = cn0; cw = wave - 4;
    const bool isB = layer & 1;
    const unsigned ar = WS_AR + layer * ARENA;
    {
      f32x4 acc[2][6];
      f32x4 ck[2], mv[2], xv[2];
      float mean[2], rs[2], cv[2];
      const float* lcl = lcs + (isB ? 96 : 0);
      {
        LANE_VARS
        const unsigned va = v16 + cw * 2048;
        const unsigned sa0 = ar + AR_X + bm * (512 * 1024);
        auto lda = [&](unsigned so, u32x4* a) AI {
          a[0] = ld16s(wsr, va, so);
          a[1] = ld16s(wsr, va + 1024, so);
        };
        bf16x8 f[2][6];
        u32x4 a[4][2];
        auto unset = [&](const u32x4* e) AI { return poisoned(umax(umax(umax(e[0][0], e[0][2]), e[1][0]), e[1][2])); };
        auto refill = [&](auto ic, unsigned cur) AI {
          constexpr int I = decltype(ic)::value;
          do {
            COUNT(0);
            SLEEP(SLP_R);
            MEMFENCE;
            lda(cur, a[I]);
            bool u[4];
#pragma unroll
            for (int j = 1; j < 4; ++j) u[j] = unset(a[(I + j) % 4]);
            SCHED0;
#pragma unroll
            for (int j = 1; j < 4; ++j)
              if (u[j]) lda(cur + j * 16384, a[(I + j) % 4]);
            __builtin_amdgcn_s_waitcnt(0x0f70);
            MEMFENCE;
          } while (unset(a[I]) && --budget > 0);
        };
        STAMP(0);
        if (layer > 0) {
          bar();
          if (cn < 8) SLEEP(SLP_X); else SLEEP(SLP_XD);
        }
#pragma unroll
        for (int j = 0; j < 4; ++j) { lda(sa0 + j * 16384, a[j]); SCHED0; }
#pragma unroll
        for (int m = 0; m < 2; ++m)
#pragma unroll
          for (int n = 0; n < 6; ++n) acc[m][n] = zero;
        asm volatile("" : "+v"(acc[0][0]), "+v"(acc[0][1]), "+v"(acc[0][2]), "+v"(acc[0][3]), "+v"(acc[0][4]), "+v"(acc[0][5]));
        asm volatile("" : "+v"(acc[1][0]), "+v"(acc[1][1]), "+v"(acc[1][2]), "+v"(acc[1][3]), "+v"(acc[1][4]), "+v"(acc[1][5]));
        if (layer == 0) { wait_vm<8>(); sync_lds(); }
#pragma unroll
        for (int nn = 0; nn < 6; ++nn) { const int n = nn < 4 ? nn + 2 : nn - 4; f[0][n] = *(const bf16x8*)(smem + v16 + n * 1024); }
        SCHED0;
        if (layer > 0) {
          const u32x2 tg = *(const u32x2*)(lflag + 10);
          wait_vm<8>();
          unsigned old;
          asm volatile("s_mov_b64 exec, 1\n\tds_add_rtn_u32 %0, %1, %2\n\ts_mov_b64 exec, -1\n\ts_waitcnt lgkmcnt(0)" : "=v"(old) : "v"(sbase + L_RED + 32), "v"(1) : "memory");
          if (__builtin_amdgcn_readfirstlane(old) == 8u * layer - 1)
            __builtin_amdgcn_raw_buffer_store_b64(tg, wsr, lane ? OOB : 0, ar + AR_FL + bm * 256 + (cm * 16 + cn) * 8, 16);
          MEMFENCE;
        }
        SCHED0;
        bool bad = layer > 0 && unset(a[0]);
        const int wrap = layer > 0 ? 6 : 4;
        unsigned ps[4] = {sbase + L_RED + 64 + cw * 128, sbase + L_RED + 2176 + (layer & 1) * 384, 0, WS_AR + AR_X + xt_off(bm * 16 + cw * 2 + cm, cn * 2)};
        ps[2] = ps[0] + 1088 + (layer & 1) * (2176 + 384 - 1088 - 64 - cw * 128);
        int early = (layer & 1) * cw;
        asm volatile("" : "+s"(ps[0]), "+s"(ps[1]), "+s"(ps[2]), "+s"(ps[3]), "+s"(early));
        STAMP(1);
        auto group = [&](auto modec, int g) AI {
          constexpr int MODE = decltype(modec)::value;
          constexpr bool LAST = MODE == 3, CHK = MODE == 0;
          const unsigned gs = (g < wrap ? g : g - 4) * QGRP, gn = (g + 1 < wrap ? g + 1 : g - 3) * QGRP;
          const unsigned sc = sa0 + g * 65536, so = sc + 4 * 16384;
          unsigned gl = sbase + gs + v16;
          if constexpr (LAST) asm volatile("" : "+v"(gl));
          unsigned pa[4];
          static_for<4>([&](auto ic) AI {
            constexpr int i = decltype(ic)::value;
            constexpr bool END = LAST && i == 3;
            SCHED0;
            if (i == 3 && !LAST) { sync_lds(); SCHED0; }
            if (i == 0 && LAST) {
              PRIO(1);
              if (early >= 4) PRIO(3);
              SCHED0;
              bar();
              SCHED0;
            }
            if constexpr (!LAST) PRIO(i == 3 ? 3 : 2 - i);
            if constexpr (CHK) { if (__builtin_expect(bad, 0)) refill(ic, sc + i * 16384); }
            SCHED0;
            const bf16x8* fc = f[i & 1];
            bf16x8* fn = f[(i + 1) & 1];
            const u32x4* an = a[(i + 1) % 4];
            const unsigned slot = i < 3 ? gs + (i + 1) * QSLOT : gn;
            const char* pb = LAST ? (const char*)((const LDS(char)*)gl + (i + 1) * QSLOT) : smem + slot + v16;
            unsigned mx = 0;
            static_for<12>([&](auto jc) AI {
              constexpr int j = decltype(jc)::value, nn = j >> 1, m = j & 1, n = nn < 4 ? nn + 2 : nn - 4;
              if constexpr (n < 4) mfma_acc(acc[m][n], fc[n], as_frag(a[i][m]));
              else mfma_acc(acc[m][n], as_frag(a[i][m]), fc[n]);
              SCHED0;
              if constexpr (!END && j < 6) { constexpr int r = j < 4 ? j + 2 : j - 4; fn[r] = *(const bf16x8*)(pb + r * 1024); }
              if constexpr (LAST && i == 0 && j == 6) {
                if (layer == 0) { xr[0] = ld16s(wsr, v16, ps[3]); xr[1] = ld16s(wsr, v16, ps[3] + 16384); }
              }
              if constexpr (LAST && i == 1) {
                if constexpr (j == 6) { pa[0] = lane & 48; asm volatile("" : "+v"(pa[0])); }
                if constexpr (j == 7) { pa[1] = v16 >> 2; asm volatile("" : "+v"(pa[1])); }
                if constexpr (j == 8) { pa[1] &= 60; asm volatile("" : "+v"(pa[1])); }
                if constexpr (j == 9) { pa[2] = pa[1] + ps[0]; asm volatile("" : "+v"(pa[2])); }
                if constexpr (j == 10) { pa[1] += ps[1]; asm volatile("" : "+v"(pa[1])); }
                if constexpr (j == 11) { pa[3] = pa[0] + ps[1]; asm volatile("" : "+v"(pa[3])); }
              }
              if constexpr (LAST && i == 2) {
                if constexpr (j == 6) ck[0] = ((const LDS(f32x4)*)pa[3])[8];
                if constexpr (j == 7) ck[1] = ((const LDS(f32x4)*)pa[3])[12];
                if constexpr (j == 8) { mean[0] = ((const LDS(float)*)pa[2])[0]; asm volatile("" : "+v"(pa[2])); }
                if constexpr (j == 9) { mean[1] = ((const LDS(float)*)pa[2])[16]; asm volatile("" : "+v"(pa[2])); }
                if constexpr (j == 10) { cv[0] = ((const LDS(float)*)pa[1])[64]; cv[1] = ((const LDS(float)*)pa[1])[80]; }
                if constexpr (j == 11) { pa[1] = pa[0] + ps[0]; asm volatile("" : "+v"(pa[1])); }
              }
              if constexpr (LAST && i == 3) {
                if constexpr (j == 0) mv[0] = ((const LDS(f32x4)*)pa[1])[0];
                if constexpr (j == 1) mv[1] = ((const LDS(f32x4)*)pa[1])[4];
                if constexpr (j == 2) { pa[0] += ps[2]; asm volatile("" : "+v"(pa[0])); }
                if constexpr (j == 3) { rs[0] = ((const LDS(float)*)pa[2])[272]; asm volatile("" : "+v"(pa[2])); }
                if constexpr (j == 4) xv[0] = ((const LDS(f32x4)*)pa[0])[0];
                if constexpr (j == 5) xv[1] = ((const LDS(f32x4)*)pa[0])[4];
                if constexpr (j == 6) rs[1] = ((const LDS(float)*)pa[2])[288];
              }
              if constexpr (CHK) {
                if constexpr (j == 8) mx = umax(umax(umax(an[0][0], an[0][2]), an[1][0]), an[1][2]);
                if constexpr (j == 10) bad = poisoned(mx);
              }
              if constexpr (j == 10 && !LAST) a[i][0] = ld16s(wsr, va, so + i * 16384);
              if constexpr (j == 11 && !LAST) a[i][1] = ld16s(wsr, va + 1024, so + i * 16384);
              SCHED0;
            });
          });
        };
        int g = 0;
        if (layer > 0) {
#pragma unroll 1
          for (; g < NCHK; ++g) group(IC<0>{}, g);
        }
#pragma unroll 1
        for (; g < 7; ++g) group(IC<1>{}, g);
        group(IC<3>{}, 7);
        if (!isB && cw >= 4) PRIO(2);
        mfma_done();
      }
      STAMP(2);
      LANE_VARS
      char* const scr = smem;

      u32x4 qf[2], kf[2];
      u32x2 vf[2][2];
      auto fold_rows = [&](int part, u32x4* out, const f32x4* c) AI {
#pragma unroll
        for (int m = 0; m < 2; ++m) {
          f32x4 a0 = acc[m][part * 2] + c[0] * mean[m], a1 = acc[m][part * 2 + 1] + c[1] * mean[m];
          out[m] = u32x4{pk2(a0[0], a0[1]), pk2(a0[2], a0[3]), pk2(a1[0], a1[1]), pk2(a1[2], a1[3])};
        }
      };
      auto fold_x = [&](int part, f32x2 (*x)[4], const f32x4* c) AI {
#pragma unroll
        for (int m = 0; m < 2; ++m) {
          const f32x4 a0 = acc[m][part * 2] + c[0] * mean[m], a1 = acc[m][part * 2 + 1] + c[1] * mean[m];
          x[m][0] = f32x2{a0[0], a0[1]}; x[m][1] = f32x2{a0[2], a0[3]}; x[m][2] = f32x2{a1[0], a1[1]}; x[m][3] = f32x2{a1[2], a1[3]};
        }
      };
      auto fold_v = [&](bool scaled) AI {
#pragma unroll
        for (int m = 0; m < 2; ++m) {
          f32x4 a0 = acc[m][4] + mv[m] * cv[0], a1 = acc[m][5] + mv[m] * cv[1];
          if (scaled) { a0 = mul4(a0, xv[m]); a1 = mul4(a1, xv[m]); }
          vf[m][0] = u32x2{pk2(a0[0], a0[1]), pk2(a0[2], a0[3])};
          vf[m][1] = u32x2{pk2(a1[0], a1[1]), pk2(a1[2], a1[3])};
        }
      };
      const unsigned otile = WS_AR + AR_O + xt_off(bm * 16 + cw * 2, head);
      auto put_o = [&](int m, f32x4 a, f32x4 b, unsigned opar) AI {
        const u32x4 v = u32x4{(pk2(a[0], a[1]) & ~1u) | opar, pk2(a[2], a[3]), (pk2(b[0], b[1]) & ~1u) | opar, pk2(b[2], b[3])};
        st16s(wsr, v16 + m * 1024, otile, v);
        return v;
      };
      auto hold = [&](u32x4 a, u32x4 b) AI { SCHED0; asm volatile("s_nop 1" :: "v"(a), "v"(b)); SCHED0; };

      if (!isB) {
        f32x2 lx[2][4];
        fold_x(1, lx, ck); fold_v(true);
        char* PK = smem + L_STG;
        char* VF = scr;
#pragma unroll
        for (int t = 0; t < 2; ++t)
          *(u32x4*)(VF + (cw * 2 + t) * 1024 + lane * 16) = u32x4{vf[0][t][0], vf[0][t][1], vf[1][t][0], vf[1][t][1]};
        {
          u32x4 pk[2][2];
          phi_map2(lx, rs, pk, [](float*) {});
          char* d0 = PK + cw * 4096 + fr * 64 + ((fr >> 2) & 1) * 32 + ((fq ^ ((fr >> 3) * 2)) * 8);
          char* d1 = PK + cw * 4096 + fr * 64 + (((fr >> 2) & 1) ^ 1) * 32 + ((fq ^ ((fr >> 3) * 2)) * 8);
#pragma unroll
          for (int m = 0; m < 2; ++m) {
            *(u32x2*)(d0 + m * 2048) = u32x2{pk[m][0][0], pk[m][0][1]};
            *(u32x2*)(d0 + m * 2048 + 1024) = u32x2{pk[m][0][2], pk[m][0][3]};
            *(u32x2*)(d1 + m * 2048) = u32x2{pk[m][1][0], pk[m][1][1]};
            *(u32x2*)(d1 + m * 2048 + 1024) = u32x2{pk[m][1][2], pk[m][1][3]};
          }
        }
        PRIO(1);
        const unsigned rec = ar + AR_KV + (unsigned)head * KVREC, dst = rec + (unsigned)bm * (32 * KVREC);
        unsigned ad = sbase + L_STG + (cw & 1) * 1024 + (fq * 4 + (fr >> 2)) * 64 + (((cw >> 1) ^ fq) & 1) * 32 + (((fr & 3) ^ (fq & 2)) * 8);
        unsigned vd = sbase + (cw >> 2) * 1024 + v16, kd = fq & 1 ? OOB : dst + cw * 512 + (fq >> 1) * 256 + fr * 16;
        asm volatile("" : "+v"(ad), "+v"(vd), "+v"(kd));
        sync_lds();
        STAMP(3);
        {
          const u32x4 ones = u32x4{0x3f803f80u, 0x3f803f80u, 0x3f803f80u, 0x3f803f80u};
          f32x4 c0 = zero;
          bf16x8 a[8], b0[8];
#pragma unroll
          for (int ks = 0; ks < 8; ++ks) {
            u32x2 a0 = trl(ad + ks * 4096), a1 = trl(ad + ks * 4096 + 2048);
            a[ks] = as_frag(u32x4{a0[0], a0[1], a1[0], a1[1]});
            b0[ks] = *(const LDS(bf16x8)*)(vd + ks * 2048);
            SCHED0;
          }
#pragma unroll
          for (int ks = 0; ks < 8; ++ks) c0 = mfma16(a[ks], b0[ks], c0);
          const unsigned p0 = umin(pk2(c0[0], c0[1]), POISON - 1), p1 = umin(pk2(c0[2], c0[3]), POISON - 1);
          const auto q0 = __builtin_amdgcn_permlane16_swap(p0, p0, false, false), q1 = __builtin_amdgcn_permlane16_swap(p1, p1, false, false);
          st16(wsr, kd, u32x4{q0[0], q1[0], q0[1], q1[1]});
          SCHED0;
          if (cw < 4) {
            f32x4 cz = zero;
#pragma unroll
            for (int ks = 0; ks < 8; ++ks) cz = mfma16(a[ks], as_frag(ones), cz);
            st16(wsr, fr ? OOB : dst + 4096 + cw * 64 + fq * 16, clean(__builtin_bit_cast(u32x4, cz)));
          }
        }
        SCHED0;
        STAMP(4);
        bar();
        const f32x4 cq[2] = {*(const f32x4*)(lcl + fq * 4), *(const f32x4*)(lcl + 16 + fq * 4)};
        fold_x(0, lx, cq);
        u32x4 pq[2][2];
        u32x4 zp, zq;
        u32x4 part[4];
        auto ldp = [&](int i) AI { part[i] = ld16s(wsr, vk, rec + i * (64 * KVREC) + cw * 512); };
        auto ldz = [&]() AI {
          zp = ld16(wsr, rec + fq * (32 * KVREC) + 4096 + fr * 16);
          zq = ld16(wsr, rec + (4 + fq) * (32 * KVREC) + 4096 + fr * 16);
        };
#pragma unroll
        for (int i = 0; i < 4; ++i) asm volatile("" : "=v"(part[i]));
        asm volatile("" : "=v"(zp), "=v"(zq));
        phi_map2(lx, rs, pq, [&](float* sp) AI {
          asm volatile("" : "+v"(sp[0]), "+v"(sp[1]));
          SCHED0;
          if (cw >= 4) {
            MEMFENCE;
#pragma unroll
            for (int i = 0; i < 4; ++i) ldp(i);
            MEMFENCE;
          }
          SCHED0;
        });
        asm volatile("" : "+v"(pq[0][0]), "+v"(pq[0][1]), "+v"(pq[1][0]), "+v"(pq[1][1]));
        SCHED0;
        STAMP(5);
        char* kvf = smem + L_A2;
        if (cw < 4) {
          SLEEP(SLP_K);
          MEMFENCE;
          if (cw == 0) ldz();
#pragma unroll
          for (int i = 0; i < 4; ++i) ldp(i);
          MEMFENCE;
        }
        unsigned kw = sbase + L_A2 + v16 + (cw >> 1) * 1024 + (cw & 1) * 8;
        unsigned ka = sbase + L_A2 + v16, za = sbase + L_Z + fq * 16;
        asm volatile("" : "+v"(kw), "+v"(ka), "+v"(za));
        u32x2 kvs;
        auto nans = [&](f32x4 v) AI { return __builtin_amdgcn_ballot_w64(__builtin_isunordered(v[0], v[1])) | __builtin_amdgcn_ballot_w64(__builtin_isunordered(v[2], v[3])); };
        auto sums = [&]() AI {
          unsigned long long nan = 0;
          if (cw == 0) {
            f32x4 z = __builtin_bit_cast(f32x4, zp) + __builtin_bit_cast(f32x4, zq);
            nan = nans(z);
            float z0 = z[0], z1 = z[1], z2 = z[2], z3 = z[3];
            xred4<false>(z0, z1, z2, z3);
            *(u32x2*)(smem + L_Z + (fr >> 3) * 64 + (fr & 3) * 16 + ((fr >> 2) & 1) * 8) = u32x2{pk2(z0, z1), pk2(z2, z3)};
          }
          f32x4 s4 = zero;
#pragma unroll
          for (int i = 0; i < 4; ++i) { s4 = mfma16(as_frag(eye), as_frag(part[i]), s4); SCHED0; }
          kvs = u32x2{pk2(s4[0], s4[1]), pk2(s4[2], s4[3])};
          asm volatile("" : "+v"(kvs));
          return nan | __builtin_amdgcn_ballot_w64(s4[0] != s4[0]);
        };
        if (__builtin_expect(sums() != 0, 0)) {
          for (;;) {
            bool again = false;
            if (cw == 0 && poisoned(umax4(umax4(zp), zq))) { again = true; ldz(); }
#pragma unroll
            for (int i = 0; i < 4; ++i)
              if (poisoned(umax4(part[i]))) { again = true; ldp(i); }
            __builtin_amdgcn_s_waitcnt(0x0f70);
            MEMFENCE;
            if (!again || --budget <= 0) break;
            COUNT(2);
          }
          sums();
        }
        STAMP(6);
        *(LDS(u32x2)*)kw = kvs;
        sync_lds();
        STAMP(7);
        bf16x8 kvr[2][2], zf[2];
#pragma unroll
        for (int s = 0; s < 2; ++s) { zf[s] = *(const LDS(bf16x8)*)(za + s * 64); SCHED0; }
#pragma unroll
        for (int s = 0; s < 2; ++s) {
          kvr[0][s] = *(const LDS(bf16x8)*)(ka + (0 * 2 + s) * 1024);
          kvr[1][s] = *(const LDS(bf16x8)*)(ka + (1 * 2 + s) * 1024);
          SCHED0;
        }
        f32x4 cd[2], c[2][2];
        u32x4 oo[2];
        auto den = [&](int m, int s) AI { cd[m] = mfma16(zf[s], as_frag(pq[m][s]), s ? cd[m] : zero); SCHED0; };
        auto num = [&](int m, int s) AI {
#pragma unroll
          for (int t = 0; t < 2; ++t) { c[m][t] = mfma16(kvr[t][s], as_frag(pq[m][s]), s ? c[m][t] : zero); SCHED0; }
        };
        auto fin = [&](int m, float id) AI { const f32x4 i4 = f32x4{id, id, id, id}; oo[m] = put_o(m, mul4(c[m][0], i4), mul4(c[m][1], i4), 0); SCHED0; };
        den(0, 0); den(1, 0); den(0, 1); den(1, 1);
        num(0, 0); num(0, 1); num(1, 0);
        const float i0 = __builtin_amdgcn_rcpf(cd[0][0] + 1e-6f), i1 = __builtin_amdgcn_rcpf(cd[1][0] + 1e-6f);
        SCHED0;
        num(1, 1);
        fin(0, i0); fin(1, i1);
        hold(oo[0], oo[1]);
        STAMP(8);
        asm volatile("" : "=v"(acc[0][0]), "=v"(acc[0][1]), "=v"(acc[0][2]), "=v"(acc[0][3]), "=v"(acc[0][4]), "=v"(acc[0][5]));
        asm volatile("" : "=v"(acc[1][0]), "=v"(acc[1][1]), "=v"(acc[1][2]), "=v"(acc[1][3]), "=v"(acc[1][4]), "=v"(acc[1][5]));
        asm volatile("" : "=v"(ck[0]), "=v"(ck[1]), "=v"(mv[0]), "=v"(mv[1]), "=v"(xv[0]), "=v"(xv[1]));
        asm volatile("" : "=v"(mean[0]), "=v"(mean[1]), "=v"(rs[0]), "=v"(rs[1]), "=v"(cv[0]), "=v"(cv[1]));
      }
      int kind = layer & 1;
      asm volatile("" : "+s"(kind));
      if (kind) {
        char* ex = scr;
        fold_rows(1, kf, ck); fold_v(false);
        const unsigned hrec = ar + AR_KV + (unsigned)(bm * 32 + head) * KVREC;
        {
          const u32x4 vl = u32x4{vf[1][0][0], vf[1][0][1], vf[1][1][0], vf[1][1][1]};
          if (cw == 7) {
            st16s(wsr, v16, hrec, clean(kf[1]));
            STPAD;
            st16s(wsr, v16 + 1024, hrec, clean(vl));
            STPAD;
          }
          char* e2 = ex + (cw + 1) * 2048;
          *(u32x4*)(e2 + lane * 16) = kf[1];
          *(u32x2*)(e2 + 1024 + lane * 8) = vf[1][0];
          *(u32x2*)(e2 + 1536 + lane * 8) = vf[1][1];
          MEMFENCE;
          lflag[cw] = layer;
          MEMFENCE;
          PRIO(1);
        }
        SCHED0;
        fold_rows(0, qf, xv);
        u32x4 kp;
        u32x4 va0 = __builtin_shufflevector(vf[0][0], vf[0][0], -1, -1, 0, 1), vb0 = __builtin_shufflevector(vf[0][1], vf[0][1], -1, -1, 0, 1);
        auto setp = [&](u32x4& d, u32x2 v) AI { d[0] = v[0]; d[1] = v[1]; };
        auto ldh = [&]() AI {
          MEMFENCE;
          kp = ld16s(wsr, v16, hrec - 32 * KVREC);
          setp(va0, __builtin_amdgcn_raw_buffer_load_b64(wsr, v16 + 1024, hrec - 32 * KVREC, 16));
          MEMFENCE;
          setp(vb0, __builtin_amdgcn_raw_buffer_load_b64(wsr, v16 + 1032, hrec - 32 * KVREC, 16));
          MEMFENCE;
        };
        unsigned fl = 0;
        auto rdl = [&]() AI {
          MEMFENCE;
          fl = lflag[cw - 1];
          MEMFENCE;
          kp = *(const u32x4*)(ex + cw * 2048 + lane * 16);
          setp(va0, *(const u32x2*)(ex + cw * 2048 + 1024 + lane * 8));
          MEMFENCE;
          setp(vb0, *(const u32x2*)(ex + cw * 2048 + 1536 + lane * 8));
          MEMFENCE;
        };
        auto prev = [&]() AI {
          if (cw > 0) {
            do { SLEEP(1); rdl(); __builtin_amdgcn_s_waitcnt(0xc07f); } while (__builtin_amdgcn_readfirstlane(fl) != (unsigned)layer && --budget > 0);
          } else if (bm > 0) {
            SLEEP(SLP_HX);
            do { ldh(); __builtin_amdgcn_s_waitcnt(0x0f70); }
            while (poisoned(umax4(umax4(kp), u32x4{va0[0], va0[1], vb0[0], vb0[1]})) && --budget > 0);
          } else {
            kp = u32x4{0u, 0u, 0u, 0u};
            setp(va0, u32x2{0u, 0u}); setp(vb0, u32x2{0u, 0u});
          }
          asm volatile("" : "+v"(kp), "+v"(va0), "+v"(vb0));
        };
        const float scale = 0.17677669529663687f * LOG2E;
        const bool odd = fr & 1;
        const bool own0 = sel[0] == 0x0c0c0c0cu, own1 = sel[1] == 0x0c0c0c0cu;
        float l[2][2], mx[2], rk[2][2], sum[2];
#pragma unroll
        for (int m = 0; m < 2; ++m) { rk[m][0] = ((const LDS(float)*)ra0)[m * 16]; rk[m][1] = ((const LDS(float)*)ra1)[m * 16]; }
        float qk[2][2];
#pragma unroll
        for (int m = 0; m < 2; ++m) { const float qs = rs[m] * scale; qk[m][0] = qs * rk[m][0]; qk[m][1] = qs * rk[m][1]; }
        u32x4 va1 = u32x4{vf[0][0][0], vf[0][0][1], vf[1][0][0], vf[1][0][1]}, vb1 = u32x4{vf[0][1][0], vf[0][1][1], vf[1][1][0], vf[1][1][1]};
        auto score = [&](int m) AI {
          f32x4 s0 = mfma16(as_frag(m == 0 ? kp : kf[0]), as_frag(qf[m]), zero);
          f32x4 s1 = mfma16(as_frag(kf[m]), as_frag(qf[m]), zero);
          float a = own0 ? (odd ? s1[1] : s1[0]) : (odd ? s0[1] : s0[0]);
          float b = own1 ? (odd ? s1[3] : s1[2]) : (odd ? s0[3] : s0[2]);
          if (m == 0) { l[m][0] = __builtin_fmaf(a, qk[m][0], off0); l[m][1] = __builtin_fmaf(b, qk[m][1], off1); }
          else { l[m][0] = a * qk[m][0]; l[m][1] = b * qk[m][1]; }
          mx[m] = fmx(l[m][0], l[m][1]);
        };
        auto expo = [&](int m) AI {
          l[m][0] = __builtin_amdgcn_exp2f(l[m][0] - mx[m]);
          l[m][1] = __builtin_amdgcn_exp2f(l[m][1] - mx[m]);
          sum[m] = l[m][0] + l[m][1];
        };
        f32x4 ov[2][2];
        auto pv = [&](int m) AI {
          const unsigned pr = pk2(mul1(mul1(l[m][0], rk[m][0]), sum[m]), mul1(mul1(l[m][1], rk[m][1]), sum[m]));
          const u32x4 pf = u32x4{__builtin_amdgcn_perm(pr, pr, sel[0]), __builtin_amdgcn_perm(pr, pr, sel[1]),
                                 __builtin_amdgcn_perm(pr, pr, sel[2]), __builtin_amdgcn_perm(pr, pr, sel[3])};
          ov[m][0] = mfma16(as_frag(m ? va1 : va0), as_frag(pf), zero);
          ov[m][1] = mfma16(as_frag(m ? vb1 : vb0), as_frag(pf), zero);
        };
        asm volatile("" : "+v"(qf[0]), "+v"(qf[1]), "+v"(va0), "+v"(vb0));
        SCHED0;
        if (cw > 0) rdl();
        SCHED0;
        score(1);
        asm volatile("" : "+v"(qf[0]), "+v"(qk[0][0]), "+v"(qk[0][1]), "+v"(mx[1]), "+v"(l[1][0]), "+v"(l[1][1]));
        asm volatile("" : "+v"(va1), "+v"(vb1));
        SCHED0;
        STAMP(3);
        if (__builtin_expect(cw == 0 || __builtin_amdgcn_readfirstlane(fl) != (unsigned)layer, 0)) prev();
        score(0);
        xred2<true>(mx[0], mx[1]);
        expo(0); expo(1);
        xred2<false, true>(sum[0], sum[1]);
        pv(0); pv(1);
        SCHED0;
        const u32x4 o0 = put_o(0, ov[0][0], ov[0][1], 1), o1 = put_o(1, ov[1][0], ov[1][1], 1);
        hold(o0, o1);
        STAMP(4);
      }
    }
    {
      LANE_VARS
      const int mt = bm * 16 + cw * 2 + cm;
      const unsigned so0 = WS_AR + AR_O + xt_off(mt, 0);
      const unsigned opar = layer & 1;
      auto diff = [&](u32x4 v) AI { return ((v[0] ^ opar) | (v[2] ^ opar)) & 1u; };
      auto dif3 = [&](u32x4 v) AI { return __builtin_amdgcn_bitop3_b32(v[0], v[2], opar, 0x7e); };
      auto any = [&](unsigned d) AI { return __builtin_amdgcn_ballot_w64(d != 0) != 0; };
      const int g0 = cm;
      u32x4 a[RO];
      auto refill = [&](auto ic, auto wc, unsigned cur, unsigned nxt) AI {
        constexpr int I = decltype(ic)::value, W = decltype(wc)::value;
        do {
          COUNT(1);
          SLEEP(SLP_R);
          MEMFENCE;
          a[I] = ld16s(wsr, v16, cur + I * 16384);
          bool u[RO];
#pragma unroll
          for (int j = I + 1; j < I + W; ++j) u[j % RO] = any(diff(a[j % RO]));
          SCHED0;
#pragma unroll
          for (int j = I + 1; j < I + W; ++j)
            if (u[j % RO]) a[j % RO] = ld16s(wsr, v16, (j < RO ? cur : nxt) + (j % RO) * 16384);
          __builtin_amdgcn_s_waitcnt(0x0f70);
          MEMFENCE;
        } while (any(diff(a[I]) | diff(a[I + 1])) && --budget > 0);
      };
      const unsigned sf = so0 + g0 * (RO * 16384);
      f32x4 acc[4];
#pragma unroll
      for (int n = 0; n < 4; ++n)
        acc[n] = f32x4{bflo(xr[n >> 1][(n & 1) * 2]), bfhi(xr[n >> 1][(n & 1) * 2]), bflo(xr[n >> 1][(n & 1) * 2 + 1]), bfhi(xr[n >> 1][(n & 1) * 2 + 1])};
      asm volatile("" : "+v"(acc[0]), "+v"(acc[1]), "+v"(acc[2]), "+v"(acc[3]));
      SCHED0;
      const bool late = isB && cw == 0;
      if (late) bar();
      if (cn < 8) { if (late) SLEEP(SLP_O0); else SLEEP(SLP_O); }
      else { if (late) SLEEP(SLP_O0D); else SLEEP(SLP_OD); }
#pragma unroll
      for (int j = 0; j < 4; ++j) { a[j] = ld16s(wsr, v16, sf + j * 16384); SCHED0; }
      if (!late) bar();
      STAMP(9);
      bf16x8 wf[2][4];
      const char* pw0 = smem + L_STG + v16;
#pragma unroll
      for (int n = 0; n < 8; ++n) wf[n >> 2][n & 3] = *(const bf16x8*)(pw0 + g0 * (RO * 4096) + n * 1024);
      SCHED0;
      STAMP(10);
      if (any(diff(a[0]) | diff(a[1]))) refill(IC<0>{}, IC<4>{}, sf, sf);
      SCHED0;
#pragma unroll
      for (int j = 4; j < RO; ++j) { a[j] = ld16s(wsr, v16, sf + j * 16384); SCHED0; }
      STAMP(11);
      bool bad = false;
      auto group = [&](auto lastc, int g) AI {
        constexpr bool LAST = decltype(lastc)::value;
        const int gc = g ^ g0, gn = (g + 1) ^ g0;
        const unsigned sc = so0 + gc * (RO * 16384), so = so0 + gn * (RO * 16384);
        const char* pc = pw0 + gc * (RO * 4096);
        const char* pn = pw0 + gn * (RO * 4096);
        static_for<RO>([&](auto ic) AI {
          constexpr int i = decltype(ic)::value;
          SCHED0;
          if constexpr (!LAST && i == 0) { if (g == 0) PRIO(3); else if (g == 2) PRIO(2); }
          if constexpr (LAST && i == 3) PRIO(1);
          if constexpr (i % 2 == 0)
            if (__builtin_expect(bad, 0)) refill(ic, IC<(LAST && i + 4 > RO) ? RO - i : 4>{}, sc, so);
          SCHED0;
          unsigned mx = 0, my = 0;
          static_for<4>([&](auto nc) AI {
            constexpr int n = decltype(nc)::value;
            mfma_acc(acc[n], wf[i & 1][n], as_frag(a[i]));
            SCHED0;
            if constexpr (i + 2 < RO) wf[i & 1][n] = *(const bf16x8*)(pc + (i + 2) * 4096 + n * 1024);
            else if constexpr (!LAST) wf[i & 1][n] = *(const bf16x8*)(pn + (i + 2 - RO) * 4096 + n * 1024);
            if constexpr (!(LAST && i == RO - 1) && (i & 1)) {
              if constexpr (n == 0) mx = dif3(a[(i + 1) % RO]);
              if constexpr (n == 1) my = dif3(a[(i + 2) % RO]);
              if constexpr (n == 2) bad = any((mx | my) & 1u);
            }
            if constexpr (n == 3 && !LAST) a[i] = ld16s(wsr, v16, so + i * 16384);
            SCHED0;
          });
        });
      };
#pragma unroll 1
      for (int g = 0; g < 3; ++g) group(std::false_type{}, g);
      group(std::true_type{}, 3);
      mfma_done();
      STAMP(12);
#pragma unroll
      for (int j = 0; j < 2; ++j)
#pragma unroll
        for (int e = 0; e < 4; ++e) xr[j][e] = pk2(acc[j * 2 + (e >> 1)][(e & 1) * 2], acc[j * 2 + (e >> 1)][(e & 1) * 2 + 1]);
      if (layer == 3) {
        u32x4 yo[2];
#pragma unroll
        for (int j = 0; j < 2; ++j) {
          auto u = __builtin_amdgcn_permlane16_swap(xr[j][0], xr[j][2], false, false);
          auto v = __builtin_amdgcn_permlane16_swap(xr[j][1], xr[j][3], false, false);
          yo[j] = u32x4{u[0], v[0], u[1], v[1]};
        }
        const unsigned vo = fr * 2048 + (fq & 1) * 32 + (fq >> 1) * 16;
        __builtin_amdgcn_raw_buffer_store_b128(yo[0], mk_rsrc(p.out, 4 * MBY), vo, mt * 32768 + cn * 128, 18);
        __builtin_amdgcn_raw_buffer_store_b128(yo[1], mk_rsrc(p.out, 4 * MBY), vo + 64, mt * 32768 + cn * 128, 18);
        STAMP(13);
        STAMP(14);
        STAMP_DUMP;
        __builtin_amdgcn_endpgm();
      }
      const u32x2 stat = put_x(xr, v16, ar + ARENA + AR_X + xt_off(mt, cn * 2));
      __builtin_amdgcn_raw_buffer_store_b64(stat, wsr, fq ? OOB : fr * 8, ar + ARENA + AR_ST + (mt * 16 + cn) * 128, 16);
      MEMFENCE;
      STAMP(14);
    }
  }
}

extern "C" void kernel_launch(void* const* d_in, const int* in_sizes, int n_in, void* d_out, int out_size,
                              void* d_ws, size_t ws_size, hipStream_t stream) {
  (void)hipFuncSetAttribute((const void*)mega_kernel, hipFuncAttributeMaxDynamicSharedMemorySize, MEGA_LDS);
  MegaParams m;
  m.ws = (char*)d_ws; m.out = (u16*)d_out; m.x = (const u16*)d_in[0]; m.wphi = (const u16*)d_in[2];
  m.wqkvA = (const u16*)d_in[1]; m.wqkvB = (const u16*)d_in[5];
  m.woA = (const u16*)d_in[3]; m.woB = (const u16*)d_in[6];
  mega_kernel<<<256, 768, MEGA_LDS, stream>>>(m);
}
```

```cpp
#include <hip/hip_runtime.h>
#include <stdint.h>
#include <type_traits>

#define DI __device__ __forceinline__
#define AI __attribute__((always_inline))
typedef unsigned short u16;
typedef __attribute__((ext_vector_type(8))) short bf16x8;
typedef __attribute__((ext_vector_type(4))) short s16x4;
typedef __attribute__((ext_vector_type(4))) float f32x4;
typedef __attribute__((ext_vector_type(2))) float f32x2;
typedef __attribute__((ext_vector_type(2))) __bf16 bf2;
typedef __attribute__((ext_vector_type(4))) unsigned u32x4;
typedef __attribute__((ext_vector_type(2))) unsigned u32x2;
typedef __attribute__((address_space(3))) s16x4* lds_s16x4_ptr;
#define LDS(T) __attribute__((address_space(3))) T
template <int N> using IC = std::integral_constant<int, N>;

constexpr float LOG2E = 1.4426950408889634f;

DI unsigned pk2(float a, float b) {
  f32x2 v = {a, b};
  bf2 r = __builtin_convertvector(v, bf2);
  return __builtin_bit_cast(unsigned, r);
}
DI float bflo(unsigned u) { return __uint_as_float(u << 16); }
DI float bfhi(unsigned u) { return __uint_as_float(u & 0xffff0000u); }
DI f32x4 mfma16(bf16x8 a, bf16x8 b, f32x4 c) { return __builtin_amdgcn_mfma_f32_16x16x32_bf16(a, b, c, 0, 0, 0); }
DI void mfma_acc(f32x4& c, bf16x8 a, bf16x8 b) { asm volatile("v_mfma_f32_16x16x32_bf16 %0, %1, %2, %0" : "+v"(c) : "v"(a), "v"(b)); }
DI void mfma_done() { asm volatile("s_nop 7\n\ts_nop 1" ::: "memory"); __builtin_amdgcn_sched_barrier(0); }
DI bf16x8 as_frag(u32x4 v) { return __builtin_bit_cast(bf16x8, v); }
DI float dot2(unsigned a, unsigned b, float c) {
  return __builtin_amdgcn_fdot2_f32_bf16(__builtin_bit_cast(bf2, a), __builtin_bit_cast(bf2, b), c, false);
}
DI u32x2 trread(const char* p) { return __builtin_bit_cast(u32x2, __builtin_amdgcn_ds_read_tr16_b64_v4i16((lds_s16x4_ptr)p)); }
DI u32x2 trl(unsigned a) { return __builtin_bit_cast(u32x2, __builtin_amdgcn_ds_read_tr16_b64_v4i16((lds_s16x4_ptr)a)); }
DI float fmx(float a, float b) { return __builtin_elementwise_maximum(a, b); }
DI float fmn(float a, float b) { return __builtin_elementwise_minimum(a, b); }

DI float mul1(float a, float b) { float r; asm("v_mul_f32 %0, %1, %2" : "=v"(r) : "v"(a), "v"(b)); return r; }
DI float add1(float a, float b) { float r; asm("v_add_f32 %0, %1, %2" : "=v"(r) : "v"(a), "v"(b)); return r; }
DI f32x4 mul4(f32x4 a, f32x4 b) {
  float r0, r1, r2, r3;
  asm("v_mul_f32 %0, %4, %8\n\tv_mul_f32 %1, %5, %9\n\tv_mul_f32 %2, %6, %10\n\tv_mul_f32 %3, %7, %11"
      : "=&v"(r0), "=&v"(r1), "=&v"(r2), "=&v"(r3) : "v"(a[0]), "v"(a[1]), "v"(a[2]), "v"(a[3]), "v"(b[0]), "v"(b[1]), "v"(b[2]), "v"(b[3]));
  return f32x4{r0, r1, r2, r3};
}
DI f32x4 add4(f32x4 a, f32x4 b) { return f32x4{add1(a[0], b[0]), add1(a[1], b[1]), add1(a[2], b[2]), add1(a[3], b[3])}; }

template <bool IS_MAX, bool INV = false>
DI void xred2(float& a, float& b) {
  auto r = __builtin_amdgcn_permlane32_swap(__float_as_uint(a), __float_as_uint(b), false, false);
  float c = IS_MAX ? fmx(__uint_as_float(r[0]), __uint_as_float(r[1])) : __uint_as_float(r[0]) + __uint_as_float(r[1]);
  auto q = __builtin_amdgcn_permlane16_swap(__float_as_uint(c), __float_as_uint(c), false, false);
  c = IS_MAX ? fmx(__uint_as_float(q[0]), __uint_as_float(q[1])) : __uint_as_float(q[0]) + __uint_as_float(q[1]);
  if (INV) c = __builtin_amdgcn_rcpf(c);
  auto w = __builtin_amdgcn_permlane32_swap(__float_as_uint(c), __float_as_uint(c), false, false);
  a = __uint_as_float(w[0]); b = __uint_as_float(w[1]);
}
template <bool IS_MAX, bool INV = false>
DI void xred4(float& a, float& b, float& c, float& d) {
  auto op = [](unsigned x, unsigned y) { return IS_MAX ? fmx(__uint_as_float(x), __uint_as_float(y)) : __uint_as_float(x) + __uint_as_float(y); };
  auto r0 = __builtin_amdgcn_permlane32_swap(__float_as_uint(a), __float_as_uint(b), false, false);
  auto r1 = __builtin_amdgcn_permlane32_swap(__float_as_uint(c), __float_as_uint(d), false, false);
  float ab = op(r0[0], r0[1]), cd = op(r1[0], r1[1]);
  auto q = __builtin_amdgcn_permlane16_swap(__float_as_uint(ab), __float_as_uint(cd), false, false);
  float e = op(q[0], q[1]);
  if (INV) e = __builtin_amdgcn_rcpf(e);
  auto w = __builtin_amdgcn_permlane16_swap(__float_as_uint(e), __float_as_uint(e), false, false);
  auto u = __builtin_amdgcn_permlane32_swap(w[0], w[0], false, false);
  auto v = __builtin_amdgcn_permlane32_swap(w[1], w[1], false, false);
  a = __uint_as_float(u[0]); b = __uint_as_float(u[1]); c = __uint_as_float(v[0]); d = __uint_as_float(v[1]);
}

DI unsigned xt_off(int mt, int ks) { return (unsigned)((((mt >> 4) * 32 + ks) * 16 + (mt & 15)) << 10); }

#define GLDS_PRE "s_mov_b32 %0, m0\n\ts_mov_b32 m0, %3\n\ts_nop 2\n\t"
#define GLDS_POST "s_mov_b32 m0, %0"
#define GLDS_OPS : "=&s"(keep) : "v"(voff), "s"(sbase), "s"(lds) : "memory"
#define GL1(c) "global_load_lds_dwordx4 %1, %2" c "\n\t"
#define GL2(c) GL1(c) "global_load_lds_dwordx4 %1, %2 offset:1024" c "\n\t"
#define GL3(c) GL2(c) "global_load_lds_dwordx4 %1, %2 offset:2048" c "\n\t"
#define GL4(c) GL3(c) "global_load_lds_dwordx4 %1, %2 offset:3072" c "\n\t"
template <int N, bool SC1 = false>
DI void glds_run(unsigned voff, const void* sbase, unsigned lds) {
  unsigned keep;
  if constexpr (SC1) {
    if constexpr (N == 2) asm volatile(GLDS_PRE GL2(" sc1") GLDS_POST GLDS_OPS);
    else if constexpr (N == 3) asm volatile(GLDS_PRE GL3(" sc1") GLDS_POST GLDS_OPS);
    else asm volatile(GLDS_PRE GL4(" sc1") GLDS_POST GLDS_OPS);
  } else {
    static_assert(SC1 || N == 4, "");
    asm volatile(GLDS_PRE GL4("") GLDS_POST GLDS_OPS);
  }
}
template <int N> DI void wait_vm_lgkm0() { asm volatile("s_waitcnt vmcnt(%0) lgkmcnt(0)" ::"n"(N) : "memory"); }
template <int N> DI void wait_vm() { asm volatile("s_waitcnt vmcnt(%0)" ::"n"(N) : "memory"); }
DI void drain() { asm volatile("s_waitcnt vmcnt(0)" ::: "memory"); __builtin_amdgcn_s_waitcnt(0x0f70); }
DI void bar() { asm volatile("" ::: "memory"); __builtin_amdgcn_s_barrier(); asm volatile("" ::: "memory"); }
DI void sync_lds() { asm volatile("s_waitcnt lgkmcnt(0)" ::: "memory"); bar(); }
#define SCHED0 __builtin_amdgcn_sched_barrier(0)
#define STPAD do { SCHED0; asm volatile("s_nop 1"); SCHED0; } while (0)
#define SLEEP(n) do { if (n) __builtin_amdgcn_s_sleep(n); } while (0)
#define PRIO(n) __builtin_amdgcn_s_setprio(n)

template <typename F>
DI void phi_map2(const f32x2 (*x)[4], const float* rs, u32x4 (*out)[2], F mid) {
  auto top = [&](float sg, float* mx) AI {
#pragma unroll
    for (int m = 0; m < 2; ++m)
      mx[m] = fmx(fmx(fmx(sg * x[m][0][0], sg * x[m][0][1]), sg * x[m][1][0]), fmx(fmx(sg * x[m][1][1], sg * x[m][2][0]), fmx(fmx(sg * x[m][2][1], sg * x[m][3][0]), sg * x[m][3][1])));
    xred2<true>(mx[0], mx[1]);
  };
  auto sum8 = [](const f32x2* e) AI { const f32x2 t = (e[0] + e[1]) + (e[2] + e[3]); float lo = t[0]; asm("" : "+v"(lo)); return lo + t[1]; };
  float mx[2], sp[2], sn[2];
  top(1.f, mx);
  f32x2 ep[2][4], en[2][4];
#pragma unroll
  for (int m = 0; m < 2; ++m) {
    const float sl = rs[m] * LOG2E, off = mx[m] * -sl;
#pragma unroll
    for (int j = 0; j < 4; ++j) {
      const f32x2 a = __builtin_elementwise_fma(x[m][j], f32x2{sl, sl}, f32x2{off, off});
      ep[m][j] = f32x2{__builtin_amdgcn_exp2f(a[0]), __builtin_amdgcn_exp2f(a[1])};
    }
#pragma unroll
    for (int j = 0; j < 4; ++j) en[m][j] = f32x2{__builtin_amdgcn_rcpf(ep[m][j][0]), __builtin_amdgcn_rcpf(ep[m][j][1])};
    sp[m] = sum8(ep[m]); sn[m] = sum8(en[m]);
  }
  xred4<false, true>(sp[0], sn[0], sp[1], sn[1]);
  if (__builtin_expect(__builtin_amdgcn_ballot_w64(!(fmn(sn[0], sn[1]) > 1e-30f)) != 0, 0)) {
    top(-1.f, mx);
#pragma unroll
    for (int m = 0; m < 2; ++m) {
      const float sl = rs[m] * -LOG2E, off = mx[m] * sl;
#pragma unroll
      for (int j = 0; j < 4; ++j) {
        const f32x2 a = __builtin_elementwise_fma(x[m][j], f32x2{sl, sl}, f32x2{off, off});
        en[m][j] = f32x2{__builtin_amdgcn_exp2f(a[0]), __builtin_amdgcn_exp2f(a[1])};
      }
      sn[m] = sum8(en[m]);
    }
    xred2<false, true>(sn[0], sn[1]);
  }
  mid(sp);
#pragma unroll
  for (int m = 0; m < 2; ++m) {
#pragma unroll
    for (int j = 0; j < 4; j += 2) {
      f32x2 a0, a1, b0, b1;
      asm("v_pk_mul_f32 %0, %4, %5 op_sel_hi:[0,1]\n\tv_pk_mul_f32 %1, %4, %6 op_sel_hi:[0,1]\n\t"
          "v_pk_mul_f32 %2, %4, %7 op_sel:[1,0]\n\tv_pk_mul_f32 %3, %4, %8 op_sel:[1,0]"
          : "=&v"(a0), "=&v"(a1), "=&v"(b0), "=&v"(b1) : "v"(f32x2{sp[m], sn[m]}), "v"(ep[m][j]), "v"(ep[m][j + 1]), "v"(en[m][j]), "v"(en[m][j + 1]));
      out[m][0][j] = pk2(a0[0], a0[1]); out[m][0][j + 1] = pk2(a1[0], a1[1]);
      out[m][1][j] = pk2(b0[0], b0[1]); out[m][1][j + 1] = pk2(b1[0], b1[1]);
    }
  }
}

#define SLP_X 8
#define SLP_XD 4
#define SLP_O 19
#define SLP_OD 10
#define SLP_O0 8
#define SLP_O0D 2
#define SLP_K 4
#define SLP_R 4
#define SLP_HX 1
#define SLP_Q 4
#define SLP_P 40
#define SLP_E 2
#define SLP_G 12
#define EV 0
#define SPR0 1
#define SW0 0
#define LANEID __builtin_amdgcn_mbcnt_hi(~0u, __builtin_amdgcn_mbcnt_lo(~0u, 0u))
#define STAMP(i) do {} while (0)
#define STAMP_DUMP do {} while (0)
#define COUNT(k) do {} while (0)
#define PSTAMP(l) do { const int layer = l; (void)layer; STAMP(15); } while (0)
constexpr unsigned MBY = 1u << 20;
constexpr unsigned WS_WQ = 0;
constexpr unsigned WS_WO = 12 * MBY;
constexpr unsigned WS_AR = 16 * MBY, ARENA = 12 * MBY;
constexpr unsigned AR_X = 0, AR_ST = 4 * MBY, AR_O = 5 * MBY, AR_KV = 9 * MBY;
constexpr unsigned AR_L2 = AR_ST + 256 * 1024, AR_FL = AR_ST + 320 * 1024;
constexpr unsigned KVREC = 9 * 1024;
constexpr unsigned WS_CS = WS_AR + 4 * ARENA;
constexpr unsigned WS_F1 = WS_CS + 512 * 1024;
constexpr unsigned WS_F2 = WS_F1 + 64 * 1024;
constexpr unsigned WS_EP = WS_F2 + 2048;
constexpr unsigned WS_END = WS_EP + 128;
constexpr unsigned POISON = 0xffffffffu;
constexpr unsigned OOB = 0x80000000u;

typedef __amdgpu_buffer_rsrc_t rsrc_t;
DI rsrc_t mk_rsrc(const void* p, unsigned bytes) { return __builtin_amdgcn_make_buffer_rsrc((void*)p, 0, bytes, 0x00020000); }
DI void st16(rsrc_t r, unsigned off, u32x4 v) { __builtin_amdgcn_raw_buffer_store_b128(v, r, off, 0, 16); }
DI void st16s(rsrc_t r, unsigned voff, unsigned soff, u32x4 v) { __builtin_amdgcn_raw_buffer_store_b128(v, r, voff, soff, 16); }
DI u32x4 ld16(rsrc_t r, unsigned off) { return __builtin_amdgcn_raw_buffer_load_b128(r, off, 0, 16); }
DI u32x4 ld16s(rsrc_t r, unsigned voff, unsigned soff) { return __builtin_amdgcn_raw_buffer_load_b128(r, voff, soff, 16); }
DI u32x4 clean(u32x4 v) { return __builtin_elementwise_min(v, u32x4{POISON - 1, POISON - 1, POISON - 1, POISON - 1}); }
DI unsigned umax(unsigned a, unsigned b) { return a > b ? a : b; }
DI unsigned umin(unsigned a, unsigned b) { return a < b ? a : b; }
DI unsigned umax4(u32x4 v) { return umax(umax(umax(v[0], v[1]), v[2]), v[3]); }
DI unsigned umax4(unsigned m, u32x4 v) { return umax(umax(umax(umax(m, v[0]), v[1]), v[2]), v[3]); }
DI bool poisoned(unsigned m) { return __builtin_amdgcn_ballot_w64(m == POISON) != 0; }
#define MEMFENCE asm volatile("" ::: "memory")

#define BL(i, o) "buffer_load_dwordx4 %1, %2, %" #i " offen" o " nt lds\n\t"
#define BL_OPS(...) : "=&s"(keep) : "v"(voff), "s"(r), "s"(lds), __VA_ARGS__ : "memory"
template <int N>
DI void blds_run(rsrc_t r, unsigned voff, unsigned lds, unsigned s0, unsigned s1 = 0, unsigned s2 = 0, unsigned s3 = 0) {
  unsigned keep;
  if constexpr (N == 2) asm volatile(GLDS_PRE BL(4, "") BL(5, " offset:1024") GLDS_POST BL_OPS("s"(s0), "s"(s1)));
  else asm volatile(GLDS_PRE BL(4, "") BL(5, " offset:1024") BL(6, " offset:2048") BL(7, " offset:3072") GLDS_POST BL_OPS("s"(s0), "s"(s1), "s"(s2), "s"(s3)));
}

#define CODEB $
constexpr int MEGA_CODE_BYTES = CODEB;
DI unsigned code_prefetch(int i) {
  unsigned long long pc; int rel; unsigned d;
  asm volatile("s_getpc_b64 %0\n\ts_mov_b32 %1, _Z11mega_kernel10MegaParams@rel32@lo+4" : "=s"(pc), "=s"(rel));
  const unsigned long long a = pc + (long long)rel + (i * 192 + 192 <= MEGA_CODE_BYTES ? i * 192 : 0);
  asm volatile("s_load_dword %0, %1, 0x0\n\ts_load_dword %0, %1, 0x40\n\ts_load_dword %0, %1, 0x80" : "=&s"(d) : "s"(a) : "memory");
  return d;
}

struct MegaParams {
  char* ws;
  const u16* x;
  const u16* wqkvA;
  const u16* woA;
  const u16* wqkvB;
  const u16* woB;
  const u16* wphi;
  u16* out;
};

constexpr int QSLOT = 6 * 1024, QGRP = 4 * QSLOT;
constexpr int L_STG = QGRP;
constexpr int L_A2 = 16 * 1024, L_Z = 21 * 1024;
constexpr int L_RT = 4 * QGRP;
constexpr int L_PB = 0;
constexpr int L_RED = L_STG + 128 * 1024;
constexpr int L_STAMP = L_RED + 3072;
constexpr int MEGA_LDS = L_STAMP + 1536;
static_assert(MEGA_LDS <= 160 * 1024, "LDS map");

template <int I, int N, typename F> DI void static_for_(F&& f) {
  if constexpr (I < N) { f(IC<I>{}); static_for_<I + 1, N>(f); }
}
template <int N, typename F> DI void static_for(F&& f) { static_for_<0, N>(f); }

constexpr int NCHK = 3;
constexpr int RO = 8;
constexpr int SPIN_BUDGET = 1 << 15;

__global__ __launch_bounds__(768, 3) void mega_kernel(MegaParams p) {
  extern __shared__ __attribute__((aligned(16))) char smem[];
  const int tid = threadIdx.x;
  const int wave = __builtin_amdgcn_readfirstlane(tid >> 6);
  const int xcd = blockIdx.x & 7, idx = blockIdx.x >> 3;
  int bm = (xcd >> 2) * 4 + (idx >> 3), head = ((xcd & 1) * 2 + ((xcd >> 1) & 1)) * 8 + (idx & 7);
  int cm = (xcd >> 1) & 1, cn = (xcd & 1) * 8 + (idx & 7);
  const int bm0 = bm, head0 = head, cm0 = cm, cn0 = cn;
  char* ws = p.ws;
  const u16 *px = p.x, *pq = p.wqkvA;
  asm volatile("" : "+s"(ws), "+s"(px), "+s"(pq));
  const int swv = wave == SW0 ? 0 : wave == 4 ? 1 : wave == 11 ? 2 : -1; (void)swv;
  float* lmean = (float*)(smem + L_RED);
  float* lrstd = lmean + 272;
  float* lcs = lrstd + 272;
  unsigned* lflag = (unsigned*)lmean;
  const rsrc_t wsr = mk_rsrc(ws, WS_END);
  int budget = SPIN_BUDGET;
  unsigned tag0 = 0, tag1 = 0;
  auto ld_ep = [&]() AI { MEMFENCE; return __builtin_amdgcn_raw_buffer_load_b64(wsr, 0, WS_EP, 16); };
  auto mk_tag = [&](u32x2 ep) AI {
    tag0 = __builtin_amdgcn_readfirstlane(ep[0]) + 0x9e3779b9u;
    tag1 = __builtin_amdgcn_readfirstlane(ep[1]) ^ 0x85ebca6bu;
  };
  const unsigned sbase = (unsigned)(size_t)smem;
  int L = wave, cw = wave - 4;
#define OPAQUE_S asm volatile("" : "+s"(L), "+s"(bm), "+s"(head), "+s"(cm), "+s"(cn));
#define LANE_VARS asm volatile("" : "+s"(cw), "+s"(bm), "+s"(head), "+s"(cm), "+s"(cn)); int lane_ = __builtin_amdgcn_mbcnt_hi(~0u, __builtin_amdgcn_mbcnt_lo(~0u, 0u)); asm volatile("" : "+v"(lane_)); \
  const int lane = lane_, fr = lane & 15, fq = lane >> 4; const unsigned v16 = lane * 16; (void)fr; (void)fq; (void)v16;
  auto put_x = [&](u32x4* xr, unsigned v16, unsigned dst) AI {
    float sm = 0.f, sq = 0.f;
#pragma unroll
    for (int j = 0; j < 2; ++j) {
      xr[j][0] = umin(xr[j][0], POISON - 1); xr[j][2] = umin(xr[j][2], POISON - 1);
      st16s(wsr, v16, dst + j * 16384, xr[j]);
    }
    SCHED0;
#pragma unroll
    for (int j = 0; j < 2; ++j)
#pragma unroll
      for (int e = 0; e < 4; ++e) { sm = dot2(xr[j][e], 0x3f803f80u, sm); sq = dot2(xr[j][e], xr[j][e], sq); }
    xred2<false>(sm, sq);
    return u32x2{__float_as_uint(sm), __float_as_uint(sq)};
  };

#define RT_VARS const bool tq = L < 3; const int tsg = tq ? (bm * 2 + (head >> 4)) * 3 + L : bm * 2 + (head >> 4), ks = (head & 15) + 16 * h;
  auto ld_rt = [&](const u16* w, int h, unsigned lds) AI {
    RT_VARS
    LANE_VARS
    const unsigned rs = tq ? 6144 : 2048, D = 8 * rs - 1024, o = ks * 32 * rs + tsg * 128;
    blds_run<4>(mk_rsrc(w, rs * 1024), (lane >> 3) * rs + ((lane & 7) ^ ((lane >> 3) & 6)) * 16, sbase + lds, o, o + D, o + 2 * D, o + 3 * D);
  };
  auto retile = [&](int side, int h, unsigned lds, f32x4* rcs, int wp = -1) AI {
    RT_VARS
    LANE_VARS
    const unsigned dst = tq ? WS_WQ + side * (6 * MBY) + (unsigned)((((tsg & 15) * 2) * 32 + ks) * 6 + (tsg >> 4) * 2) * 1024
                            : WS_WO + side * (2 * MBY) + (unsigned)((tsg * 32 + ks) * 4) * 1024;
    const unsigned gstr = tq ? 32 * 6 * 1024 : 2048;
    const unsigned a0 = (lane >> 2) * 128 + ((lane >> 3) & 3) * 32 + (lane & 3) * 8;
    u32x4 fv[4];
    if (wp >= 0 && tsg < 32) {
      const f32x4 zero = f32x4{0.f, 0.f, 0.f, 0.f};
#pragma unroll
      for (int g = 0; g < 2; ++g) {
        const char* ra = smem + lds + fr * 128 + (((g * 4 + fq) ^ (fr & 6)) * 16);
        const bf16x8 w0 = *(const bf16x8*)ra, w1 = *(const bf16x8*)(ra + 2048);
#pragma unroll
        for (int u = 0; u < 2; ++u) {
          const char* pa = smem + wp + g * 2048 + (fq * 8 + (fr >> 2)) * 64 + u * 32 + (fr & 3) * 8;
          const u32x2 t0 = trread(pa), t1 = trread(pa + 256);
          const bf16x8 wf = as_frag(u32x4{t0[0], t0[1], t1[0], t1[1]});
          const f32x4 d0 = mfma16(w0, wf, zero), d1 = mfma16(w1, wf, zero);
          fv[g * 2 + u] = u32x4{pk2(d0[0], d0[1]), pk2(d0[2], d0[3]), pk2(d1[0], d1[1]), pk2(d1[2], d1[3])};
        }
      }
    } else {
#pragma unroll
      for (int pc = 0; pc < 4; ++pc) {
        const char* aa = smem + lds + (a0 ^ (pc * 32));
        const u32x2 r0_ = trread(aa), r1_ = trread(aa + 2048);
        fv[pc] = u32x4{r0_[0], r0_[1], r1_[0], r1_[1]};
      }
    }
#pragma unroll
    for (int pc = 0; pc < 4; ++pc) {
      const u32x4 v = fv[pc];
      st16s(wsr, v16 + (pc & 1) * 1024, dst + (pc >> 1) * gstr, v);
      if (rcs) rcs[pc] = mfma16(as_frag(u32x4{0x3f803f80u, 0x3f803f80u, 0x3f803f80u, 0x3f803f80u}), as_frag(v), rcs[pc]);
    }
    if (rcs && h == 1) {
      const f32x4 c0 = rcs[0], c1 = rcs[1], c2 = rcs[2], c3 = rcs[3];
      const float ca = fq & 1 ? c1[0] : c0[0], cb = fq & 1 ? c3[0] : c2[0], cc = fq & 2 ? cb : ca;
      if (tq) __builtin_amdgcn_raw_buffer_store_b32(__float_as_uint(cc), wsr, ((fq >> 1) * 1536 + (fq & 1) * 16 + fr) * 4,
                                                    WS_CS + ((side * 32 + (tsg & 15) * 2) * 1536 + (tsg >> 4) * 512 + (head & 15) * 32) * 4, 16);
    }
  };
  auto flag = [&](unsigned table) AI {
    if (LANEID == 0) __builtin_amdgcn_raw_buffer_store_b64(u32x2{tag0, tag1}, wsr, 0, table + (bm * 32 + head) * 8, 16);
  };
  auto poll = [&](unsigned table, auto between, int rows = 0) AI {
    LANE_VARS
    const int q = (umin(fq, 2) * 16 + (head >> 1)) * 43 >> 7;
    const unsigned vo = rows ? bm * 256 + (fr & rows) * 16 : fq == 3 ? bm * 256 + fr * 16 : q * 128 + (fr & 7) * 16;
    auto miss = [&](u32x4 f) AI { return __builtin_amdgcn_ballot_w64(((f[0] ^ tag0) | (f[1] ^ tag1) | (f[2] ^ tag0) | (f[3] ^ tag1)) != 0) != 0; };
    MEMFENCE;
    const u32x4 f0 = ld16s(wsr, vo, table);
    MEMFENCE;
    between();
    MEMFENCE;
    bool wait = miss(f0);
#pragma unroll 1
    while (wait && --budget > 0) {
      SLEEP(SLP_Q);
      MEMFENCE;
      wait = miss(ld16s(wsr, vo, table));
      asm volatile("" : "+s"(budget) :: "memory");
    }
    MEMFENCE;
  };
  auto ld_x = [&](int cx, unsigned lds) AI {
    LANE_VARS
    const unsigned o = (bm * 16 + (head >> 1)) * 32768 + cx * 128;
    blds_run<2>(mk_rsrc(px, 4 * MBY), (lane >> 3) * 2048 + ((lane & 7) ^ (lane >> 3)) * 16, sbase + lds, o, o + 15360);
  };
  auto st_x = [&](int cx, unsigned lds, int w) AI {
    LANE_VARS
    const int mt = bm * 16 + (head >> 1);
    u32x4 xr[2];
#pragma unroll
    for (int j = 0; j < 2; ++j) {
      const char* a = smem + lds + fr * 128 + (fq & 1) * 8;
      const u32x2 lo = *(const u32x2*)(a + ((j * 4 + (fq >> 1)) ^ (fr & 7)) * 16), hi = *(const u32x2*)(a + ((j * 4 + 2 + (fq >> 1)) ^ (fr & 7)) * 16);
      xr[j] = u32x4{lo[0], lo[1], hi[0], hi[1]};
    }
    const u32x2 stat = put_x(xr, v16, WS_AR + AR_X + xt_off(mt, cx * 2));
    if (fq == 0) *(u32x2*)(lmean + 16 + fr * 4 + w) = stat;
  };

  {
    const int lane = LANEID;
    const bool tq = wave < 4;
    const unsigned rs = tq ? 6144 : 2048, D = 8 * rs - 1024;
    const unsigned o = (tq ? (head & 15) * 32 : (bm * 16 + (head >> 1)) * 16) * rs + (tq ? (bm * 2 + (head >> 4)) * 3 + wave : (head & 1) * 4 + cw) * 128;
    const unsigned lds = sbase + L_PB + (tq ? 8192 + wave * 4096 : cw * 2048);
    const unsigned vo = (lane >> 3) * rs + ((lane & 7) ^ ((lane >> 3) & (tq ? 6 : 7))) * 16;
    const rsrc_t r = mk_rsrc(tq ? pq : px, tq ? 6 * MBY : 4 * MBY);
    if ((wave & 7) != 3 && wave < 8) blds_run<2>(r, vo, lds, o, o + D);
    if (wave < 3) {
      blds_run<2>(r, vo, lds + 2048, o + 2 * D + 2048, o + 3 * D + 2048);
      const int tsg = (bm * 2 + (head >> 4)) * 3 + wave;
      if (tsg < 32) glds_run<4>(lane * 16, (const char*)p.wphi + (tsg & 15) * 4096, sbase + L_PB + 45056 + wave * 4096);
    }
  }
  if (wave >= 8) {
    const unsigned cp = code_prefetch(idx * 9 + cw), cq = code_prefetch(idx * 9 + cw - 4);
    L = wave - 8;
    bar();
    ld_x(8 + (head & 1) * 4 + L, L_PB + 32768 + L * 2048);
    if (L < 3) ld_rt(pq, 1, L_PB + 20480 + L * 4096);
    sync_lds();
    asm volatile("; pi" :: "s"(cp), "s"(cq) : "memory");
    wait_vm<0>();
    PSTAMP(1);
    st_x(8 + (head & 1) * 4 + L, L_PB + 32768 + L * 2048, L * 64 + 2);
    if (L < 3) {
      const f32x4 t = *(const f32x4*)(smem + L_PB + 40960 + L * 1024 + LANEID * 16);
      f32x4 rca[4] = {{t[0], 0.f, 0.f, 0.f}, {t[1], 0.f, 0.f, 0.f}, {t[2], 0.f, 0.f, 0.f}, {t[3], 0.f, 0.f, 0.f}};
      retile(0, 1, L_PB + 20480 + L * 4096, rca, L_PB + 45056 + L * 4096);
    }
    asm volatile("s_waitcnt lgkmcnt(0)" ::: "memory");
    PSTAMP(2);
  } else if (wave >= 4) {
    wait_vm<0>();
    PSTAMP(1);
    st_x((head & 1) * 4 + cw, L_PB + cw * 2048, cw * 64);
    bar();
    wait_vm_lgkm0<0>();
    PSTAMP(2);
    bar();
    asm volatile("; pc" ::: "memory");
    if (!(EV & 1)) {
      SLEEP(SLP_E);
      L = 3;
      if (cw < 2) ld_rt(p.woA, cw, L_RT + 3 * 14336 + cw * 4096);
    }
  } else if (wave < 3) {
    const u32x2 ep = ld_ep();
    wait_vm<1>();
    PSTAMP(1);
    f32x4 rca[4] = {};
    retile(0, 0, L_PB + 8192 + L * 4096, rca, L_PB + 45056 + L * 4096);
    *(f32x4*)(smem + L_PB + 40960 + L * 1024 + LANEID * 16) = f32x4{rca[0][0], rca[1][0], rca[2][0], rca[3][0]};
    bar();
    drain();
    mk_tag(ep);
    PSTAMP(2);
    sync_lds();
    asm volatile("; pl" ::: "memory");
    if (!(EV & 2)) {
      SLEEP(SLP_E);
      ld_rt(p.wqkvB, 0, L_RT + L * 14336);
    }
  } else {
    const u32x2 ep = ld_ep();
    const unsigned cp = code_prefetch(idx * 9 + 8);
    if (LANEID < 9) lflag[LANEID] = 0;
    wait_vm_lgkm0<0>();
    asm volatile("" :: "s"(cp));
    mk_tag(ep);
    bar();
    bar();
    flag(WS_F1);
    if (LANEID == 0) *(u32x2*)(lflag + 10) = u32x2{tag0, tag1};
    PSTAMP(2);
    SLEEP(SLP_P);
    poll(WS_F1, []() {});
    asm volatile("; pp" ::: "memory");
  }
  bar();
  PSTAMP(3);
  int role = wave;
  asm volatile("" : "+s"(role));

  if (__builtin_expect(role < 4, 1)) {
    const unsigned v16 = __builtin_amdgcn_mbcnt_hi(~0u, __builtin_amdgcn_mbcnt_lo(~0u, 0u)) * 16;
#define OPAQUE_LANE int lane = v16 >> 4; asm volatile("" : "+v"(lane));
    auto q_groupW = [&](int layer, int g, int pos) AI {
      const char* gW = ws + WS_WQ + (layer & 1) * (6 * MBY) + head * (192 * 1024) + (g * 4 + L) * 6144;
      const unsigned d = sbase + (pos * 4 + L) * QSLOT;
      glds_run<3, true>(v16, gW, d);
      glds_run<3, true>(v16, gW + 3072, d + 3072);
    };
    u32x4 wn[4][6];
    auto ld_wn = [&](int layer, int g0, int g1) AI {
      const unsigned so = WS_WQ + (layer & 1) * (6 * MBY) + head * (192 * 1024) + L * 6144;
#pragma unroll
      for (int g = g0; g < g1; ++g)
#pragma unroll
        for (int n = 0; n < 6; ++n) wn[g][n] = ld16s(wsr, v16 + (n % 3) * 1024, so + g * 24576 + (n / 3) * 3072);
      MEMFENCE;
    };
    auto wr_wn = [&](int g) AI {
#pragma unroll
      for (int n = 0; n < 6; ++n) *(u32x4*)(smem + (g * 4 + L) * QSLOT + n * 1024 + v16) = wn[g][n];
    };
    const u32x4 pz = u32x4{POISON, POISON, POISON, POISON};
    auto poison = [&](unsigned so) AI { st16s(wsr, v16, so, pz); };
    auto poison_o = [&]() AI {
      const unsigned o = WS_AR + AR_O + xt_off(bm * 16 + L * 4, head);
#pragma unroll
      for (int i = 0; i < 4; ++i) poison(o + i * 1024);
      STPAD;
    };
    auto poison_x = [&](int l) AI {
      if (L < NCHK) {
        const unsigned o = WS_AR + l * ARENA + AR_X + bm * (512 * 1024) + head * (NCHK * 2048) + L * 2048;
        poison(o); poison(o + 1024);
        STPAD;
      }
    };
    auto poison_s = [&](int l) AI {
      if (L == 3) {
        const unsigned o = WS_AR + l * ARENA + AR_KV + (unsigned)(bm * 32 + head) * KVREC;
        poison(o); poison(o + 1024);
        STPAD;
      }
    };
    auto poison_kv = [&](int l) AI {
      const unsigned kvo = WS_AR + l * ARENA + AR_KV + (unsigned)(bm * 32 + head) * KVREC;
      poison(kvo + L * 1024);
      if (L == (l ? 0 : 1)) st16s(wsr, v16 < 256 ? v16 : OOB, kvo + 4096, pz);
      STPAD;
    };
    q_groupW(0, 0, 0);
    q_groupW(0, 1, 1);
    auto put_l2 = [&](float sm, float sq, unsigned vo, unsigned so) AI {
      xred2<false>(sm, sq);
      if (v16 < 256) st16s(wsr, vo, so, u32x4{__float_as_uint(sm), tag0, __float_as_uint(sq), tag1});
      MEMFENCE;
    };
    auto put_st0 = [&]() AI {
      if (L == 0) {
        const f32x4 t = *(const f32x4*)((const char*)(lmean + 16) + v16);
        put_l2(t[0] + t[2], t[1] + t[3], v16, WS_AR + AR_L2 + (head & 1) * 32768 + (bm * 16 + (head >> 1)) * 256);
      }
    };
    wait_vm<6>();
    auto body = [&](auto kind, const int layer) AI {
      constexpr bool isB = decltype(kind)::value == 1, is0 = decltype(kind)::value == 0;
      const unsigned ar = WS_AR + layer * ARENA;
      f32x4 rcs[4] = {};
      auto sW = [&]() AI { return WS_WO + (layer & 1) * (2 * MBY) + cn * (128 * 1024); };
      auto stg = [&](unsigned off, auto n) AI { glds_run<decltype(n)::value, true>(v16, ws + sW() + off, sbase + L_STG + off); };
      auto stg7 = [&](unsigned o) AI { stg(o, IC<4>{}); stg(o + 4096, IC<3>{}); };
      auto stg6 = [&](unsigned o) AI { stg(o, IC<3>{}); stg(o + 3072, IC<3>{}); };

      auto extra = [&](int b) AI {
        if (!is0) return;
        const unsigned lu = L_RT + L * 14336;
        if (b < 7) { SCHED0; PRIO(3); SCHED0; }
        if (b == 1) retile(L < 3, 0, lu, rcs);
        if (b == 2 && L == 3) retile(0, 1, lu + 4096, nullptr);
        if (b == 3) {
          ld_rt(L < 3 ? p.wqkvB : p.woB, 1, lu + 4096);
          if (L == 3) ld_rt(p.woB, 0, lu + 8192);
        }
        if (b == 6 && L < 3) retile(1, 1, lu + 4096, rcs);
        if (b == 8 && L == 3) { retile(1, 0, lu + 8192, nullptr); retile(1, 1, lu + 4096, nullptr); }
        if (b < 7) { SCHED0; PRIO(0); SCHED0; }
      };
      auto extra2 = [&](int b) AI {
        if (!is0) return;
        if (b < 7) { SCHED0; PRIO(3); SCHED0; }
        if (b == 2) put_st0();
        if (b == 1) poison_o();
        if (b == 2) poison_kv(0);
        if (b == 5) poison_kv(2);
        if (b == 8) { poison_s(1); poison_x(1); }
        if (b == 10) { poison_x(2); poison_s(3); }
        if (b < 7) { SCHED0; PRIO(0); SCHED0; }
      };
      if (is0) sync_lds();
      STAMP(0);
      {
        OPAQUE_S
        OPAQUE_LANE
        u32x4 c0, c1;
        asm volatile("" : "=v"(c0), "=v"(c1));
        auto ldcs = [&]() AI {
          if (L > 0) {
            const unsigned so = WS_CS + (((layer & 1) * 32 + head) * 1536 + (L - 1) * 512) * 4;
            c0 = ld16s(wsr, v16, so); c1 = ld16s(wsr, v16 + 1024, so);
          }
          MEMFENCE;
        };
        auto wrcs = [&]() AI {
          if (L > 0) {
            f32x4 t = add4(__builtin_bit_cast(f32x4, c0), __builtin_bit_cast(f32x4, c1));
#pragma unroll
            for (int e = 0; e < 4; ++e)
              t[e] += __uint_as_float(__builtin_amdgcn_update_dpp(0u, __float_as_uint(t[e]), 0x128, 0xf, 0xf, false));
            float t0 = t[0], t1 = t[1], t2 = t[2], t3 = t[3];
            xred4<false>(t0, t1, t2, t3);
            *(f32x4*)(lcs + (isB ? 96 : 0) + (L - 1) * 32 + (lane & 7) * 4) = f32x4{t0, t1, t2, t3};
          }
        };
        if (isB && layer == 1) ldcs();
        if constexpr (is0) {
          if (L == 3) flag(WS_F1 + 2048);
          q_groupW(0, 2, 2); q_groupW(0, 3, 3);
          if ((EV & 1) && L == 3) { ld_rt(p.woA, 0, L_RT + L * 14336); ld_rt(p.woA, 1, L_RT + L * 14336 + 4096); wait_vm<20>(); }
          else if ((EV & 2) && L < 3) { ld_rt(p.wqkvB, 0, L_RT + L * 14336); wait_vm<16>(); }
          else wait_vm<12>();
        }
        STAMP(1);
        if (!is0) PRIO(3);
        sync_lds();
        STAMP(2);
        OPAQUE_S
        u32x4 s0 = u32x4{0u, 0u, 0u, 0u}, s1 = s0;
        auto ldst = [&]() AI {
          const unsigned so = ar + AR_L2 + (bm * 16 + L * 4) * 256;
          s0 = ld16s(wsr, v16, so);
          if (is0) s1 = ld16s(wsr, v16, so + 32768);
          if (isB && L == 0) s1 = ld16s(wsr, v16 & 255, bm > 0 ? so - 256 : so);
          MEMFENCE;
        };
        auto fin = [&](float sm, float sq, int r, bool own) AI {
          float mean = sm * (1.0f / 1024.0f);
          float var = fmaxf(__builtin_fmaf(sq, 1.0f / 1024.0f, -mul1(mean, mean)), 0.f);
          if (own) lmean[r] = -mean;
          lrstd[r] = __builtin_amdgcn_rsqf(var + 1e-5f);
        };
        auto wrst = [&]() AI {
          while (true) {
            unsigned d = (s0[1] ^ tag0) | (s0[3] ^ tag1);
            if (is0 || (isB && L == 0)) d |= (s1[1] ^ tag0) | (s1[3] ^ tag1);
            if (__builtin_expect(__builtin_amdgcn_ballot_w64(d != 0) == 0, 1) || --budget <= 0) break;
            COUNT(1);
            ldst();
          }
          MEMFENCE;
          float sm = __uint_as_float(s0[0]), sq = __uint_as_float(s0[2]);
          if (is0) { sm += __uint_as_float(s1[0]); sq += __uint_as_float(s1[2]); }
          fin(sm, sq, 16 + L * 64 + lane, true);
          if (isB && L == 0) fin(__uint_as_float(s1[0]), __uint_as_float(s1[2]), lane & 15, false);
        };
        auto q_group = [&](int g) AI {
          if (is0) {
            if (L & 1) SLEEP(SPR0);
            if (L & 2) SLEEP(2 * SPR0);
            q_groupW(layer, g, g & 3);
          } else if (g >= 5) q_groupW(layer, g, g < 6 ? g : g - 4);
        };
        if (!is0 && L == 0) {
          poll(ar + AR_FL, []() {}, 15);
          if (head < 16) {
            const unsigned so = ar + AR_ST + (bm * 16 + head) * 2048;
            s0 = ld16s(wsr, v16, so); s1 = ld16s(wsr, v16 + 1024, so);
            MEMFENCE;
          }
        }
        if (is0) {
          drain();
          poll(WS_F1 + 2048, [&]() AI { extra(1); });
          ldcs();
          extra2(1);
        }
        if (isB && layer == 1) wrcs();
        MEMFENCE;
        STAMP(3);
        if (!is0) PRIO(0);
        sync_lds();
        STAMP(4);
        OPAQUE_S
        if (!is0) drain();
        if (!is0 && L == 0 && head < 16) {
          f32x4 t = add4(__builtin_bit_cast(f32x4, s0), __builtin_bit_cast(f32x4, s1));
#pragma unroll
          for (int e = 0; e < 4; ++e)
            t[e] += __uint_as_float(__builtin_amdgcn_update_dpp(0u, __float_as_uint(t[e]), 0x128, 0xf, 0xf, false));
          const bool hi = lane & 8;
          put_l2(hi ? t[2] : t[0], hi ? t[3] : t[1], (lane & 7) * 32 + (lane & 8) * 2, ar + AR_L2 + (bm * 16 + head) * 256);
        }
        q_group(4);
        extra(2); extra2(2);
        STAMP(5);
        sync_lds();
        STAMP(6);
        OPAQUE_S
        drain();
        q_group(5);
        extra(3);
        if (is0) wrcs();
        STAMP(7);
        bar();
        OPAQUE_S
        drain();
        if (is0 && L == 0 && v16 == 0) __builtin_amdgcn_raw_buffer_store_b64(u32x2{tag0, tag1}, wsr, 0, WS_F2 + (bm * 32 + head) * 8, 16);
        q_group(6);
        ldst();
        STAMP(8);
        bar();
        STAMP(9);
        OPAQUE_S
        drain();
        q_group(7);
        extra2(5);
        SCHED0;
        wrst();
        STAMP(10);
        bar();
        STAMP(11);
        OPAQUE_S
        drain();
        extra(6);
        if (isB) stg6(96 * 1024 + L * 6144);
        SCHED0;
      }
      STAMP(12);
      sync_lds();
      STAMP(13);
      OPAQUE_S
      u32x4 fl[2];
      fl[0] = fl[1] = u32x4{tag0, tag1, tag0, tag1};
      auto ldfl = [&]() AI { fl[0] = ld16s(wsr, v16, WS_F2); fl[1] = ld16s(wsr, v16 + 1024, WS_F2); };
      if (is0) ldfl();
      bar();
      constexpr int NR = isB ? 6 : 9;
      const unsigned ro = isB ? 48 * 1024 + L * 6144 : L * 9216;
      u32x4 r[NR];
      bool pend = false;
      auto flags = [&]() AI {
        const u32x4 d = (fl[0] ^ u32x4{tag0, tag1, tag0, tag1}) | (fl[1] ^ u32x4{tag0, tag1, tag0, tag1});
        pend = __builtin_amdgcn_ballot_w64((d[0] | d[1] | d[2] | d[3]) != 0) != 0;
      };
      if (isB) {
        stg6(L * 6144); stg(120 * 1024 + L * 2048, IC<2>{});
        stg6(24 * 1024 + L * 6144);
        stg6(72 * 1024 + L * 6144);
      } else {
        drain();
        if (is0) flags();
        MEMFENCE;
        extra(8); extra2(8);
        if (!is0) poison_x(3);
        while (is0 && __builtin_expect(pend, 0) && --budget > 0) {
          ldfl();
          flags();
          MEMFENCE;
        }
        stg(36 * 1024 + L * 3072, IC<3>{}); stg7(72 * 1024 + L * 14336); stg7(72 * 1024 + L * 14336 + 7168);
      }
#pragma unroll
      for (int i = 0; i < NR; ++i) r[i] = ld16s(wsr, v16 + (i & 3) * 1024, sW() + ro + (i >> 2) * 4096);
      MEMFENCE;
      if (is0 && bm + head + L == 0 && v16 == 0) __builtin_amdgcn_raw_buffer_store_b32(tag0, wsr, 0, WS_EP, 16);
      auto wr_r = [&]() AI {
        unsigned vv = v16;
        asm volatile("" : "+v"(vv));
#pragma unroll
        for (int i = 0; i < NR; ++i) *(u32x4*)(smem + L_STG + ro + i * 1024 + vv) = r[i];
      };
      auto left = [&]() AI {
        OPAQUE_LANE
        for (;;) {
          MEMFENCE;
          if (__builtin_amdgcn_ballot_w64(lflag[lane & 7] != (unsigned)layer) == 0 || --budget <= 0) break;
          SLEEP(2);
        }
        MEMFENCE;
      };
      if (isB) {
#pragma unroll
        for (int i = 0; i < 24; ++i) asm volatile("" : "=v"(wn[i / 6][i % 6]));
        if (layer == 1) ld_wn(layer + 1, 0, 2);
        STAMP(14);
        left();
        wr_r();
        MEMFENCE;
        if (layer == 1) ld_wn(layer + 1, 2, 4);
        wait_vm_lgkm0<12>();
        if (layer == 3) wait_vm<0>();
      } else {
        ld_wn(layer + 1, 0, is0 ? 1 : 2);
        if (is0) wait_vm<32>();
        STAMP(14);
        bar();
        stg6(48 * 1024 + L * 6144);
        if (is0) ld_wn(layer + 1, 1, 2);
        bar();
        ld_wn(layer + 1, 2, 4);
        wr_r();
        wait_vm<12>();
        bar();
        extra2(10);
        wait_vm_lgkm0<0>();
      }
      bar();
      if (isB && layer == 3) { STAMP_DUMP; __builtin_amdgcn_endpgm(); }
      OPAQUE_S
      wr_wn(0);
      drain();
      sync_lds();
      OPAQUE_S
      SCHED0;
#pragma unroll
      for (int g = 1; g < 4; ++g) wr_wn(g);
      MEMFENCE;
      SLEEP(SLP_G);
      q_groupW(layer + 1, 4, 4);
    };
    body(IC<0>{}, 0);
#pragma unroll 1
    for (int layer = 1;; layer += 2) {
      body(IC<1>{}, layer);
      body(IC<2>{}, 2);
    }
  }

  const f32x4 zero = f32x4{0.f, 0.f, 0.f, 0.f};
  u32x4 xr[2];
  asm volatile("" : "=v"(xr[0]), "=v"(xr[1]));
  u32x4 sel;
  u32x4 eye;
  unsigned vk;
  unsigned ra0, ra1;
  float off0, off1;
  {
    const int lane = LANEID, fr = lane & 15, fq = lane >> 4;
    const bool odd = fr & 1, own0 = fr >= fq * 4 + (fr & 1), own1 = fr >= fq * 4 + (fr & 1) + 2, nopre = bm0 == 0 && wave == 4;
    const unsigned e0 = odd ? 0x01000c0cu : 0x0c0c0100u, e1 = odd ? 0x03020c0cu : 0x0c0c0302u, ez = 0x0c0c0c0cu;
    sel = u32x4{own0 ? ez : e0, own1 ? ez : e1, own0 ? e0 : ez, own1 ? e1 : ez};
    const unsigned rb = sbase + L_RED + 1088 + ((wave - 4) * 32 + fq * 4 + (fr & 1)) * 4;
    ra0 = rb + (own0 ? 64 : 0); ra1 = rb + (own1 ? 72 : 8);
    off0 = nopre && !own0 ? -3.0e38f : 0.f; off1 = nopre && !own1 ? -3.0e38f : 0.f;
    const unsigned one = (fq & 1) == (fr >> 3) ? (fr & 1 ? 0x3f800000u : 0x00003f80u) : 0u;
    eye = u32x4{(fr & 6) == 0 ? one : 0u, (fr & 6) == 2 ? one : 0u, (fr & 6) == 4 ? one : 0u, (fr & 6) == 6 ? one : 0u};
    vk = (fq >> 1) * (32 * KVREC) + (fq & 1) * 256 + fr * 16;
    asm volatile("" : "+v"(sel), "+v"(ra0), "+v"(ra1), "+v"(off0), "+v"(off1), "+v"(eye), "+v"(vk));
  }
#pragma unroll 1
  for (int layer = 0; layer < 4; ++layer) {
    bm = bm0; head = head0; cm = cm0; cn = cn0; cw = wave - 4;
    const bool isB = layer & 1;
    const unsigned ar = WS_AR + layer * ARENA;
    {
      f32x4 acc[2][6];
      f32x4 ck[2], mv[2], xv[2];
      float mean[2], rs[2], cv[2];
      const float* lcl = lcs + (isB ? 96 : 0);
      {
        LANE_VARS
        const unsigned va = v16 + cw * 2048;
        const unsigned sa0 = ar + AR_X + bm * (512 * 1024);
        auto lda = [&](unsigned so, u32x4* a) AI {
          a[0] = ld16s(wsr, va, so);
          a[1] = ld16s(wsr, va + 1024, so);
        };
        bf16x8 f[2][6];
        u32x4 a[4][2];
        auto unset = [&](const u32x4* e) AI { return poisoned(umax(umax(umax(e[0][0], e[0][2]), e[1][0]), e[1][2])); };
        auto refill = [&](auto ic, unsigned cur) AI {
          constexpr int I = decltype(ic)::value;
          do {
            COUNT(0);
            SLEEP(SLP_R);
            MEMFENCE;
            lda(cur, a[I]);
            bool u[4];
#pragma unroll
            for (int j = 1; j < 4; ++j) u[j] = unset(a[(I + j) % 4]);
            SCHED0;
#pragma unroll
            for (int j = 1; j < 4; ++j)
              if (u[j]) lda(cur + j * 16384, a[(I + j) % 4]);
            __builtin_amdgcn_s_waitcnt(0x0f70);
            MEMFENCE;
          } while (unset(a[I]) && --budget > 0);
        };
        STAMP(0);
        if (layer > 0) {
          bar();
          if (cn < 8) SLEEP(SLP_X); else SLEEP(SLP_XD);
        }
#pragma unroll
        for (int j = 0; j < 4; ++j) { lda(sa0 + j * 16384, a[j]); SCHED0; }
#pragma unroll
        for (int m = 0; m < 2; ++m)
#pragma unroll
          for (int n = 0; n < 6; ++n) acc[m][n] = zero;
        asm volatile("" : "+v"(acc[0][0]), "+v"(acc[0][1]), "+v"(acc[0][2]), "+v"(acc[0][3]), "+v"(acc[0][4]), "+v"(acc[0][5]));
        asm volatile("" : "+v"(acc[1][0]), "+v"(acc[1][1]), "+v"(acc[1][2]), "+v"(acc[1][3]), "+v"(acc[1][4]), "+v"(acc[1][5]));
        if (layer == 0) { wait_vm<8>(); sync_lds(); }
#pragma unroll
        for (int nn = 0; nn < 6; ++nn) { const int n = nn < 4 ? nn + 2 : nn - 4; f[0][n] = *(const bf16x8*)(smem + v16 + n * 1024); }
        SCHED0;
        if (layer > 0) {
          const u32x2 tg = *(const u32x2*)(lflag + 10);
          wait_vm<8>();
          unsigned old;
          asm volatile("s_mov_b64 exec, 1\n\tds_add_rtn_u32 %0, %1, %2\n\ts_mov_b64 exec, -1\n\ts_waitcnt lgkmcnt(0)" : "=v"(old) : "v"(sbase + L_RED + 32), "v"(1) : "memory");
          if (__builtin_amdgcn_readfirstlane(old) == 8u * layer - 1)
            __builtin_amdgcn_raw_buffer_store_b64(tg, wsr, lane ? OOB : 0, ar + AR_FL + bm * 256 + (cm * 16 + cn) * 8, 16);
          MEMFENCE;
        }
        SCHED0;
        bool bad = layer > 0 && unset(a[0]);
        const int wrap = layer > 0 ? 6 : 4;
        unsigned ps[4] = {sbase + L_RED + 64 + cw * 128, sbase + L_RED + 2176 + (layer & 1) * 384, 0, WS_AR + AR_X + xt_off(bm * 16 + cw * 2 + cm, cn * 2)};
        ps[2] = ps[0] + 1088 + (layer & 1) * (2176 + 384 - 1088 - 64 - cw * 128);
        int early = (layer & 1) * cw;
        asm volatile("" : "+s"(ps[0]), "+s"(ps[1]), "+s"(ps[2]), "+s"(ps[3]), "+s"(early));
        STAMP(1);
        auto group = [&](auto modec, int g) AI {
          constexpr int MODE = decltype(modec)::value;
          constexpr bool LAST = MODE == 3, CHK = MODE == 0;
          const unsigned gs = (g < wrap ? g : g - 4) * QGRP, gn = (g + 1 < wrap ? g + 1 : g - 3) * QGRP;
          const unsigned sc = sa0 + g * 65536, so = sc + 4 * 16384;
          unsigned gl = sbase + gs + v16;
          if constexpr (LAST) asm volatile("" : "+v"(gl));
          unsigned pa[4];
          static_for<4>([&](auto ic) AI {
            constexpr int i = decltype(ic)::value;
            constexpr bool END = LAST && i == 3;
            SCHED0;
            if (i == 3 && !LAST) { sync_lds(); SCHED0; }
            if (i == 0 && LAST) {
              PRIO(1);
              if (early >= 4) PRIO(3);
              SCHED0;
              bar();
              SCHED0;
            }
            if constexpr (!LAST) PRIO(i == 3 ? 3 : 2 - i);
            if constexpr (CHK) { if (__builtin_expect(bad, 0)) refill(ic, sc + i * 16384); }
            SCHED0;
            const bf16x8* fc = f[i & 1];
            bf16x8* fn = f[(i + 1) & 1];
            const u32x4* an = a[(i + 1) % 4];
            const unsigned slot = i < 3 ? gs + (i + 1) * QSLOT : gn;
            const char* pb = LAST ? (const char*)((const LDS(char)*)gl + (i + 1) * QSLOT) : smem + slot + v16;
            unsigned mx = 0;
            static_for<12>([&](auto jc) AI {
              constexpr int j = decltype(jc)::value, nn = j >> 1, m = j & 1, n = nn < 4 ? nn + 2 : nn - 4;
              if constexpr (n < 4) mfma_acc(acc[m][n], fc[n], as_frag(a[i][m]));
              else mfma_acc(acc[m][n], as_frag(a[i][m]), fc[n]);
              SCHED0;
              if constexpr (!END && j < 6) { constexpr int r = j < 4 ? j + 2 : j - 4; fn[r] = *(const bf16x8*)(pb + r * 1024); }
              if constexpr (LAST && i == 0 && j == 6) {
                if (layer == 0) { xr[0] = ld16s(wsr, v16, ps[3]); xr[1] = ld16s(wsr, v16, ps[3] + 16384); }
              }
              if constexpr (LAST && i == 1) {
                if constexpr (j == 6) { pa[0] = lane & 48; asm volatile("" : "+v"(pa[0])); }
                if constexpr (j == 7) { pa[1] = v16 >> 2; asm volatile("" : "+v"(pa[1])); }
                if constexpr (j == 8) { pa[1] &= 60; asm volatile("" : "+v"(pa[1])); }
                if constexpr (j == 9) { pa[2] = pa[1] + ps[0]; asm volatile("" : "+v"(pa[2])); }
                if constexpr (j == 10) { pa[1] += ps[1]; asm volatile("" : "+v"(pa[1])); }
                if constexpr (j == 11) { pa[3] = pa[0] + ps[1]; asm volatile("" : "+v"(pa[3])); }
              }
              if constexpr (LAST && i == 2) {
                if constexpr (j == 6) ck[0] = ((const LDS(f32x4)*)pa[3])[8];
                if constexpr (j == 7) ck[1] = ((const LDS(f32x4)*)pa[3])[12];
                if constexpr (j == 8) { mean[0] = ((const LDS(float)*)pa[2])[0]; asm volatile("" : "+v"(pa[2])); }
                if constexpr (j == 9) { mean[1] = ((const LDS(float)*)pa[2])[16]; asm volatile("" : "+v"(pa[2])); }
                if constexpr (j == 10) { cv[0] = ((const LDS(float)*)pa[1])[64]; cv[1] = ((const LDS(float)*)pa[1])[80]; }
                if constexpr (j == 11) { pa[1] = pa[0] + ps[0]; asm volatile("" : "+v"(pa[1])); }
              }
              if constexpr (LAST && i == 3) {
                if constexpr (j == 0) mv[0] = ((const LDS(f32x4)*)pa[1])[0];
                if constexpr (j == 1) mv[1] = ((const LDS(f32x4)*)pa[1])[4];
                if constexpr (j == 2) { pa[0] += ps[2]; asm volatile("" : "+v"(pa[0])); }
                if constexpr (j == 3) { rs[0] = ((const LDS(float)*)pa[2])[272]; asm volatile("" : "+v"(pa[2])); }
                if constexpr (j == 4) xv[0] = ((const LDS(f32x4)*)pa[0])[0];
                if constexpr (j == 5) xv[1] = ((const LDS(f32x4)*)pa[0])[4];
                if constexpr (j == 6) rs[1] = ((const LDS(float)*)pa[2])[288];
              }
              if constexpr (CHK) {
                if constexpr (j == 8) mx = umax(umax(umax(an[0][0], an[0][2]), an[1][0]), an[1][2]);
                if constexpr (j == 10) bad = poisoned(mx);
              }
              if constexpr (j == 10 && !LAST) a[i][0] = ld16s(wsr, va, so + i * 16384);
              if constexpr (j == 11 && !LAST) a[i][1] = ld16s(wsr, va + 1024, so + i * 16384);
              SCHED0;
            });
          });
        };
        int g = 0;
        if (layer > 0) {
#pragma unroll 1
          for (; g < NCHK; ++g) group(IC<0>{}, g);
        }
#pragma unroll 1
        for (; g < 7; ++g) group(IC<1>{}, g);
        group(IC<3>{}, 7);
        if (!isB && cw >= 4) PRIO(2);
        mfma_done();
      }
      STAMP(2);
      LANE_VARS
      char* const scr = smem;

      u32x4 qf[2], kf[2];
      u32x2 vf[2][2];
      auto fold_rows = [&](int part, u32x4* out, const f32x4* c) AI {
#pragma unroll
        for (int m = 0; m < 2; ++m) {
          f32x4 a0 = acc[m][part * 2] + c[0] * mean[m], a1 = acc[m][part * 2 + 1] + c[1] * mean[m];
          out[m] = u32x4{pk2(a0[0], a0[1]), pk2(a0[2], a0[3]), pk2(a1[0], a1[1]), pk2(a1[2], a1[3])};
        }
      };
      auto fold_x = [&](int part, f32x2 (*x)[4], const f32x4* c) AI {
#pragma unroll
        for (int m = 0; m < 2; ++m) {
          const f32x4 a0 = acc[m][part * 2] + c[0] * mean[m], a1 = acc[m][part * 2 + 1] + c[1] * mean[m];
          x[m][0] = f32x2{a0[0], a0[1]}; x[m][1] = f32x2{a0[2], a0[3]}; x[m][2] = f32x2{a1[0], a1[1]}; x[m][3] = f32x2{a1[2], a1[3]};
        }
      };
      auto fold_v = [&](bool scaled) AI {
#pragma unroll
        for (int m = 0; m < 2; ++m) {
          f32x4 a0 = acc[m][4] + mv[m] * cv[0], a1 = acc[m][5] + mv[m] * cv[1];
          if (scaled) { a0 = mul4(a0, xv[m]); a1 = mul4(a1, xv[m]); }
          vf[m][0] = u32x2{pk2(a0[0], a0[1]), pk2(a0[2], a0[3])};
          vf[m][1] = u32x2{pk2(a1[0], a1[1]), pk2(a1[2], a1[3])};
        }
      };
      const unsigned otile = WS_AR + AR_O + xt_off(bm * 16 + cw * 2, head);
      auto put_o = [&](int m, f32x4 a, f32x4 b, unsigned opar) AI {
        const u32x4 v = u32x4{(pk2(a[0], a[1]) & ~1u) | opar, pk2(a[2], a[3]), (pk2(b[0], b[1]) & ~1u) | opar, pk2(b[2], b[3])};
        st16s(wsr, v16 + m * 1024, otile, v);
        return v;
      };
      auto hold = [&](u32x4 a, u32x4 b) AI { SCHED0; asm volatile("s_nop 1" :: "v"(a), "v"(b)); SCHED0; };

      if (!isB) {
        f32x2 lx[2][4];
        fold_x(1, lx, ck); fold_v(true);
        char* PK = smem + L_STG;
        char* VF = scr;
#pragma unroll
        for (int t = 0; t < 2; ++t)
          *(u32x4*)(VF + (cw * 2 + t) * 1024 + lane * 16) = u32x4{vf[0][t][0], vf[0][t][1], vf[1][t][0], vf[1][t][1]};
        {
          u32x4 pk[2][2];
          phi_map2(lx, rs, pk, [](float*) {});
          char* d0 = PK + cw * 4096 + fr * 64 + ((fr >> 2) & 1) * 32 + ((fq ^ ((fr >> 3) * 2)) * 8);
          char* d1 = PK + cw * 4096 + fr * 64 + (((fr >> 2) & 1) ^ 1) * 32 + ((fq ^ ((fr >> 3) * 2)) * 8);
#pragma unroll
          for (int m = 0; m < 2; ++m) {
            *(u32x2*)(d0 + m * 2048) = u32x2{pk[m][0][0], pk[m][0][1]};
            *(u32x2*)(d0 + m * 2048 + 1024) = u32x2{pk[m][0][2], pk[m][0][3]};
            *(u32x2*)(d1 + m * 2048) = u32x2{pk[m][1][0], pk[m][1][1]};
            *(u32x2*)(d1 + m * 2048 + 1024) = u32x2{pk[m][1][2], pk[m][1][3]};
          }
        }
        PRIO(1);
        const unsigned rec = ar + AR_KV + (unsigned)head * KVREC, dst = rec + (unsigned)bm * (32 * KVREC);
        unsigned ad = sbase + L_STG + (cw & 1) * 1024 + (fq * 4 + (fr >> 2)) * 64 + (((cw >> 1) ^ fq) & 1) * 32 + (((fr & 3) ^ (fq & 2)) * 8);
        unsigned vd = sbase + (cw >> 2) * 1024 + v16, kd = fq & 1 ? OOB : dst + cw * 512 + (fq >> 1) * 256 + fr * 16;
        asm volatile("" : "+v"(ad), "+v"(vd), "+v"(kd));
        sync_lds();
        STAMP(3);
        {
          const u32x4 ones = u32x4{0x3f803f80u, 0x3f803f80u, 0x3f803f80u, 0x3f803f80u};
          f32x4 c0 = zero;
          bf16x8 a[8], b0[8];
#pragma unroll
          for (int ks = 0; ks < 8; ++ks) {
            u32x2 a0 = trl(ad + ks * 4096), a1 = trl(ad + ks * 4096 + 2048);
            a[ks] = as_frag(u32x4{a0[0], a0[1], a1[0], a1[1]});
            b0[ks] = *(const LDS(bf16x8)*)(vd + ks * 2048);
            SCHED0;
          }
#pragma unroll
          for (int ks = 0; ks < 8; ++ks) c0 = mfma16(a[ks], b0[ks], c0);
          const unsigned p0 = umin(pk2(c0[0], c0[1]), POISON - 1), p1 = umin(pk2(c0[2], c0[3]), POISON - 1);
          const auto q0 = __builtin_amdgcn_permlane16_swap(p0, p0, false, false), q1 = __builtin_amdgcn_permlane16_swap(p1, p1, false, false);
          st16(wsr, kd, u32x4{q0[0], q1[0], q0[1], q1[1]});
          SCHED0;
          if (cw < 4) {
            f32x4 cz = zero;
#pragma unroll
            for (int ks = 0; ks < 8; ++ks) cz = mfma16(a[ks], as_frag(ones), cz);
            st16(wsr, fr ? OOB : dst + 4096 + cw * 64 + fq * 16, clean(__builtin_bit_cast(u32x4, cz)));
          }
        }
        SCHED0;
        STAMP(4);
        bar();
        const f32x4 cq[2] = {*(const f32x4*)(lcl + fq * 4), *(const f32x4*)(lcl + 16 + fq * 4)};
        fold_x(0, lx, cq);
        u32x4 pq[2][2];
        u32x4 zp, zq;
        u32x4 part[4];
        auto ldp = [&](int i) AI { part[i] = ld16s(wsr, vk, rec + i * (64 * KVREC) + cw * 512); };
        auto ldz = [&]() AI {
          zp = ld16(wsr, rec + fq * (32 * KVREC) + 4096 + fr * 16);
          zq = ld16(wsr, rec + (4 + fq) * (32 * KVREC) + 4096 + fr * 16);
        };
#pragma unroll
        for (int i = 0; i < 4; ++i) asm volatile("" : "=v"(part[i]));
        asm volatile("" : "=v"(zp), "=v"(zq));
        phi_map2(lx, rs, pq, [&](float* sp) AI {
          asm volatile("" : "+v"(sp[0]), "+v"(sp[1]));
          SCHED0;
          if (cw >= 4) {
            MEMFENCE;
#pragma unroll
            for (int i = 0; i < 4; ++i) ldp(i);
            MEMFENCE;
          }
          SCHED0;
        });
        asm volatile("" : "+v"(pq[0][0]), "+v"(pq[0][1]), "+v"(pq[1][0]), "+v"(pq[1][1]));
        SCHED0;
        STAMP(5);
        char* kvf = smem + L_A2;
        if (cw < 4) {
          SLEEP(SLP_K);
          MEMFENCE;
          if (cw == 0) ldz();
#pragma unroll
          for (int i = 0; i < 4; ++i) ldp(i);
          MEMFENCE;
        }
        unsigned kw = sbase + L_A2 + v16 + (cw >> 1) * 1024 + (cw & 1) * 8;
        unsigned ka = sbase + L_A2 + v16, za = sbase + L_Z + fq * 16;
        asm volatile("" : "+v"(kw), "+v"(ka), "+v"(za));
        u32x2 kvs;
        auto nans = [&](f32x4 v) AI { return __builtin_amdgcn_ballot_w64(__builtin_isunordered(v[0], v[1])) | __builtin_amdgcn_ballot_w64(__builtin_isunordered(v[2], v[3])); };
        auto sums = [&]() AI {
          unsigned long long nan = 0;
          if (cw == 0) {
            f32x4 z = __builtin_bit_cast(f32x4, zp) + __builtin_bit_cast(f32x4, zq);
            nan = nans(z);
            float z0 = z[0], z1 = z[1], z2 = z[2], z3 = z[3];
            xred4<false>(z0, z1, z2, z3);
            *(u32x2*)(smem + L_Z + (fr >> 3) * 64 + (fr & 3) * 16 + ((fr >> 2) & 1) * 8) = u32x2{pk2(z0, z1), pk2(z2, z3)};
          }
          f32x4 s4 = zero;
#pragma unroll
          for (int i = 0; i < 4; ++i) { s4 = mfma16(as_frag(eye), as_frag(part[i]), s4); SCHED0; }
          kvs = u32x2{pk2(s4[0], s4[1]), pk2(s4[2], s4[3])};
          asm volatile("" : "+v"(kvs));
          return nan | __builtin_amdgcn_ballot_w64(s4[0] != s4[0]);
        };
        if (__builtin_expect(sums() != 0, 0)) {
          for (;;) {
            bool again = false;
            if (cw == 0 && poisoned(umax4(umax4(zp), zq))) { again = true; ldz(); }
#pragma unroll
            for (int i = 0; i < 4; ++i)
              if (poisoned(umax4(part[i]))) { again = true; ldp(i); }
            __builtin_amdgcn_s_waitcnt(0x0f70);
            MEMFENCE;
            if (!again || --budget <= 0) break;
            COUNT(2);
          }
          sums();
        }
        STAMP(6);
        *(LDS(u32x2)*)kw = kvs;
        sync_lds();
        STAMP(7);
        bf16x8 kvr[2][2], zf[2];
#pragma unroll
        for (int s = 0; s < 2; ++s) { zf[s] = *(const LDS(bf16x8)*)(za + s * 64); SCHED0; }
#pragma unroll
        for (int s = 0; s < 2; ++s) {
          kvr[0][s] = *(const LDS(bf16x8)*)(ka + (0 * 2 + s) * 1024);
          kvr[1][s] = *(const LDS(bf16x8)*)(ka + (1 * 2 + s) * 1024);
          SCHED0;
        }
        f32x4 cd[2], c[2][2];
        u32x4 oo[2];
        auto den = [&](int m, int s) AI { cd[m] = mfma16(zf[s], as_frag(pq[m][s]), s ? cd[m] : zero); SCHED0; };
        auto num = [&](int m, int s) AI {
#pragma unroll
          for (int t = 0; t < 2; ++t) { c[m][t] = mfma16(kvr[t][s], as_frag(pq[m][s]), s ? c[m][t] : zero); SCHED0; }
        };
        auto fin = [&](int m, float id) AI { const f32x4 i4 = f32x4{id, id, id, id}; oo[m] = put_o(m, mul4(c[m][0], i4), mul4(c[m][1], i4), 0); SCHED0; };
        den(0, 0); den(1, 0); den(0, 1); den(1, 1);
        num(0, 0); num(0, 1); num(1, 0);
        const float i0 = __builtin_amdgcn_rcpf(cd[0][0] + 1e-6f), i1 = __builtin_amdgcn_rcpf(cd[1][0] + 1e-6f);
        SCHED0;
        num(1, 1);
        fin(0, i0); fin(1, i1);
        hold(oo[0], oo[1]);
        STAMP(8);
        asm volatile("" : "=v"(acc[0][0]), "=v"(acc[0][1]), "=v"(acc[0][2]), "=v"(acc[0][3]), "=v"(acc[0][4]), "=v"(acc[0][5]));
        asm volatile("" : "=v"(acc[1][0]), "=v"(acc[1][1]), "=v"(acc[1][2]), "=v"(acc[1][3]), "=v"(acc[1][4]), "=v"(acc[1][5]));
        asm volatile("" : "=v"(ck[0]), "=v"(ck[1]), "=v"(mv[0]), "=v"(mv[1]), "=v"(xv[0]), "=v"(xv[1]));
        asm volatile("" : "=v"(mean[0]), "=v"(mean[1]), "=v"(rs[0]), "=v"(rs[1]), "=v"(cv[0]), "=v"(cv[1]));
      }
      int kind = layer & 1;
      asm volatile("" : "+s"(kind));
      if (kind) {
        char* ex = scr;
        fold_rows(1, kf, ck); fold_v(false);
        const unsigned hrec = ar + AR_KV + (unsigned)(bm * 32 + head) * KVREC;
        {
          const u32x4 vl = u32x4{vf[1][0][0], vf[1][0][1], vf[1][1][0], vf[1][1][1]};
          if (cw == 7) {
            st16s(wsr, v16, hrec, clean(kf[1]));
            STPAD;
            st16s(wsr, v16 + 1024, hrec, clean(vl));
            STPAD;
          }
          char* e2 = ex + (cw + 1) * 2048;
          *(u32x4*)(e2 + lane * 16) = kf[1];
          *(u32x2*)(e2 + 1024 + lane * 8) = vf[1][0];
          *(u32x2*)(e2 + 1536 + lane * 8) = vf[1][1];
          MEMFENCE;
          lflag[cw] = layer;
          MEMFENCE;
          PRIO(1);
        }
        SCHED0;
        fold_rows(0, qf, xv);
        u32x4 kp;
        u32x4 va0 = __builtin_shufflevector(vf[0][0], vf[0][0], -1, -1, 0, 1), vb0 = __builtin_shufflevector(vf[0][1], vf[0][1], -1, -1, 0, 1);
        auto setp = [&](u32x4& d, u32x2 v) AI { d[0] = v[0]; d[1] = v[1]; };
        auto ldh = [&]() AI {
          MEMFENCE;
          kp = ld16s(wsr, v16, hrec - 32 * KVREC);
          setp(va0, __builtin_amdgcn_raw_buffer_load_b64(wsr, v16 + 1024, hrec - 32 * KVREC, 16));
          MEMFENCE;
          setp(vb0, __builtin_amdgcn_raw_buffer_load_b64(wsr, v16 + 1032, hrec - 32 * KVREC, 16));
          MEMFENCE;
        };
        unsigned fl = 0;
        auto rdl = [&]() AI {
          MEMFENCE;
          fl = lflag[cw - 1];
          MEMFENCE;
          kp = *(const u32x4*)(ex + cw * 2048 + lane * 16);
          setp(va0, *(const u32x2*)(ex + cw * 2048 + 1024 + lane * 8));
          MEMFENCE;
          setp(vb0, *(const u32x2*)(ex + cw * 2048 + 1536 + lane * 8));
          MEMFENCE;
        };
        auto prev = [&]() AI {
          if (cw > 0) {
            do { SLEEP(1); rdl(); __builtin_amdgcn_s_waitcnt(0xc07f); } while (__builtin_amdgcn_readfirstlane(fl) != (unsigned)layer && --budget > 0);
          } else if (bm > 0) {
            SLEEP(SLP_HX);
            do { ldh(); __builtin_amdgcn_s_waitcnt(0x0f70); }
            while (poisoned(umax4(umax4(kp), u32x4{va0[0], va0[1], vb0[0], vb0[1]})) && --budget > 0);
          } else {
            kp = u32x4{0u, 0u, 0u, 0u};
            setp(va0, u32x2{0u, 0u}); setp(vb0, u32x2{0u, 0u});
          }
          asm volatile("" : "+v"(kp), "+v"(va0), "+v"(vb0));
        };
        const float scale = 0.17677669529663687f * LOG2E;
        const bool odd = fr & 1;
        const bool own0 = sel[0] == 0x0c0c0c0cu, own1 = sel[1] == 0x0c0c0c0cu;
        float l[2][2], mx[2], rk[2][2], sum[2];
#pragma unroll
        for (int m = 0; m < 2; ++m) { rk[m][0] = ((const LDS(float)*)ra0)[m * 16]; rk[m][1] = ((const LDS(float)*)ra1)[m * 16]; }
        float qk[2][2];
#pragma unroll
        for (int m = 0; m < 2; ++m) { const float qs = rs[m] * scale; qk[m][0] = qs * rk[m][0]; qk[m][1] = qs * rk[m][1]; }
        u32x4 va1 = u32x4{vf[0][0][0], vf[0][0][1], vf[1][0][0], vf[1][0][1]}, vb1 = u32x4{vf[0][1][0], vf[0][1][1], vf[1][1][0], vf[1][1][1]};
        auto score = [&](int m) AI {
          f32x4 s0 = mfma16(as_frag(m == 0 ? kp : kf[0]), as_frag(qf[m]), zero);
          f32x4 s1 = mfma16(as_frag(kf[m]), as_frag(qf[m]), zero);
          float a = own0 ? (odd ? s1[1] : s1[0]) : (odd ? s0[1] : s0[0]);
          float b = own1 ? (odd ? s1[3] : s1[2]) : (odd ? s0[3] : s0[2]);
          if (m == 0) { l[m][0] = __builtin_fmaf(a, qk[m][0], off0); l[m][1] = __builtin_fmaf(b, qk[m][1], off1); }
          else { l[m][0] = a * qk[m][0]; l[m][1] = b * qk[m][1]; }
          mx[m] = fmx(l[m][0], l[m][1]);
        };
        auto expo = [&](int m) AI {
          l[m][0] = __builtin_amdgcn_exp2f(l[m][0] - mx[m]);
          l[m][1] = __builtin_amdgcn_exp2f(l[m][1] - mx[m]);
          sum[m] = l[m][0] + l[m][1];
        };
        f32x4 ov[2][2];
        auto pv = [&](int m) AI {
          const unsigned pr = pk2(mul1(mul1(l[m][0], rk[m][0]), sum[m]), mul1(mul1(l[m][1], rk[m][1]), sum[m]));
          const u32x4 pf = u32x4{__builtin_amdgcn_perm(pr, pr, sel[0]), __builtin_amdgcn_perm(pr, pr, sel[1]),
                                 __builtin_amdgcn_perm(pr, pr, sel[2]), __builtin_amdgcn_perm(pr, pr, sel[3])};
          ov[m][0] = mfma16(as_frag(m ? va1 : va0), as_frag(pf), zero);
          ov[m][1] = mfma16(as_frag(m ? vb1 : vb0), as_frag(pf), zero);
        };
        asm volatile("" : "+v"(qf[0]), "+v"(qf[1]), "+v"(va0), "+v"(vb0));
        SCHED0;
        if (cw > 0) rdl();
        SCHED0;
        score(1);
        asm volatile("" : "+v"(qf[0]), "+v"(qk[0][0]), "+v"(qk[0][1]), "+v"(mx[1]), "+v"(l[1][0]), "+v"(l[1][1]));
        asm volatile("" : "+v"(va1), "+v"(vb1));
        SCHED0;
        STAMP(3);
        if (__builtin_expect(cw == 0 || __builtin_amdgcn_readfirstlane(fl) != (unsigned)layer, 0)) prev();
        score(0);
        xred2<true>(mx[0], mx[1]);
        expo(0); expo(1);
        xred2<false, true>(sum[0], sum[1]);
        pv(0); pv(1);
        SCHED0;
        const u32x4 o0 = put_o(0, ov[0][0], ov[0][1], 1), o1 = put_o(1, ov[1][0], ov[1][1], 1);
        hold(o0, o1);
        STAMP(4);
      }
    }
    {
      LANE_VARS
      const int mt = bm * 16 + cw * 2 + cm;
      const unsigned so0 = WS_AR + AR_O + xt_off(mt, 0);
      const unsigned opar = layer & 1;
      auto diff = [&](u32x4 v) AI { return ((v[0] ^ opar) | (v[2] ^ opar)) & 1u; };
      auto dif3 = [&](u32x4 v) AI { return __builtin_amdgcn_bitop3_b32(v[0], v[2], opar, 0x7e); };
      auto any = [&](unsigned d) AI { return __builtin_amdgcn_ballot_w64(d != 0) != 0; };
      const int g0 = cm;
      u32x4 a[RO];
      auto refill = [&](auto ic, auto wc, unsigned cur, unsigned nxt) AI {
        constexpr int I = decltype(ic)::value, W = decltype(wc)::value;
        do {
          COUNT(1);
          SLEEP(SLP_R);
          MEMFENCE;
          a[I] = ld16s(wsr, v16, cur + I * 16384);
          bool u[RO];
#pragma unroll
          for (int j = I + 1; j < I + W; ++j) u[j % RO] = any(diff(a[j % RO]));
          SCHED0;
#pragma unroll
          for (int j = I + 1; j < I + W; ++j)
            if (u[j % RO]) a[j % RO] = ld16s(wsr, v16, (j < RO ? cur : nxt) + (j % RO) * 16384);
          __builtin_amdgcn_s_waitcnt(0x0f70);
          MEMFENCE;
        } while (any(diff(a[I]) | diff(a[I + 1])) && --budget > 0);
      };
      const unsigned sf = so0 + g0 * (RO * 16384);
      f32x4 acc[4];
#pragma unroll
      for (int n = 0; n < 4; ++n)
        acc[n] = f32x4{bflo(xr[n >> 1][(n & 1) * 2]), bfhi(xr[n >> 1][(n & 1) * 2]), bflo(xr[n >> 1][(n & 1) * 2 + 1]), bfhi(xr[n >> 1][(n & 1) * 2 + 1])};
      asm volatile("" : "+v"(acc[0]), "+v"(acc[1]), "+v"(acc[2]), "+v"(acc[3]));
      SCHED0;
      const bool late = isB && cw == 0;
      if (late) bar();
      if (cn < 8) { if (late) SLEEP(SLP_O0); else SLEEP(SLP_O); }
      else { if (late) SLEEP(SLP_O0D); else SLEEP(SLP_OD); }
#pragma unroll
      for (int j = 0; j < 4; ++j) { a[j] = ld16s(wsr, v16, sf + j * 16384); SCHED0; }
      if (!late) bar();
      STAMP(9);
      bf16x8 wf[2][4];
      const char* pw0 = smem + L_STG + v16;
#pragma unroll
      for (int n = 0; n < 8; ++n) wf[n >> 2][n & 3] = *(const bf16x8*)(pw0 + g0 * (RO * 4096) + n * 1024);
      SCHED0;
      STAMP(10);
      if (any(diff(a[0]) | diff(a[1]))) refill(IC<0>{}, IC<4>{}, sf, sf);
      SCHED0;
#pragma unroll
      for (int j = 4; j < RO; ++j) { a[j] = ld16s(wsr, v16, sf + j * 16384); SCHED0; }
      STAMP(11);
      bool bad = false;
      auto group = [&](auto lastc, int g) AI {
        constexpr bool LAST = decltype(lastc)::value;
        const int gc = g ^ g0, gn = (g + 1) ^ g0;
        const unsigned sc = so0 + gc * (RO * 16384), so = so0 + gn * (RO * 16384);
        const char* pc = pw0 + gc * (RO * 4096);
        const char* pn = pw0 + gn * (RO * 4096);
        static_for<RO>([&](auto ic) AI {
          constexpr int i = decltype(ic)::value;
          SCHED0;
          if constexpr (!LAST && i == 0) { if (g == 0) PRIO(3); else if (g == 2) PRIO(2); }
          if constexpr (LAST && i == 3) PRIO(1);
          if constexpr (i % 2 == 0)
            if (__builtin_expect(bad, 0)) refill(ic, IC<(LAST && i + 4 > RO) ? RO - i : 4>{}, sc, so);
          SCHED0;
          unsigned mx = 0, my = 0;
          static_for<4>([&](auto nc) AI {
            constexpr int n = decltype(nc)::value;
            mfma_acc(acc[n], wf[i & 1][n], as_frag(a[i]));
            SCHED0;
            if constexpr (i + 2 < RO) wf[i & 1][n] = *(const bf16x8*)(pc + (i + 2) * 4096 + n * 1024);
            else if constexpr (!LAST) wf[i & 1][n] = *(const bf16x8*)(pn + (i + 2 - RO) * 4096 + n * 1024);
            if constexpr (!(LAST && i == RO - 1) && (i & 1)) {
              if constexpr (n == 0) mx = dif3(a[(i + 1) % RO]);
              if constexpr (n == 1) my = dif3(a[(i + 2) % RO]);
              if constexpr (n == 2) bad = any((mx | my) & 1u);
            }
            if constexpr (n == 3 && !LAST) a[i] = ld16s(wsr, v16, so + i * 16384);
            SCHED0;
          });
        });
      };
#pragma unroll 1
      for (int g = 0; g < 3; ++g) group(std::false_type{}, g);
      group(std::true_type{}, 3);
      mfma_done();
      STAMP(12);
#pragma unroll
      for (int j = 0; j < 2; ++j)
#pragma unroll
        for (int e = 0; e < 4; ++e) xr[j][e] = pk2(acc[j * 2 + (e >> 1)][(e & 1) * 2], acc[j * 2 + (e >> 1)][(e & 1) * 2 + 1]);
      if (layer == 3) {
        u32x4 yo[2];
#pragma unroll
        for (int j = 0; j < 2; ++j) {
          auto u = __builtin_amdgcn_permlane16_swap(xr[j][0], xr[j][2], false, false);
          auto v = __builtin_amdgcn_permlane16_swap(xr[j][1], xr[j][3], false, false);
          yo[j] = u32x4{u[0], v[0], u[1], v[1]};
        }
        const unsigned vo = fr * 2048 + (fq & 1) * 32 + (fq >> 1) * 16;
        __builtin_amdgcn_raw_buffer_store_b128(yo[0], mk_rsrc(p.out, 4 * MBY), vo, mt * 32768 + cn * 128, 18);
        __builtin_amdgcn_raw_buffer_store_b128(yo[1], mk_rsrc(p.out, 4 * MBY), vo + 64, mt * 32768 + cn * 128, 18);
        STAMP(13);
        STAMP(14);
        STAMP_DUMP;
        __builtin_amdgcn_endpgm();
      }
      const u32x2 stat = put_x(xr, v16, ar + ARENA + AR_X + xt_off(mt, cn * 2));
      __builtin_amdgcn_raw_buffer_store_b64(stat, wsr, fq ? OOB : fr * 8, ar + ARENA + AR_ST + (mt * 16 + cn) * 128, 16);
      MEMFENCE;
      STAMP(14);
    }
  }
}

extern "C" void kernel_launch(void* const* d_in, const int* in_sizes, int n_in, void* d_out, int out_size,
                              void* d_ws, size_t ws_size, hipStream_t stream) {
  (void)hipFuncSetAttribute((const void*)mega_kernel, hipFuncAttributeMaxDynamicSharedMemorySize, MEGA_LDS);
  MegaParams m;
  m.ws = (char*)d_ws; m.out = (u16*)d_out; m.x = (const u16*)d_in[0]; m.wphi = (const u16*)d_in[2];
  m.wqkvA = (const u16*)d_in[1]; m.wqkvB = (const u16*)d_in[5];
  m.woA = (const u16*)d_in[3]; m.woB = (const u16*)d_in[6];
  mega_kernel<<<256, 768, MEGA_LDS, stream>>>(m);
}
```

```cpp
#include <hip/hip_runtime.h>
#include <stdint.h>
#include <type_traits>

#define DI __device__ __forceinline__
#define AI __attribute__((always_inline))
typedef unsigned short u16;
typedef __attribute__((ext_vector_type(8))) short bf16x8;
typedef __attribute__((ext_vector_type(4))) short s16x4;
typedef __attribute__((ext_vector_type(4))) float f32x4;
typedef __attribute__((ext_vector_type(2))) float f32x2;
typedef __attribute__((ext_vector_type(2))) __bf16 bf2;
typedef __attribute__((ext_vector_type(4))) unsigned u32x4;
typedef __attribute__((ext_vector_type(2))) unsigned u32x2;
typedef __attribute__((address_space(3))) s16x4* lds_s16x4_ptr;
#define LDS(T) __attribute__((address_space(3))) T
template <int N> using IC = std::integral_constant<int, N>;

constexpr float LOG2E = 1.4426950408889634f;

DI unsigned pk2(float a, float b) {
  f32x2 v = {a, b};
  bf2 r = __builtin_convertvector(v, bf2);
  return __builtin_bit_cast(unsigned, r);
}
DI float bflo(unsigned u) { return __uint_as_float(u << 16); }
DI float bfhi(unsigned u) { return __uint_as_float(u & 0xffff0000u); }
DI f32x4 mfma16(bf16x8 a, bf16x8 b, f32x4 c) { return __builtin_amdgcn_mfma_f32_16x16x32_bf16(a, b, c, 0, 0, 0); }
DI void mfma_acc(f32x4& c, bf16x8 a, bf16x8 b) { asm volatile("v_mfma_f32_16x16x32_bf16 %0, %1, %2, %0" : "+v"(c) : "v"(a), "v"(b)); }
DI void mfma_done() { asm volatile("s_nop 7\n\ts_nop 1" ::: "memory"); __builtin_amdgcn_sched_barrier(0); }
DI bf16x8 as_frag(u32x4 v) { return __builtin_bit_cast(bf16x8, v); }
DI float dot2(unsigned a, unsigned b, float c) {
  return __builtin_amdgcn_fdot2_f32_bf16(__builtin_bit_cast(bf2, a), __builtin_bit_cast(bf2, b), c, false);
}
DI u32x2 trread(const char* p) { return __builtin_bit_cast(u32x2, __builtin_amdgcn_ds_read_tr16_b64_v4i16((lds_s16x4_ptr)p)); }
DI u32x2 trl(unsigned a) { return __builtin_bit_cast(u32x2, __builtin_amdgcn_ds_read_tr16_b64_v4i16((lds_s16x4_ptr)a)); }
DI float fmx(float a, float b) { return __builtin_elementwise_maximum(a, b); }
DI float fmn(float a, float b) { return __builtin_elementwise_minimum(a, b); }

DI float mul1(float a, float b) { float r; asm("v_mul_f32 %0, %1, %2" : "=v"(r) : "v"(a), "v"(b)); return r; }
DI float add1(float a, float b) { float r; asm("v_add_f32 %0, %1, %2" : "=v"(r) : "v"(a), "v"(b)); return r; }
DI f32x4 mul4(f32x4 a, f32x4 b) {
  float r0, r1, r2, r3;
  asm("v_mul_f32 %0, %4, %8\n\tv_mul_f32 %1, %5, %9\n\tv_mul_f32 %2, %6, %10\n\tv_mul_f32 %3, %7, %11"
      : "=&v"(r0), "=&v"(r1), "=&v"(r2), "=&v"(r3) : "v"(a[0]), "v"(a[1]), "v"(a[2]), "v"(a[3]), "v"(b[0]), "v"(b[1]), "v"(b[2]), "v"(b[3]));
  return f32x4{r0, r1, r2, r3};
}
DI f32x4 add4(f32x4 a, f32x4 b) { return f32x4{add1(a[0], b[0]), add1(a[1], b[1]), add1(a[2], b[2]), add1(a[3], b[3])}; }

template <bool IS_MAX, bool INV = false>
DI void xred2(float& a, float& b) {
  auto r = __builtin_amdgcn_permlane32_swap(__float_as_uint(a), __float_as_uint(b), false, false);
  float c = IS_MAX ? fmx(__uint_as_float(r[0]), __uint_as_float(r[1])) : __uint_as_float(r[0]) + __uint_as_float(r[1]);
  auto q = __builtin_amdgcn_permlane16_swap(__float_as_uint(c), __float_as_uint(c), false, false);
  c = IS_MAX ? fmx(__uint_as_float(q[0]), __uint_as_float(q[1])) : __uint_as_float(q[0]) + __uint_as_float(q[1]);
  if (INV) c = __builtin_amdgcn_rcpf(c);
  auto w = __builtin_amdgcn_permlane32_swap(__float_as_uint(c), __float_as_uint(c), false, false);
  a = __uint_as_float(w[0]); b = __uint_as_float(w[1]);
}
template <bool IS_MAX, bool INV = false>
DI void xred4(float& a, float& b, float& c, float& d) {
  auto op = [](unsigned x, unsigned y) { return IS_MAX ? fmx(__uint_as_float(x), __uint_as_float(y)) : __uint_as_float(x) + __uint_as_float(y); };
  auto r0 = __builtin_amdgcn_permlane32_swap(__float_as_uint(a), __float_as_uint(b), false, false);
  auto r1 = __builtin_amdgcn_permlane32_swap(__float_as_uint(c), __float_as_uint(d), false, false);
  float ab = op(r0[0], r0[1]), cd = op(r1[0], r1[1]);
  auto q = __builtin_amdgcn_permlane16_swap(__float_as_uint(ab), __float_as_uint(cd), false, false);
  float e = op(q[0], q[1]);
  if (INV) e = __builtin_amdgcn_rcpf(e);
  auto w = __builtin_amdgcn_permlane16_swap(__float_as_uint(e), __float_as_uint(e), false, false);
  auto u = __builtin_amdgcn_permlane32_swap(w[0], w[0], false, false);
  auto v = __builtin_amdgcn_permlane32_swap(w[1], w[1], false, false);
  a = __uint_as_float(u[0]); b = __uint_as_float(u[1]); c = __uint_as_float(v[0]); d = __uint_as_float(v[1]);
}

DI unsigned xt_off(int mt, int ks) { return (unsigned)((((mt >> 4) * 32 + ks) * 16 + (mt & 15)) << 10); }

#define GLDS_PRE "s_mov_b32 %0, m0\n\ts_mov_b32 m0, %3\n\ts_nop 2\n\t"
#define GLDS_POST "s_mov_b32 m0, %0"
#define GLDS_OPS : "=&s"(keep) : "v"(voff), "s"(sbase), "s"(lds) : "memory"
#define GL1(c) "global_load_lds_dwordx4 %1, %2" c "\n\t"
#define GL2(c) GL1(c) "global_load_lds_dwordx4 %1, %2 offset:1024" c "\n\t"
#define GL3(c) GL2(c) "global_load_lds_dwordx4 %1, %2 offset:2048" c "\n\t"
#define GL4(c) GL3(c) "global_load_lds_dwordx4 %1, %2 offset:3072" c "\n\t"
template <int N, bool SC1 = false>
DI void glds_run(unsigned voff, const void* sbase, unsigned lds) {
  unsigned keep;
  if constexpr (SC1) {
    if constexpr (N == 2) asm volatile(GLDS_PRE GL2(" sc1") GLDS_POST GLDS_OPS);
    else if constexpr (N == 3) asm volatile(GLDS_PRE GL3(" sc1") GLDS_POST GLDS_OPS);
    else asm volatile(GLDS_PRE GL4(" sc1") GLDS_POST GLDS_OPS);
  } else {
    static_assert(SC1 || N == 4, "");
    asm volatile(GLDS_PRE GL4("") GLDS_POST GLDS_OPS);
  }
}
template <int N> DI void wait_vm_lgkm0() { asm volatile("s_waitcnt vmcnt(%0) lgkmcnt(0)" ::"n"(N) : "memory"); }
template <int N> DI void wait_vm() { asm volatile("s_waitcnt vmcnt(%0)" ::"n"(N) : "memory"); }
DI void drain() { asm volatile("s_waitcnt vmcnt(0)" ::: "memory"); __builtin_amdgcn_s_waitcnt(0x0f70); }
DI void bar() { asm volatile("" ::: "memory"); __builtin_amdgcn_s_barrier(); asm volatile("" ::: "memory"); }
DI void sync_lds() { asm volatile("s_waitcnt lgkmcnt(0)" ::: "memory"); bar(); }
#define SCHED0 __builtin_amdgcn_sched_barrier(0)
#define STPAD do { SCHED0; asm volatile("s_nop 1"); SCHED0; } while (0)
#define SLEEP(n) do { if (n) __builtin_amdgcn_s_sleep(n); } while (0)
#define PRIO(n) __builtin_amdgcn_s_setprio(n)

template <typename F>
DI void phi_map2(const f32x2 (*x)[4], const float* rs, u32x4 (*out)[2], F mid) {
  auto top = [&](float sg, float* mx) AI {
#pragma unroll
    for (int m = 0; m < 2; ++m)
      mx[m] = fmx(fmx(fmx(sg * x[m][0][0], sg * x[m][0][1]), sg * x[m][1][0]), fmx(fmx(sg * x[m][1][1], sg * x[m][2][0]), fmx(fmx(sg * x[m][2][1], sg * x[m][3][0]), sg * x[m][3][1])));
    xred2<true>(mx[0], mx[1]);
  };
  auto sum8 = [](const f32x2* e) AI { const f32x2 t = (e[0] + e[1]) + (e[2] + e[3]); float lo = t[0]; asm("" : "+v"(lo)); return lo + t[1]; };
  float mx[2], sp[2], sn[2];
  top(1.f, mx);
  f32x2 ep[2][4], en[2][4];
#pragma unroll
  for (int m = 0; m < 2; ++m) {
    const float sl = rs[m] * LOG2E, off = mx[m] * -sl;
#pragma unroll
    for (int j = 0; j < 4; ++j) {
      const f32x2 a = __builtin_elementwise_fma(x[m][j], f32x2{sl, sl}, f32x2{off, off});
      ep[m][j] = f32x2{__builtin_amdgcn_exp2f(a[0]), __builtin_amdgcn_exp2f(a[1])};
    }
#pragma unroll
    for (int j = 0; j < 4; ++j) en[m][j] = f32x2{__builtin_amdgcn_rcpf(ep[m][j][0]), __builtin_amdgcn_rcpf(ep[m][j][1])};
    sp[m] = sum8(ep[m]); sn[m] = sum8(en[m]);
  }
  xred4<false, true>(sp[0], sn[0], sp[1], sn[1]);
  if (__builtin_expect(__builtin_amdgcn_ballot_w64(!(fmn(sn[0], sn[1]) > 1e-30f)) != 0, 0)) {
    top(-1.f, mx);
#pragma unroll
    for (int m = 0; m < 2; ++m) {
      const float sl = rs[m] * -LOG2E, off = mx[m] * sl;
#pragma unroll
      for (int j = 0; j < 4; ++j) {
        const f32x2 a = __builtin_elementwise_fma(x[m][j], f32x2{sl, sl}, f32x2{off, off});
        en[m][j] = f32x2{__builtin_amdgcn_exp2f(a[0]), __builtin_amdgcn_exp2f(a[1])};
      }
      sn[m] = sum8(en[m]);
    }
    xred2<false, true>(sn[0], sn[1]);
  }
  mid(sp);
#pragma unroll
  for (int m = 0; m < 2; ++m) {
#pragma unroll
    for (int j = 0; j < 4; j += 2) {
      f32x2 a0, a1, b0, b1;
      asm("v_pk_mul_f32 %0, %4, %5 op_sel_hi:[0,1]\n\tv_pk_mul_f32 %1, %4, %6 op_sel_hi:[0,1]\n\t"
          "v_pk_mul_f32 %2, %4, %7 op_sel:[1,0]\n\tv_pk_mul_f32 %3, %4, %8 op_sel:[1,0]"
          : "=&v"(a0), "=&v"(a1), "=&v"(b0), "=&v"(b1) : "v"(f32x2{sp[m], sn[m]}), "v"(ep[m][j]), "v"(ep[m][j + 1]), "v"(en[m][j]), "v"(en[m][j + 1]));
      out[m][0][j] = pk2(a0[0], a0[1]); out[m][0][j + 1] = pk2(a1[0], a1[1]);
      out[m][1][j] = pk2(b0[0], b0[1]); out[m][1][j + 1] = pk2(b1[0], b1[1]);
    }
  }
}

#define SLP_X 8
#define SLP_XD 4
#define SLP_O 19
#define SLP_OD 10
#define SLP_O0 8
#define SLP_O0D 2
#define SLP_K 4
#define SLP_R 4
#define SLP_HX 1
#define SLP_Q 4
#define SLP_P 40
#define SLP_E 14
#define SLP_G 12
#define SPR0 1
#define SW0 0
#define LANEID __builtin_amdgcn_mbcnt_hi(~0u, __builtin_amdgcn_mbcnt_lo(~0u, 0u))
#define STAMP(i) do {} while (0)
#define STAMP_DUMP do {} while (0)
#define COUNT(k) do {} while (0)
#define PSTAMP(l) do { const int layer = l; (void)layer; STAMP(15); } while (0)
constexpr unsigned MBY = 1u << 20;
constexpr unsigned WS_WQ = 0;
constexpr unsigned WS_WO = 12 * MBY;
constexpr unsigned WS_AR = 16 * MBY, ARENA = 12 * MBY;
constexpr unsigned AR_X = 0, AR_ST = 4 * MBY, AR_O = 5 * MBY, AR_KV = 9 * MBY;
constexpr unsigned AR_L2 = AR_ST + 256 * 1024, AR_FL = AR_ST + 320 * 1024;
constexpr unsigned KVREC = 9 * 1024;
constexpr unsigned WS_CS = WS_AR + 4 * ARENA;
constexpr unsigned WS_F1 = WS_CS + 512 * 1024;
constexpr unsigned WS_F2 = WS_F1 + 64 * 1024;
constexpr unsigned WS_EP = WS_F2 + 2048;
constexpr unsigned WS_END = WS_EP + 128;
constexpr unsigned POISON = 0xffffffffu;
constexpr unsigned OOB = 0x80000000u;

typedef __amdgpu_buffer_rsrc_t rsrc_t;
DI rsrc_t mk_rsrc(const void* p, unsigned bytes) { return __builtin_amdgcn_make_buffer_rsrc((void*)p, 0, bytes, 0x00020000); }
DI void st16(rsrc_t r, unsigned off, u32x4 v) { __builtin_amdgcn_raw_buffer_store_b128(v, r, off, 0, 16); }
DI void st16s(rsrc_t r, unsigned voff, unsigned soff, u32x4 v) { __builtin_amdgcn_raw_buffer_store_b128(v, r, voff, soff, 16); }
DI u32x4 ld16(rsrc_t r, unsigned off) { return __builtin_amdgcn_raw_buffer_load_b128(r, off, 0, 16); }
DI u32x4 ld16s(rsrc_t r, unsigned voff, unsigned soff) { return __builtin_amdgcn_raw_buffer_load_b128(r, voff, soff, 16); }
DI u32x4 clean(u32x4 v) { return __builtin_elementwise_min(v, u32x4{POISON - 1, POISON - 1, POISON - 1, POISON - 1}); }
DI unsigned umax(unsigned a, unsigned b) { return a > b ? a : b; }
DI unsigned umin(unsigned a, unsigned b) { return a < b ? a : b; }
DI unsigned umax4(u32x4 v) { return umax(umax(umax(v[0], v[1]), v[2]), v[3]); }
DI unsigned umax4(unsigned m, u32x4 v) { return umax(umax(umax(umax(m, v[0]), v[1]), v[2]), v[3]); }
DI bool poisoned(unsigned m) { return __builtin_amdgcn_ballot_w64(m == POISON) != 0; }
#define MEMFENCE asm volatile("" ::: "memory")

#define BL(i, o) "buffer_load_dwordx4 %1, %2, %" #i " offen" o " nt lds\n\t"
#define BL_OPS(...) : "=&s"(keep) : "v"(voff), "s"(r), "s"(lds), __VA_ARGS__ : "memory"
template <int N>
DI void blds_run(rsrc_t r, unsigned voff, unsigned lds, unsigned s0, unsigned s1 = 0, unsigned s2 = 0, unsigned s3 = 0) {
  unsigned keep;
  if constexpr (N == 2) asm volatile(GLDS_PRE BL(4, "") BL(5, " offset:1024") GLDS_POST BL_OPS("s"(s0), "s"(s1)));
  else asm volatile(GLDS_PRE BL(4, "") BL(5, " offset:1024") BL(6, " offset:2048") BL(7, " offset:3072") GLDS_POST BL_OPS("s"(s0), "s"(s1), "s"(s2), "s"(s3)));
}

#define CODEB 36360
constexpr int MEGA_CODE_BYTES = CODEB;
DI unsigned code_prefetch(int i) {
  unsigned long long pc; int rel; unsigned d;
  asm volatile("s_getpc_b64 %0\n\ts_mov_b32 %1, _Z11mega_kernel10MegaParams@rel32@lo+4" : "=s"(pc), "=s"(rel));
  const unsigned long long a = pc + (long long)rel + (i * 192 + 192 <= MEGA_CODE_BYTES ? i * 192 : 0);
  asm volatile("s_load_dword %0, %1, 0x0\n\ts_load_dword %0, %1, 0x40\n\ts_load_dword %0, %1, 0x80" : "=&s"(d) : "s"(a) : "memory");
  return d;
}

struct MegaParams {
  char* ws;
  const u16* x;
  const u16* wqkvA;
  const u16* woA;
  const u16* wqkvB;
  const u16* woB;
  const u16* wphi;
  u16* out;
};

constexpr int QSLOT = 6 * 1024, QGRP = 4 * QSLOT;
constexpr int L_STG = QGRP;
constexpr int L_A2 = 16 * 1024, L_Z = 21 * 1024;
constexpr int L_RT = 4 * QGRP;
constexpr int L_PB = 0;
constexpr int L_RED = L_STG + 128 * 1024;
constexpr int L_STAMP = L_RED + 3072;
constexpr int MEGA_LDS = L_STAMP + 1536;
static_assert(MEGA_LDS <= 160 * 1024, "LDS map");

template <int I, int N, typename F> DI void static_for_(F&& f) {
  if constexpr (I < N) { f(IC<I>{}); static_for_<I + 1, N>(f); }
}
template <int N, typename F> DI void static_for(F&& f) { static_for_<0, N>(f); }

constexpr int NCHK = 3;
constexpr int RO = 8;
constexpr int SPIN_BUDGET = 1 << 15;

__global__ __launch_bounds__(768, 3) void mega_kernel(MegaParams p) {
  extern __shared__ __attribute__((aligned(16))) char smem[];
  const int tid = threadIdx.x;
  const int wave = __builtin_amdgcn_readfirstlane(tid >> 6);
  const int xcd = blockIdx.x & 7, idx = blockIdx.x >> 3;
  int bm = (xcd >> 2) * 4 + (idx >> 3), head = ((xcd & 1) * 2 + ((xcd >> 1) & 1)) * 8 + (idx & 7);
  int cm = (xcd >> 1) & 1, cn = (xcd & 1) * 8 + (idx & 7);
  const int bm0 = bm, head0 = head, cm0 = cm, cn0 = cn;
  char* ws = p.ws;
  const u16 *px = p.x, *pq = p.wqkvA;
  asm volatile("" : "+s"(ws), "+s"(px), "+s"(pq));
  const int swv = wave == SW0 ? 0 : wave == 4 ? 1 : wave == 11 ? 2 : -1; (void)swv;
  float* lmean = (float*)(smem + L_RED);
  float* lrstd = lmean + 272;
  float* lcs = lrstd + 272;
  unsigned* lflag = (unsigned*)lmean;
  const rsrc_t wsr = mk_rsrc(ws, WS_END);
  int budget = SPIN_BUDGET;
  unsigned tag0 = 0, tag1 = 0;
  auto ld_ep = [&]() AI { MEMFENCE; return __builtin_amdgcn_raw_buffer_load_b64(wsr, 0, WS_EP, 16); };
  auto mk_tag = [&](u32x2 ep) AI {
    tag0 = __builtin_amdgcn_readfirstlane(ep[0]) + 0x9e3779b9u;
    tag1 = __builtin_amdgcn_readfirstlane(ep[1]) ^ 0x85ebca6bu;
  };
  const unsigned sbase = (unsigned)(size_t)smem;
  int L = wave, cw = wave - 4;
#define OPAQUE_S asm volatile("" : "+s"(L), "+s"(bm), "+s"(head), "+s"(cm), "+s"(cn));
#define LANE_VARS asm volatile("" : "+s"(cw), "+s"(bm), "+s"(head), "+s"(cm), "+s"(cn)); int lane_ = __builtin_amdgcn_mbcnt_hi(~0u, __builtin_amdgcn_mbcnt_lo(~0u, 0u)); asm volatile("" : "+v"(lane_)); \
  const int lane = lane_, fr = lane & 15, fq = lane >> 4; const unsigned v16 = lane * 16; (void)fr; (void)fq; (void)v16;
  auto put_x = [&](u32x4* xr, unsigned v16, unsigned dst) AI {
    float sm = 0.f, sq = 0.f;
#pragma unroll
    for (int j = 0; j < 2; ++j) {
      xr[j][0] = umin(xr[j][0], POISON - 1); xr[j][2] = umin(xr[j][2], POISON - 1);
      st16s(wsr, v16, dst + j * 16384, xr[j]);
    }
    SCHED0;
#pragma unroll
    for (int j = 0; j < 2; ++j)
#pragma unroll
      for (int e = 0; e < 4; ++e) { sm = dot2(xr[j][e], 0x3f803f80u, sm); sq = dot2(xr[j][e], xr[j][e], sq); }
    xred2<false>(sm, sq);
    return u32x2{__float_as_uint(sm), __float_as_uint(sq)};
  };

#define RT_VARS const bool tq = L < 3; const int tsg = tq ? (bm * 2 + (head >> 4)) * 3 + L : bm * 2 + (head >> 4), ks = (head & 15) + 16 * h;
  auto ld_rt = [&](const u16* w, int h, unsigned lds) AI {
    RT_VARS
    LANE_VARS
    const unsigned rs = tq ? 6144 : 2048, D = 8 * rs - 1024, o = ks * 32 * rs + tsg * 128;
    blds_run<4>(mk_rsrc(w, rs * 1024), (lane >> 3) * rs + ((lane & 7) ^ ((lane >> 3) & 6)) * 16, sbase + lds, o, o + D, o + 2 * D, o + 3 * D);
  };
  auto retile = [&](int side, int h, unsigned lds, f32x4* rcs, int wp = -1) AI {
    RT_VARS
    LANE_VARS
    const unsigned dst = tq ? WS_WQ + side * (6 * MBY) + (unsigned)((((tsg & 15) * 2) * 32 + ks) * 6 + (tsg >> 4) * 2) * 1024
                            : WS_WO + side * (2 * MBY) + (unsigned)((tsg * 32 + ks) * 4) * 1024;
    const unsigned gstr = tq ? 32 * 6 * 1024 : 2048;
    const unsigned a0 = (lane >> 2) * 128 + ((lane >> 3) & 3) * 32 + (lane & 3) * 8;
    u32x4 fv[4];
    if (wp >= 0 && tsg < 32) {
      const f32x4 zero = f32x4{0.f, 0.f, 0.f, 0.f};
#pragma unroll
      for (int g = 0; g < 2; ++g) {
        const char* ra = smem + lds + fr * 128 + (((g * 4 + fq) ^ (fr & 6)) * 16);
        const bf16x8 w0 = *(const bf16x8*)ra, w1 = *(const bf16x8*)(ra + 2048);
#pragma unroll
        for (int u = 0; u < 2; ++u) {
          const char* pa = smem + wp + g * 2048 + (fq * 8 + (fr >> 2)) * 64 + u * 32 + (fr & 3) * 8;
          const u32x2 t0 = trread(pa), t1 = trread(pa + 256);
          const bf16x8 wf = as_frag(u32x4{t0[0], t0[1], t1[0], t1[1]});
          const f32x4 d0 = mfma16(w0, wf, zero), d1 = mfma16(w1, wf, zero);
          fv[g * 2 + u] = u32x4{pk2(d0[0], d0[1]), pk2(d0[2], d0[3]), pk2(d1[0], d1[1]), pk2(d1[2], d1[3])};
        }
      }
    } else {
#pragma unroll
      for (int pc = 0; pc < 4; ++pc) {
        const char* aa = smem + lds + (a0 ^ (pc * 32));
        const u32x2 r0_ = trread(aa), r1_ = trread(aa + 2048);
        fv[pc] = u32x4{r0_[0], r0_[1], r1_[0], r1_[1]};
      }
    }
#pragma unroll
    for (int pc = 0; pc < 4; ++pc) {
      const u32x4 v = fv[pc];
      st16s(wsr, v16 + (pc & 1) * 1024, dst + (pc >> 1) * gstr, v);
      if (rcs) rcs[pc] = mfma16(as_frag(u32x4{0x3f803f80u, 0x3f803f80u, 0x3f803f80u, 0x3f803f80u}), as_frag(v), rcs[pc]);
    }
    if (rcs && h == 1) {
      const f32x4 c0 = rcs[0], c1 = rcs[1], c2 = rcs[2], c3 = rcs[3];
      const float ca = fq & 1 ? c1[0] : c0[0], cb = fq & 1 ? c3[0] : c2[0], cc = fq & 2 ? cb : ca;
      if (tq) __builtin_amdgcn_raw_buffer_store_b32(__float_as_uint(cc), wsr, ((fq >> 1) * 1536 + (fq & 1) * 16 + fr) * 4,
                                                    WS_CS + ((side * 32 + (tsg & 15) * 2) * 1536 + (tsg >> 4) * 512 + (head & 15) * 32) * 4, 16);
    }
  };
  auto flag = [&](unsigned table) AI {
    if (LANEID == 0) __builtin_amdgcn_raw_buffer_store_b64(u32x2{tag0, tag1}, wsr, 0, table + (bm * 32 + head) * 8, 16);
  };
  auto poll = [&](unsigned table, auto between, int rows = 0) AI {
    LANE_VARS
    const int q = (umin(fq, 2) * 16 + (head >> 1)) * 43 >> 7;
    const unsigned vo = rows ? bm * 256 + (fr & rows) * 16 : fq == 3 ? bm * 256 + fr * 16 : q * 128 + (fr & 7) * 16;
    auto miss = [&](u32x4 f) AI { return __builtin_amdgcn_ballot_w64(((f[0] ^ tag0) | (f[1] ^ tag1) | (f[2] ^ tag0) | (f[3] ^ tag1)) != 0) != 0; };
    MEMFENCE;
    const u32x4 f0 = ld16s(wsr, vo, table);
    MEMFENCE;
    between();
    MEMFENCE;
    bool wait = miss(f0);
#pragma unroll 1
    while (wait && --budget > 0) {
      SLEEP(SLP_Q);
      MEMFENCE;
      wait = miss(ld16s(wsr, vo, table));
      asm volatile("" : "+s"(budget) :: "memory");
    }
    MEMFENCE;
  };
  auto ld_x = [&](int cx, unsigned lds) AI {
    LANE_VARS
    const unsigned o = (bm * 16 + (head >> 1)) * 32768 + cx * 128;
    blds_run<2>(mk_rsrc(px, 4 * MBY), (lane >> 3) * 2048 + ((lane & 7) ^ (lane >> 3)) * 16, sbase + lds, o, o + 15360);
  };
  auto st_x = [&](int cx, unsigned lds, int w) AI {
    LANE_VARS
    const int mt = bm * 16 + (head >> 1);
    u32x4 xr[2];
#pragma unroll
    for (int j = 0; j < 2; ++j) {
      const char* a = smem + lds + fr * 128 + (fq & 1) * 8;
      const u32x2 lo = *(const u32x2*)(a + ((j * 4 + (fq >> 1)) ^ (fr & 7)) * 16), hi = *(const u32x2*)(a + ((j * 4 + 2 + (fq >> 1)) ^ (fr & 7)) * 16);
      xr[j] = u32x4{lo[0], lo[1], hi[0], hi[1]};
    }
    const u32x2 stat = put_x(xr, v16, WS_AR + AR_X + xt_off(mt, cx * 2));
    if (fq == 0) *(u32x2*)(lmean + 16 + fr * 4 + w) = stat;
  };

  {
    const int lane = LANEID;
    const bool tq = wave < 4;
    const unsigned rs = tq ? 6144 : 2048, D = 8 * rs - 1024;
    const unsigned o = (tq ? (head & 15) * 32 : (bm * 16 + (head >> 1)) * 16) * rs + (tq ? (bm * 2 + (head >> 4)) * 3 + wave : (head & 1) * 4 + cw) * 128;
    const unsigned lds = sbase + L_PB + (tq ? 8192 + wave * 4096 : cw * 2048);
    const unsigned vo = (lane >> 3) * rs + ((lane & 7) ^ ((lane >> 3) & (tq ? 6 : 7))) * 16;
    const rsrc_t r = mk_rsrc(tq ? pq : px, tq ? 6 * MBY : 4 * MBY);
    if ((wave & 7) != 3 && wave < 8) blds_run<2>(r, vo, lds, o, o + D);
    if (wave < 3) {
      blds_run<2>(r, vo, lds + 2048, o + 2 * D + 2048, o + 3 * D + 2048);
      const int tsg = (bm * 2 + (head >> 4)) * 3 + wave;
      if (tsg < 32) glds_run<4>(lane * 16, (const char*)p.wphi + (tsg & 15) * 4096, sbase + L_PB + 45056 + wave * 4096);
    }
  }
  if (wave >= 8) {
    const unsigned cp = code_prefetch(idx * 9 + cw), cq = code_prefetch(idx * 9 + cw - 4);
    L = wave - 8;
    bar();
    ld_x(8 + (head & 1) * 4 + L, L_PB + 32768 + L * 2048);
    if (L < 3) ld_rt(pq, 1, L_PB + 20480 + L * 4096);
    sync_lds();
    asm volatile("; pi" :: "s"(cp), "s"(cq) : "memory");
    wait_vm<0>();
    PSTAMP(1);
    st_x(8 + (head & 1) * 4 + L, L_PB + 32768 + L * 2048, L * 64 + 2);
    if (L < 3) {
      const f32x4 t = *(const f32x4*)(smem + L_PB + 40960 + L * 1024 + LANEID * 16);
      f32x4 rca[4] = {{t[0], 0.f, 0.f, 0.f}, {t[1], 0.f, 0.f, 0.f}, {t[2], 0.f, 0.f, 0.f}, {t[3], 0.f, 0.f, 0.f}};
      retile(0, 1, L_PB + 20480 + L * 4096, rca, L_PB + 45056 + L * 4096);
    }
    asm volatile("s_waitcnt lgkmcnt(0)" ::: "memory");
    PSTAMP(2);
  } else if (wave >= 4) {
    wait_vm<0>();
    PSTAMP(1);
    st_x((head & 1) * 4 + cw, L_PB + cw * 2048, cw * 64);
    bar();
    wait_vm_lgkm0<0>();
    PSTAMP(2);
    bar();
    asm volatile("; pc" ::: "memory");
    SLEEP(SLP_E);
    L = 3;
    if (cw < 2) ld_rt(p.woA, cw, L_RT + 3 * 14336 + cw * 4096);
  } else if (wave < 3) {
    const u32x2 ep = ld_ep();
    wait_vm<1>();
    PSTAMP(1);
    f32x4 rca[4] = {};
    retile(0, 0, L_PB + 8192 + L * 4096, rca, L_PB + 45056 + L * 4096);
    *(f32x4*)(smem + L_PB + 40960 + L * 1024 + LANEID * 16) = f32x4{rca[0][0], rca[1][0], rca[2][0], rca[3][0]};
    bar();
    drain();
    mk_tag(ep);
    PSTAMP(2);
    sync_lds();
    asm volatile("; pl" ::: "memory");
    SLEEP(SLP_E);
    ld_rt(p.wqkvB, 0, L_RT + L * 14336);
  } else {
    const u32x2 ep = ld_ep();
    const unsigned cp = code_prefetch(idx * 9 + 8);
    if (LANEID < 9) lflag[LANEID] = 0;
    wait_vm_lgkm0<0>();
    asm volatile("" :: "s"(cp));
    mk_tag(ep);
    bar();
    bar();
    flag(WS_F1);
    if (LANEID == 0) *(u32x2*)(lflag + 10) = u32x2{tag0, tag1};
    PSTAMP(2);
    SLEEP(SLP_P);
    poll(WS_F1, []() {});
    asm volatile("; pp" ::: "memory");
  }
  bar();
  PSTAMP(3);
  int role = wave;
  asm volatile("" : "+s"(role));

  if (__builtin_expect(role < 4, 1)) {
    const unsigned v16 = __builtin_amdgcn_mbcnt_hi(~0u, __builtin_amdgcn_mbcnt_lo(~0u, 0u)) * 16;
#define OPAQUE_LANE int lane = v16 >> 4; asm volatile("" : "+v"(lane));
    auto q_groupW = [&](int layer, int g, int pos) AI {
      const char* gW = ws + WS_WQ + (layer & 1) * (6 * MBY) + head * (192 * 1024) + (g * 4 + L) * 6144;
      const unsigned d = sbase + (pos * 4 + L) * QSLOT;
      glds_run<3, true>(v16, gW, d);
      glds_run<3, true>(v16, gW + 3072, d + 3072);
    };
    u32x4 wn[4][6];
    auto ld_wn = [&](int layer, int g0, int g1) AI {
      const unsigned so = WS_WQ + (layer & 1) * (6 * MBY) + head * (192 * 1024) + L * 6144;
#pragma unroll
      for (int g = g0; g < g1; ++g)
#pragma unroll
        for (int n = 0; n < 6; ++n) wn[g][n] = ld16s(wsr, v16 + (n % 3) * 1024, so + g * 24576 + (n / 3) * 3072);
      MEMFENCE;
    };
    auto wr_wn = [&](int g) AI {
#pragma unroll
      for (int n = 0; n < 6; ++n) *(u32x4*)(smem + (g * 4 + L) * QSLOT + n * 1024 + v16) = wn[g][n];
    };
    const u32x4 pz = u32x4{POISON, POISON, POISON, POISON};
    auto poison = [&](unsigned so) AI { st16s(wsr, v16, so, pz); };
    auto poison_o = [&]() AI {
      const unsigned o = WS_AR + AR_O + xt_off(bm * 16 + L * 4, head);
#pragma unroll
      for (int i = 0; i < 4; ++i) poison(o + i * 1024);
      STPAD;
    };
    auto poison_x = [&](int l) AI {
      if (L < NCHK) {
        const unsigned o = WS_AR + l * ARENA + AR_X + bm * (512 * 1024) + head * (NCHK * 2048) + L * 2048;
        poison(o); poison(o + 1024);
        STPAD;
      }
    };
    auto poison_s = [&](int l) AI {
      if (L == 3) {
        const unsigned o = WS_AR + l * ARENA + AR_KV + (unsigned)(bm * 32 + head) * KVREC;
        poison(o); poison(o + 1024);
        STPAD;
      }
    };
    auto poison_kv = [&](int l) AI {
      const unsigned kvo = WS_AR + l * ARENA + AR_KV + (unsigned)(bm * 32 + head) * KVREC;
      poison(kvo + L * 1024);
      if (L == (l ? 0 : 1)) st16s(wsr, v16 < 256 ? v16 : OOB, kvo + 4096, pz);
      STPAD;
    };
    q_groupW(0, 0, 0);
    q_groupW(0, 1, 1);
    auto put_l2 = [&](float sm, float sq, unsigned vo, unsigned so) AI {
      xred2<false>(sm, sq);
      if (v16 < 256) st16s(wsr, vo, so, u32x4{__float_as_uint(sm), tag0, __float_as_uint(sq), tag1});
      MEMFENCE;
    };
    auto put_st0 = [&]() AI {
      if (L == 0) {
        const f32x4 t = *(const f32x4*)((const char*)(lmean + 16) + v16);
        put_l2(t[0] + t[2], t[1] + t[3], v16, WS_AR + AR_L2 + (head & 1) * 32768 + (bm * 16 + (head >> 1)) * 256);
      }
    };
    wait_vm<6>();
    auto body = [&](auto kind, const int layer) AI {
      constexpr bool isB = decltype(kind)::value == 1, is0 = decltype(kind)::value == 0;
      const unsigned ar = WS_AR + layer * ARENA;
      f32x4 rcs[4] = {};
      auto sW = [&]() AI { return WS_WO + (layer & 1) * (2 * MBY) + cn * (128 * 1024); };
      auto stg = [&](unsigned off, auto n) AI { glds_run<decltype(n)::value, true>(v16, ws + sW() + off, sbase + L_STG + off); };
      auto stg7 = [&](unsigned o) AI { stg(o, IC<4>{}); stg(o + 4096, IC<3>{}); };
      auto stg6 = [&](unsigned o) AI { stg(o, IC<3>{}); stg(o + 3072, IC<3>{}); };

      auto extra = [&](int b) AI {
        if (!is0) return;
        const unsigned lu = L_RT + L * 14336;
        if (b < 7) { SCHED0; PRIO(3); SCHED0; }
        if (b == 1) { retile(L < 3, 0, lu, rcs); if (L == 3) retile(0, 1, lu + 4096, nullptr); }
        if (b == 3) {
          ld_rt(L < 3 ? p.wqkvB : p.woB, 1, lu + 4096);
          if (L == 3) ld_rt(p.woB, 0, lu + 8192);
        }
        if (b == 6 && L < 3) retile(1, 1, lu + 4096, rcs);
        if (b == 8 && L == 3) { retile(1, 0, lu + 8192, nullptr); retile(1, 1, lu + 4096, nullptr); }
        if (b < 7) { SCHED0; PRIO(0); SCHED0; }
      };
      auto extra2 = [&](int b) AI {
        if (!is0) return;
        if (b < 7) { SCHED0; PRIO(3); SCHED0; }
        if (b == 1) { put_st0(); poison_o(); poison_kv(0); poison_kv(2); }
        if (b == 8) { poison_s(1); poison_x(1); }
        if (b == 10) { poison_x(2); poison_s(3); }
        if (b < 7) { SCHED0; PRIO(0); SCHED0; }
      };
      if (is0) sync_lds();
      STAMP(0);
      {
        OPAQUE_S
        OPAQUE_LANE
        u32x4 c0, c1;
        asm volatile("" : "=v"(c0), "=v"(c1));
        auto ldcs = [&]() AI {
          if (L > 0) {
            const unsigned so = WS_CS + (((layer & 1) * 32 + head) * 1536 + (L - 1) * 512) * 4;
            c0 = ld16s(wsr, v16, so); c1 = ld16s(wsr, v16 + 1024, so);
          }
          MEMFENCE;
        };
        auto wrcs = [&]() AI {
          if (L > 0) {
            f32x4 t = add4(__builtin_bit_cast(f32x4, c0), __builtin_bit_cast(f32x4, c1));
#pragma unroll
            for (int e = 0; e < 4; ++e)
              t[e] += __uint_as_float(__builtin_amdgcn_update_dpp(0u, __float_as_uint(t[e]), 0x128, 0xf, 0xf, false));
            float t0 = t[0], t1 = t[1], t2 = t[2], t3 = t[3];
            xred4<false>(t0, t1, t2, t3);
            *(f32x4*)(lcs + (isB ? 96 : 0) + (L - 1) * 32 + (lane & 7) * 4) = f32x4{t0, t1, t2, t3};
          }
        };
        if (isB && layer == 1) ldcs();
        if constexpr (is0) {
          if (L == 3) flag(WS_F1 + 2048);
          q_groupW(0, 2, 2); q_groupW(0, 3, 3);
          wait_vm<12>();
        }
        STAMP(1);
        if (!is0) PRIO(3);
        sync_lds();
        STAMP(2);
        OPAQUE_S
        u32x4 s0 = u32x4{0u, 0u, 0u, 0u}, s1 = s0;
        auto ldst = [&]() AI {
          const unsigned so = ar + AR_L2 + (bm * 16 + L * 4) * 256;
          s0 = ld16s(wsr, v16, so);
          if (is0) s1 = ld16s(wsr, v16, so + 32768);
          if (isB && L == 0) s1 = ld16s(wsr, v16 & 255, bm > 0 ? so - 256 : so);
          MEMFENCE;
        };
        auto fin = [&](float sm, float sq, int r, bool own) AI {
          float mean = sm * (1.0f / 1024.0f);
          float var = fmaxf(__builtin_fmaf(sq, 1.0f / 1024.0f, -mul1(mean, mean)), 0.f);
          if (own) lmean[r] = -mean;
          lrstd[r] = __builtin_amdgcn_rsqf(var + 1e-5f);
        };
        auto wrst = [&]() AI {
          while (true) {
            unsigned d = (s0[1] ^ tag0) | (s0[3] ^ tag1);
            if (is0 || (isB && L == 0)) d |= (s1[1] ^ tag0) | (s1[3] ^ tag1);
            if (__builtin_expect(__builtin_amdgcn_ballot_w64(d != 0) == 0, 1) || --budget <= 0) break;
            COUNT(1);
            ldst();
          }
          MEMFENCE;
          float sm = __uint_as_float(s0[0]), sq = __uint_as_float(s0[2]);
          if (is0) { sm += __uint_as_float(s1[0]); sq += __uint_as_float(s1[2]); }
          fin(sm, sq, 16 + L * 64 + lane, true);
          if (isB && L == 0) fin(__uint_as_float(s1[0]), __uint_as_float(s1[2]), lane & 15, false);
        };
        auto q_group = [&](int g) AI {
          if (is0) {
            if (L & 1) SLEEP(SPR0);
            if (L & 2) SLEEP(2 * SPR0);
            q_groupW(layer, g, g & 3);
          } else if (g >= 5) q_groupW(layer, g, g < 6 ? g : g - 4);
        };
        if (!is0 && L == 0) {
          poll(ar + AR_FL, []() {}, 15);
          if (head < 16) {
            const unsigned so = ar + AR_ST + (bm * 16 + head) * 2048;
            s0 = ld16s(wsr, v16, so); s1 = ld16s(wsr, v16 + 1024, so);
            MEMFENCE;
          }
        }
        if (is0) {
          drain();
          poll(WS_F1 + 2048, [&]() AI { extra(1); });
          ldcs();
          extra2(1);
        }
        if (isB && layer == 1) wrcs();
        MEMFENCE;
        STAMP(3);
        if (!is0) PRIO(0);
        sync_lds();
        STAMP(4);
        OPAQUE_S
        if (!is0) drain();
        if (!is0 && L == 0 && head < 16) {
          f32x4 t = add4(__builtin_bit_cast(f32x4, s0), __builtin_bit_cast(f32x4, s1));
#pragma unroll
          for (int e = 0; e < 4; ++e)
            t[e] += __uint_as_float(__builtin_amdgcn_update_dpp(0u, __float_as_uint(t[e]), 0x128, 0xf, 0xf, false));
          const bool hi = lane & 8;
          put_l2(hi ? t[2] : t[0], hi ? t[3] : t[1], (lane & 7) * 32 + (lane & 8) * 2, ar + AR_L2 + (bm * 16 + head) * 256);
        }
        q_group(4);
        STAMP(5);
        sync_lds();
        STAMP(6);
        OPAQUE_S
        drain();
        q_group(5);
        extra(3);
        if (is0) wrcs();
        STAMP(7);
        bar();
        OPAQUE_S
        drain();
        if (is0 && L == 0 && v16 == 0) __builtin_amdgcn_raw_buffer_store_b64(u32x2{tag0, tag1}, wsr, 0, WS_F2 + (bm * 32 + head) * 8, 16);
        q_group(6);
        ldst();
        STAMP(8);
        bar();
        STAMP(9);
        OPAQUE_S
        drain();
        q_group(7);
        SCHED0;
        wrst();
        STAMP(10);
        bar();
        STAMP(11);
        OPAQUE_S
        drain();
        extra(6);
        if (isB) stg6(96 * 1024 + L * 6144);
        SCHED0;
      }
      STAMP(12);
      sync_lds();
      STAMP(13);
      OPAQUE_S
      u32x4 fl[2];
      fl[0] = fl[1] = u32x4{tag0, tag1, tag0, tag1};
      auto ldfl = [&]() AI { fl[0] = ld16s(wsr, v16, WS_F2); fl[1] = ld16s(wsr, v16 + 1024, WS_F2); };
      if (is0) ldfl();
      bar();
      constexpr int NR = isB ? 6 : 9;
      const unsigned ro = isB ? 48 * 1024 + L * 6144 : L * 9216;
      u32x4 r[NR];
      bool pend = false;
      auto flags = [&]() AI {
        const u32x4 d = (fl[0] ^ u32x4{tag0, tag1, tag0, tag1}) | (fl[1] ^ u32x4{tag0, tag1, tag0, tag1});
        pend = __builtin_amdgcn_ballot_w64((d[0] | d[1] | d[2] | d[3]) != 0) != 0;
      };
      if (isB) {
        stg6(L * 6144); stg(120 * 1024 + L * 2048, IC<2>{});
        stg6(24 * 1024 + L * 6144);
        stg6(72 * 1024 + L * 6144);
      } else {
        drain();
        if (is0) flags();
        MEMFENCE;
        extra(8); extra2(8);
        if (!is0) poison_x(3);
        while (is0 && __builtin_expect(pend, 0) && --budget > 0) {
          ldfl();
          flags();
          MEMFENCE;
        }
        stg(36 * 1024 + L * 3072, IC<3>{}); stg7(72 * 1024 + L * 14336); stg7(72 * 1024 + L * 14336 + 7168);
      }
#pragma unroll
      for (int i = 0; i < NR; ++i) r[i] = ld16s(wsr, v16 + (i & 3) * 1024, sW() + ro + (i >> 2) * 4096);
      MEMFENCE;
      if (is0 && bm + head + L == 0 && v16 == 0) __builtin_amdgcn_raw_buffer_store_b32(tag0, wsr, 0, WS_EP, 16);
      auto wr_r = [&]() AI {
        unsigned vv = v16;
        asm volatile("" : "+v"(vv));
#pragma unroll
        for (int i = 0; i < NR; ++i) *(u32x4*)(smem + L_STG + ro + i * 1024 + vv) = r[i];
      };
      auto left = [&]() AI {
        OPAQUE_LANE
        for (;;) {
          MEMFENCE;
          if (__builtin_amdgcn_ballot_w64(lflag[lane & 7] != (unsigned)layer) == 0 || --budget <= 0) break;
          SLEEP(2);
        }
        MEMFENCE;
      };
      if (isB) {
#pragma unroll
        for (int i = 0; i < 24; ++i) asm volatile("" : "=v"(wn[i / 6][i % 6]));
        if (layer == 1) ld_wn(layer + 1, 0, 2);
        STAMP(14);
        left();
        wr_r();
        MEMFENCE;
        if (layer == 1) ld_wn(layer + 1, 2, 4);
        wait_vm_lgkm0<12>();
        if (layer == 3) wait_vm<0>();
      } else {
        ld_wn(layer + 1, 0, is0 ? 1 : 2);
        if (is0) wait_vm<32>();
        STAMP(14);
        bar();
        stg6(48 * 1024 + L * 6144);
        if (is0) ld_wn(layer + 1, 1, 2);
        bar();
        ld_wn(layer + 1, 2, 4);
        wr_r();
        wait_vm<12>();
        bar();
        extra2(10);
        wait_vm_lgkm0<0>();
      }
      bar();
      if (isB && layer == 3) { STAMP_DUMP; __builtin_amdgcn_endpgm(); }
      OPAQUE_S
      wr_wn(0);
      drain();
      sync_lds();
      OPAQUE_S
      SCHED0;
#pragma unroll
      for (int g = 1; g < 4; ++g) wr_wn(g);
      MEMFENCE;
      SLEEP(SLP_G);
      q_groupW(layer + 1, 4, 4);
    };
    body(IC<0>{}, 0);
#pragma unroll 1
    for (int layer = 1;; layer += 2) {
      body(IC<1>{}, layer);
      body(IC<2>{}, 2);
    }
  }

  const f32x4 zero = f32x4{0.f, 0.f, 0.f, 0.f};
  u32x4 xr[2];
  asm volatile("" : "=v"(xr[0]), "=v"(xr[1]));
  u32x4 sel;
  u32x4 eye;
  unsigned vk;
  unsigned ra0, ra1;
  float off0, off1;
  {
    const int lane = LANEID, fr = lane & 15, fq = lane >> 4;
    const bool odd = fr & 1, own0 = fr >= fq * 4 + (fr & 1), own1 = fr >= fq * 4 + (fr & 1) + 2, nopre = bm0 == 0 && wave == 4;
    const unsigned e0 = odd ? 0x01000c0cu : 0x0c0c0100u, e1 = odd ? 0x03020c0cu : 0x0c0c0302u, ez = 0x0c0c0c0cu;
    sel = u32x4{own0 ? ez : e0, own1 ? ez : e1, own0 ? e0 : ez, own1 ? e1 : ez};
    const unsigned rb = sbase + L_RED + 1088 + ((wave - 4) * 32 + fq * 4 + (fr & 1)) * 4;
    ra0 = rb + (own0 ? 64 : 0); ra1 = rb + (own1 ? 72 : 8);
    off0 = nopre && !own0 ? -3.0e38f : 0.f; off1 = nopre && !own1 ? -3.0e38f : 0.f;
    const unsigned one = (fq & 1) == (fr >> 3) ? (fr & 1 ? 0x3f800000u : 0x00003f80u) : 0u;
    eye = u32x4{(fr & 6) == 0 ? one : 0u, (fr & 6) == 2 ? one : 0u, (fr & 6) == 4 ? one : 0u, (fr & 6) == 6 ? one : 0u};
    vk = (fq >> 1) * (32 * KVREC) + (fq & 1) * 256 + fr * 16;
    asm volatile("" : "+v"(sel), "+v"(ra0), "+v"(ra1), "+v"(off0), "+v"(off1), "+v"(eye), "+v"(vk));
  }
#pragma unroll 1
  for (int layer = 0; layer < 4; ++layer) {
    bm = bm0; head = head0; cm = cm0; cn = cn0; cw = wave - 4;
    const bool isB = layer & 1;
    const unsigned ar = WS_AR + layer * ARENA;
    {
      f32x4 acc[2][6];
      f32x4 ck[2], mv[2], xv[2];
      float mean[2], rs[2], cv[2];
      const float* lcl = lcs + (isB ? 96 : 0);
      {
        LANE_VARS
        const unsigned va = v16 + cw * 2048;
        const unsigned sa0 = ar + AR_X + bm * (512 * 1024);
        auto lda = [&](unsigned so, u32x4* a) AI {
          a[0] = ld16s(wsr, va, so);
          a[1] = ld16s(wsr, va + 1024, so);
        };
        bf16x8 f[2][6];
        u32x4 a[4][2];
        auto unset = [&](const u32x4* e) AI { return poisoned(umax(umax(umax(e[0][0], e[0][2]), e[1][0]), e[1][2])); };
        auto refill = [&](auto ic, unsigned cur) AI {
          constexpr int I = decltype(ic)::value;
          do {
            COUNT(0);
            SLEEP(SLP_R);
            MEMFENCE;
            lda(cur, a[I]);
            bool u[4];
#pragma unroll
            for (int j = 1; j < 4; ++j) u[j] = unset(a[(I + j) % 4]);
            SCHED0;
#pragma unroll
            for (int j = 1; j < 4; ++j)
              if (u[j]) lda(cur + j * 16384, a[(I + j) % 4]);
            __builtin_amdgcn_s_waitcnt(0x0f70);
            MEMFENCE;
          } while (unset(a[I]) && --budget > 0);
        };
        STAMP(0);
        if (layer > 0) {
          bar();
          if (cn < 8) SLEEP(SLP_X); else SLEEP(SLP_XD);
        }
#pragma unroll
        for (int j = 0; j < 4; ++j) { lda(sa0 + j * 16384, a[j]); SCHED0; }
#pragma unroll
        for (int m = 0; m < 2; ++m)
#pragma unroll
          for (int n = 0; n < 6; ++n) acc[m][n] = zero;
        asm volatile("" : "+v"(acc[0][0]), "+v"(acc[0][1]), "+v"(acc[0][2]), "+v"(acc[0][3]), "+v"(acc[0][4]), "+v"(acc[0][5]));
        asm volatile("" : "+v"(acc[1][0]), "+v"(acc[1][1]), "+v"(acc[1][2]), "+v"(acc[1][3]), "+v"(acc[1][4]), "+v"(acc[1][5]));
        if (layer == 0) { wait_vm<8>(); sync_lds(); }
#pragma unroll
        for (int nn = 0; nn < 6; ++nn) { const int n = nn < 4 ? nn + 2 : nn - 4; f[0][n] = *(const bf16x8*)(smem + v16 + n * 1024); }
        SCHED0;
        if (layer > 0) {
          const u32x2 tg = *(const u32x2*)(lflag + 10);
          wait_vm<8>();
          unsigned old;
          asm volatile("s_mov_b64 exec, 1\n\tds_add_rtn_u32 %0, %1, %2\n\ts_mov_b64 exec, -1\n\ts_waitcnt lgkmcnt(0)" : "=v"(old) : "v"(sbase + L_RED + 32), "v"(1) : "memory");
          if (__builtin_amdgcn_readfirstlane(old) == 8u * layer - 1)
            __builtin_amdgcn_raw_buffer_store_b64(tg, wsr, lane ? OOB : 0, ar + AR_FL + bm * 256 + (cm * 16 + cn) * 8, 16);
          MEMFENCE;
        }
        SCHED0;
        bool bad = layer > 0 && unset(a[0]);
        const int wrap = layer > 0 ? 6 : 4;
        unsigned ps[4] = {sbase + L_RED + 64 + cw * 128, sbase + L_RED + 2176 + (layer & 1) * 384, 0, WS_AR + AR_X + xt_off(bm * 16 + cw * 2 + cm, cn * 2)};
        ps[2] = ps[0] + 1088 + (layer & 1) * (2176 + 384 - 1088 - 64 - cw * 128);
        int early = (layer & 1) * cw;
        asm volatile("" : "+s"(ps[0]), "+s"(ps[1]), "+s"(ps[2]), "+s"(ps[3]), "+s"(early));
        STAMP(1);
        auto group = [&](auto modec, int g) AI {
          constexpr int MODE = decltype(modec)::value;
          constexpr bool LAST = MODE == 3, CHK = MODE == 0;
          const unsigned gs = (g < wrap ? g : g - 4) * QGRP, gn = (g + 1 < wrap ? g + 1 : g - 3) * QGRP;
          const unsigned sc = sa0 + g * 65536, so = sc + 4 * 16384;
          unsigned gl = sbase + gs + v16;
          if constexpr (LAST) asm volatile("" : "+v"(gl));
          unsigned pa[4];
          static_for<4>([&](auto ic) AI {
            constexpr int i = decltype(ic)::value;
            constexpr bool END = LAST && i == 3;
            SCHED0;
            if (i == 3 && !LAST) { sync_lds(); SCHED0; }
            if (i == 0 && LAST) {
              PRIO(1);
              if (early >= 4) PRIO(3);
              SCHED0;
              bar();
              SCHED0;
            }
            if constexpr (!LAST) PRIO(i == 3 ? 3 : 2 - i);
            if constexpr (CHK) { if (__builtin_expect(bad, 0)) refill(ic, sc + i * 16384); }
            SCHED0;
            const bf16x8* fc = f[i & 1];
            bf16x8* fn = f[(i + 1) & 1];
            const u32x4* an = a[(i + 1) % 4];
            const unsigned slot = i < 3 ? gs + (i + 1) * QSLOT : gn;
            const char* pb = LAST ? (const char*)((const LDS(char)*)gl + (i + 1) * QSLOT) : smem + slot + v16;
            unsigned mx = 0;
            static_for<12>([&](auto jc) AI {
              constexpr int j = decltype(jc)::value, nn = j >> 1, m = j & 1, n = nn < 4 ? nn + 2 : nn - 4;
              if constexpr (n < 4) mfma_acc(acc[m][n], fc[n], as_frag(a[i][m]));
              else mfma_acc(acc[m][n], as_frag(a[i][m]), fc[n]);
              SCHED0;
              if constexpr (!END && j < 6) { constexpr int r = j < 4 ? j + 2 : j - 4; fn[r] = *(const bf16x8*)(pb + r * 1024); }
              if constexpr (LAST && i == 0 && j == 6) {
                if (layer == 0) { xr[0] = ld16s(wsr, v16, ps[3]); xr[1] = ld16s(wsr, v16, ps[3] + 16384); }
              }
              if constexpr (LAST && i == 1) {
                if constexpr (j == 6) { pa[0] = lane & 48; asm volatile("" : "+v"(pa[0])); }
                if constexpr (j == 7) { pa[1] = v16 >> 2; asm volatile("" : "+v"(pa[1])); }
                if constexpr (j == 8) { pa[1] &= 60; asm volatile("" : "+v"(pa[1])); }
                if constexpr (j == 9) { pa[2] = pa[1] + ps[0]; asm volatile("" : "+v"(pa[2])); }
                if constexpr (j == 10) { pa[1] += ps[1]; asm volatile("" : "+v"(pa[1])); }
                if constexpr (j == 11) { pa[3] = pa[0] + ps[1]; asm volatile("" : "+v"(pa[3])); }
              }
              if constexpr (LAST && i == 2) {
                if constexpr (j == 6) ck[0] = ((const LDS(f32x4)*)pa[3])[8];
                if constexpr (j == 7) ck[1] = ((const LDS(f32x4)*)pa[3])[12];
                if constexpr (j == 8) { mean[0] = ((const LDS(float)*)pa[2])[0]; asm volatile("" : "+v"(pa[2])); }
                if constexpr (j == 9) { mean[1] = ((const LDS(float)*)pa[2])[16]; asm volatile("" : "+v"(pa[2])); }
                if constexpr (j == 10) { cv[0] = ((const LDS(float)*)pa[1])[64]; cv[1] = ((const LDS(float)*)pa[1])[80]; }
                if constexpr (j == 11) { pa[1] = pa[0] + ps[0]; asm volatile("" : "+v"(pa[1])); }
              }
              if constexpr (LAST && i == 3) {
                if constexpr (j == 0) mv[0] = ((const LDS(f32x4)*)pa[1])[0];
                if constexpr (j == 1) mv[1] = ((const LDS(f32x4)*)pa[1])[4];
                if constexpr (j == 2) { pa[0] += ps[2]; asm volatile("" : "+v"(pa[0])); }
                if constexpr (j == 3) { rs[0] = ((const LDS(float)*)pa[2])[272]; asm volatile("" : "+v"(pa[2])); }
                if constexpr (j == 4) xv[0] = ((const LDS(f32x4)*)pa[0])[0];
                if constexpr (j == 5) xv[1] = ((const LDS(f32x4)*)pa[0])[4];
                if constexpr (j == 6) rs[1] = ((const LDS(float)*)pa[2])[288];
              }
              if constexpr (CHK) {
                if constexpr (j == 8) mx = umax(umax(umax(an[0][0], an[0][2]), an[1][0]), an[1][2]);
                if constexpr (j == 10) bad = poisoned(mx);
              }
              if constexpr (j == 10 && !LAST) a[i][0] = ld16s(wsr, va, so + i * 16384);
              if constexpr (j == 11 && !LAST) a[i][1] = ld16s(wsr, va + 1024, so + i * 16384);
              SCHED0;
            });
          });
        };
        int g = 0;
        if (layer > 0) {
#pragma unroll 1
          for (; g < NCHK; ++g) group(IC<0>{}, g);
        }
#pragma unroll 1
        for (; g < 7; ++g) group(IC<1>{}, g);
        group(IC<3>{}, 7);
        if (!isB && cw >= 4) PRIO(2);
        mfma_done();
      }
      STAMP(2);
      LANE_VARS
      char* const scr = smem;

      u32x4 qf[2], kf[2];
      u32x2 vf[2][2];
      auto fold_rows = [&](int part, u32x4* out, const f32x4* c) AI {
#pragma unroll
        for (int m = 0; m < 2; ++m) {
          f32x4 a0 = acc[m][part * 2] + c[0] * mean[m], a1 = acc[m][part * 2 + 1] + c[1] * mean[m];
          out[m] = u32x4{pk2(a0[0], a0[1]), pk2(a0[2], a0[3]), pk2(a1[0], a1[1]), pk2(a1[2], a1[3])};
        }
      };
      auto fold_x = [&](int part, f32x2 (*x)[4], const f32x4* c) AI {
#pragma unroll
        for (int m = 0; m < 2; ++m) {
          const f32x4 a0 = acc[m][part * 2] + c[0] * mean[m], a1 = acc[m][part * 2 + 1] + c[1] * mean[m];
          x[m][0] = f32x2{a0[0], a0[1]}; x[m][1] = f32x2{a0[2], a0[3]}; x[m][2] = f32x2{a1[0], a1[1]}; x[m][3] = f32x2{a1[2], a1[3]};
        }
      };
      auto fold_v = [&](bool scaled) AI {
#pragma unroll
        for (int m = 0; m < 2; ++m) {
          f32x4 a0 = acc[m][4] + mv[m] * cv[0], a1 = acc[m][5] + mv[m] * cv[1];
          if (scaled) { a0 = mul4(a0, xv[m]); a1 = mul4(a1, xv[m]); }
          vf[m][0] = u32x2{pk2(a0[0], a0[1]), pk2(a0[2], a0[3])};
          vf[m][1] = u32x2{pk2(a1[0], a1[1]), pk2(a1[2], a1[3])};
        }
      };
      const unsigned otile = WS_AR + AR_O + xt_off(bm * 16 + cw * 2, head);
      auto put_o = [&](int m, f32x4 a, f32x4 b, unsigned opar) AI {
        const u32x4 v = u32x4{(pk2(a[0], a[1]) & ~1u) | opar, pk2(a[2], a[3]), (pk2(b[0], b[1]) & ~1u) | opar, pk2(b[2], b[3])};
        st16s(wsr, v16 + m * 1024, otile, v);
        return v;
      };
      auto hold = [&](u32x4 a, u32x4 b) AI { SCHED0; asm volatile("s_nop 1" :: "v"(a), "v"(b)); SCHED0; };

      if (!isB) {
        f32x2 lx[2][4];
        fold_x(1, lx, ck); fold_v(true);
        char* PK = smem + L_STG;
        char* VF = scr;
#pragma unroll
        for (int t = 0; t < 2; ++t)
          *(u32x4*)(VF + (cw * 2 + t) * 1024 + lane * 16) = u32x4{vf[0][t][0], vf[0][t][1], vf[1][t][0], vf[1][t][1]};
        {
          u32x4 pk[2][2];
          phi_map2(lx, rs, pk, [](float*) {});
          char* d0 = PK + cw * 4096 + fr * 64 + ((fr >> 2) & 1) * 32 + ((fq ^ ((fr >> 3) * 2)) * 8);
          char* d1 = PK + cw * 4096 + fr * 64 + (((fr >> 2) & 1) ^ 1) * 32 + ((fq ^ ((fr >> 3) * 2)) * 8);
#pragma unroll
          for (int m = 0; m < 2; ++m) {
            *(u32x2*)(d0 + m * 2048) = u32x2{pk[m][0][0], pk[m][0][1]};
            *(u32x2*)(d0 + m * 2048 + 1024) = u32x2{pk[m][0][2], pk[m][0][3]};
            *(u32x2*)(d1 + m * 2048) = u32x2{pk[m][1][0], pk[m][1][1]};
            *(u32x2*)(d1 + m * 2048 + 1024) = u32x2{pk[m][1][2], pk[m][1][3]};
          }
        }
        PRIO(1);
        const unsigned rec = ar + AR_KV + (unsigned)head * KVREC, dst = rec + (unsigned)bm * (32 * KVREC);
        unsigned ad = sbase + L_STG + (cw & 1) * 1024 + (fq * 4 + (fr >> 2)) * 64 + (((cw >> 1) ^ fq) & 1) * 32 + (((fr & 3) ^ (fq & 2)) * 8);
        unsigned vd = sbase + (cw >> 2) * 1024 + v16, kd = fq & 1 ? OOB : dst + cw * 512 + (fq >> 1) * 256 + fr * 16;
        asm volatile("" : "+v"(ad), "+v"(vd), "+v"(kd));
        sync_lds();
        STAMP(3);
        {
          const u32x4 ones = u32x4{0x3f803f80u, 0x3f803f80u, 0x3f803f80u, 0x3f803f80u};
          f32x4 c0 = zero;
          bf16x8 a[8], b0[8];
#pragma unroll
          for (int ks = 0; ks < 8; ++ks) {
            u32x2 a0 = trl(ad + ks * 4096), a1 = trl(ad + ks * 4096 + 2048);
            a[ks] = as_frag(u32x4{a0[0], a0[1], a1[0], a1[1]});
            b0[ks] = *(const LDS(bf16x8)*)(vd + ks * 2048);
            SCHED0;
          }
#pragma unroll
          for (int ks = 0; ks < 8; ++ks) c0 = mfma16(a[ks], b0[ks], c0);
          const unsigned p0 = umin(pk2(c0[0], c0[1]), POISON - 1), p1 = umin(pk2(c0[2], c0[3]), POISON - 1);
          const auto q0 = __builtin_amdgcn_permlane16_swap(p0, p0, false, false), q1 = __builtin_amdgcn_permlane16_swap(p1, p1, false, false);
          st16(wsr, kd, u32x4{q0[0], q1[0], q0[1], q1[1]});
          SCHED0;
          if (cw < 4) {
            f32x4 cz = zero;
#pragma unroll
            for (int ks = 0; ks < 8; ++ks) cz = mfma16(a[ks], as_frag(ones), cz);
            st16(wsr, fr ? OOB : dst + 4096 + cw * 64 + fq * 16, clean(__builtin_bit_cast(u32x4, cz)));
          }
        }
        SCHED0;
        STAMP(4);
        bar();
        const f32x4 cq[2] = {*(const f32x4*)(lcl + fq * 4), *(const f32x4*)(lcl + 16 + fq * 4)};
        fold_x(0, lx, cq);
        u32x4 pq[2][2];
        u32x4 zp, zq;
        u32x4 part[4];
        auto ldp = [&](int i) AI { part[i] = ld16s(wsr, vk, rec + i * (64 * KVREC) + cw * 512); };
        auto ldz = [&]() AI {
          zp = ld16(wsr, rec + fq * (32 * KVREC) + 4096 + fr * 16);
          zq = ld16(wsr, rec + (4 + fq) * (32 * KVREC) + 4096 + fr * 16);
        };
#pragma unroll
        for (int i = 0; i < 4; ++i) asm volatile("" : "=v"(part[i]));
        asm volatile("" : "=v"(zp), "=v"(zq));
        phi_map2(lx, rs, pq, [&](float* sp) AI {
          asm volatile("" : "+v"(sp[0]), "+v"(sp[1]));
          SCHED0;
          if (cw >= 4) {
            MEMFENCE;
#pragma unroll
            for (int i = 0; i < 4; ++i) ldp(i);
            MEMFENCE;
          }
          SCHED0;
        });
        asm volatile("" : "+v"(pq[0][0]), "+v"(pq[0][1]), "+v"(pq[1][0]), "+v"(pq[1][1]));
        SCHED0;
        STAMP(5);
        char* kvf = smem + L_A2;
        if (cw < 4) {
          SLEEP(SLP_K);
          MEMFENCE;
          if (cw == 0) ldz();
#pragma unroll
          for (int i = 0; i < 4; ++i) ldp(i);
          MEMFENCE;
        }
        unsigned kw = sbase + L_A2 + v16 + (cw >> 1) * 1024 + (cw & 1) * 8;
        unsigned ka = sbase + L_A2 + v16, za = sbase + L_Z + fq * 16;
        asm volatile("" : "+v"(kw), "+v"(ka), "+v"(za));
        u32x2 kvs;
        auto nans = [&](f32x4 v) AI { return __builtin_amdgcn_ballot_w64(__builtin_isunordered(v[0], v[1])) | __builtin_amdgcn_ballot_w64(__builtin_isunordered(v[2], v[3])); };
        auto sums = [&]() AI {
          unsigned long long nan = 0;
          if (cw == 0) {
            f32x4 z = __builtin_bit_cast(f32x4, zp) + __builtin_bit_cast(f32x4, zq);
            nan = nans(z);
            float z0 = z[0], z1 = z[1], z2 = z[2], z3 = z[3];
            xred4<false>(z0, z1, z2, z3);
            *(u32x2*)(smem + L_Z + (fr >> 3) * 64 + (fr & 3) * 16 + ((fr >> 2) & 1) * 8) = u32x2{pk2(z0, z1), pk2(z2, z3)};
          }
          f32x4 s4 = zero;
#pragma unroll
          for (int i = 0; i < 4; ++i) { s4 = mfma16(as_frag(eye), as_frag(part[i]), s4); SCHED0; }
          kvs = u32x2{pk2(s4[0], s4[1]), pk2(s4[2], s4[3])};
          asm volatile("" : "+v"(kvs));
          return nan | __builtin_amdgcn_ballot_w64(s4[0] != s4[0]);
        };
        if (__builtin_expect(sums() != 0, 0)) {
          for (;;) {
            bool again = false;
            if (cw == 0 && poisoned(umax4(umax4(zp), zq))) { again = true; ldz(); }
#pragma unroll
            for (int i = 0; i < 4; ++i)
              if (poisoned(umax4(part[i]))) { again = true; ldp(i); }
            __builtin_amdgcn_s_waitcnt(0x0f70);
            MEMFENCE;
            if (!again || --budget <= 0) break;
            COUNT(2);
          }
          sums();
        }
        STAMP(6);
        *(LDS(u32x2)*)kw = kvs;
        sync_lds();
        STAMP(7);
        bf16x8 kvr[2][2], zf[2];
#pragma unroll
        for (int s = 0; s < 2; ++s) { zf[s] = *(const LDS(bf16x8)*)(za + s * 64); SCHED0; }
#pragma unroll
        for (int s = 0; s < 2; ++s) {
          kvr[0][s] = *(const LDS(bf16x8)*)(ka + (0 * 2 + s) * 1024);
          kvr[1][s] = *(const LDS(bf16x8)*)(ka + (1 * 2 + s) * 1024);
          SCHED0;
        }
        f32x4 cd[2], c[2][2];
        u32x4 oo[2];
        auto den = [&](int m, int s) AI { cd[m] = mfma16(zf[s], as_frag(pq[m][s]), s ? cd[m] : zero); SCHED0; };
        auto num = [&](int m, int s) AI {
#pragma unroll
          for (int t = 0; t < 2; ++t) { c[m][t] = mfma16(kvr[t][s], as_frag(pq[m][s]), s ? c[m][t] : zero); SCHED0; }
        };
        auto fin = [&](int m, float id) AI { const f32x4 i4 = f32x4{id, id, id, id}; oo[m] = put_o(m, mul4(c[m][0], i4), mul4(c[m][1], i4), 0); SCHED0; };
        den(0, 0); den(1, 0); den(0, 1); den(1, 1);
        num(0, 0); num(0, 1); num(1, 0);
        const float i0 = __builtin_amdgcn_rcpf(cd[0][0] + 1e-6f), i1 = __builtin_amdgcn_rcpf(cd[1][0] + 1e-6f);
        SCHED0;
        num(1, 1);
        fin(0, i0); fin(1, i1);
        hold(oo[0], oo[1]);
        STAMP(8);
        asm volatile("" : "=v"(acc[0][0]), "=v"(acc[0][1]), "=v"(acc[0][2]), "=v"(acc[0][3]), "=v"(acc[0][4]), "=v"(acc[0][5]));
        asm volatile("" : "=v"(acc[1][0]), "=v"(acc[1][1]), "=v"(acc[1][2]), "=v"(acc[1][3]), "=v"(acc[1][4]), "=v"(acc[1][5]));
        asm volatile("" : "=v"(ck[0]), "=v"(ck[1]), "=v"(mv[0]), "=v"(mv[1]), "=v"(xv[0]), "=v"(xv[1]));
        asm volatile("" : "=v"(mean[0]), "=v"(mean[1]), "=v"(rs[0]), "=v"(rs[1]), "=v"(cv[0]), "=v"(cv[1]));
      }
      int kind = layer & 1;
      asm volatile("" : "+s"(kind));
      if (kind) {
        char* ex = scr;
        fold_rows(1, kf, ck); fold_v(false);
        const unsigned hrec = ar + AR_KV + (unsigned)(bm * 32 + head) * KVREC;
        {
          const u32x4 vl = u32x4{vf[1][0][0], vf[1][0][1], vf[1][1][0], vf[1][1][1]};
          if (cw == 7) {
            st16s(wsr, v16, hrec, clean(kf[1]));
            STPAD;
            st16s(wsr, v16 + 1024, hrec, clean(vl));
            STPAD;
          }
          char* e2 = ex + (cw + 1) * 2048;
          *(u32x4*)(e2 + lane * 16) = kf[1];
          *(u32x2*)(e2 + 1024 + lane * 8) = vf[1][0];
          *(u32x2*)(e2 + 1536 + lane * 8) = vf[1][1];
          MEMFENCE;
          lflag[cw] = layer;
          MEMFENCE;
          PRIO(1);
        }
        SCHED0;
        fold_rows(0, qf, xv);
        u32x4 kp;
        u32x4 va0 = __builtin_shufflevector(vf[0][0], vf[0][0], -1, -1, 0, 1), vb0 = __builtin_shufflevector(vf[0][1], vf[0][1], -1, -1, 0, 1);
        auto setp = [&](u32x4& d, u32x2 v) AI { d[0] = v[0]; d[1] = v[1]; };
        auto ldh = [&]() AI {
          MEMFENCE;
          kp = ld16s(wsr, v16, hrec - 32 * KVREC);
          setp(va0, __builtin_amdgcn_raw_buffer_load_b64(wsr, v16 + 1024, hrec - 32 * KVREC, 16));
          MEMFENCE;
          setp(vb0, __builtin_amdgcn_raw_buffer_load_b64(wsr, v16 + 1032, hrec - 32 * KVREC, 16));
          MEMFENCE;
        };
        unsigned fl = 0;
        auto rdl = [&]() AI {
          MEMFENCE;
          fl = lflag[cw - 1];
          MEMFENCE;
          kp = *(const u32x4*)(ex + cw * 2048 + lane * 16);
          setp(va0, *(const u32x2*)(ex + cw * 2048 + 1024 + lane * 8));
          MEMFENCE;
          setp(vb0, *(const u32x2*)(ex + cw * 2048 + 1536 + lane * 8));
          MEMFENCE;
        };
        auto prev = [&]() AI {
          if (cw > 0) {
            do { SLEEP(1); rdl(); __builtin_amdgcn_s_waitcnt(0xc07f); } while (__builtin_amdgcn_readfirstlane(fl) != (unsigned)layer && --budget > 0);
          } else if (bm > 0) {
            SLEEP(SLP_HX);
            do { ldh(); __builtin_amdgcn_s_waitcnt(0x0f70); }
            while (poisoned(umax4(umax4(kp), u32x4{va0[0], va0[1], vb0[0], vb0[1]})) && --budget > 0);
          } else {
            kp = u32x4{0u, 0u, 0u, 0u};
            setp(va0, u32x2{0u, 0u}); setp(vb0, u32x2{0u, 0u});
          }
          asm volatile("" : "+v"(kp), "+v"(va0), "+v"(vb0));
        };
        const float scale = 0.17677669529663687f * LOG2E;
        const bool odd = fr & 1;
        const bool own0 = sel[0] == 0x0c0c0c0cu, own1 = sel[1] == 0x0c0c0c0cu;
        float l[2][2], mx[2], rk[2][2], sum[2];
#pragma unroll
        for (int m = 0; m < 2; ++m) { rk[m][0] = ((const LDS(float)*)ra0)[m * 16]; rk[m][1] = ((const LDS(float)*)ra1)[m * 16]; }
        float qk[2][2];
#pragma unroll
        for (int m = 0; m < 2; ++m) { const float qs = rs[m] * scale; qk[m][0] = qs * rk[m][0]; qk[m][1] = qs * rk[m][1]; }
        u32x4 va1 = u32x4{vf[0][0][0], vf[0][0][1], vf[1][0][0], vf[1][0][1]}, vb1 = u32x4{vf[0][1][0], vf[0][1][1], vf[1][1][0], vf[1][1][1]};
        auto score = [&](int m) AI {
          f32x4 s0 = mfma16(as_frag(m == 0 ? kp : kf[0]), as_frag(qf[m]), zero);
          f32x4 s1 = mfma16(as_frag(kf[m]), as_frag(qf[m]), zero);
          float a = own0 ? (odd ? s1[1] : s1[0]) : (odd ? s0[1] : s0[0]);
          float b = own1 ? (odd ? s1[3] : s1[2]) : (odd ? s0[3] : s0[2]);
          if (m == 0) { l[m][0] = __builtin_fmaf(a, qk[m][0], off0); l[m][1] = __builtin_fmaf(b, qk[m][1], off1); }
          else { l[m][0] = a * qk[m][0]; l[m][1] = b * qk[m][1]; }
          mx[m] = fmx(l[m][0], l[m][1]);
        };
        auto expo = [&](int m) AI {
          l[m][0] = __builtin_amdgcn_exp2f(l[m][0] - mx[m]);
          l[m][1] = __builtin_amdgcn_exp2f(l[m][1] - mx[m]);
          sum[m] = l[m][0] + l[m][1];
        };
        f32x4 ov[2][2];
        auto pv = [&](int m) AI {
          const unsigned pr = pk2(mul1(mul1(l[m][0], rk[m][0]), sum[m]), mul1(mul1(l[m][1], rk[m][1]), sum[m]));
          const u32x4 pf = u32x4{__builtin_amdgcn_perm(pr, pr, sel[0]), __builtin_amdgcn_perm(pr, pr, sel[1]),
                                 __builtin_amdgcn_perm(pr, pr, sel[2]), __builtin_amdgcn_perm(pr, pr, sel[3])};
          ov[m][0] = mfma16(as_frag(m ? va1 : va0), as_frag(pf), zero);
          ov[m][1] = mfma16(as_frag(m ? vb1 : vb0), as_frag(pf), zero);
        };
        asm volatile("" : "+v"(qf[0]), "+v"(qf[1]), "+v"(va0), "+v"(vb0));
        SCHED0;
        if (cw > 0) rdl();
        SCHED0;
        score(1);
        asm volatile("" : "+v"(qf[0]), "+v"(qk[0][0]), "+v"(qk[0][1]), "+v"(mx[1]), "+v"(l[1][0]), "+v"(l[1][1]));
        asm volatile("" : "+v"(va1), "+v"(vb1));
        SCHED0;
        STAMP(3);
        if (__builtin_expect(cw == 0 || __builtin_amdgcn_readfirstlane(fl) != (unsigned)layer, 0)) prev();
        score(0);
        xred2<true>(mx[0], mx[1]);
        expo(0); expo(1);
        xred2<false, true>(sum[0], sum[1]);
        pv(0); pv(1);
        SCHED0;
        const u32x4 o0 = put_o(0, ov[0][0], ov[0][1], 1), o1 = put_o(1, ov[1][0], ov[1][1], 1);
        hold(o0, o1);
        STAMP(4);
      }
    }
    {
      LANE_VARS
      const int mt = bm * 16 + cw * 2 + cm;
      const unsigned so0 = WS_AR + AR_O + xt_off(mt, 0);
      const unsigned opar = layer & 1;
      auto diff = [&](u32x4 v) AI { return ((v[0] ^ opar) | (v[2] ^ opar)) & 1u; };
      auto dif3 = [&](u32x4 v) AI { return __builtin_amdgcn_bitop3_b32(v[0], v[2], opar, 0x7e); };
      auto any = [&](unsigned d) AI { return __builtin_amdgcn_ballot_w64(d != 0) != 0; };
      const int g0 = cm;
      u32x4 a[RO];
      auto refill = [&](auto ic, auto wc, unsigned cur, unsigned nxt) AI {
        constexpr int I = decltype(ic)::value, W = decltype(wc)::value;
        do {
          COUNT(1);
          SLEEP(SLP_R);
          MEMFENCE;
          a[I] = ld16s(wsr, v16, cur + I * 16384);
          bool u[RO];
#pragma unroll
          for (int j = I + 1; j < I + W; ++j) u[j % RO] = any(diff(a[j % RO]));
          SCHED0;
#pragma unroll
          for (int j = I + 1; j < I + W; ++j)
            if (u[j % RO]) a[j % RO] = ld16s(wsr, v16, (j < RO ? cur : nxt) + (j % RO) * 16384);
          __builtin_amdgcn_s_waitcnt(0x0f70);
          MEMFENCE;
        } while (any(diff(a[I]) | diff(a[I + 1])) && --budget > 0);
      };
      const unsigned sf = so0 + g0 * (RO * 16384);
      f32x4 acc[4];
#pragma unroll
      for (int n = 0; n < 4; ++n)
        acc[n] = f32x4{bflo(xr[n >> 1][(n & 1) * 2]), bfhi(xr[n >> 1][(n & 1) * 2]), bflo(xr[n >> 1][(n & 1) * 2 + 1]), bfhi(xr[n >> 1][(n & 1) * 2 + 1])};
      asm volatile("" : "+v"(acc[0]), "+v"(acc[1]), "+v"(acc[2]), "+v"(acc[3]));
      SCHED0;
      const bool late = isB && cw == 0;
      if (late) bar();
      if (cn < 8) { if (late) SLEEP(SLP_O0); else SLEEP(SLP_O); }
      else { if (late) SLEEP(SLP_O0D); else SLEEP(SLP_OD); }
#pragma unroll
      for (int j = 0; j < 4; ++j) { a[j] = ld16s(wsr, v16, sf + j * 16384); SCHED0; }
      if (!late) bar();
      STAMP(9);
      bf16x8 wf[2][4];
      const char* pw0 = smem + L_STG + v16;
#pragma unroll
      for (int n = 0; n < 8; ++n) wf[n >> 2][n & 3] = *(const bf16x8*)(pw0 + g0 * (RO * 4096) + n * 1024);
      SCHED0;
      STAMP(10);
      if (any(diff(a[0]) | diff(a[1]))) refill(IC<0>{}, IC<4>{}, sf, sf);
      SCHED0;
#pragma unroll
      for (int j = 4; j < RO; ++j) { a[j] = ld16s(wsr, v16, sf + j * 16384); SCHED0; }
      STAMP(11);
      bool bad = false;
      auto group = [&](auto lastc, int g) AI {
        constexpr bool LAST = decltype(lastc)::value;
        const int gc = g ^ g0, gn = (g + 1) ^ g0;
        const unsigned sc = so0 + gc * (RO * 16384), so = so0 + gn * (RO * 16384);
        const char* pc = pw0 + gc * (RO * 4096);
        const char* pn = pw0 + gn * (RO * 4096);
        static_for<RO>([&](auto ic) AI {
          constexpr int i = decltype(ic)::value;
          SCHED0;
          if constexpr (!LAST && i == 0) { if (g == 0) PRIO(3); else if (g == 2) PRIO(2); }
          if constexpr (LAST && i == 3) PRIO(1);
          if constexpr (i % 2 == 0)
            if (__builtin_expect(bad, 0)) refill(ic, IC<(LAST && i + 4 > RO) ? RO - i : 4>{}, sc, so);
          SCHED0;
          unsigned mx = 0, my = 0;
          static_for<4>([&](auto nc) AI {
            constexpr int n = decltype(nc)::value;
            mfma_acc(acc[n], wf[i & 1][n], as_frag(a[i]));
            SCHED0;
            if constexpr (i + 2 < RO) wf[i & 1][n] = *(const bf16x8*)(pc + (i + 2) * 4096 + n * 1024);
            else if constexpr (!LAST) wf[i & 1][n] = *(const bf16x8*)(pn + (i + 2 - RO) * 4096 + n * 1024);
            if constexpr (!(LAST && i == RO - 1) && (i & 1)) {
              if constexpr (n == 0) mx = dif3(a[(i + 1) % RO]);
              if constexpr (n == 1) my = dif3(a[(i + 2) % RO]);
              if constexpr (n == 2) bad = any((mx | my) & 1u);
            }
            if constexpr (n == 3 && !LAST) a[i] = ld16s(wsr, v16, so + i * 16384);
            SCHED0;
          });
        });
      };
#pragma unroll 1
      for (int g = 0; g < 3; ++g) group(std::false_type{}, g);
      group(std::true_type{}, 3);
      mfma_done();
      STAMP(12);
#pragma unroll
      for (int j = 0; j < 2; ++j)
#pragma unroll
        for (int e = 0; e < 4; ++e) xr[j][e] = pk2(acc[j * 2 + (e >> 1)][(e & 1) * 2], acc[j * 2 + (e >> 1)][(e & 1) * 2 + 1]);
      if (layer == 3) {
        u32x4 yo[2];
#pragma unroll
        for (int j = 0; j < 2; ++j) {
          auto u = __builtin_amdgcn_permlane16_swap(xr[j][0], xr[j][2], false, false);
          auto v = __builtin_amdgcn_permlane16_swap(xr[j][1], xr[j][3], false, false);
          yo[j] = u32x4{u[0], v[0], u[1], v[1]};
        }
        const unsigned vo = fr * 2048 + (fq & 1) * 32 + (fq >> 1) * 16;
        __builtin_amdgcn_raw_buffer_store_b128(yo[0], mk_rsrc(p.out, 4 * MBY), vo, mt * 32768 + cn * 128, 18);
        __builtin_amdgcn_raw_buffer_store_b128(yo[1], mk_rsrc(p.out, 4 * MBY), vo + 64, mt * 32768 + cn * 128, 18);
        STAMP(13);
        STAMP(14);
        STAMP_DUMP;
        __builtin_amdgcn_endpgm();
      }
      const u32x2 stat = put_x(xr, v16, ar + ARENA + AR_X + xt_off(mt, cn * 2));
      __builtin_amdgcn_raw_buffer_store_b64(stat, wsr, fq ? OOB : fr * 8, ar + ARENA + AR_ST + (mt * 16 + cn) * 128, 16);
      MEMFENCE;
      STAMP(14);
    }
  }
}

extern "C" void kernel_launch(void* const* d_in, const int* in_sizes, int n_in, void* d_out, int out_size,
                              void* d_ws, size_t ws_size, hipStream_t stream) {
  (void)hipFuncSetAttribute((const void*)mega_kernel, hipFuncAttributeMaxDynamicSharedMemorySize, MEGA_LDS);
  MegaParams m;
  m.ws = (char*)d_ws; m.out = (u16*)d_out; m.x = (const u16*)d_in[0]; m.wphi = (const u16*)d_in[2];
  m.wqkvA = (const u16*)d_in[1]; m.wqkvB = (const u16*)d_in[5];
  m.woA = (const u16*)d_in[3]; m.woB = (const u16*)d_in[6];
  mega_kernel<<<256, 768, MEGA_LDS, stream>>>(m);
}
```
